```python
import torch

def _softmax(x, axis=-1):
    return torch.softmax(x, dim=axis)
def _gelu(x):
    return torch.nn.functional.gelu(x, approximate="tanh")
def _silu(x):
    return torch.nn.functional.silu(x)
def _sigmoid(x):
    return torch.sigmoid(x)
def _layernorm(x, axis=-1, eps=1e-5):
    m = x.mean(dim=axis, keepdim=True)
    v = x.var(dim=axis, keepdim=True, unbiased=False)
    return (x - m) / torch.sqrt(v + eps)
def _scatter_add(tgt, idx, src, axis=0):
    return tgt.clone().index_add_(axis, idx, src)
def _segment_sum(src, seg, n):
    out = torch.zeros((n,) + src.shape[1:], dtype=src.dtype)
    return out.index_add_(0, seg, src)
def _pad(x, pw):
    flat = tuple(v for pair in reversed(pw) for v in pair)
    return torch.nn.functional.pad(x, flat)

kernel_name = 'concat_heads__compressed_kv_cla__ssm_conv_s5__prompt'

def setup_inputs():

    _w = (lambda a: (lambda t: t.to(torch.bfloat16) if t.is_floating_point() else t)(torch.as_tensor(a)))
    def _A_setup():
        B, H, Sq, Sk, D, M = 1, 32, 2048, 2048, 128, 512
        Dc = 4 * D
        return {
            "x": _w((lambda *s: torch.randn(*s, dtype=torch.bfloat16) * 3.2)(B, Sq, M)),
            "c_kv": _w((lambda *s: torch.randn(*s, dtype=torch.bfloat16) * 0.1)(B, Sk, Dc)),
            "w_q": _w((lambda *s: torch.randn(*s, dtype=torch.bfloat16) * 6.4)(M, H, D) * (M ** -0.5)),
            "w_uk": _w((lambda *s: torch.randn(*s, dtype=torch.bfloat16) * 6.4)(Dc, H, D) * (Dc ** -0.5)),
            "w_uv": _w((lambda *s: torch.randn(*s, dtype=torch.bfloat16) * 0.1)(Dc, H, D) * (Dc ** -0.5)),
            "w_o": _w((lambda *s: torch.randn(*s, dtype=torch.bfloat16) * 0.1)(H, D, M) * ((H * D) ** -0.5)),
        }
    _A_in = _A_setup()

    def _B_setup():
        B, S, D = 1, 2048, 512
        P = D
        Lambda_re = -0.5 * (lambda _x: torch.exp(torch.as_tensor(_x)))((lambda *s: torch.randn(*s, dtype=torch.bfloat16) * 0.2)(P))
        Lambda_im = (lambda *s: torch.randn(*s, dtype=torch.bfloat16) * 0.4)(P) * 1.0
        B_re = (lambda *s: torch.randn(*s, dtype=torch.bfloat16) * 0.2)(P, D) * (D ** -0.5)
        B_im = (lambda *s: torch.randn(*s, dtype=torch.bfloat16) * 0.2)(P, D) * (D ** -0.5)
        C_re = (lambda *s: torch.randn(*s, dtype=torch.bfloat16) * 0.2)(D, P) * (P ** -0.5)
        C_im = (lambda *s: torch.randn(*s, dtype=torch.bfloat16) * 0.2)(D, P) * (P ** -0.5)
        log_dt = (lambda *s: torch.randn(*s, dtype=torch.bfloat16) * 0.2)(P) * 0.3 - 2.0
        Dskip = (lambda *s: torch.randn(*s, dtype=torch.bfloat16) * 0.05)(D)
        return {
            "x": _w((lambda *s: torch.randn(*s, dtype=torch.bfloat16) * 0.05)(B, S, D)),
            "Lambda_re": _w(Lambda_re),
            "Lambda_im": _w(Lambda_im),
            "B_re": _w(B_re),
            "B_im": _w(B_im),
            "C_re": _w(C_re),
            "C_im": _w(C_im),
            "log_dt": _w(log_dt),
            "Dskip": _w(Dskip),
        }
    _B_in = _B_setup()

    return {
        "A_x": _A_in["x"],
        "A_c_kv": _A_in["c_kv"],
        "A_w_q": _A_in["w_q"],
        "A_w_uk": _A_in["w_uk"],
        "A_w_uv": _A_in["w_uv"],
        "A_w_o": _A_in["w_o"],
        "B_x": _B_in["x"],
        "B_Lambda_re": _B_in["Lambda_re"],
        "B_Lambda_im": _B_in["Lambda_im"],
        "B_B_re": _B_in["B_re"],
        "B_B_im": _B_in["B_im"],
        "B_C_re": _B_in["C_re"],
        "B_C_im": _B_in["C_im"],
        "B_log_dt": _B_in["log_dt"],
        "B_Dskip": _B_in["Dskip"],
    }

def reference(A_x, A_c_kv, A_w_q, A_w_uk, A_w_uv, A_w_o, B_x, B_Lambda_re, B_Lambda_im, B_B_re, B_B_im, B_C_re, B_C_im, B_log_dt, B_Dskip):
    def _A_ref(x, c_kv, w_q, w_uk, w_uv, w_o):
        B, Sq, M = x.shape[0], x.shape[1], x.shape[2]
        Sk = c_kv.shape[1]
        H, D = w_q.shape[1], w_q.shape[2]
        q = torch.einsum("bqm,mhd->bhqd", x, w_q)
        k = torch.einsum("bkc,chd->bhkd", c_kv, w_uk)
        v = torch.einsum("bkc,chd->bhkd", c_kv, w_uv)
        scores = torch.einsum("bhqd,bhkd->bhqk", q, k) * (D ** -0.5)
        off = Sk - Sq
        qi = torch.arange(Sq)[:, None]
        kj = torch.arange(Sk)[None, :]
        cmask = kj <= (qi + off)
        scores = torch.where(cmask[None, None], scores, -1e9)
        p = _softmax(scores, axis=-1)
        ctx = torch.einsum("bhqk,bhkd->bhqd", p, v)
        return torch.einsum("bhqd,hdm->bqm", ctx, w_o)

    def _B_ref(x, Lambda_re, Lambda_im, B_re, B_im, C_re, C_im, log_dt, Dskip):
        Bsz, L, H = x.shape
        Lam = Lambda_re + 1j * Lambda_im
        Bmat = B_re + 1j * B_im
        Cmat = C_re + 1j * C_im
        dt = torch.exp(log_dt)
        Lam_bar = torch.exp(dt * Lam)
        B_bar = ((Lam_bar - 1.0) / Lam)[:, None] * Bmat
        Bu = torch.einsum("bld,pd->blp", x + 0j, B_bar)
        h = torch.zeros_like(Bu[:, 0, :])
        hs = []
        for t in range(L):
            h = Lam_bar[None, :] * h + Bu[:, t, :]
            hs.append(h)
        hseq = torch.stack(hs, 1)
        y = 2.0 * torch.einsum("blp,dp->bld", hseq, Cmat).real + x * Dskip[None, None, :]
        return y

    a = _A_ref(A_x, A_c_kv, A_w_q, A_w_uk, A_w_uv, A_w_o)
    b = _B_ref(B_x, B_Lambda_re, B_Lambda_im, B_B_re, B_B_im, B_C_re, B_C_im, B_log_dt, B_Dskip)
    return torch.concatenate([a, b], axis=-1)
```

```cpp
#include <hip/hip_runtime.h>
#include <hip/hip_bf16.h>
#include <stdint.h>
typedef unsigned short u16;
__device__ __forceinline__ float bf2f(u16 v) { return __uint_as_float(((unsigned)v) << 16); }
__device__ __forceinline__ u16 f2bf(float f) { unsigned u = __float_as_uint(f); u += 0x7fffu + ((u >> 16) & 1u); return (u16)(u >> 16); }

__global__ void gemm_nn(const u16* A, const u16* W, u16* C, int M, int N, int K) {
  int n = blockIdx.x * 256 + threadIdx.x, m = blockIdx.y;
  float acc = 0;
  for (int k = 0; k < K; ++k) acc += bf2f(A[m * K + k]) * bf2f(W[(size_t)k * N + n]);
  C[(size_t)m * N + n] = f2bf(acc);
}
__global__ void gemm_nn_f32out(const u16* A, const u16* W, float* C, int M, int N, int K, int ldc) {
  int n = blockIdx.x * 256 + threadIdx.x, m = blockIdx.y;
  float acc = 0;
  for (int k = 0; k < K; ++k) acc += bf2f(A[(size_t)m * K + k]) * bf2f(W[(size_t)k * N + n]);
  C[(size_t)m * ldc + n] = acc;
}
__global__ void attn_naive(const u16* Q, const u16* Kk, const u16* V, u16* ctx) {
  __shared__ float sc[2048];
  __shared__ float qs[128];
  __shared__ float red[128];
  int q = blockIdx.x, h = blockIdx.y, t = threadIdx.x;
  qs[t] = bf2f(Q[(size_t)q * 4096 + h * 128 + t]);
  __syncthreads();
  float mx = -1e30f;
  for (int k = t; k <= q; k += 128) {
    float s = 0;
    for (int d = 0; d < 128; ++d) s += qs[d] * bf2f(Kk[(size_t)k * 4096 + h * 128 + d]);
    s *= 0.08838834764831845f;
    sc[k] = s; mx = fmaxf(mx, s);
  }
  red[t] = mx; __syncthreads();
  for (int o = 64; o > 0; o >>= 1) { if (t < o) red[t] = fmaxf(red[t], red[t + o]); __syncthreads(); }
  mx = red[0]; __syncthreads();
  float sum = 0;
  for (int k = t; k <= q; k += 128) { float p = __expf(sc[k] - mx); sc[k] = p; sum += p; }
  red[t] = sum; __syncthreads();
  for (int o = 64; o > 0; o >>= 1) { if (t < o) red[t] += red[t + o]; __syncthreads(); }
  sum = red[0];
  float acc = 0;
  for (int k = 0; k <= q; ++k) acc += sc[k] * bf2f(V[(size_t)k * 4096 + h * 128 + t]);
  ctx[(size_t)q * 4096 + h * 128 + t] = f2bf(acc / sum);
}
__global__ void ssm_bu(const u16* x, const u16* Bre, const u16* Bim, float* bure, float* buim) {
  int p = blockIdx.x * 256 + threadIdx.x, l = blockIdx.y;
  float ar = 0, ai = 0;
  for (int d = 0; d < 512; ++d) { float xv = bf2f(x[l * 512 + d]); ar += xv * bf2f(Bre[p * 512 + d]); ai += xv * bf2f(Bim[p * 512 + d]); }
  bure[l * 512 + p] = ar; buim[l * 512 + p] = ai;
}
__global__ void ssm_scan(const u16* Lre, const u16* Lim, const u16* logdt, float* bure, float* buim) {
  int p = blockIdx.x * 64 + threadIdx.x;
  float lr = bf2f(Lre[p]), li = bf2f(Lim[p]), dt = expf(bf2f(logdt[p]));
  float e = expf(dt * lr), br = e * cosf(dt * li), bi = e * sinf(dt * li);
  float nr = br - 1.f, ni = bi, den = lr * lr + li * li;
  float cr = (nr * lr + ni * li) / den, ci = (ni * lr - nr * li) / den;
  float hr = 0, hi = 0;
  for (int l = 0; l < 2048; ++l) {
    float ur = bure[l * 512 + p], ui = buim[l * 512 + p];
    float vr = cr * ur - ci * ui, vi = cr * ui + ci * ur;
    float nhr = br * hr - bi * hi + vr, nhi = br * hi + bi * hr + vi;
    hr = nhr; hi = nhi;
    bure[l * 512 + p] = hr; buim[l * 512 + p] = hi;
  }
}
__global__ void ssm_y(const u16* x, const u16* Cre, const u16* Cim, const u16* Dsk, const float* hre, const float* him, float* out) {
  int d = blockIdx.x * 256 + threadIdx.x, l = blockIdx.y;
  float acc = 0;
  for (int p = 0; p < 512; ++p) acc += hre[l * 512 + p] * bf2f(Cre[d * 512 + p]) - him[l * 512 + p] * bf2f(Cim[d * 512 + p]);
  out[(size_t)l * 1024 + 512 + d] = 2.f * acc + bf2f(x[l * 512 + d]) * bf2f(Dsk[d]);
}
extern "C" void kernel_launch(void* const* d_in, const int* in_sizes, int n_in, void* d_out, int out_size, void* d_ws, size_t ws_size, hipStream_t stream) {
  const u16* Ax = (const u16*)d_in[0]; const u16* ckv = (const u16*)d_in[1]; const u16* wq = (const u16*)d_in[2];
  const u16* wuk = (const u16*)d_in[3]; const u16* wuv = (const u16*)d_in[4]; const u16* wo = (const u16*)d_in[5];
  const u16* Bx = (const u16*)d_in[6]; const u16* Lre = (const u16*)d_in[7]; const u16* Lim = (const u16*)d_in[8];
  const u16* Bre = (const u16*)d_in[9]; const u16* Bim = (const u16*)d_in[10]; const u16* Cre = (const u16*)d_in[11];
  const u16* Cim = (const u16*)d_in[12]; const u16* logdt = (const u16*)d_in[13]; const u16* Dsk = (const u16*)d_in[14];
  float* out = (float*)d_out;
  char* ws = (char*)d_ws;
  u16* Q = (u16*)ws; u16* K = (u16*)(ws + (16 << 20)); u16* V = (u16*)(ws + (32 << 20)); u16* ctx = (u16*)(ws + (48 << 20));
  float* bure = (float*)(ws + (64 << 20)); float* buim = (float*)(ws + (72 << 20));
  gemm_nn<<<dim3(16, 2048), 256, 0, stream>>>(Ax, wq, Q, 2048, 4096, 512);
  gemm_nn<<<dim3(16, 2048), 256, 0, stream>>>(ckv, wuk, K, 2048, 4096, 512);
  gemm_nn<<<dim3(16, 2048), 256, 0, stream>>>(ckv, wuv, V, 2048, 4096, 512);
  attn_naive<<<dim3(2048, 32), 128, 0, stream>>>(Q, K, V, ctx);
  gemm_nn_f32out<<<dim3(2, 2048), 256, 0, stream>>>(ctx, wo, out, 2048, 512, 4096, 1024);
  ssm_bu<<<dim3(2, 2048), 256, 0, stream>>>(Bx, Bre, Bim, bure, buim);
  ssm_scan<<<8, 64, 0, stream>>>(Lre, Lim, logdt, bure, buim);
  ssm_y<<<dim3(2, 2048), 256, 0, stream>>>(Bx, Cre, Cim, Dsk, bure, buim, out);
}
```

```cpp
#include <hip/hip_runtime.h>
#include <stdint.h>
typedef unsigned short u16;
typedef short bf16x8 __attribute__((ext_vector_type(8)));
typedef float f32x4 __attribute__((ext_vector_type(4)));
typedef float f32x2 __attribute__((ext_vector_type(2)));
typedef float f32x16 __attribute__((ext_vector_type(16)));
typedef float f32x8 __attribute__((ext_vector_type(8)));
typedef unsigned u32x4 __attribute__((ext_vector_type(4)));
typedef unsigned u32x2 __attribute__((ext_vector_type(2)));
typedef __bf16 bf16x2v __attribute__((ext_vector_type(2)));
#define GAS __attribute__((address_space(1)))
struct Params { const void* p[18]; };
constexpr unsigned WS_E = 131072u;
__device__ __forceinline__ float bf2f(u16 v) { return __uint_as_float(((unsigned)v) << 16); }
__device__ __forceinline__ unsigned cvtpk(float lo, float hi) {
  f32x2 v = {lo, hi}; bf16x2v b = __builtin_convertvector(v, bf16x2v); return __builtin_bit_cast(unsigned, b);
}
__device__ __forceinline__ int lane_id() { unsigned l; asm volatile("v_mbcnt_lo_u32_b32 %0, -1, 0\n\tv_mbcnt_hi_u32_b32 %0, -1, %0" : "=v"(l)); return (int)l; }
__device__ __forceinline__ void cpow(float ar, float ai, float n, float& re, float& im) {
  const float e = __expf(n * ar);
  float rev = n * ai * 0.15915494309189535f; rev -= floorf(rev);
  re = e * __builtin_amdgcn_cosf(rev); im = e * __builtin_amdgcn_sinf(rev);
}

__global__ __launch_bounds__(256, 1) void mega_kernel(Params prm) {
  const int wid = __builtin_amdgcn_readfirstlane(threadIdx.x >> 6);
  const int b = blockIdx.x;
  auto ka = __builtin_amdgcn_kernarg_segment_ptr();
  char* ws = (char*)prm.p[16];
  const int chunk = 4 * (b & 7) + ((b >> 3) & 3), pg = b >> 5;
  f32x16 hr, hi; f32x2 cc; f32x8 cn;
  u32x4 par;
  asm volatile(
"s_mov_b64 s[36:37], %3\n\t"
"s_mov_b32 s38, %4\n\t"
"s_mov_b32 s39, %5\n\t"
"s_load_dwordx2 s[42:43], s[36:37], 128\n\t"
"s_and_b32 s40, s38, 7\n\t"
"s_lshl_b32 s40, s40, 2\n\t"
"s_lshr_b32 s41, s38, 6\n\t"
"s_add_u32 s40, s40, s41\n\t"
"s_lshr_b32 s41, s38, 3\n\t"
"s_and_b32 s41, s41, 7\n\t"
"s_and_b32 s44, s39, 1\n\t"
"s_lshr_b32 s45, s39, 1\n\t"
"s_and_b32 s46, s38, 7\n\t"
"s_lshl_b32 s46, s46, 2\n\t"
"s_and_b32 s47, s41, 3\n\t"
"s_add_u32 s46, s46, s47\n\t"
"s_lshr_b32 s47, s38, 5\n\t"
"s_waitcnt lgkmcnt(0)\n\t"
"s_load_dwordx2 s[48:49], s[36:37], 8\n\t"
"s_load_dwordx2 s[56:57], s[36:37], 24\n\t"
"s_load_dwordx2 s[52:53], s[36:37], 48\n\t"
"s_load_dwordx2 s[60:61], s[36:37], 72\n\t"
"s_load_dwordx2 s[64:65], s[36:37], 80\n\t"
"v_mbcnt_lo_u32_b32 v15, -1, 0\n\t"
"v_mbcnt_hi_u32_b32 v15, -1, v15\n\t"
"v_lshlrev_b32 v14, 4, v15\n\t"
"v_lshrrev_b32 v16, 3, v15\n\t"
"v_lshrrev_b32 v17, 4, v15\n\t"
"v_and_b32 v18, 7, v15\n\t"
"v_xor_b32 v19, v18, v17\n\t"
"v_lshlrev_b32 v19, 4, v19\n\t"
"v_lshl_add_u32 v6, v16, 10, v19\n\t"
"v_lshrrev_b32 v20, 5, v15\n\t"
"v_lshlrev_b32 v20, 4, v20\n\t"
"v_and_b32 v21, 3, v16\n\t"
"v_add_u32 v20, v20, v21\n\t"
"v_lshl_add_u32 v24, v20, 10, v19\n\t"
"v_xor_b32 v19, v18, v17\n\t"
"v_xor_b32 v19, 4, v19\n\t"
"v_lshlrev_b32 v19, 4, v19\n\t"
"v_lshl_add_u32 v7, v16, 10, v19\n\t"
"v_add_u32 v7, 7168, v7\n\t"
"v_lshrrev_b32 v20, 5, v15\n\t"
"v_lshlrev_b32 v20, 4, v20\n\t"
"v_and_b32 v21, 3, v16\n\t"
"v_add_u32 v20, v20, v21\n\t"
"v_lshl_add_u32 v25, v20, 10, v19\n\t"
"v_add_u32 v25, 31744, v25\n\t"
"v_xor_b32 v19, v18, v17\n\t"
"v_lshlrev_b32 v19, 4, v19\n\t"
"v_lshl_add_u32 v8, v16, 10, v19\n\t"
"v_add_u32 v8, 14336, v8\n\t"
"v_xor_b32 v19, v18, v17\n\t"
"v_xor_b32 v19, 4, v19\n\t"
"v_lshlrev_b32 v19, 4, v19\n\t"
"v_lshl_add_u32 v9, v16, 10, v19\n\t"
"v_add_u32 v9, 21504, v9\n\t"
"v_bfe_u32 v19, v15, 1, 3\n\t"
"v_lshlrev_b32 v20, 1, v17\n\t"
"v_xor_b32 v19, v19, v20\n\t"
"v_and_b32 v20, 1, v15\n\t"
"v_lshlrev_b32 v20, 4, v20\n\t"
"v_lshl_add_u32 v20, v17, 13, v20\n\t"
"v_xor_b32 v21, 0, v19\n\t"
"v_lshl_add_u32 v10, v21, 5, v20\n\t"
"v_xor_b32 v21, 0, v19\n\t"
"v_lshl_add_u32 v11, v21, 5, v20\n\t"
"v_add_u32 v11, 31744, v11\n\t"
"v_xor_b32 v21, 1, v19\n\t"
"v_lshl_add_u32 v12, v21, 5, v20\n\t"
"v_add_u32 v12, 63488, v12\n\t"
"v_xor_b32 v21, 1, v19\n\t"
"v_lshl_add_u32 v13, v21, 5, v20\n\t"
"v_add_u32 v13, 95232, v13\n\t"
"s_waitcnt lgkmcnt(0)\n\t"
"s_and_b32 s49, s49, 0xffff\n\t"
"s_mov_b32 s50, 2097152\n\t"
"s_mov_b32 s51, 0x00020000\n\t"
"s_and_b32 s53, s53, 0xffff\n\t"
"s_mov_b32 s54, 2097152\n\t"
"s_mov_b32 s55, 0x00020000\n\t"
"s_lshl_b32 s81, s40, 8\n\t"
"s_add_u32 s56, s56, s81\n\t"
"s_addc_u32 s57, s57, 0\n\t"
"s_and_b32 s57, s57, 0xffff\n\t"
"s_mov_b32 s58, 4186368\n\t"
"s_mov_b32 s59, 0x00020000\n\t"
"s_and_b32 s61, s61, 0xffff\n\t"
"s_mov_b32 s62, 524288\n\t"
"s_mov_b32 s63, 0x00020000\n\t"
"s_and_b32 s65, s65, 0xffff\n\t"
"s_mov_b32 s66, 524288\n\t"
"s_mov_b32 s67, 0x00020000\n\t"
"s_lshl_b32 s81, s41, 18\n\t"
"s_lshl_b32 s82, s39, 16\n\t"
"s_add_u32 s76, s81, s82\n\t"
"s_lshl_b32 s79, s39, 13\n\t"
"s_lshl_b32 s78, s39, 17\n\t"
"s_lshl_b32 s80, s39, 12\n\t"
"s_add_u32 s80, s80, 32768\n\t"
"s_lshl_b32 s81, s46, 16\n\t"
"s_lshl_b32 s82, s39, 12\n\t"
"s_add_u32 s77, s81, s82\n\t"
"s_lshl_b32 s81, s47, 16\n\t"
"s_lshl_b32 s82, s39, 14\n\t"
"s_add_u32 s84, s81, s82\n\t"
"s_lshl_b32 s85, s39, 11\n\t"
"s_cmp_lg_u32 s45, 0\n\t"
"s_cbranch_scc1 LS1P1_%=\n\t"
"s_add_u32 m0, s85, 0\n\t"
"s_add_u32 s81, s77, 0\n\t"
"buffer_load_dwordx4 v24, s[52:55], s81 offen offset:0 lds\n\t"
"buffer_load_dwordx4 v25, s[52:55], s81 offen offset:1024 lds\n\t"
"s_add_u32 m0, s85, 8192\n\t"
"s_add_u32 s81, s84, 0\n\t"
"buffer_load_dwordx4 v6, s[60:63], s81 offen offset:0 lds\n\t"
"buffer_load_dwordx4 v7, s[60:63], s81 offen offset:1024 lds\n\t"
"s_add_u32 m0, s85, 16384\n\t"
"s_add_u32 s81, s84, 0\n\t"
"buffer_load_dwordx4 v6, s[64:67], s81 offen offset:0 lds\n\t"
"buffer_load_dwordx4 v7, s[64:67], s81 offen offset:1024 lds\n\t"
"s_add_u32 m0, s85, 24576\n\t"
"s_add_u32 s81, s77, 128\n\t"
"buffer_load_dwordx4 v24, s[52:55], s81 offen offset:0 lds\n\t"
"buffer_load_dwordx4 v25, s[52:55], s81 offen offset:1024 lds\n\t"
"s_add_u32 m0, s85, 32768\n\t"
"s_add_u32 s81, s84, 128\n\t"
"buffer_load_dwordx4 v6, s[60:63], s81 offen offset:0 lds\n\t"
"buffer_load_dwordx4 v7, s[60:63], s81 offen offset:1024 lds\n\t"
"s_add_u32 m0, s85, 40960\n\t"
"s_add_u32 s81, s84, 128\n\t"
"buffer_load_dwordx4 v6, s[64:67], s81 offen offset:0 lds\n\t"
"buffer_load_dwordx4 v7, s[64:67], s81 offen offset:1024 lds\n\t"
"s_add_u32 m0, s85, 4096\n\t"
"s_add_u32 s81, s77, 8192\n\t"
"buffer_load_dwordx4 v24, s[52:55], s81 offen offset:0 lds\n\t"
"buffer_load_dwordx4 v25, s[52:55], s81 offen offset:1024 lds\n\t"
"s_add_u32 m0, s85, 12288\n\t"
"s_add_u32 s81, s84, 32768\n\t"
"buffer_load_dwordx4 v6, s[60:63], s81 offen offset:0 lds\n\t"
"buffer_load_dwordx4 v7, s[60:63], s81 offen offset:1024 lds\n\t"
"s_add_u32 m0, s85, 20480\n\t"
"s_add_u32 s81, s84, 32768\n\t"
"buffer_load_dwordx4 v6, s[64:67], s81 offen offset:0 lds\n\t"
"buffer_load_dwordx4 v7, s[64:67], s81 offen offset:1024 lds\n\t"
"s_add_u32 m0, s85, 28672\n\t"
"s_add_u32 s81, s77, 8320\n\t"
"buffer_load_dwordx4 v24, s[52:55], s81 offen offset:0 lds\n\t"
"buffer_load_dwordx4 v25, s[52:55], s81 offen offset:1024 lds\n\t"
"s_add_u32 m0, s85, 36864\n\t"
"s_add_u32 s81, s84, 32896\n\t"
"buffer_load_dwordx4 v6, s[60:63], s81 offen offset:0 lds\n\t"
"buffer_load_dwordx4 v7, s[60:63], s81 offen offset:1024 lds\n\t"
"s_add_u32 m0, s85, 45056\n\t"
"s_add_u32 s81, s84, 32896\n\t"
"buffer_load_dwordx4 v6, s[64:67], s81 offen offset:0 lds\n\t"
"buffer_load_dwordx4 v7, s[64:67], s81 offen offset:1024 lds\n\t"
"s_add_u32 m0, s85, 49152\n\t"
"s_add_u32 s81, s77, 256\n\t"
"buffer_load_dwordx4 v24, s[52:55], s81 offen offset:0 lds\n\t"
"buffer_load_dwordx4 v25, s[52:55], s81 offen offset:1024 lds\n\t"
"s_add_u32 m0, s85, 57344\n\t"
"s_add_u32 s81, s84, 256\n\t"
"buffer_load_dwordx4 v6, s[60:63], s81 offen offset:0 lds\n\t"
"buffer_load_dwordx4 v7, s[60:63], s81 offen offset:1024 lds\n\t"
"s_add_u32 m0, s85, 65536\n\t"
"s_add_u32 s81, s84, 256\n\t"
"buffer_load_dwordx4 v6, s[64:67], s81 offen offset:0 lds\n\t"
"buffer_load_dwordx4 v7, s[64:67], s81 offen offset:1024 lds\n\t"
"s_add_u32 m0, s85, 73728\n\t"
"s_add_u32 s81, s77, 384\n\t"
"buffer_load_dwordx4 v24, s[52:55], s81 offen offset:0 lds\n\t"
"buffer_load_dwordx4 v25, s[52:55], s81 offen offset:1024 lds\n\t"
"s_add_u32 m0, s85, 81920\n\t"
"s_add_u32 s81, s84, 384\n\t"
"buffer_load_dwordx4 v6, s[60:63], s81 offen offset:0 lds\n\t"
"buffer_load_dwordx4 v7, s[60:63], s81 offen offset:1024 lds\n\t"
"s_add_u32 m0, s85, 90112\n\t"
"s_add_u32 s81, s84, 384\n\t"
"buffer_load_dwordx4 v6, s[64:67], s81 offen offset:0 lds\n\t"
"buffer_load_dwordx4 v7, s[64:67], s81 offen offset:1024 lds\n\t"
"s_add_u32 m0, s85, 53248\n\t"
"s_add_u32 s81, s77, 8448\n\t"
"buffer_load_dwordx4 v24, s[52:55], s81 offen offset:0 lds\n\t"
"buffer_load_dwordx4 v25, s[52:55], s81 offen offset:1024 lds\n\t"
"s_add_u32 m0, s85, 61440\n\t"
"s_add_u32 s81, s84, 33024\n\t"
"buffer_load_dwordx4 v6, s[60:63], s81 offen offset:0 lds\n\t"
"buffer_load_dwordx4 v7, s[60:63], s81 offen offset:1024 lds\n\t"
"s_add_u32 m0, s85, 69632\n\t"
"s_add_u32 s81, s84, 33024\n\t"
"buffer_load_dwordx4 v6, s[64:67], s81 offen offset:0 lds\n\t"
"buffer_load_dwordx4 v7, s[64:67], s81 offen offset:1024 lds\n\t"
"s_add_u32 m0, s85, 77824\n\t"
"s_add_u32 s81, s77, 8576\n\t"
"buffer_load_dwordx4 v24, s[52:55], s81 offen offset:0 lds\n\t"
"buffer_load_dwordx4 v25, s[52:55], s81 offen offset:1024 lds\n\t"
"s_add_u32 m0, s85, 86016\n\t"
"s_add_u32 s81, s84, 33152\n\t"
"buffer_load_dwordx4 v6, s[60:63], s81 offen offset:0 lds\n\t"
"buffer_load_dwordx4 v7, s[60:63], s81 offen offset:1024 lds\n\t"
"s_add_u32 m0, s85, 94208\n\t"
"s_add_u32 s81, s84, 33152\n\t"
"buffer_load_dwordx4 v6, s[64:67], s81 offen offset:0 lds\n\t"
"buffer_load_dwordx4 v7, s[64:67], s81 offen offset:1024 lds\n\t"
"s_add_u32 m0, s85, 98304\n\t"
"s_add_u32 s81, s77, 512\n\t"
"buffer_load_dwordx4 v24, s[52:55], s81 offen offset:0 lds\n\t"
"buffer_load_dwordx4 v25, s[52:55], s81 offen offset:1024 lds\n\t"
"s_add_u32 m0, s85, 106496\n\t"
"s_add_u32 s81, s84, 512\n\t"
"buffer_load_dwordx4 v6, s[60:63], s81 offen offset:0 lds\n\t"
"buffer_load_dwordx4 v7, s[60:63], s81 offen offset:1024 lds\n\t"
"s_add_u32 m0, s85, 114688\n\t"
"s_add_u32 s81, s84, 512\n\t"
"buffer_load_dwordx4 v6, s[64:67], s81 offen offset:0 lds\n\t"
"buffer_load_dwordx4 v7, s[64:67], s81 offen offset:1024 lds\n\t"
"s_add_u32 m0, s85, 122880\n\t"
"s_add_u32 s81, s77, 640\n\t"
"buffer_load_dwordx4 v24, s[52:55], s81 offen offset:0 lds\n\t"
"buffer_load_dwordx4 v25, s[52:55], s81 offen offset:1024 lds\n\t"
"s_add_u32 m0, s85, 131072\n\t"
"s_add_u32 s81, s84, 640\n\t"
"buffer_load_dwordx4 v6, s[60:63], s81 offen offset:0 lds\n\t"
"buffer_load_dwordx4 v7, s[60:63], s81 offen offset:1024 lds\n\t"
"s_add_u32 m0, s85, 139264\n\t"
"s_add_u32 s81, s84, 640\n\t"
"buffer_load_dwordx4 v6, s[64:67], s81 offen offset:0 lds\n\t"
"buffer_load_dwordx4 v7, s[64:67], s81 offen offset:1024 lds\n\t"
"s_add_u32 m0, s85, 102400\n\t"
"s_add_u32 s81, s77, 8704\n\t"
"buffer_load_dwordx4 v24, s[52:55], s81 offen offset:0 lds\n\t"
"buffer_load_dwordx4 v25, s[52:55], s81 offen offset:1024 lds\n\t"
"s_add_u32 m0, s85, 110592\n\t"
"s_add_u32 s81, s84, 33280\n\t"
"buffer_load_dwordx4 v6, s[60:63], s81 offen offset:0 lds\n\t"
"buffer_load_dwordx4 v7, s[60:63], s81 offen offset:1024 lds\n\t"
"s_add_u32 m0, s85, 118784\n\t"
"s_add_u32 s81, s84, 33280\n\t"
"buffer_load_dwordx4 v6, s[64:67], s81 offen offset:0 lds\n\t"
"buffer_load_dwordx4 v7, s[64:67], s81 offen offset:1024 lds\n\t"
"s_add_u32 m0, s85, 126976\n\t"
"s_add_u32 s81, s77, 8832\n\t"
"buffer_load_dwordx4 v24, s[52:55], s81 offen offset:0 lds\n\t"
"buffer_load_dwordx4 v25, s[52:55], s81 offen offset:1024 lds\n\t"
"s_add_u32 m0, s85, 135168\n\t"
"s_add_u32 s81, s84, 33408\n\t"
"buffer_load_dwordx4 v6, s[60:63], s81 offen offset:0 lds\n\t"
"buffer_load_dwordx4 v7, s[60:63], s81 offen offset:1024 lds\n\t"
"s_add_u32 m0, s85, 143360\n\t"
"s_add_u32 s81, s84, 33408\n\t"
"buffer_load_dwordx4 v6, s[64:67], s81 offen offset:0 lds\n\t"
"buffer_load_dwordx4 v7, s[64:67], s81 offen offset:1024 lds\n\t"
"s_lshl_b32 s82, s47, 7\n\t"
"s_lshl_b32 s83, s39, 5\n\t"
"s_add_u32 s82, s82, s83\n\t"
"v_and_b32 v22, 15, v15\n\t"
"v_lshlrev_b32 v22, 1, v22\n\t"
"s_load_dwordx2 s[72:73], s[36:37], 56\n\t"
"s_waitcnt lgkmcnt(0)\n\t"
"s_and_b32 s73, s73, 0xffff\n\t"
"s_mov_b32 s74, 1024\n\t"
"s_mov_b32 s75, 0x00020000\n\t"
"buffer_load_ushort v244, v22, s[72:75], s82 offen\n\t"
"s_load_dwordx2 s[72:73], s[36:37], 64\n\t"
"s_waitcnt lgkmcnt(0)\n\t"
"s_and_b32 s73, s73, 0xffff\n\t"
"s_mov_b32 s74, 1024\n\t"
"s_mov_b32 s75, 0x00020000\n\t"
"buffer_load_ushort v245, v22, s[72:75], s82 offen\n\t"
"s_load_dwordx2 s[72:73], s[36:37], 104\n\t"
"s_waitcnt lgkmcnt(0)\n\t"
"s_and_b32 s73, s73, 0xffff\n\t"
"s_mov_b32 s74, 1024\n\t"
"s_mov_b32 s75, 0x00020000\n\t"
"buffer_load_ushort v246, v22, s[72:75], s82 offen\n\t"
"v_and_b32 v16, 15, v15\n\t"
"v_lshrrev_b32 v17, 4, v15\n\t"
"v_bfe_u32 v18, v15, 1, 3\n\t"
"v_xor_b32 v18, v18, v17\n\t"
"v_lshlrev_b32 v16, 7, v16\n\t"
"s_lshl_b32 s82, s39, 11\n\t"
"s_add_u32 s82, s82, 8192\n\t"
"v_xor_b32 v19, 0, v18\n\t"
"v_lshl_add_u32 v0, v19, 4, v16\n\t"
"v_add_u32 v2, s82, v0\n\t"
"v_xor_b32 v19, 4, v18\n\t"
"v_lshl_add_u32 v1, v19, 4, v16\n\t"
"v_add_u32 v3, s82, v1\n\t"
"s_waitcnt vmcnt(51) lgkmcnt(0)\n\t"
"s_barrier\n\t"
"ds_read_b128 v[48:51], v2 offset:0\n\t"
"ds_read_b128 v[52:55], v2 offset:8192\n\t"
"ds_read_b128 v[32:35], v0 offset:0\n\t"
"ds_read_b128 v[36:39], v0 offset:2048\n\t"
"ds_read_b128 v[40:43], v0 offset:4096\n\t"
"ds_read_b128 v[44:47], v0 offset:6144\n\t"
"ds_read_b128 v[72:75], v3 offset:0\n\t"
"ds_read_b128 v[76:79], v3 offset:8192\n\t"
"ds_read_b128 v[56:59], v1 offset:0\n\t"
"ds_read_b128 v[60:63], v1 offset:2048\n\t"
"ds_read_b128 v[64:67], v1 offset:4096\n\t"
"ds_read_b128 v[68:71], v1 offset:6144\n\t"
"s_waitcnt lgkmcnt(9)\n\t"
"v_mfma_f32_16x16x32_bf16 v[212:215], v[32:35], v[48:51], 0\n\t"
"ds_read_b128 v[96:99], v2 offset:24576\n\t"
"v_mfma_f32_16x16x32_bf16 v[228:231], v[32:35], v[52:55], 0\n\t"
"ds_read_b128 v[100:103], v2 offset:32768\n\t"
"s_waitcnt lgkmcnt(10)\n\t"
"v_mfma_f32_16x16x32_bf16 v[216:219], v[36:39], v[48:51], 0\n\t"
"ds_read_b128 v[80:83], v0 offset:24576\n\t"
"v_mfma_f32_16x16x32_bf16 v[232:235], v[36:39], v[52:55], 0\n\t"
"s_waitcnt lgkmcnt(10)\n\t"
"v_mfma_f32_16x16x32_bf16 v[220:223], v[40:43], v[48:51], 0\n\t"
"ds_read_b128 v[84:87], v0 offset:26624\n\t"
"v_mfma_f32_16x16x32_bf16 v[236:239], v[40:43], v[52:55], 0\n\t"
"ds_read_b128 v[88:91], v0 offset:28672\n\t"
"s_waitcnt lgkmcnt(11)\n\t"
"v_mfma_f32_16x16x32_bf16 v[224:227], v[44:47], v[48:51], 0\n\t"
"ds_read_b128 v[92:95], v0 offset:30720\n\t"
"v_mfma_f32_16x16x32_bf16 v[240:243], v[44:47], v[52:55], 0\n\t"
"s_waitcnt lgkmcnt(6)\n\t"
"v_mfma_f32_16x16x32_bf16 v[212:215], v[56:59], v[72:75], v[212:215]\n\t"
"ds_read_b128 v[120:123], v3 offset:24576\n\t"
"ds_read_b128 v[124:127], v3 offset:32768\n\t"
"v_mfma_f32_16x16x32_bf16 v[228:231], v[56:59], v[76:79], v[228:231]\n\t"
"ds_read_b128 v[104:107], v1 offset:24576\n\t"
"v_mfma_f32_16x16x32_bf16 v[216:219], v[60:63], v[72:75], v[216:219]\n\t"
"ds_read_b128 v[108:111], v1 offset:26624\n\t"
"v_mfma_f32_16x16x32_bf16 v[232:235], v[60:63], v[76:79], v[232:235]\n\t"
"ds_read_b128 v[112:115], v1 offset:28672\n\t"
"v_mfma_f32_16x16x32_bf16 v[220:223], v[64:67], v[72:75], v[220:223]\n\t"
"ds_read_b128 v[116:119], v1 offset:30720\n\t"
"v_add_u32 v0, 49152, v0\n\t"
"v_mfma_f32_16x16x32_bf16 v[236:239], v[64:67], v[76:79], v[236:239]\n\t"
"v_add_u32 v1, 49152, v1\n\t"
"v_mfma_f32_16x16x32_bf16 v[224:227], v[68:71], v[72:75], v[224:227]\n\t"
"v_add_u32 v2, 49152, v2\n\t"
"v_mfma_f32_16x16x32_bf16 v[240:243], v[68:71], v[76:79], v[240:243]\n\t"
"v_add_u32 v3, 49152, v3\n\t"
"s_waitcnt lgkmcnt(4)\n\t"
"v_mfma_f32_16x16x32_bf16 v[212:215], v[80:83], v[96:99], v[212:215]\n\t"
"v_mfma_f32_16x16x32_bf16 v[228:231], v[80:83], v[100:103], v[228:231]\n\t"
"v_mfma_f32_16x16x32_bf16 v[216:219], v[84:87], v[96:99], v[216:219]\n\t"
"v_mfma_f32_16x16x32_bf16 v[232:235], v[84:87], v[100:103], v[232:235]\n\t"
"v_mfma_f32_16x16x32_bf16 v[220:223], v[88:91], v[96:99], v[220:223]\n\t"
"v_mfma_f32_16x16x32_bf16 v[236:239], v[88:91], v[100:103], v[236:239]\n\t"
"v_mfma_f32_16x16x32_bf16 v[224:227], v[92:95], v[96:99], v[224:227]\n\t"
"v_mfma_f32_16x16x32_bf16 v[240:243], v[92:95], v[100:103], v[240:243]\n\t"
"s_waitcnt lgkmcnt(0)\n\t"
"v_mfma_f32_16x16x32_bf16 v[212:215], v[104:107], v[120:123], v[212:215]\n\t"
"v_mfma_f32_16x16x32_bf16 v[228:231], v[104:107], v[124:127], v[228:231]\n\t"
"v_mfma_f32_16x16x32_bf16 v[216:219], v[108:111], v[120:123], v[216:219]\n\t"
"v_mfma_f32_16x16x32_bf16 v[232:235], v[108:111], v[124:127], v[232:235]\n\t"
"v_mfma_f32_16x16x32_bf16 v[220:223], v[112:115], v[120:123], v[220:223]\n\t"
"v_mfma_f32_16x16x32_bf16 v[236:239], v[112:115], v[124:127], v[236:239]\n\t"
"v_mfma_f32_16x16x32_bf16 v[224:227], v[116:119], v[120:123], v[224:227]\n\t"
"v_mfma_f32_16x16x32_bf16 v[240:243], v[116:119], v[124:127], v[240:243]\n\t"
"s_waitcnt vmcnt(27) lgkmcnt(0)\n\t"
"s_barrier\n\t"
"ds_read_b128 v[48:51], v2 offset:0\n\t"
"ds_read_b128 v[52:55], v2 offset:8192\n\t"
"ds_read_b128 v[32:35], v0 offset:0\n\t"
"ds_read_b128 v[36:39], v0 offset:2048\n\t"
"ds_read_b128 v[40:43], v0 offset:4096\n\t"
"ds_read_b128 v[44:47], v0 offset:6144\n\t"
"ds_read_b128 v[72:75], v3 offset:0\n\t"
"ds_read_b128 v[76:79], v3 offset:8192\n\t"
"ds_read_b128 v[56:59], v1 offset:0\n\t"
"ds_read_b128 v[60:63], v1 offset:2048\n\t"
"ds_read_b128 v[64:67], v1 offset:4096\n\t"
"ds_read_b128 v[68:71], v1 offset:6144\n\t"
"s_waitcnt lgkmcnt(9)\n\t"
"v_mfma_f32_16x16x32_bf16 v[212:215], v[32:35], v[48:51], v[212:215]\n\t"
"ds_read_b128 v[96:99], v2 offset:24576\n\t"
"s_add_u32 m0, s85, 0\n\t"
"s_add_u32 s81, s77, 768\n\t"
"buffer_load_dwordx4 v24, s[52:55], s81 offen offset:0 lds\n\t"
"v_mfma_f32_16x16x32_bf16 v[228:231], v[32:35], v[52:55], v[228:231]\n\t"
"ds_read_b128 v[100:103], v2 offset:32768\n\t"
"s_waitcnt lgkmcnt(10)\n\t"
"v_mfma_f32_16x16x32_bf16 v[216:219], v[36:39], v[48:51], v[216:219]\n\t"
"buffer_load_dwordx4 v25, s[52:55], s81 offen offset:1024 lds\n\t"
"ds_read_b128 v[80:83], v0 offset:24576\n\t"
"v_mfma_f32_16x16x32_bf16 v[232:235], v[36:39], v[52:55], v[232:235]\n\t"
"s_add_u32 m0, s85, 8192\n\t"
"s_add_u32 s81, s84, 768\n\t"
"buffer_load_dwordx4 v6, s[60:63], s81 offen offset:0 lds\n\t"
"s_waitcnt lgkmcnt(10)\n\t"
"v_mfma_f32_16x16x32_bf16 v[220:223], v[40:43], v[48:51], v[220:223]\n\t"
"ds_read_b128 v[84:87], v0 offset:26624\n\t"
"buffer_load_dwordx4 v7, s[60:63], s81 offen offset:1024 lds\n\t"
"v_mfma_f32_16x16x32_bf16 v[236:239], v[40:43], v[52:55], v[236:239]\n\t"
"ds_read_b128 v[88:91], v0 offset:28672\n\t"
"s_waitcnt lgkmcnt(5)\n\t"
"v_mfma_f32_16x16x32_bf16 v[224:227], v[44:47], v[48:51], v[224:227]\n\t"
"s_add_u32 m0, s85, 16384\n\t"
"s_add_u32 s81, s84, 768\n\t"
"buffer_load_dwordx4 v6, s[64:67], s81 offen offset:0 lds\n\t"
"ds_read_b128 v[92:95], v0 offset:30720\n\t"
"v_mfma_f32_16x16x32_bf16 v[240:243], v[44:47], v[52:55], v[240:243]\n\t"
"buffer_load_dwordx4 v7, s[64:67], s81 offen offset:1024 lds\n\t"
"v_mfma_f32_16x16x32_bf16 v[212:215], v[56:59], v[72:75], v[212:215]\n\t"
"ds_read_b128 v[120:123], v3 offset:24576\n\t"
"s_add_u32 m0, s85, 24576\n\t"
"s_add_u32 s81, s77, 896\n\t"
"buffer_load_dwordx4 v24, s[52:55], s81 offen offset:0 lds\n\t"
"v_mfma_f32_16x16x32_bf16 v[228:231], v[56:59], v[76:79], v[228:231]\n\t"
"ds_read_b128 v[124:127], v3 offset:32768\n\t"
"buffer_load_dwordx4 v25, s[52:55], s81 offen offset:1024 lds\n\t"
"v_mfma_f32_16x16x32_bf16 v[216:219], v[60:63], v[72:75], v[216:219]\n\t"
"ds_read_b128 v[104:107], v1 offset:24576\n\t"
"ds_read_b128 v[108:111], v1 offset:26624\n\t"
"v_mfma_f32_16x16x32_bf16 v[232:235], v[60:63], v[76:79], v[232:235]\n\t"
"s_add_u32 m0, s85, 32768\n\t"
"s_add_u32 s81, s84, 896\n\t"
"buffer_load_dwordx4 v6, s[60:63], s81 offen offset:0 lds\n\t"
"ds_read_b128 v[112:115], v1 offset:28672\n\t"
"v_mfma_f32_16x16x32_bf16 v[220:223], v[64:67], v[72:75], v[220:223]\n\t"
"ds_read_b128 v[116:119], v1 offset:30720\n\t"
"buffer_load_dwordx4 v7, s[60:63], s81 offen offset:1024 lds\n\t"
"v_mfma_f32_16x16x32_bf16 v[236:239], v[64:67], v[76:79], v[236:239]\n\t"
"v_add_u32 v0, 49152, v0\n\t"
"s_add_u32 m0, s85, 40960\n\t"
"s_add_u32 s81, s84, 896\n\t"
"buffer_load_dwordx4 v6, s[64:67], s81 offen offset:0 lds\n\t"
"v_mfma_f32_16x16x32_bf16 v[224:227], v[68:71], v[72:75], v[224:227]\n\t"
"v_add_u32 v1, 49152, v1\n\t"
"v_add_u32 v2, 49152, v2\n\t"
"v_mfma_f32_16x16x32_bf16 v[240:243], v[68:71], v[76:79], v[240:243]\n\t"
"buffer_load_dwordx4 v7, s[64:67], s81 offen offset:1024 lds\n\t"
"v_add_u32 v3, 49152, v3\n\t"
"s_waitcnt lgkmcnt(4)\n\t"
"v_mfma_f32_16x16x32_bf16 v[212:215], v[80:83], v[96:99], v[212:215]\n\t"
"s_add_u32 m0, s85, 4096\n\t"
"s_add_u32 s81, s77, 8960\n\t"
"buffer_load_dwordx4 v24, s[52:55], s81 offen offset:0 lds\n\t"
"v_mfma_f32_16x16x32_bf16 v[228:231], v[80:83], v[100:103], v[228:231]\n\t"
"buffer_load_dwordx4 v25, s[52:55], s81 offen offset:1024 lds\n\t"
"v_mfma_f32_16x16x32_bf16 v[216:219], v[84:87], v[96:99], v[216:219]\n\t"
"s_add_u32 m0, s85, 12288\n\t"
"s_add_u32 s81, s84, 33536\n\t"
"buffer_load_dwordx4 v6, s[60:63], s81 offen offset:0 lds\n\t"
"v_mfma_f32_16x16x32_bf16 v[232:235], v[84:87], v[100:103], v[232:235]\n\t"
"v_mfma_f32_16x16x32_bf16 v[220:223], v[88:91], v[96:99], v[220:223]\n\t"
"buffer_load_dwordx4 v7, s[60:63], s81 offen offset:1024 lds\n\t"
"v_mfma_f32_16x16x32_bf16 v[236:239], v[88:91], v[100:103], v[236:239]\n\t"
"s_add_u32 m0, s85, 20480\n\t"
"s_add_u32 s81, s84, 33536\n\t"
"buffer_load_dwordx4 v6, s[64:67], s81 offen offset:0 lds\n\t"
"v_mfma_f32_16x16x32_bf16 v[224:227], v[92:95], v[96:99], v[224:227]\n\t"
"buffer_load_dwordx4 v7, s[64:67], s81 offen offset:1024 lds\n\t"
"v_mfma_f32_16x16x32_bf16 v[240:243], v[92:95], v[100:103], v[240:243]\n\t"
"s_waitcnt lgkmcnt(0)\n\t"
"v_mfma_f32_16x16x32_bf16 v[212:215], v[104:107], v[120:123], v[212:215]\n\t"
"s_add_u32 m0, s85, 28672\n\t"
"s_add_u32 s81, s77, 9088\n\t"
"buffer_load_dwordx4 v24, s[52:55], s81 offen offset:0 lds\n\t"
"v_mfma_f32_16x16x32_bf16 v[228:231], v[104:107], v[124:127], v[228:231]\n\t"
"buffer_load_dwordx4 v25, s[52:55], s81 offen offset:1024 lds\n\t"
"v_mfma_f32_16x16x32_bf16 v[216:219], v[108:111], v[120:123], v[216:219]\n\t"
"s_add_u32 m0, s85, 36864\n\t"
"s_add_u32 s81, s84, 33664\n\t"
"buffer_load_dwordx4 v6, s[60:63], s81 offen offset:0 lds\n\t"
"v_mfma_f32_16x16x32_bf16 v[232:235], v[108:111], v[124:127], v[232:235]\n\t"
"v_mfma_f32_16x16x32_bf16 v[220:223], v[112:115], v[120:123], v[220:223]\n\t"
"buffer_load_dwordx4 v7, s[60:63], s81 offen offset:1024 lds\n\t"
"v_mfma_f32_16x16x32_bf16 v[236:239], v[112:115], v[124:127], v[236:239]\n\t"
"s_add_u32 m0, s85, 45056\n\t"
"s_add_u32 s81, s84, 33664\n\t"
"buffer_load_dwordx4 v6, s[64:67], s81 offen offset:0 lds\n\t"
"v_mfma_f32_16x16x32_bf16 v[224:227], v[116:119], v[120:123], v[224:227]\n\t"
"buffer_load_dwordx4 v7, s[64:67], s81 offen offset:1024 lds\n\t"
"v_mfma_f32_16x16x32_bf16 v[240:243], v[116:119], v[124:127], v[240:243]\n\t"
"s_waitcnt vmcnt(27) lgkmcnt(0)\n\t"
"s_barrier\n\t"
"ds_read_b128 v[48:51], v2 offset:0\n\t"
"ds_read_b128 v[52:55], v2 offset:8192\n\t"
"ds_read_b128 v[32:35], v0 offset:0\n\t"
"ds_read_b128 v[36:39], v0 offset:2048\n\t"
"ds_read_b128 v[40:43], v0 offset:4096\n\t"
"ds_read_b128 v[44:47], v0 offset:6144\n\t"
"ds_read_b128 v[72:75], v3 offset:0\n\t"
"ds_read_b128 v[76:79], v3 offset:8192\n\t"
"ds_read_b128 v[56:59], v1 offset:0\n\t"
"ds_read_b128 v[60:63], v1 offset:2048\n\t"
"ds_read_b128 v[64:67], v1 offset:4096\n\t"
"ds_read_b128 v[68:71], v1 offset:6144\n\t"
"s_waitcnt lgkmcnt(9)\n\t"
"v_mfma_f32_16x16x32_bf16 v[212:215], v[32:35], v[48:51], v[212:215]\n\t"
"ds_read_b128 v[96:99], v2 offset:24576\n\t"
"s_add_u32 m0, s79, 49152\n\t"
"s_add_u32 s81, s76, 0\n\t"
"buffer_load_dwordx4 v6, s[48:51], s81 offen offset:0 lds\n\t"
"v_mfma_f32_16x16x32_bf16 v[228:231], v[32:35], v[52:55], v[228:231]\n\t"
"ds_read_b128 v[100:103], v2 offset:32768\n\t"
"s_waitcnt lgkmcnt(10)\n\t"
"v_mfma_f32_16x16x32_bf16 v[216:219], v[36:39], v[48:51], v[216:219]\n\t"
"buffer_load_dwordx4 v7, s[48:51], s81 offen offset:1024 lds\n\t"
"ds_read_b128 v[80:83], v0 offset:24576\n\t"
"v_mfma_f32_16x16x32_bf16 v[232:235], v[36:39], v[52:55], v[232:235]\n\t"
"buffer_load_dwordx4 v8, s[48:51], s81 offen offset:2048 lds\n\t"
"s_waitcnt lgkmcnt(10)\n\t"
"v_mfma_f32_16x16x32_bf16 v[220:223], v[40:43], v[48:51], v[220:223]\n\t"
"ds_read_b128 v[84:87], v0 offset:26624\n\t"
"buffer_load_dwordx4 v9, s[48:51], s81 offen offset:3072 lds\n\t"
"v_mfma_f32_16x16x32_bf16 v[236:239], v[40:43], v[52:55], v[236:239]\n\t"
"ds_read_b128 v[88:91], v0 offset:28672\n\t"
"s_waitcnt lgkmcnt(6)\n\t"
"v_mfma_f32_16x16x32_bf16 v[224:227], v[44:47], v[48:51], v[224:227]\n\t"
"s_add_u32 m0, s79, 53248\n\t"
"s_add_u32 s81, s76, 32768\n\t"
"buffer_load_dwordx4 v6, s[48:51], s81 offen offset:0 lds\n\t"
"ds_read_b128 v[92:95], v0 offset:30720\n\t"
"v_mfma_f32_16x16x32_bf16 v[240:243], v[44:47], v[52:55], v[240:243]\n\t"
"buffer_load_dwordx4 v7, s[48:51], s81 offen offset:1024 lds\n\t"
"v_mfma_f32_16x16x32_bf16 v[212:215], v[56:59], v[72:75], v[212:215]\n\t"
"ds_read_b128 v[120:123], v3 offset:24576\n\t"
"buffer_load_dwordx4 v8, s[48:51], s81 offen offset:2048 lds\n\t"
"v_mfma_f32_16x16x32_bf16 v[228:231], v[56:59], v[76:79], v[228:231]\n\t"
"ds_read_b128 v[124:127], v3 offset:32768\n\t"
"buffer_load_dwordx4 v9, s[48:51], s81 offen offset:3072 lds\n\t"
"v_mfma_f32_16x16x32_bf16 v[216:219], v[60:63], v[72:75], v[216:219]\n\t"
"ds_read_b128 v[104:107], v1 offset:24576\n\t"
"ds_read_b128 v[108:111], v1 offset:26624\n\t"
"v_mfma_f32_16x16x32_bf16 v[232:235], v[60:63], v[76:79], v[232:235]\n\t"
"s_add_u32 m0, s80, 49152\n\t"
"s_add_u32 s81, s78, 0\n\t"
"buffer_load_dwordx4 v10, s[56:59], s81 offen offset:0 lds\n\t"
"ds_read_b128 v[112:115], v1 offset:28672\n\t"
"v_mfma_f32_16x16x32_bf16 v[220:223], v[64:67], v[72:75], v[220:223]\n\t"
"ds_read_b128 v[116:119], v1 offset:30720\n\t"
"buffer_load_dwordx4 v11, s[56:59], s81 offen offset:1024 lds\n\t"
"v_mfma_f32_16x16x32_bf16 v[236:239], v[64:67], v[76:79], v[236:239]\n\t"
"v_add_u32 v0, -98304, v0\n\t"
"buffer_load_dwordx4 v12, s[56:59], s81 offen offset:2048 lds\n\t"
"s_waitcnt lgkmcnt(6)\n\t"
"v_mfma_f32_16x16x32_bf16 v[224:227], v[68:71], v[72:75], v[224:227]\n\t"
"v_add_u32 v1, -98304, v1\n\t"
"v_add_u32 v2, -98304, v2\n\t"
"v_mfma_f32_16x16x32_bf16 v[240:243], v[68:71], v[76:79], v[240:243]\n\t"
"buffer_load_dwordx4 v13, s[56:59], s81 offen offset:3072 lds\n\t"
"v_add_u32 v3, -98304, v3\n\t"
"v_mfma_f32_16x16x32_bf16 v[212:215], v[80:83], v[96:99], v[212:215]\n\t"
"s_add_u32 m0, s79, 65536\n\t"
"s_add_u32 s81, s76, 131072\n\t"
"buffer_load_dwordx4 v6, s[48:51], s81 offen offset:0 lds\n\t"
"v_mfma_f32_16x16x32_bf16 v[228:231], v[80:83], v[100:103], v[228:231]\n\t"
"buffer_load_dwordx4 v7, s[48:51], s81 offen offset:1024 lds\n\t"
"v_mfma_f32_16x16x32_bf16 v[216:219], v[84:87], v[96:99], v[216:219]\n\t"
"buffer_load_dwordx4 v8, s[48:51], s81 offen offset:2048 lds\n\t"
"v_mfma_f32_16x16x32_bf16 v[232:235], v[84:87], v[100:103], v[232:235]\n\t"
"v_mfma_f32_16x16x32_bf16 v[220:223], v[88:91], v[96:99], v[220:223]\n\t"
"buffer_load_dwordx4 v9, s[48:51], s81 offen offset:3072 lds\n\t"
"v_mfma_f32_16x16x32_bf16 v[236:239], v[88:91], v[100:103], v[236:239]\n\t"
"s_add_u32 m0, s79, 69632\n\t"
"s_add_u32 s81, s76, 163840\n\t"
"buffer_load_dwordx4 v6, s[48:51], s81 offen offset:0 lds\n\t"
"v_mfma_f32_16x16x32_bf16 v[224:227], v[92:95], v[96:99], v[224:227]\n\t"
"buffer_load_dwordx4 v7, s[48:51], s81 offen offset:1024 lds\n\t"
"v_mfma_f32_16x16x32_bf16 v[240:243], v[92:95], v[100:103], v[240:243]\n\t"
"s_waitcnt lgkmcnt(0)\n\t"
"v_mfma_f32_16x16x32_bf16 v[212:215], v[104:107], v[120:123], v[212:215]\n\t"
"buffer_load_dwordx4 v8, s[48:51], s81 offen offset:2048 lds\n\t"
"v_mfma_f32_16x16x32_bf16 v[228:231], v[104:107], v[124:127], v[228:231]\n\t"
"buffer_load_dwordx4 v9, s[48:51], s81 offen offset:3072 lds\n\t"
"v_mfma_f32_16x16x32_bf16 v[216:219], v[108:111], v[120:123], v[216:219]\n\t"
"s_add_u32 m0, s80, 57344\n\t"
"s_add_u32 s81, s78, 262144\n\t"
"buffer_load_dwordx4 v10, s[56:59], s81 offen offset:0 lds\n\t"
"v_mfma_f32_16x16x32_bf16 v[232:235], v[108:111], v[124:127], v[232:235]\n\t"
"v_mfma_f32_16x16x32_bf16 v[220:223], v[112:115], v[120:123], v[220:223]\n\t"
"buffer_load_dwordx4 v11, s[56:59], s81 offen offset:1024 lds\n\t"
"v_mfma_f32_16x16x32_bf16 v[236:239], v[112:115], v[124:127], v[236:239]\n\t"
"buffer_load_dwordx4 v12, s[56:59], s81 offen offset:2048 lds\n\t"
"v_mfma_f32_16x16x32_bf16 v[224:227], v[116:119], v[120:123], v[224:227]\n\t"
"buffer_load_dwordx4 v13, s[56:59], s81 offen offset:3072 lds\n\t"
"v_mfma_f32_16x16x32_bf16 v[240:243], v[116:119], v[124:127], v[240:243]\n\t"
"s_waitcnt vmcnt(24) lgkmcnt(0)\n\t"
"s_barrier\n\t"
"ds_read_b128 v[48:51], v2 offset:0\n\t"
"ds_read_b128 v[52:55], v2 offset:8192\n\t"
"ds_read_b128 v[32:35], v0 offset:0\n\t"
"ds_read_b128 v[36:39], v0 offset:2048\n\t"
"ds_read_b128 v[40:43], v0 offset:4096\n\t"
"ds_read_b128 v[44:47], v0 offset:6144\n\t"
"ds_read_b128 v[72:75], v3 offset:0\n\t"
"ds_read_b128 v[76:79], v3 offset:8192\n\t"
"ds_read_b128 v[56:59], v1 offset:0\n\t"
"ds_read_b128 v[60:63], v1 offset:2048\n\t"
"ds_read_b128 v[64:67], v1 offset:4096\n\t"
"ds_read_b128 v[68:71], v1 offset:6144\n\t"
"s_waitcnt lgkmcnt(9)\n\t"
"v_mfma_f32_16x16x32_bf16 v[212:215], v[32:35], v[48:51], v[212:215]\n\t"
"ds_read_b128 v[96:99], v2 offset:24576\n\t"
"s_add_u32 m0, s79, 98304\n\t"
"s_add_u32 s81, s76, 128\n\t"
"buffer_load_dwordx4 v6, s[48:51], s81 offen offset:0 lds\n\t"
"v_mfma_f32_16x16x32_bf16 v[228:231], v[32:35], v[52:55], v[228:231]\n\t"
"ds_read_b128 v[100:103], v2 offset:32768\n\t"
"s_waitcnt lgkmcnt(10)\n\t"
"v_mfma_f32_16x16x32_bf16 v[216:219], v[36:39], v[48:51], v[216:219]\n\t"
"buffer_load_dwordx4 v7, s[48:51], s81 offen offset:1024 lds\n\t"
"ds_read_b128 v[80:83], v0 offset:24576\n\t"
"v_mfma_f32_16x16x32_bf16 v[232:235], v[36:39], v[52:55], v[232:235]\n\t"
"buffer_load_dwordx4 v8, s[48:51], s81 offen offset:2048 lds\n\t"
"s_waitcnt lgkmcnt(10)\n\t"
"v_mfma_f32_16x16x32_bf16 v[220:223], v[40:43], v[48:51], v[220:223]\n\t"
"ds_read_b128 v[84:87], v0 offset:26624\n\t"
"buffer_load_dwordx4 v9, s[48:51], s81 offen offset:3072 lds\n\t"
"v_mfma_f32_16x16x32_bf16 v[236:239], v[40:43], v[52:55], v[236:239]\n\t"
"ds_read_b128 v[88:91], v0 offset:28672\n\t"
"s_waitcnt lgkmcnt(6)\n\t"
"v_mfma_f32_16x16x32_bf16 v[224:227], v[44:47], v[48:51], v[224:227]\n\t"
"s_add_u32 m0, s79, 102400\n\t"
"s_add_u32 s81, s76, 32896\n\t"
"buffer_load_dwordx4 v6, s[48:51], s81 offen offset:0 lds\n\t"
"ds_read_b128 v[92:95], v0 offset:30720\n\t"
"v_mfma_f32_16x16x32_bf16 v[240:243], v[44:47], v[52:55], v[240:243]\n\t"
"buffer_load_dwordx4 v7, s[48:51], s81 offen offset:1024 lds\n\t"
"v_mfma_f32_16x16x32_bf16 v[212:215], v[56:59], v[72:75], v[212:215]\n\t"
"ds_read_b128 v[120:123], v3 offset:24576\n\t"
"buffer_load_dwordx4 v8, s[48:51], s81 offen offset:2048 lds\n\t"
"v_mfma_f32_16x16x32_bf16 v[228:231], v[56:59], v[76:79], v[228:231]\n\t"
"ds_read_b128 v[124:127], v3 offset:32768\n\t"
"buffer_load_dwordx4 v9, s[48:51], s81 offen offset:3072 lds\n\t"
"v_mfma_f32_16x16x32_bf16 v[216:219], v[60:63], v[72:75], v[216:219]\n\t"
"ds_read_b128 v[104:107], v1 offset:24576\n\t"
"ds_read_b128 v[108:111], v1 offset:26624\n\t"
"v_mfma_f32_16x16x32_bf16 v[232:235], v[60:63], v[76:79], v[232:235]\n\t"
"s_add_u32 m0, s80, 98304\n\t"
"s_add_u32 s81, s78, 524288\n\t"
"buffer_load_dwordx4 v10, s[56:59], s81 offen offset:0 lds\n\t"
"ds_read_b128 v[112:115], v1 offset:28672\n\t"
"v_mfma_f32_16x16x32_bf16 v[220:223], v[64:67], v[72:75], v[220:223]\n\t"
"ds_read_b128 v[116:119], v1 offset:30720\n\t"
"buffer_load_dwordx4 v11, s[56:59], s81 offen offset:1024 lds\n\t"
"v_mfma_f32_16x16x32_bf16 v[236:239], v[64:67], v[76:79], v[236:239]\n\t"
"v_add_u32 v0, 49152, v0\n\t"
"buffer_load_dwordx4 v12, s[56:59], s81 offen offset:2048 lds\n\t"
"s_waitcnt lgkmcnt(6)\n\t"
"v_mfma_f32_16x16x32_bf16 v[224:227], v[68:71], v[72:75], v[224:227]\n\t"
"v_add_u32 v1, 49152, v1\n\t"
"v_add_u32 v2, 49152, v2\n\t"
"v_mfma_f32_16x16x32_bf16 v[240:243], v[68:71], v[76:79], v[240:243]\n\t"
"buffer_load_dwordx4 v13, s[56:59], s81 offen offset:3072 lds\n\t"
"v_add_u32 v3, 49152, v3\n\t"
"v_mfma_f32_16x16x32_bf16 v[212:215], v[80:83], v[96:99], v[212:215]\n\t"
"s_add_u32 m0, s79, 114688\n\t"
"s_add_u32 s81, s76, 131200\n\t"
"buffer_load_dwordx4 v6, s[48:51], s81 offen offset:0 lds\n\t"
"v_mfma_f32_16x16x32_bf16 v[228:231], v[80:83], v[100:103], v[228:231]\n\t"
"buffer_load_dwordx4 v7, s[48:51], s81 offen offset:1024 lds\n\t"
"v_mfma_f32_16x16x32_bf16 v[216:219], v[84:87], v[96:99], v[216:219]\n\t"
"buffer_load_dwordx4 v8, s[48:51], s81 offen offset:2048 lds\n\t"
"v_mfma_f32_16x16x32_bf16 v[232:235], v[84:87], v[100:103], v[232:235]\n\t"
"v_mfma_f32_16x16x32_bf16 v[220:223], v[88:91], v[96:99], v[220:223]\n\t"
"buffer_load_dwordx4 v9, s[48:51], s81 offen offset:3072 lds\n\t"
"v_mfma_f32_16x16x32_bf16 v[236:239], v[88:91], v[100:103], v[236:239]\n\t"
"s_add_u32 m0, s79, 118784\n\t"
"s_add_u32 s81, s76, 163968\n\t"
"buffer_load_dwordx4 v6, s[48:51], s81 offen offset:0 lds\n\t"
"v_mfma_f32_16x16x32_bf16 v[224:227], v[92:95], v[96:99], v[224:227]\n\t"
"buffer_load_dwordx4 v7, s[48:51], s81 offen offset:1024 lds\n\t"
"v_mfma_f32_16x16x32_bf16 v[240:243], v[92:95], v[100:103], v[240:243]\n\t"
"s_waitcnt lgkmcnt(0)\n\t"
"v_mfma_f32_16x16x32_bf16 v[212:215], v[104:107], v[120:123], v[212:215]\n\t"
"buffer_load_dwordx4 v8, s[48:51], s81 offen offset:2048 lds\n\t"
"v_mfma_f32_16x16x32_bf16 v[228:231], v[104:107], v[124:127], v[228:231]\n\t"
"buffer_load_dwordx4 v9, s[48:51], s81 offen offset:3072 lds\n\t"
"v_mfma_f32_16x16x32_bf16 v[216:219], v[108:111], v[120:123], v[216:219]\n\t"
"s_add_u32 m0, s80, 106496\n\t"
"s_add_u32 s81, s78, 786432\n\t"
"buffer_load_dwordx4 v10, s[56:59], s81 offen offset:0 lds\n\t"
"v_mfma_f32_16x16x32_bf16 v[232:235], v[108:111], v[124:127], v[232:235]\n\t"
"v_mfma_f32_16x16x32_bf16 v[220:223], v[112:115], v[120:123], v[220:223]\n\t"
"buffer_load_dwordx4 v11, s[56:59], s81 offen offset:1024 lds\n\t"
"v_mfma_f32_16x16x32_bf16 v[236:239], v[112:115], v[124:127], v[236:239]\n\t"
"buffer_load_dwordx4 v12, s[56:59], s81 offen offset:2048 lds\n\t"
"v_mfma_f32_16x16x32_bf16 v[224:227], v[116:119], v[120:123], v[224:227]\n\t"
"buffer_load_dwordx4 v13, s[56:59], s81 offen offset:3072 lds\n\t"
"v_mfma_f32_16x16x32_bf16 v[240:243], v[116:119], v[124:127], v[240:243]\n\t"
"s_waitcnt vmcnt(63)\n\t"
"s_nop 7\n\t"
"s_branch LS1J2_%=\n\t"
"LS1P1_%=:\n\t"
"s_waitcnt lgkmcnt(0)\n\t"
"s_lshl_b32 s82, s47, 7\n\t"
"s_lshl_b32 s83, s39, 5\n\t"
"s_add_u32 s82, s82, s83\n\t"
"v_and_b32 v22, 15, v15\n\t"
"v_lshlrev_b32 v22, 1, v22\n\t"
"s_load_dwordx2 s[72:73], s[36:37], 56\n\t"
"s_waitcnt lgkmcnt(0)\n\t"
"s_and_b32 s73, s73, 0xffff\n\t"
"s_mov_b32 s74, 1024\n\t"
"s_mov_b32 s75, 0x00020000\n\t"
"buffer_load_ushort v244, v22, s[72:75], s82 offen\n\t"
"s_load_dwordx2 s[72:73], s[36:37], 64\n\t"
"s_waitcnt lgkmcnt(0)\n\t"
"s_and_b32 s73, s73, 0xffff\n\t"
"s_mov_b32 s74, 1024\n\t"
"s_mov_b32 s75, 0x00020000\n\t"
"buffer_load_ushort v245, v22, s[72:75], s82 offen\n\t"
"s_load_dwordx2 s[72:73], s[36:37], 104\n\t"
"s_waitcnt lgkmcnt(0)\n\t"
"s_and_b32 s73, s73, 0xffff\n\t"
"s_mov_b32 s74, 1024\n\t"
"s_mov_b32 s75, 0x00020000\n\t"
"buffer_load_ushort v246, v22, s[72:75], s82 offen\n\t"
".Lmega_begin:\n\t"
"s_getpc_b64 s[72:73]\n\t"
"s_and_b32 s73, s73, 0xffff\n\t"
"s_mov_b32 s74, .Lmega_end-.Lmega_begin\n\t"
"s_mov_b32 s75, 0x00020000\n\t"
"s_and_b32 s72, s72, 0xfffffff0\n\t"
"s_lshr_b32 s97, s38, 3\n\t"
"s_lshl_b32 s97, s97, 1\n\t"
"s_add_u32 s97, s97, s44\n\t"
"s_lshl_b32 s97, s97, 11\n\t"
"s_mov_b32 m0, 147456\n\t"
"s_nop 0\n\t"
"buffer_load_dwordx4 v14, s[72:75], s97 offen offset:0 lds\n\t"
"buffer_load_dwordx4 v14, s[72:75], s97 offen offset:1024 lds\n\t"
"s_lshl_b32 s98, s38, 1\n\t"
"s_add_u32 s98, s98, s44\n\t"
"s_mov_b32 m0, 147456\n\t"
"s_load_dwordx2 s[72:73], s[36:37], 24\n\t"
"s_waitcnt lgkmcnt(0)\n\t"
"s_and_b32 s73, s73, 0xffff\n\t"
"s_mov_b32 s74, 4194304\n\t"
"s_mov_b32 s75, 0x00020000\n\t"
"s_mul_i32 s97, s98, 8192\n\t"
"buffer_load_dwordx4 v14, s[72:75], s97 offen offset:0 lds\n\t"
"buffer_load_dwordx4 v14, s[72:75], s97 offen offset:1024 lds\n\t"
"buffer_load_dwordx4 v14, s[72:75], s97 offen offset:2048 lds\n\t"
"buffer_load_dwordx4 v14, s[72:75], s97 offen offset:3072 lds\n\t"
"s_add_u32 s97, s97, 4096\n\t"
"buffer_load_dwordx4 v14, s[72:75], s97 offen offset:0 lds\n\t"
"buffer_load_dwordx4 v14, s[72:75], s97 offen offset:1024 lds\n\t"
"buffer_load_dwordx4 v14, s[72:75], s97 offen offset:2048 lds\n\t"
"buffer_load_dwordx4 v14, s[72:75], s97 offen offset:3072 lds\n\t"
"s_load_dwordx2 s[72:73], s[36:37], 8\n\t"
"s_waitcnt lgkmcnt(0)\n\t"
"s_and_b32 s73, s73, 0xffff\n\t"
"s_mov_b32 s74, 2097152\n\t"
"s_mov_b32 s75, 0x00020000\n\t"
"s_mul_i32 s97, s98, 4096\n\t"
"buffer_load_dwordx4 v14, s[72:75], s97 offen offset:0 lds\n\t"
"buffer_load_dwordx4 v14, s[72:75], s97 offen offset:1024 lds\n\t"
"buffer_load_dwordx4 v14, s[72:75], s97 offen offset:2048 lds\n\t"
"buffer_load_dwordx4 v14, s[72:75], s97 offen offset:3072 lds\n\t"
"v_and_b32 v16, 15, v15\n\t"
"v_lshrrev_b32 v17, 4, v15\n\t"
"v_bfe_u32 v18, v15, 1, 3\n\t"
"v_xor_b32 v18, v18, v17\n\t"
"v_lshlrev_b32 v16, 7, v16\n\t"
"s_lshl_b32 s82, s39, 11\n\t"
"s_add_u32 s82, s82, 8192\n\t"
"v_xor_b32 v19, 0, v18\n\t"
"v_lshl_add_u32 v0, v19, 4, v16\n\t"
"v_add_u32 v2, s82, v0\n\t"
"v_xor_b32 v19, 4, v18\n\t"
"v_lshl_add_u32 v1, v19, 4, v16\n\t"
"v_add_u32 v3, s82, v1\n\t"
"s_waitcnt vmcnt(63) lgkmcnt(0)\n\t"
"s_barrier\n\t"
"ds_read_b128 v[48:51], v2 offset:0\n\t"
"ds_read_b128 v[52:55], v2 offset:8192\n\t"
"ds_read_b128 v[32:35], v0 offset:0\n\t"
"ds_read_b128 v[36:39], v0 offset:2048\n\t"
"ds_read_b128 v[40:43], v0 offset:4096\n\t"
"ds_read_b128 v[44:47], v0 offset:6144\n\t"
"ds_read_b128 v[72:75], v3 offset:0\n\t"
"ds_read_b128 v[76:79], v3 offset:8192\n\t"
"ds_read_b128 v[56:59], v1 offset:0\n\t"
"ds_read_b128 v[60:63], v1 offset:2048\n\t"
"ds_read_b128 v[64:67], v1 offset:4096\n\t"
"ds_read_b128 v[68:71], v1 offset:6144\n\t"
"s_waitcnt lgkmcnt(9)\n\t"
"v_mfma_f32_16x16x32_bf16 v[212:215], v[32:35], v[48:51], 0\n\t"
"ds_read_b128 v[96:99], v2 offset:24576\n\t"
"v_mfma_f32_16x16x32_bf16 v[228:231], v[32:35], v[52:55], 0\n\t"
"ds_read_b128 v[100:103], v2 offset:32768\n\t"
"s_waitcnt lgkmcnt(10)\n\t"
"v_mfma_f32_16x16x32_bf16 v[216:219], v[36:39], v[48:51], 0\n\t"
"ds_read_b128 v[80:83], v0 offset:24576\n\t"
"v_mfma_f32_16x16x32_bf16 v[232:235], v[36:39], v[52:55], 0\n\t"
"s_waitcnt lgkmcnt(10)\n\t"
"v_mfma_f32_16x16x32_bf16 v[220:223], v[40:43], v[48:51], 0\n\t"
"ds_read_b128 v[84:87], v0 offset:26624\n\t"
"v_mfma_f32_16x16x32_bf16 v[236:239], v[40:43], v[52:55], 0\n\t"
"ds_read_b128 v[88:91], v0 offset:28672\n\t"
"s_waitcnt lgkmcnt(11)\n\t"
"v_mfma_f32_16x16x32_bf16 v[224:227], v[44:47], v[48:51], 0\n\t"
"ds_read_b128 v[92:95], v0 offset:30720\n\t"
"v_mfma_f32_16x16x32_bf16 v[240:243], v[44:47], v[52:55], 0\n\t"
"s_waitcnt lgkmcnt(6)\n\t"
"v_mfma_f32_16x16x32_bf16 v[212:215], v[56:59], v[72:75], v[212:215]\n\t"
"ds_read_b128 v[120:123], v3 offset:24576\n\t"
"ds_read_b128 v[124:127], v3 offset:32768\n\t"
"v_mfma_f32_16x16x32_bf16 v[228:231], v[56:59], v[76:79], v[228:231]\n\t"
"ds_read_b128 v[104:107], v1 offset:24576\n\t"
"v_mfma_f32_16x16x32_bf16 v[216:219], v[60:63], v[72:75], v[216:219]\n\t"
"ds_read_b128 v[108:111], v1 offset:26624\n\t"
"v_mfma_f32_16x16x32_bf16 v[232:235], v[60:63], v[76:79], v[232:235]\n\t"
"ds_read_b128 v[112:115], v1 offset:28672\n\t"
"v_mfma_f32_16x16x32_bf16 v[220:223], v[64:67], v[72:75], v[220:223]\n\t"
"ds_read_b128 v[116:119], v1 offset:30720\n\t"
"v_add_u32 v0, 49152, v0\n\t"
"v_mfma_f32_16x16x32_bf16 v[236:239], v[64:67], v[76:79], v[236:239]\n\t"
"v_add_u32 v1, 49152, v1\n\t"
"v_mfma_f32_16x16x32_bf16 v[224:227], v[68:71], v[72:75], v[224:227]\n\t"
"v_add_u32 v2, 49152, v2\n\t"
"v_mfma_f32_16x16x32_bf16 v[240:243], v[68:71], v[76:79], v[240:243]\n\t"
"v_add_u32 v3, 49152, v3\n\t"
"s_waitcnt lgkmcnt(4)\n\t"
"v_mfma_f32_16x16x32_bf16 v[212:215], v[80:83], v[96:99], v[212:215]\n\t"
"v_mfma_f32_16x16x32_bf16 v[228:231], v[80:83], v[100:103], v[228:231]\n\t"
"v_mfma_f32_16x16x32_bf16 v[216:219], v[84:87], v[96:99], v[216:219]\n\t"
"v_mfma_f32_16x16x32_bf16 v[232:235], v[84:87], v[100:103], v[232:235]\n\t"
"v_mfma_f32_16x16x32_bf16 v[220:223], v[88:91], v[96:99], v[220:223]\n\t"
"v_mfma_f32_16x16x32_bf16 v[236:239], v[88:91], v[100:103], v[236:239]\n\t"
"v_mfma_f32_16x16x32_bf16 v[224:227], v[92:95], v[96:99], v[224:227]\n\t"
"v_mfma_f32_16x16x32_bf16 v[240:243], v[92:95], v[100:103], v[240:243]\n\t"
"s_waitcnt lgkmcnt(0)\n\t"
"v_mfma_f32_16x16x32_bf16 v[212:215], v[104:107], v[120:123], v[212:215]\n\t"
"v_mfma_f32_16x16x32_bf16 v[228:231], v[104:107], v[124:127], v[228:231]\n\t"
"v_mfma_f32_16x16x32_bf16 v[216:219], v[108:111], v[120:123], v[216:219]\n\t"
"v_mfma_f32_16x16x32_bf16 v[232:235], v[108:111], v[124:127], v[232:235]\n\t"
"v_mfma_f32_16x16x32_bf16 v[220:223], v[112:115], v[120:123], v[220:223]\n\t"
"v_mfma_f32_16x16x32_bf16 v[236:239], v[112:115], v[124:127], v[236:239]\n\t"
"v_mfma_f32_16x16x32_bf16 v[224:227], v[116:119], v[120:123], v[224:227]\n\t"
"v_mfma_f32_16x16x32_bf16 v[240:243], v[116:119], v[124:127], v[240:243]\n\t"
"s_waitcnt vmcnt(63) lgkmcnt(0)\n\t"
"s_barrier\n\t"
"ds_read_b128 v[48:51], v2 offset:0\n\t"
"ds_read_b128 v[52:55], v2 offset:8192\n\t"
"ds_read_b128 v[32:35], v0 offset:0\n\t"
"ds_read_b128 v[36:39], v0 offset:2048\n\t"
"ds_read_b128 v[40:43], v0 offset:4096\n\t"
"ds_read_b128 v[44:47], v0 offset:6144\n\t"
"ds_read_b128 v[72:75], v3 offset:0\n\t"
"ds_read_b128 v[76:79], v3 offset:8192\n\t"
"ds_read_b128 v[56:59], v1 offset:0\n\t"
"ds_read_b128 v[60:63], v1 offset:2048\n\t"
"ds_read_b128 v[64:67], v1 offset:4096\n\t"
"ds_read_b128 v[68:71], v1 offset:6144\n\t"
"s_waitcnt lgkmcnt(9)\n\t"
"v_mfma_f32_16x16x32_bf16 v[212:215], v[32:35], v[48:51], v[212:215]\n\t"
"ds_read_b128 v[96:99], v2 offset:24576\n\t"
"v_mfma_f32_16x16x32_bf16 v[228:231], v[32:35], v[52:55], v[228:231]\n\t"
"ds_read_b128 v[100:103], v2 offset:32768\n\t"
"s_waitcnt lgkmcnt(10)\n\t"
"v_mfma_f32_16x16x32_bf16 v[216:219], v[36:39], v[48:51], v[216:219]\n\t"
"ds_read_b128 v[80:83], v0 offset:24576\n\t"
"v_mfma_f32_16x16x32_bf16 v[232:235], v[36:39], v[52:55], v[232:235]\n\t"
"s_waitcnt lgkmcnt(10)\n\t"
"v_mfma_f32_16x16x32_bf16 v[220:223], v[40:43], v[48:51], v[220:223]\n\t"
"ds_read_b128 v[84:87], v0 offset:26624\n\t"
"v_mfma_f32_16x16x32_bf16 v[236:239], v[40:43], v[52:55], v[236:239]\n\t"
"ds_read_b128 v[88:91], v0 offset:28672\n\t"
"s_waitcnt lgkmcnt(11)\n\t"
"v_mfma_f32_16x16x32_bf16 v[224:227], v[44:47], v[48:51], v[224:227]\n\t"
"ds_read_b128 v[92:95], v0 offset:30720\n\t"
"v_mfma_f32_16x16x32_bf16 v[240:243], v[44:47], v[52:55], v[240:243]\n\t"
"s_waitcnt lgkmcnt(6)\n\t"
"v_mfma_f32_16x16x32_bf16 v[212:215], v[56:59], v[72:75], v[212:215]\n\t"
"ds_read_b128 v[120:123], v3 offset:24576\n\t"
"ds_read_b128 v[124:127], v3 offset:32768\n\t"
"v_mfma_f32_16x16x32_bf16 v[228:231], v[56:59], v[76:79], v[228:231]\n\t"
"ds_read_b128 v[104:107], v1 offset:24576\n\t"
"v_mfma_f32_16x16x32_bf16 v[216:219], v[60:63], v[72:75], v[216:219]\n\t"
"ds_read_b128 v[108:111], v1 offset:26624\n\t"
"v_mfma_f32_16x16x32_bf16 v[232:235], v[60:63], v[76:79], v[232:235]\n\t"
"ds_read_b128 v[112:115], v1 offset:28672\n\t"
"v_mfma_f32_16x16x32_bf16 v[220:223], v[64:67], v[72:75], v[220:223]\n\t"
"ds_read_b128 v[116:119], v1 offset:30720\n\t"
"v_add_u32 v0, 49152, v0\n\t"
"v_mfma_f32_16x16x32_bf16 v[236:239], v[64:67], v[76:79], v[236:239]\n\t"
"v_add_u32 v1, 49152, v1\n\t"
"v_mfma_f32_16x16x32_bf16 v[224:227], v[68:71], v[72:75], v[224:227]\n\t"
"v_add_u32 v2, 49152, v2\n\t"
"v_mfma_f32_16x16x32_bf16 v[240:243], v[68:71], v[76:79], v[240:243]\n\t"
"v_add_u32 v3, 49152, v3\n\t"
"s_waitcnt lgkmcnt(4)\n\t"
"v_mfma_f32_16x16x32_bf16 v[212:215], v[80:83], v[96:99], v[212:215]\n\t"
"v_mfma_f32_16x16x32_bf16 v[228:231], v[80:83], v[100:103], v[228:231]\n\t"
"v_mfma_f32_16x16x32_bf16 v[216:219], v[84:87], v[96:99], v[216:219]\n\t"
"v_mfma_f32_16x16x32_bf16 v[232:235], v[84:87], v[100:103], v[232:235]\n\t"
"v_mfma_f32_16x16x32_bf16 v[220:223], v[88:91], v[96:99], v[220:223]\n\t"
"v_mfma_f32_16x16x32_bf16 v[236:239], v[88:91], v[100:103], v[236:239]\n\t"
"v_mfma_f32_16x16x32_bf16 v[224:227], v[92:95], v[96:99], v[224:227]\n\t"
"v_mfma_f32_16x16x32_bf16 v[240:243], v[92:95], v[100:103], v[240:243]\n\t"
"s_waitcnt lgkmcnt(0)\n\t"
"v_mfma_f32_16x16x32_bf16 v[212:215], v[104:107], v[120:123], v[212:215]\n\t"
"v_mfma_f32_16x16x32_bf16 v[228:231], v[104:107], v[124:127], v[228:231]\n\t"
"v_mfma_f32_16x16x32_bf16 v[216:219], v[108:111], v[120:123], v[216:219]\n\t"
"v_mfma_f32_16x16x32_bf16 v[232:235], v[108:111], v[124:127], v[232:235]\n\t"
"v_mfma_f32_16x16x32_bf16 v[220:223], v[112:115], v[120:123], v[220:223]\n\t"
"v_mfma_f32_16x16x32_bf16 v[236:239], v[112:115], v[124:127], v[236:239]\n\t"
"v_mfma_f32_16x16x32_bf16 v[224:227], v[116:119], v[120:123], v[224:227]\n\t"
"v_mfma_f32_16x16x32_bf16 v[240:243], v[116:119], v[124:127], v[240:243]\n\t"
"s_waitcnt vmcnt(63) lgkmcnt(0)\n\t"
"s_barrier\n\t"
"ds_read_b128 v[48:51], v2 offset:0\n\t"
"ds_read_b128 v[52:55], v2 offset:8192\n\t"
"ds_read_b128 v[32:35], v0 offset:0\n\t"
"ds_read_b128 v[36:39], v0 offset:2048\n\t"
"ds_read_b128 v[40:43], v0 offset:4096\n\t"
"ds_read_b128 v[44:47], v0 offset:6144\n\t"
"ds_read_b128 v[72:75], v3 offset:0\n\t"
"ds_read_b128 v[76:79], v3 offset:8192\n\t"
"ds_read_b128 v[56:59], v1 offset:0\n\t"
"ds_read_b128 v[60:63], v1 offset:2048\n\t"
"ds_read_b128 v[64:67], v1 offset:4096\n\t"
"ds_read_b128 v[68:71], v1 offset:6144\n\t"
"s_waitcnt lgkmcnt(9)\n\t"
"v_mfma_f32_16x16x32_bf16 v[212:215], v[32:35], v[48:51], v[212:215]\n\t"
"ds_read_b128 v[96:99], v2 offset:24576\n\t"
"v_mfma_f32_16x16x32_bf16 v[228:231], v[32:35], v[52:55], v[228:231]\n\t"
"ds_read_b128 v[100:103], v2 offset:32768\n\t"
"s_waitcnt lgkmcnt(10)\n\t"
"v_mfma_f32_16x16x32_bf16 v[216:219], v[36:39], v[48:51], v[216:219]\n\t"
"ds_read_b128 v[80:83], v0 offset:24576\n\t"
"v_mfma_f32_16x16x32_bf16 v[232:235], v[36:39], v[52:55], v[232:235]\n\t"
"s_waitcnt lgkmcnt(10)\n\t"
"v_mfma_f32_16x16x32_bf16 v[220:223], v[40:43], v[48:51], v[220:223]\n\t"
"ds_read_b128 v[84:87], v0 offset:26624\n\t"
"v_mfma_f32_16x16x32_bf16 v[236:239], v[40:43], v[52:55], v[236:239]\n\t"
"ds_read_b128 v[88:91], v0 offset:28672\n\t"
"s_waitcnt lgkmcnt(11)\n\t"
"v_mfma_f32_16x16x32_bf16 v[224:227], v[44:47], v[48:51], v[224:227]\n\t"
"ds_read_b128 v[92:95], v0 offset:30720\n\t"
"v_mfma_f32_16x16x32_bf16 v[240:243], v[44:47], v[52:55], v[240:243]\n\t"
"s_waitcnt lgkmcnt(6)\n\t"
"v_mfma_f32_16x16x32_bf16 v[212:215], v[56:59], v[72:75], v[212:215]\n\t"
"ds_read_b128 v[120:123], v3 offset:24576\n\t"
"ds_read_b128 v[124:127], v3 offset:32768\n\t"
"v_mfma_f32_16x16x32_bf16 v[228:231], v[56:59], v[76:79], v[228:231]\n\t"
"ds_read_b128 v[104:107], v1 offset:24576\n\t"
"v_mfma_f32_16x16x32_bf16 v[216:219], v[60:63], v[72:75], v[216:219]\n\t"
"ds_read_b128 v[108:111], v1 offset:26624\n\t"
"v_mfma_f32_16x16x32_bf16 v[232:235], v[60:63], v[76:79], v[232:235]\n\t"
"ds_read_b128 v[112:115], v1 offset:28672\n\t"
"v_mfma_f32_16x16x32_bf16 v[220:223], v[64:67], v[72:75], v[220:223]\n\t"
"ds_read_b128 v[116:119], v1 offset:30720\n\t"
"v_add_u32 v0, -98304, v0\n\t"
"v_mfma_f32_16x16x32_bf16 v[236:239], v[64:67], v[76:79], v[236:239]\n\t"
"v_add_u32 v1, -98304, v1\n\t"
"v_mfma_f32_16x16x32_bf16 v[224:227], v[68:71], v[72:75], v[224:227]\n\t"
"v_add_u32 v2, -98304, v2\n\t"
"v_mfma_f32_16x16x32_bf16 v[240:243], v[68:71], v[76:79], v[240:243]\n\t"
"v_add_u32 v3, -98304, v3\n\t"
"s_waitcnt lgkmcnt(4)\n\t"
"v_mfma_f32_16x16x32_bf16 v[212:215], v[80:83], v[96:99], v[212:215]\n\t"
"v_mfma_f32_16x16x32_bf16 v[228:231], v[80:83], v[100:103], v[228:231]\n\t"
"v_mfma_f32_16x16x32_bf16 v[216:219], v[84:87], v[96:99], v[216:219]\n\t"
"v_mfma_f32_16x16x32_bf16 v[232:235], v[84:87], v[100:103], v[232:235]\n\t"
"v_mfma_f32_16x16x32_bf16 v[220:223], v[88:91], v[96:99], v[220:223]\n\t"
"v_mfma_f32_16x16x32_bf16 v[236:239], v[88:91], v[100:103], v[236:239]\n\t"
"v_mfma_f32_16x16x32_bf16 v[224:227], v[92:95], v[96:99], v[224:227]\n\t"
"v_mfma_f32_16x16x32_bf16 v[240:243], v[92:95], v[100:103], v[240:243]\n\t"
"s_waitcnt lgkmcnt(0)\n\t"
"v_mfma_f32_16x16x32_bf16 v[212:215], v[104:107], v[120:123], v[212:215]\n\t"
"v_mfma_f32_16x16x32_bf16 v[228:231], v[104:107], v[124:127], v[228:231]\n\t"
"v_mfma_f32_16x16x32_bf16 v[216:219], v[108:111], v[120:123], v[216:219]\n\t"
"v_mfma_f32_16x16x32_bf16 v[232:235], v[108:111], v[124:127], v[232:235]\n\t"
"v_mfma_f32_16x16x32_bf16 v[220:223], v[112:115], v[120:123], v[220:223]\n\t"
"v_mfma_f32_16x16x32_bf16 v[236:239], v[112:115], v[124:127], v[236:239]\n\t"
"v_mfma_f32_16x16x32_bf16 v[224:227], v[116:119], v[120:123], v[224:227]\n\t"
"v_mfma_f32_16x16x32_bf16 v[240:243], v[116:119], v[124:127], v[240:243]\n\t"
"s_waitcnt vmcnt(63) lgkmcnt(0)\n\t"
"s_barrier\n\t"
"ds_read_b128 v[48:51], v2 offset:0\n\t"
"ds_read_b128 v[52:55], v2 offset:8192\n\t"
"ds_read_b128 v[32:35], v0 offset:0\n\t"
"ds_read_b128 v[36:39], v0 offset:2048\n\t"
"ds_read_b128 v[40:43], v0 offset:4096\n\t"
"ds_read_b128 v[44:47], v0 offset:6144\n\t"
"ds_read_b128 v[72:75], v3 offset:0\n\t"
"ds_read_b128 v[76:79], v3 offset:8192\n\t"
"ds_read_b128 v[56:59], v1 offset:0\n\t"
"ds_read_b128 v[60:63], v1 offset:2048\n\t"
"ds_read_b128 v[64:67], v1 offset:4096\n\t"
"ds_read_b128 v[68:71], v1 offset:6144\n\t"
"s_waitcnt lgkmcnt(9)\n\t"
"v_mfma_f32_16x16x32_bf16 v[212:215], v[32:35], v[48:51], v[212:215]\n\t"
"ds_read_b128 v[96:99], v2 offset:24576\n\t"
"v_mfma_f32_16x16x32_bf16 v[228:231], v[32:35], v[52:55], v[228:231]\n\t"
"ds_read_b128 v[100:103], v2 offset:32768\n\t"
"s_waitcnt lgkmcnt(10)\n\t"
"v_mfma_f32_16x16x32_bf16 v[216:219], v[36:39], v[48:51], v[216:219]\n\t"
"ds_read_b128 v[80:83], v0 offset:24576\n\t"
"v_mfma_f32_16x16x32_bf16 v[232:235], v[36:39], v[52:55], v[232:235]\n\t"
"s_waitcnt lgkmcnt(10)\n\t"
"v_mfma_f32_16x16x32_bf16 v[220:223], v[40:43], v[48:51], v[220:223]\n\t"
"ds_read_b128 v[84:87], v0 offset:26624\n\t"
"v_mfma_f32_16x16x32_bf16 v[236:239], v[40:43], v[52:55], v[236:239]\n\t"
"ds_read_b128 v[88:91], v0 offset:28672\n\t"
"s_waitcnt lgkmcnt(11)\n\t"
"v_mfma_f32_16x16x32_bf16 v[224:227], v[44:47], v[48:51], v[224:227]\n\t"
"ds_read_b128 v[92:95], v0 offset:30720\n\t"
"v_mfma_f32_16x16x32_bf16 v[240:243], v[44:47], v[52:55], v[240:243]\n\t"
"s_waitcnt lgkmcnt(6)\n\t"
"v_mfma_f32_16x16x32_bf16 v[212:215], v[56:59], v[72:75], v[212:215]\n\t"
"ds_read_b128 v[120:123], v3 offset:24576\n\t"
"ds_read_b128 v[124:127], v3 offset:32768\n\t"
"v_mfma_f32_16x16x32_bf16 v[228:231], v[56:59], v[76:79], v[228:231]\n\t"
"ds_read_b128 v[104:107], v1 offset:24576\n\t"
"v_mfma_f32_16x16x32_bf16 v[216:219], v[60:63], v[72:75], v[216:219]\n\t"
"ds_read_b128 v[108:111], v1 offset:26624\n\t"
"v_mfma_f32_16x16x32_bf16 v[232:235], v[60:63], v[76:79], v[232:235]\n\t"
"ds_read_b128 v[112:115], v1 offset:28672\n\t"
"v_mfma_f32_16x16x32_bf16 v[220:223], v[64:67], v[72:75], v[220:223]\n\t"
"ds_read_b128 v[116:119], v1 offset:30720\n\t"
"v_add_u32 v0, 49152, v0\n\t"
"v_mfma_f32_16x16x32_bf16 v[236:239], v[64:67], v[76:79], v[236:239]\n\t"
"v_add_u32 v1, 49152, v1\n\t"
"v_mfma_f32_16x16x32_bf16 v[224:227], v[68:71], v[72:75], v[224:227]\n\t"
"v_add_u32 v2, 49152, v2\n\t"
"v_mfma_f32_16x16x32_bf16 v[240:243], v[68:71], v[76:79], v[240:243]\n\t"
"v_add_u32 v3, 49152, v3\n\t"
"s_waitcnt lgkmcnt(4)\n\t"
"v_mfma_f32_16x16x32_bf16 v[212:215], v[80:83], v[96:99], v[212:215]\n\t"
"v_mfma_f32_16x16x32_bf16 v[228:231], v[80:83], v[100:103], v[228:231]\n\t"
"v_mfma_f32_16x16x32_bf16 v[216:219], v[84:87], v[96:99], v[216:219]\n\t"
"v_mfma_f32_16x16x32_bf16 v[232:235], v[84:87], v[100:103], v[232:235]\n\t"
"v_mfma_f32_16x16x32_bf16 v[220:223], v[88:91], v[96:99], v[220:223]\n\t"
"v_mfma_f32_16x16x32_bf16 v[236:239], v[88:91], v[100:103], v[236:239]\n\t"
"v_mfma_f32_16x16x32_bf16 v[224:227], v[92:95], v[96:99], v[224:227]\n\t"
"v_mfma_f32_16x16x32_bf16 v[240:243], v[92:95], v[100:103], v[240:243]\n\t"
"s_waitcnt lgkmcnt(0)\n\t"
"v_mfma_f32_16x16x32_bf16 v[212:215], v[104:107], v[120:123], v[212:215]\n\t"
"v_mfma_f32_16x16x32_bf16 v[228:231], v[104:107], v[124:127], v[228:231]\n\t"
"v_mfma_f32_16x16x32_bf16 v[216:219], v[108:111], v[120:123], v[216:219]\n\t"
"v_mfma_f32_16x16x32_bf16 v[232:235], v[108:111], v[124:127], v[232:235]\n\t"
"v_mfma_f32_16x16x32_bf16 v[220:223], v[112:115], v[120:123], v[220:223]\n\t"
"v_mfma_f32_16x16x32_bf16 v[236:239], v[112:115], v[124:127], v[236:239]\n\t"
"v_mfma_f32_16x16x32_bf16 v[224:227], v[116:119], v[120:123], v[224:227]\n\t"
"v_mfma_f32_16x16x32_bf16 v[240:243], v[116:119], v[124:127], v[240:243]\n\t"
"s_waitcnt vmcnt(14)\n\t"
"LS1J2_%=:\n\t"
"s_nop 7\n\t"
    : "={v[212:227]}"(hr), "={v[228:243]}"(hi), "={v[244:247]}"(par) : "s"(ka), "s"(b), "s"(wid), "s"(0)
    :
"v0", "v1", "v2", "v3", "v4", "v5", "v6", "v7", "v8", "v9", "v10", "v11", "v12", "v13", "v14", "v15", "v16", "v17", "v18", "v19", "v20", "v21", "v22", "v23", "v24", "v25", "v26", "v27", "v28", "v29", "v30", "v31", "v32", "v33", "v34", "v35", "v36", "v37", "v38", "v39", "v40", "v41", "v42", "v43", "v44", "v45", "v46", "v47", "v48", "v49", "v50", "v51", "v52", "v53", "v54", "v55", "v56", "v57", "v58", "v59", "v60", "v61", "v62", "v63", "v64", "v65", "v66", "v67", "v68", "v69", "v70", "v71", "v72", "v73", "v74", "v75", "v76", "v77", "v78", "v79", "v80", "v81", "v82", "v83", "v84", "v85", "v86", "v87", "v88", "v89", "v90", "v91", "v92", "v93", "v94", "v95", "v96", "v97", "v98", "v99", "v100", "v101", "v102", "v103", "v104", "v105", "v106", "v107", "v108", "v109", "v110", "v111", "v112", "v113", "v114", "v115", "v116", "v117", "v118", "v119", "v120", "v121", "v122", "v123", "v124", "v125", "v126", "v127", "v128", "v129", "v130", "v131", "v132", "v133", "v134", "v135", "v136", "v137", "v138", "v139", "v140", "v141", "v142", "v143", "v144", "v145", "v146", "v147", "v148", "v149", "v150", "v151", "v152", "v153", "v154", "v155", "v156", "v157", "v158", "v159", "v160", "v161", "v162", "v163", "v164", "v165", "v166", "v167", "v168", "v169", "v170", "v171", "v172", "v173", "v174", "v175", "v176", "v177", "v178", "v179", "v180", "v181", "v182", "v183", "v184", "v185", "v186", "v187", "v188", "v189", "v190", "v191", "v192", "v193", "v194", "v195", "v196", "v197", "v198", "v199", "v200", "v201", "v202", "v203", "v204", "v205", "v206", "v207", "v208", "v209", "v210", "v211", "a0", "a1", "a2", "a3", "a4", "a5", "a6", "a7", "a8", "a9", "a10", "a11", "a12", "a13", "a14", "a15", "a16", "a17", "a18", "a19", "a20", "a21", "a22", "a23", "a24", "a25", "a26", "a27", "a28", "a29", "a30", "a31", "a32", "a33", "a34", "a35", "a36", "a37", "a38", "a39", "a40", "a41", "a42", "a43", "a44", "a45", "a46", "a47", "a48", "a49", "a50", "a51", "a52", "a53", "a54", "a55", "a56", "a57", "a58", "a59", "a60", "a61", "a62", "a63", "a64", "a65", "a66", "a67", "a68", "a69", "a70", "a71", "a72", "a73", "a74", "a75", "a76", "a77", "a78", "a79", "a80", "a81", "a82", "a83", "a84", "a85", "a86", "a87", "a88", "a89", "a90", "a91", "a92", "a93", "a94", "a95", "a96", "a97", "a98", "a99", "a100", "a101", "a102", "a103", "a104", "a105", "a106", "a107", "a108", "a109", "a110", "a111", "a112", "a113", "a114", "a115", "a116", "a117", "a118", "a119", "a120", "a121", "a122", "a123", "a124", "a125", "a126", "a127", "a128", "a129", "a130", "a131", "a132", "a133", "a134", "a135", "a136", "a137", "a138", "a139", "a140", "a141", "a142", "a143", "a144", "a145", "a146", "a147", "a148", "a149", "a150", "a151", "a152", "a153", "a154", "a155", "a156", "a157", "a158", "a159", "a160", "a161", "a162", "a163", "a164", "a165", "a166", "a167", "a168", "a169", "a170", "a171", "a172", "a173", "a174", "a175", "a176", "a177", "a178", "a179", "a180", "a181", "a182", "a183", "a184", "a185", "a186", "a187", "a188", "a189", "a190", "a191", "a192", "a193", "a194", "a195", "a196", "a197", "a198", "a199", "a200", "a201", "a202", "a203", "a204", "a205", "a206", "a207", "a208", "a209", "a210", "a211", "a212", "a213", "a214", "a215", "a216", "a217", "a218", "a219", "a220", "a221", "a222", "a223", "a224", "a225", "a226", "a227", "a228", "a229", "a230", "a231", "a232", "a233", "a234", "a235", "a236", "a237", "a238", "a239", "a240", "a241", "a242", "a243", "a244", "a245", "a246", "a247", "a248", "a249", "a250", "a251", "a252", "a253", "a254", "a255", "s36", "s37", "s38", "s39", "s40", "s41", "s42", "s43", "s44", "s45", "s46", "s47", "s48", "s49", "s50", "s51", "s52", "s53", "s54", "s55", "s56", "s57", "s58", "s59", "s60", "s61", "s62", "s63", "s64", "s65", "s66", "s67", "s68", "s69", "s70", "s71", "s72", "s73", "s74", "s75", "s76", "s77", "s78", "s79", "s80", "s81", "s82", "s83", "s84", "s85", "s86", "s87", "s88", "s89", "s90", "s91", "s92", "s93", "s94", "s95", "s96", "s97", "s98", "s99", "s26", "s27", "s28", "s29", "s30", "s31", "vcc", "scc", "memory"
  );
  {
    const int lane = lane_id(), r = lane & 15, g = lane >> 4;
    const int p = 64 * pg + 16 * wid + r;
    const float lr = __uint_as_float(par[0] << 16), li = __uint_as_float(par[1] << 16);
    const float dt = __expf(__uint_as_float(par[2] << 16));
    const float ar = dt * lr, ai = dt * li;
    float l1r, l1i; cpow(ar, ai, 1.f, l1r, l1i);
    const float nr = l1r - 1.f, ni = l1i, den = 1.f / (lr * lr + li * li);
    const float cfr = (nr * lr + ni * li) * den, cfi = (ni * lr - nr * li) * den;
    float pr = 0.f, pi = 0.f;
#pragma unroll
    for (int i = 0; i < 16; ++i) {
      const float ur = hr[i], ui = hi[i];
      const float vr = cfr * ur - cfi * ui, vi = cfr * ui + cfi * ur;
      const float nr2 = l1r * pr - l1i * pi + vr, ni2 = l1r * pi + l1i * pr + vi;
      pr = nr2; pi = ni2; hr[i] = pr; hi[i] = pi;
    }
    float l16r, l16i; cpow(ar, ai, 16.f, l16r, l16i);
    float cr = 0.f, ci = 0.f, er = 0.f, ei = 0.f;
#pragma unroll
    for (int gg = 0; gg < 4; ++gg) {
      const float xr = __uint_as_float(__builtin_amdgcn_ds_bpermute((r + 16 * gg) * 4, __float_as_uint(pr)));
      const float xi = __uint_as_float(__builtin_amdgcn_ds_bpermute((r + 16 * gg) * 4, __float_as_uint(pi)));
      if (gg == g) { cr = er; ci = ei; }
      const float t = l16r * er - l16i * ei + xr; ei = l16r * ei + l16i * er + xi; er = t;
    }
    cc[0] = cr; cc[1] = ci;
    float t0, t1;
    cn[0] = l1r; cn[1] = l1i;
    cpow(ar, ai, 64.f, t0, t1); cn[2] = t0; cn[3] = t1;
    cpow(ar, ai, 512.f * g, t0, t1); cn[4] = t0; cn[5] = t1;
    cpow(ar, ai, 16.f * g, t0, t1); cn[6] = t0; cn[7] = t1;
    if (g == 0) {
      unsigned long long pk = ((unsigned long long)__float_as_uint(ei) << 32) | __float_as_uint(er);
      __hip_atomic_store((GAS unsigned long long*)(ws + WS_E + (size_t)(chunk * 512 + p) * 8), pk, __ATOMIC_RELAXED, __HIP_MEMORY_SCOPE_AGENT);
    }
  }
  asm volatile(
"s_mov_b64 s[36:37], %0\n\t"
"s_mov_b32 s38, %1\n\t"
"s_mov_b32 s39, %2\n\t"
"s_load_dwordx2 s[42:43], s[36:37], 128\n\t"
"s_and_b32 s40, s38, 7\n\t"
"s_lshl_b32 s40, s40, 2\n\t"
"s_lshr_b32 s41, s38, 6\n\t"
"s_add_u32 s40, s40, s41\n\t"
"s_lshr_b32 s41, s38, 3\n\t"
"s_and_b32 s41, s41, 7\n\t"
"s_and_b32 s44, s39, 1\n\t"
"s_lshr_b32 s45, s39, 1\n\t"
"s_and_b32 s46, s38, 7\n\t"
"s_lshl_b32 s46, s46, 2\n\t"
"s_and_b32 s47, s41, 3\n\t"
"s_add_u32 s46, s46, s47\n\t"
"s_lshr_b32 s47, s38, 5\n\t"
"s_waitcnt lgkmcnt(0)\n\t"
"s_load_dword s26, s[42:43], 69632\n\t"
"s_load_dwordx2 s[48:49], s[36:37], 8\n\t"
"s_load_dwordx2 s[56:57], s[36:37], 24\n\t"
"s_load_dwordx2 s[52:53], s[36:37], 0\n\t"
"s_load_dwordx2 s[60:61], s[36:37], 32\n\t"
"s_load_dwordx2 s[64:65], s[36:37], 16\n\t"
"v_mbcnt_lo_u32_b32 v15, -1, 0\n\t"
"v_mbcnt_hi_u32_b32 v15, -1, v15\n\t"
"v_lshlrev_b32 v14, 4, v15\n\t"
"v_lshrrev_b32 v16, 3, v15\n\t"
"v_lshrrev_b32 v17, 4, v15\n\t"
"v_and_b32 v18, 7, v15\n\t"
"v_xor_b32 v19, v18, v17\n\t"
"v_lshlrev_b32 v19, 4, v19\n\t"
"v_lshl_add_u32 v6, v16, 10, v19\n\t"
"v_xor_b32 v19, v18, v17\n\t"
"v_xor_b32 v19, 4, v19\n\t"
"v_lshlrev_b32 v19, 4, v19\n\t"
"v_lshl_add_u32 v7, v16, 10, v19\n\t"
"v_add_u32 v7, 7168, v7\n\t"
"v_xor_b32 v19, v18, v17\n\t"
"v_lshlrev_b32 v19, 4, v19\n\t"
"v_lshl_add_u32 v8, v16, 10, v19\n\t"
"v_add_u32 v8, 14336, v8\n\t"
"v_xor_b32 v19, v18, v17\n\t"
"v_xor_b32 v19, 4, v19\n\t"
"v_lshlrev_b32 v19, 4, v19\n\t"
"v_lshl_add_u32 v9, v16, 10, v19\n\t"
"v_add_u32 v9, 21504, v9\n\t"
"v_bfe_u32 v19, v15, 1, 3\n\t"
"v_lshlrev_b32 v20, 1, v17\n\t"
"v_xor_b32 v19, v19, v20\n\t"
"v_and_b32 v20, 1, v15\n\t"
"v_lshlrev_b32 v20, 4, v20\n\t"
"v_lshl_add_u32 v20, v17, 13, v20\n\t"
"v_xor_b32 v21, 0, v19\n\t"
"v_lshl_add_u32 v10, v21, 5, v20\n\t"
"v_xor_b32 v21, 0, v19\n\t"
"v_lshl_add_u32 v11, v21, 5, v20\n\t"
"v_add_u32 v11, 31744, v11\n\t"
"v_xor_b32 v21, 1, v19\n\t"
"v_lshl_add_u32 v12, v21, 5, v20\n\t"
"v_add_u32 v12, 63488, v12\n\t"
"v_xor_b32 v21, 1, v19\n\t"
"v_lshl_add_u32 v13, v21, 5, v20\n\t"
"v_add_u32 v13, 95232, v13\n\t"
"s_waitcnt lgkmcnt(0)\n\t"
"s_add_u32 s26, s26, 1\n\t"
"s_xor_b32 s27, s26, 0x5bd1e995\n\t"
"s_and_b32 s49, s49, 0xffff\n\t"
"s_mov_b32 s50, 2097152\n\t"
"s_mov_b32 s51, 0x00020000\n\t"
"s_and_b32 s53, s53, 0xffff\n\t"
"s_mov_b32 s54, 2097152\n\t"
"s_mov_b32 s55, 0x00020000\n\t"
"s_lshl_b32 s81, s40, 8\n\t"
"s_add_u32 s56, s56, s81\n\t"
"s_addc_u32 s57, s57, 0\n\t"
"s_and_b32 s57, s57, 0xffff\n\t"
"s_mov_b32 s58, 4186368\n\t"
"s_mov_b32 s59, 0x00020000\n\t"
"s_add_u32 s60, s60, s81\n\t"
"s_addc_u32 s61, s61, 0\n\t"
"s_and_b32 s61, s61, 0xffff\n\t"
"s_mov_b32 s62, 4186368\n\t"
"s_mov_b32 s63, 0x00020000\n\t"
"s_add_u32 s64, s64, s81\n\t"
"s_addc_u32 s65, s65, 0\n\t"
"s_and_b32 s65, s65, 0xffff\n\t"
"s_mov_b32 s66, 4186368\n\t"
"s_mov_b32 s67, 0x00020000\n\t"
"s_lshl_b32 s81, s41, 18\n\t"
"s_lshl_b32 s82, s39, 16\n\t"
"s_add_u32 s76, s81, s82\n\t"
"s_lshl_b32 s79, s39, 13\n\t"
"s_lshl_b32 s78, s39, 17\n\t"
"s_lshl_b32 s80, s39, 12\n\t"
"s_add_u32 s80, s80, 32768\n\t"
"s_sub_u32 s81, 15, s41\n\t"
"s_cmp_lg_u32 s45, 0\n\t"
"s_cselect_b32 s81, s41, s81\n\t"
"s_lshl_b32 s81, s81, 17\n\t"
"s_lshl_b32 s82, s44, 16\n\t"
"s_add_u32 s77, s81, s82\n\t"
"s_lshl_b32 s82, s40, 19\n\t"
"s_lshl_b32 s83, s41, 16\n\t"
"s_add_u32 s82, s82, s83\n\t"
"s_add_u32 s68, s42, 8388608\n\t"
"s_addc_u32 s69, s43, 0\n\t"
"s_add_u32 s68, s68, s82\n\t"
"s_addc_u32 s69, s69, 0\n\t"
"s_and_b32 s69, s69, 0xffff\n\t"
"s_mov_b32 s70, 65536\n\t"
"s_mov_b32 s71, 0x00020000\n\t"
"s_add_u32 s72, s42, 25165824\n\t"
"s_addc_u32 s73, s43, 0\n\t"
"s_add_u32 s72, s72, s82\n\t"
"s_addc_u32 s73, s73, 0\n\t"
"s_and_b32 s73, s73, 0xffff\n\t"
"s_mov_b32 s74, 65536\n\t"
"s_mov_b32 s75, 0x00020000\n\t"
"s_lshl_b32 s82, s45, 15\n\t"
"s_lshl_b32 s83, s44, 11\n\t"
"s_add_u32 s84, s82, s83\n\t"
"s_lshl_b32 s83, s44, 12\n\t"
"s_add_u32 s85, s82, s83\n\t"
"v_and_b32 v16, 15, v15\n\t"
"v_lshrrev_b32 v17, 4, v15\n\t"
"v_bfe_u32 v18, v15, 1, 3\n\t"
"v_xor_b32 v18, v18, v17\n\t"
"v_lshlrev_b32 v16, 7, v16\n\t"
"s_lshl_b32 s82, s45, 14\n\t"
"s_add_u32 s82, s82, 49152\n\t"
"s_lshl_b32 s83, s44, 2\n\t"
"v_add_u32 v16, s82, v16\n\t"
"v_xor_b32 v19, 0, v18\n\t"
"v_lshl_add_u32 v0, v19, 4, v16\n\t"
"v_xor_b32 v19, 4, v18\n\t"
"v_lshl_add_u32 v1, v19, 4, v16\n\t"
"v_bfe_u32 v18, v15, 2, 2\n\t"
"v_lshl_add_u32 v19, v17, 3, v18\n\t"
"v_lshlrev_b32 v19, 8, v19\n\t"
"v_and_b32 v20, 3, v15\n\t"
"v_lshl_add_u32 v19, v20, 3, v19\n\t"
"v_add_u32 v19, 81920, v19\n\t"
"v_and_b32 v20, 1, v17\n\t"
"v_lshl_add_u32 v20, v18, 1, v20\n\t"
"v_xor_b32 v20, s83, v20\n\t"
"v_xor_b32 v21, 0, v20\n\t"
"v_lshl_add_u32 v2, v21, 5, v19\n\t"
"v_xor_b32 v21, 1, v20\n\t"
"v_lshl_add_u32 v3, v21, 5, v19\n\t"
"v_xor_b32 v21, 2, v20\n\t"
"v_lshl_add_u32 v4, v21, 5, v19\n\t"
"v_xor_b32 v21, 3, v20\n\t"
"v_lshl_add_u32 v5, v21, 5, v19\n\t"
"s_cmp_lg_u32 s45, 0\n\t"
"s_cbranch_scc1 LLW3_%=\n\t"
"s_waitcnt vmcnt(25)\n\t"
"LLW3_%=:\n\t"
"s_barrier\n\t"
"ds_read_b128 v[32:35], v0 offset:0\n\t"
"ds_read_b64_tr_b16 v[64:65], v2 offset:0\n\t"
"ds_read_b64_tr_b16 v[66:67], v2 offset:1024\n\t"
"ds_read_b64_tr_b16 v[68:69], v3 offset:0\n\t"
"ds_read_b64_tr_b16 v[70:71], v3 offset:1024\n\t"
"ds_read_b64_tr_b16 v[72:73], v4 offset:0\n\t"
"ds_read_b64_tr_b16 v[74:75], v4 offset:1024\n\t"
"ds_read_b64_tr_b16 v[76:77], v5 offset:0\n\t"
"ds_read_b64_tr_b16 v[78:79], v5 offset:1024\n\t"
"ds_read_b128 v[36:39], v0 offset:2048\n\t"
"ds_read_b128 v[40:43], v0 offset:4096\n\t"
"ds_read_b128 v[44:47], v0 offset:6144\n\t"
"ds_read_b128 v[48:51], v0 offset:8192\n\t"
"ds_read_b128 v[52:55], v0 offset:10240\n\t"
"ds_read_b128 v[56:59], v0 offset:12288\n\t"
"ds_read_b128 v[60:63], v0 offset:14336\n\t"
"s_waitcnt lgkmcnt(14)\n\t"
"s_waitcnt lgkmcnt(13)\n\t"
"v_mfma_f32_16x16x32_bf16 a[0:3], v[64:67], v[32:35], 0\n\t"
"ds_read_b128 v[80:83], v1 offset:0\n\t"
"s_add_u32 m0, s79, 0\n\t"
"s_add_u32 s81, s76, 256\n\t"
"buffer_load_dwordx4 v6, s[48:51], s81 offen offset:0 lds\n\t"
"s_waitcnt lgkmcnt(12)\n\t"
"v_mfma_f32_16x16x32_bf16 a[4:7], v[68:71], v[32:35], 0\n\t"
"ds_read_b64_tr_b16 v[112:113], v2 offset:8192\n\t"
"s_waitcnt lgkmcnt(11)\n\t"
"v_mfma_f32_16x16x32_bf16 a[8:11], v[72:75], v[32:35], 0\n\t"
"buffer_load_dwordx4 v7, s[48:51], s81 offen offset:1024 lds\n\t"
"s_waitcnt lgkmcnt(9)\n\t"
"v_mfma_f32_16x16x32_bf16 a[12:15], v[76:79], v[32:35], 0\n\t"
"ds_read_b64_tr_b16 v[114:115], v2 offset:9216\n\t"
"s_waitcnt lgkmcnt(9)\n\t"
"v_mfma_f32_16x16x32_bf16 a[16:19], v[64:67], v[36:39], 0\n\t"
"ds_read_b64_tr_b16 v[116:117], v3 offset:8192\n\t"
"buffer_load_dwordx4 v8, s[48:51], s81 offen offset:2048 lds\n\t"
"v_mfma_f32_16x16x32_bf16 a[20:23], v[68:71], v[36:39], 0\n\t"
"ds_read_b64_tr_b16 v[118:119], v3 offset:9216\n\t"
"v_mfma_f32_16x16x32_bf16 a[24:27], v[72:75], v[36:39], 0\n\t"
"ds_read_b64_tr_b16 v[120:121], v4 offset:8192\n\t"
"v_mfma_f32_16x16x32_bf16 a[28:31], v[76:79], v[36:39], 0\n\t"
"buffer_load_dwordx4 v9, s[48:51], s81 offen offset:3072 lds\n\t"
"s_waitcnt lgkmcnt(5)\n\t"
"v_mfma_f32_16x16x32_bf16 a[32:35], v[64:67], v[40:43], 0\n\t"
"ds_read_b64_tr_b16 v[122:123], v4 offset:9216\n\t"
"v_mfma_f32_16x16x32_bf16 a[36:39], v[68:71], v[40:43], 0\n\t"
"ds_read_b64_tr_b16 v[124:125], v5 offset:8192\n\t"
"s_add_u32 m0, s79, 4096\n\t"
"s_add_u32 s81, s76, 33024\n\t"
"buffer_load_dwordx4 v6, s[48:51], s81 offen offset:0 lds\n\t"
"v_mfma_f32_16x16x32_bf16 a[40:43], v[72:75], v[40:43], 0\n\t"
"ds_read_b64_tr_b16 v[126:127], v5 offset:9216\n\t"
"v_mfma_f32_16x16x32_bf16 a[44:47], v[76:79], v[40:43], 0\n\t"
"ds_read_b128 v[84:87], v1 offset:2048\n\t"
"v_mfma_f32_16x16x32_bf16 a[48:51], v[64:67], v[44:47], 0\n\t"
"buffer_load_dwordx4 v7, s[48:51], s81 offen offset:1024 lds\n\t"
"v_mfma_f32_16x16x32_bf16 a[52:55], v[68:71], v[44:47], 0\n\t"
"ds_read_b128 v[88:91], v1 offset:4096\n\t"
"v_mfma_f32_16x16x32_bf16 a[56:59], v[72:75], v[44:47], 0\n\t"
"ds_read_b128 v[92:95], v1 offset:6144\n\t"
"buffer_load_dwordx4 v8, s[48:51], s81 offen offset:2048 lds\n\t"
"v_mfma_f32_16x16x32_bf16 a[60:63], v[76:79], v[44:47], 0\n\t"
"ds_read_b128 v[96:99], v1 offset:8192\n\t"
"v_mfma_f32_16x16x32_bf16 a[64:67], v[64:67], v[48:51], 0\n\t"
"buffer_load_dwordx4 v9, s[48:51], s81 offen offset:3072 lds\n\t"
"v_mfma_f32_16x16x32_bf16 a[68:71], v[68:71], v[48:51], 0\n\t"
"ds_read_b128 v[100:103], v1 offset:10240\n\t"
"v_mfma_f32_16x16x32_bf16 a[72:75], v[72:75], v[48:51], 0\n\t"
"ds_read_b128 v[104:107], v1 offset:12288\n\t"
"s_add_u32 m0, s80, 0\n\t"
"s_add_u32 s81, s78, 1048576\n\t"
"buffer_load_dwordx4 v10, s[56:59], s81 offen offset:0 lds\n\t"
"v_mfma_f32_16x16x32_bf16 a[76:79], v[76:79], v[48:51], 0\n\t"
"ds_read_b128 v[108:111], v1 offset:14336\n\t"
"v_mfma_f32_16x16x32_bf16 a[80:83], v[64:67], v[52:55], 0\n\t"
"v_add_u32 v0, 49152, v0\n\t"
"v_mfma_f32_16x16x32_bf16 a[84:87], v[68:71], v[52:55], 0\n\t"
"buffer_load_dwordx4 v11, s[56:59], s81 offen offset:1024 lds\n\t"
"v_mfma_f32_16x16x32_bf16 a[88:91], v[72:75], v[52:55], 0\n\t"
"v_add_u32 v1, 49152, v1\n\t"
"v_mfma_f32_16x16x32_bf16 a[92:95], v[76:79], v[52:55], 0\n\t"
"v_add_u32 v2, 49152, v2\n\t"
"buffer_load_dwordx4 v12, s[56:59], s81 offen offset:2048 lds\n\t"
"v_mfma_f32_16x16x32_bf16 a[96:99], v[64:67], v[56:59], 0\n\t"
"v_add_u32 v3, 49152, v3\n\t"
"v_mfma_f32_16x16x32_bf16 a[100:103], v[68:71], v[56:59], 0\n\t"
"v_add_u32 v4, 49152, v4\n\t"
"v_mfma_f32_16x16x32_bf16 a[104:107], v[72:75], v[56:59], 0\n\t"
"buffer_load_dwordx4 v13, s[56:59], s81 offen offset:3072 lds\n\t"
"v_mfma_f32_16x16x32_bf16 a[108:111], v[76:79], v[56:59], 0\n\t"
"v_add_u32 v5, 49152, v5\n\t"
"v_mfma_f32_16x16x32_bf16 a[112:115], v[64:67], v[60:63], 0\n\t"
"v_mfma_f32_16x16x32_bf16 a[116:119], v[68:71], v[60:63], 0\n\t"
"v_mfma_f32_16x16x32_bf16 a[120:123], v[72:75], v[60:63], 0\n\t"
"v_mfma_f32_16x16x32_bf16 a[124:127], v[76:79], v[60:63], 0\n\t"
"s_cmp_lg_u32 s45, 0\n\t"
"s_cbranch_scc1 LLW4_%=\n\t"
"s_waitcnt vmcnt(13)\n\t"
"LLW4_%=:\n\t"
"s_waitcnt lgkmcnt(0)\n\t"
"s_barrier\n\t"
"v_mfma_f32_16x16x32_bf16 a[0:3], v[112:115], v[80:83], a[0:3]\n\t"
"ds_read_b128 v[32:35], v0 offset:0\n\t"
"v_mfma_f32_16x16x32_bf16 a[4:7], v[116:119], v[80:83], a[4:7]\n\t"
"s_add_u32 m0, s79, 49152\n\t"
"s_add_u32 s81, s76, 384\n\t"
"buffer_load_dwordx4 v6, s[48:51], s81 offen offset:0 lds\n\t"
"v_mfma_f32_16x16x32_bf16 a[8:11], v[120:123], v[80:83], a[8:11]\n\t"
"ds_read_b64_tr_b16 v[64:65], v2 offset:0\n\t"
"v_mfma_f32_16x16x32_bf16 a[12:15], v[124:127], v[80:83], a[12:15]\n\t"
"ds_read_b64_tr_b16 v[66:67], v2 offset:1024\n\t"
"v_mfma_f32_16x16x32_bf16 a[16:19], v[112:115], v[84:87], a[16:19]\n\t"
"v_mfma_f32_16x16x32_bf16 a[20:23], v[116:119], v[84:87], a[20:23]\n\t"
"buffer_load_dwordx4 v7, s[48:51], s81 offen offset:1024 lds\n\t"
"v_mfma_f32_16x16x32_bf16 a[24:27], v[120:123], v[84:87], a[24:27]\n\t"
"ds_read_b64_tr_b16 v[68:69], v3 offset:0\n\t"
"v_mfma_f32_16x16x32_bf16 a[28:31], v[124:127], v[84:87], a[28:31]\n\t"
"ds_read_b64_tr_b16 v[70:71], v3 offset:1024\n\t"
"v_mfma_f32_16x16x32_bf16 a[32:35], v[112:115], v[88:91], a[32:35]\n\t"
"ds_read_b64_tr_b16 v[72:73], v4 offset:0\n\t"
"v_mfma_f32_16x16x32_bf16 a[36:39], v[116:119], v[88:91], a[36:39]\n\t"
"v_mfma_f32_16x16x32_bf16 a[40:43], v[120:123], v[88:91], a[40:43]\n\t"
"buffer_load_dwordx4 v8, s[48:51], s81 offen offset:2048 lds\n\t"
"v_mfma_f32_16x16x32_bf16 a[44:47], v[124:127], v[88:91], a[44:47]\n\t"
"ds_read_b64_tr_b16 v[74:75], v4 offset:1024\n\t"
"v_mfma_f32_16x16x32_bf16 a[48:51], v[112:115], v[92:95], a[48:51]\n\t"
"ds_read_b64_tr_b16 v[76:77], v5 offset:0\n\t"
"v_mfma_f32_16x16x32_bf16 a[52:55], v[116:119], v[92:95], a[52:55]\n\t"
"v_mfma_f32_16x16x32_bf16 a[56:59], v[120:123], v[92:95], a[56:59]\n\t"
"ds_read_b64_tr_b16 v[78:79], v5 offset:1024\n\t"
"v_mfma_f32_16x16x32_bf16 a[60:63], v[124:127], v[92:95], a[60:63]\n\t"
"buffer_load_dwordx4 v9, s[48:51], s81 offen offset:3072 lds\n\t"
"v_mfma_f32_16x16x32_bf16 a[64:67], v[112:115], v[96:99], a[64:67]\n\t"
"ds_read_b128 v[36:39], v0 offset:2048\n\t"
"v_mfma_f32_16x16x32_bf16 a[68:71], v[116:119], v[96:99], a[68:71]\n\t"
"ds_read_b128 v[40:43], v0 offset:4096\n\t"
"v_mfma_f32_16x16x32_bf16 a[72:75], v[120:123], v[96:99], a[72:75]\n\t"
"v_mfma_f32_16x16x32_bf16 a[76:79], v[124:127], v[96:99], a[76:79]\n\t"
"s_add_u32 m0, s79, 53248\n\t"
"s_add_u32 s81, s76, 33152\n\t"
"buffer_load_dwordx4 v6, s[48:51], s81 offen offset:0 lds\n\t"
"v_mfma_f32_16x16x32_bf16 a[80:83], v[112:115], v[100:103], a[80:83]\n\t"
"ds_read_b128 v[44:47], v0 offset:6144\n\t"
"v_mfma_f32_16x16x32_bf16 a[84:87], v[116:119], v[100:103], a[84:87]\n\t"
"ds_read_b128 v[48:51], v0 offset:8192\n\t"
"v_mfma_f32_16x16x32_bf16 a[88:91], v[120:123], v[100:103], a[88:91]\n\t"
"ds_read_b128 v[52:55], v0 offset:10240\n\t"
"v_mfma_f32_16x16x32_bf16 a[92:95], v[124:127], v[100:103], a[92:95]\n\t"
"v_mfma_f32_16x16x32_bf16 a[96:99], v[112:115], v[104:107], a[96:99]\n\t"
"buffer_load_dwordx4 v7, s[48:51], s81 offen offset:1024 lds\n\t"
"v_mfma_f32_16x16x32_bf16 a[100:103], v[116:119], v[104:107], a[100:103]\n\t"
"ds_read_b128 v[56:59], v0 offset:12288\n\t"
"v_mfma_f32_16x16x32_bf16 a[104:107], v[120:123], v[104:107], a[104:107]\n\t"
"ds_read_b128 v[60:63], v0 offset:14336\n\t"
"s_waitcnt lgkmcnt(5)\n\t"
"v_mfma_f32_16x16x32_bf16 a[108:111], v[124:127], v[104:107], a[108:111]\n\t"
"v_mfma_f32_16x16x32_bf16 a[112:115], v[112:115], v[108:111], a[112:115]\n\t"
"v_mfma_f32_16x16x32_bf16 a[116:119], v[116:119], v[108:111], a[116:119]\n\t"
"v_mfma_f32_16x16x32_bf16 a[120:123], v[120:123], v[108:111], a[120:123]\n\t"
"v_mfma_f32_16x16x32_bf16 a[124:127], v[124:127], v[108:111], a[124:127]\n\t"
"v_mfma_f32_16x16x32_bf16 a[0:3], v[64:67], v[32:35], a[0:3]\n\t"
"ds_read_b128 v[80:83], v1 offset:0\n\t"
"v_mfma_f32_16x16x32_bf16 a[4:7], v[68:71], v[32:35], a[4:7]\n\t"
"buffer_load_dwordx4 v8, s[48:51], s81 offen offset:2048 lds\n\t"
"v_mfma_f32_16x16x32_bf16 a[8:11], v[72:75], v[32:35], a[8:11]\n\t"
"ds_read_b64_tr_b16 v[112:113], v2 offset:8192\n\t"
"v_mfma_f32_16x16x32_bf16 a[12:15], v[76:79], v[32:35], a[12:15]\n\t"
"ds_read_b64_tr_b16 v[114:115], v2 offset:9216\n\t"
"v_mfma_f32_16x16x32_bf16 a[16:19], v[64:67], v[36:39], a[16:19]\n\t"
"ds_read_b64_tr_b16 v[116:117], v3 offset:8192\n\t"
"v_mfma_f32_16x16x32_bf16 a[20:23], v[68:71], v[36:39], a[20:23]\n\t"
"buffer_load_dwordx4 v9, s[48:51], s81 offen offset:3072 lds\n\t"
"v_mfma_f32_16x16x32_bf16 a[24:27], v[72:75], v[36:39], a[24:27]\n\t"
"ds_read_b64_tr_b16 v[118:119], v3 offset:9216\n\t"
"v_mfma_f32_16x16x32_bf16 a[28:31], v[76:79], v[36:39], a[28:31]\n\t"
"ds_read_b64_tr_b16 v[120:121], v4 offset:8192\n\t"
"v_mfma_f32_16x16x32_bf16 a[32:35], v[64:67], v[40:43], a[32:35]\n\t"
"ds_read_b64_tr_b16 v[122:123], v4 offset:9216\n\t"
"v_mfma_f32_16x16x32_bf16 a[36:39], v[68:71], v[40:43], a[36:39]\n\t"
"ds_read_b64_tr_b16 v[124:125], v5 offset:8192\n\t"
"v_mfma_f32_16x16x32_bf16 a[40:43], v[72:75], v[40:43], a[40:43]\n\t"
"s_add_u32 m0, s80, 49152\n\t"
"s_add_u32 s81, s78, 1572864\n\t"
"buffer_load_dwordx4 v10, s[56:59], s81 offen offset:0 lds\n\t"
"v_mfma_f32_16x16x32_bf16 a[44:47], v[76:79], v[40:43], a[44:47]\n\t"
"ds_read_b64_tr_b16 v[126:127], v5 offset:9216\n\t"
"s_waitcnt lgkmcnt(5)\n\t"
"v_mfma_f32_16x16x32_bf16 a[48:51], v[64:67], v[44:47], a[48:51]\n\t"
"ds_read_b128 v[84:87], v1 offset:2048\n\t"
"v_mfma_f32_16x16x32_bf16 a[52:55], v[68:71], v[44:47], a[52:55]\n\t"
"ds_read_b128 v[88:91], v1 offset:4096\n\t"
"v_mfma_f32_16x16x32_bf16 a[56:59], v[72:75], v[44:47], a[56:59]\n\t"
"ds_read_b128 v[92:95], v1 offset:6144\n\t"
"v_mfma_f32_16x16x32_bf16 a[60:63], v[76:79], v[44:47], a[60:63]\n\t"
"buffer_load_dwordx4 v11, s[56:59], s81 offen offset:1024 lds\n\t"
"v_mfma_f32_16x16x32_bf16 a[64:67], v[64:67], v[48:51], a[64:67]\n\t"
"ds_read_b128 v[96:99], v1 offset:8192\n\t"
"v_mfma_f32_16x16x32_bf16 a[68:71], v[68:71], v[48:51], a[68:71]\n\t"
"ds_read_b128 v[100:103], v1 offset:10240\n\t"
"v_mfma_f32_16x16x32_bf16 a[72:75], v[72:75], v[48:51], a[72:75]\n\t"
"ds_read_b128 v[104:107], v1 offset:12288\n\t"
"v_mfma_f32_16x16x32_bf16 a[76:79], v[76:79], v[48:51], a[76:79]\n\t"
"buffer_load_dwordx4 v12, s[56:59], s81 offen offset:2048 lds\n\t"
"v_mfma_f32_16x16x32_bf16 a[80:83], v[64:67], v[52:55], a[80:83]\n\t"
"ds_read_b128 v[108:111], v1 offset:14336\n\t"
"v_mfma_f32_16x16x32_bf16 a[84:87], v[68:71], v[52:55], a[84:87]\n\t"
"v_add_u32 v0, -98304, v0\n\t"
"v_mfma_f32_16x16x32_bf16 a[88:91], v[72:75], v[52:55], a[88:91]\n\t"
"v_add_u32 v1, -98304, v1\n\t"
"v_mfma_f32_16x16x32_bf16 a[92:95], v[76:79], v[52:55], a[92:95]\n\t"
"v_add_u32 v2, -98304, v2\n\t"
"v_mfma_f32_16x16x32_bf16 a[96:99], v[64:67], v[56:59], a[96:99]\n\t"
"buffer_load_dwordx4 v13, s[56:59], s81 offen offset:3072 lds\n\t"
"v_mfma_f32_16x16x32_bf16 a[100:103], v[68:71], v[56:59], a[100:103]\n\t"
"v_add_u32 v3, -98304, v3\n\t"
"v_mfma_f32_16x16x32_bf16 a[104:107], v[72:75], v[56:59], a[104:107]\n\t"
"v_add_u32 v4, -98304, v4\n\t"
"v_mfma_f32_16x16x32_bf16 a[108:111], v[76:79], v[56:59], a[108:111]\n\t"
"v_add_u32 v5, -98304, v5\n\t"
"v_mfma_f32_16x16x32_bf16 a[112:115], v[64:67], v[60:63], a[112:115]\n\t"
"v_mfma_f32_16x16x32_bf16 a[116:119], v[68:71], v[60:63], a[116:119]\n\t"
"v_mfma_f32_16x16x32_bf16 a[120:123], v[72:75], v[60:63], a[120:123]\n\t"
"v_mfma_f32_16x16x32_bf16 a[124:127], v[76:79], v[60:63], a[124:127]\n\t"
"s_waitcnt vmcnt(12) lgkmcnt(0)\n\t"
"s_barrier\n\t"
"s_load_dwordx2 s[28:29], s[36:37], 32\n\t"
"s_waitcnt lgkmcnt(0)\n\t"
"s_and_b32 s29, s29, 0xffff\n\t"
"s_mov_b32 s30, 4194304\n\t"
"s_mov_b32 s31, 0x00020000\n\t"
"s_lshl_b32 s98, s38, 2\n\t"
"s_add_u32 s98, s98, s39\n\t"
"s_mul_i32 s97, s98, 4096\n\t"
"s_mov_b32 m0, 147456\n\t"
"s_nop 0\n\t"
"buffer_load_dwordx4 v14, s[28:31], s97 offen offset:0 lds\n\t"
"v_mfma_f32_16x16x32_bf16 a[0:3], v[112:115], v[80:83], a[0:3]\n\t"
"ds_read_b128 v[32:35], v0 offset:0\n\t"
"v_mfma_f32_16x16x32_bf16 a[4:7], v[116:119], v[80:83], a[4:7]\n\t"
"s_add_u32 m0, s79, 98304\n\t"
"s_add_u32 s81, s76, 512\n\t"
"buffer_load_dwordx4 v6, s[48:51], s81 offen offset:0 lds\n\t"
"v_mfma_f32_16x16x32_bf16 a[8:11], v[120:123], v[80:83], a[8:11]\n\t"
"ds_read_b64_tr_b16 v[64:65], v2 offset:0\n\t"
"v_mfma_f32_16x16x32_bf16 a[12:15], v[124:127], v[80:83], a[12:15]\n\t"
"ds_read_b64_tr_b16 v[66:67], v2 offset:1024\n\t"
"v_mfma_f32_16x16x32_bf16 a[16:19], v[112:115], v[84:87], a[16:19]\n\t"
"v_mfma_f32_16x16x32_bf16 a[20:23], v[116:119], v[84:87], a[20:23]\n\t"
"buffer_load_dwordx4 v7, s[48:51], s81 offen offset:1024 lds\n\t"
"v_mfma_f32_16x16x32_bf16 a[24:27], v[120:123], v[84:87], a[24:27]\n\t"
"ds_read_b64_tr_b16 v[68:69], v3 offset:0\n\t"
"v_mfma_f32_16x16x32_bf16 a[28:31], v[124:127], v[84:87], a[28:31]\n\t"
"ds_read_b64_tr_b16 v[70:71], v3 offset:1024\n\t"
"v_mfma_f32_16x16x32_bf16 a[32:35], v[112:115], v[88:91], a[32:35]\n\t"
"ds_read_b64_tr_b16 v[72:73], v4 offset:0\n\t"
"v_mfma_f32_16x16x32_bf16 a[36:39], v[116:119], v[88:91], a[36:39]\n\t"
"v_mfma_f32_16x16x32_bf16 a[40:43], v[120:123], v[88:91], a[40:43]\n\t"
"buffer_load_dwordx4 v8, s[48:51], s81 offen offset:2048 lds\n\t"
"v_mfma_f32_16x16x32_bf16 a[44:47], v[124:127], v[88:91], a[44:47]\n\t"
"ds_read_b64_tr_b16 v[74:75], v4 offset:1024\n\t"
"v_mfma_f32_16x16x32_bf16 a[48:51], v[112:115], v[92:95], a[48:51]\n\t"
"ds_read_b64_tr_b16 v[76:77], v5 offset:0\n\t"
"v_mfma_f32_16x16x32_bf16 a[52:55], v[116:119], v[92:95], a[52:55]\n\t"
"v_mfma_f32_16x16x32_bf16 a[56:59], v[120:123], v[92:95], a[56:59]\n\t"
"ds_read_b64_tr_b16 v[78:79], v5 offset:1024\n\t"
"v_mfma_f32_16x16x32_bf16 a[60:63], v[124:127], v[92:95], a[60:63]\n\t"
"buffer_load_dwordx4 v9, s[48:51], s81 offen offset:3072 lds\n\t"
"v_mfma_f32_16x16x32_bf16 a[64:67], v[112:115], v[96:99], a[64:67]\n\t"
"ds_read_b128 v[36:39], v0 offset:2048\n\t"
"v_mfma_f32_16x16x32_bf16 a[68:71], v[116:119], v[96:99], a[68:71]\n\t"
"ds_read_b128 v[40:43], v0 offset:4096\n\t"
"v_mfma_f32_16x16x32_bf16 a[72:75], v[120:123], v[96:99], a[72:75]\n\t"
"v_mfma_f32_16x16x32_bf16 a[76:79], v[124:127], v[96:99], a[76:79]\n\t"
"s_add_u32 m0, s79, 102400\n\t"
"s_add_u32 s81, s76, 33280\n\t"
"buffer_load_dwordx4 v6, s[48:51], s81 offen offset:0 lds\n\t"
"v_mfma_f32_16x16x32_bf16 a[80:83], v[112:115], v[100:103], a[80:83]\n\t"
"ds_read_b128 v[44:47], v0 offset:6144\n\t"
"v_mfma_f32_16x16x32_bf16 a[84:87], v[116:119], v[100:103], a[84:87]\n\t"
"ds_read_b128 v[48:51], v0 offset:8192\n\t"
"v_mfma_f32_16x16x32_bf16 a[88:91], v[120:123], v[100:103], a[88:91]\n\t"
"ds_read_b128 v[52:55], v0 offset:10240\n\t"
"v_mfma_f32_16x16x32_bf16 a[92:95], v[124:127], v[100:103], a[92:95]\n\t"
"v_mfma_f32_16x16x32_bf16 a[96:99], v[112:115], v[104:107], a[96:99]\n\t"
"buffer_load_dwordx4 v7, s[48:51], s81 offen offset:1024 lds\n\t"
"v_mfma_f32_16x16x32_bf16 a[100:103], v[116:119], v[104:107], a[100:103]\n\t"
"ds_read_b128 v[56:59], v0 offset:12288\n\t"
"v_mfma_f32_16x16x32_bf16 a[104:107], v[120:123], v[104:107], a[104:107]\n\t"
"ds_read_b128 v[60:63], v0 offset:14336\n\t"
"s_waitcnt lgkmcnt(5)\n\t"
"v_mfma_f32_16x16x32_bf16 a[108:111], v[124:127], v[104:107], a[108:111]\n\t"
"v_mfma_f32_16x16x32_bf16 a[112:115], v[112:115], v[108:111], a[112:115]\n\t"
"v_mfma_f32_16x16x32_bf16 a[116:119], v[116:119], v[108:111], a[116:119]\n\t"
"v_mfma_f32_16x16x32_bf16 a[120:123], v[120:123], v[108:111], a[120:123]\n\t"
"v_mfma_f32_16x16x32_bf16 a[124:127], v[124:127], v[108:111], a[124:127]\n\t"
"v_mfma_f32_16x16x32_bf16 a[0:3], v[64:67], v[32:35], a[0:3]\n\t"
"ds_read_b128 v[80:83], v1 offset:0\n\t"
"v_mfma_f32_16x16x32_bf16 a[4:7], v[68:71], v[32:35], a[4:7]\n\t"
"buffer_load_dwordx4 v8, s[48:51], s81 offen offset:2048 lds\n\t"
"v_mfma_f32_16x16x32_bf16 a[8:11], v[72:75], v[32:35], a[8:11]\n\t"
"ds_read_b64_tr_b16 v[112:113], v2 offset:8192\n\t"
"v_mfma_f32_16x16x32_bf16 a[12:15], v[76:79], v[32:35], a[12:15]\n\t"
"ds_read_b64_tr_b16 v[114:115], v2 offset:9216\n\t"
"v_mfma_f32_16x16x32_bf16 a[16:19], v[64:67], v[36:39], a[16:19]\n\t"
"ds_read_b64_tr_b16 v[116:117], v3 offset:8192\n\t"
"v_mfma_f32_16x16x32_bf16 a[20:23], v[68:71], v[36:39], a[20:23]\n\t"
"buffer_load_dwordx4 v9, s[48:51], s81 offen offset:3072 lds\n\t"
"v_mfma_f32_16x16x32_bf16 a[24:27], v[72:75], v[36:39], a[24:27]\n\t"
"ds_read_b64_tr_b16 v[118:119], v3 offset:9216\n\t"
"v_mfma_f32_16x16x32_bf16 a[28:31], v[76:79], v[36:39], a[28:31]\n\t"
"ds_read_b64_tr_b16 v[120:121], v4 offset:8192\n\t"
"v_mfma_f32_16x16x32_bf16 a[32:35], v[64:67], v[40:43], a[32:35]\n\t"
"ds_read_b64_tr_b16 v[122:123], v4 offset:9216\n\t"
"v_mfma_f32_16x16x32_bf16 a[36:39], v[68:71], v[40:43], a[36:39]\n\t"
"ds_read_b64_tr_b16 v[124:125], v5 offset:8192\n\t"
"v_mfma_f32_16x16x32_bf16 a[40:43], v[72:75], v[40:43], a[40:43]\n\t"
"s_add_u32 m0, s80, 98304\n\t"
"s_add_u32 s81, s78, 2097152\n\t"
"buffer_load_dwordx4 v10, s[56:59], s81 offen offset:0 lds\n\t"
"v_mfma_f32_16x16x32_bf16 a[44:47], v[76:79], v[40:43], a[44:47]\n\t"
"ds_read_b64_tr_b16 v[126:127], v5 offset:9216\n\t"
"s_waitcnt lgkmcnt(5)\n\t"
"v_mfma_f32_16x16x32_bf16 a[48:51], v[64:67], v[44:47], a[48:51]\n\t"
"ds_read_b128 v[84:87], v1 offset:2048\n\t"
"v_mfma_f32_16x16x32_bf16 a[52:55], v[68:71], v[44:47], a[52:55]\n\t"
"ds_read_b128 v[88:91], v1 offset:4096\n\t"
"v_mfma_f32_16x16x32_bf16 a[56:59], v[72:75], v[44:47], a[56:59]\n\t"
"ds_read_b128 v[92:95], v1 offset:6144\n\t"
"v_mfma_f32_16x16x32_bf16 a[60:63], v[76:79], v[44:47], a[60:63]\n\t"
"buffer_load_dwordx4 v11, s[56:59], s81 offen offset:1024 lds\n\t"
"v_mfma_f32_16x16x32_bf16 a[64:67], v[64:67], v[48:51], a[64:67]\n\t"
"ds_read_b128 v[96:99], v1 offset:8192\n\t"
"v_mfma_f32_16x16x32_bf16 a[68:71], v[68:71], v[48:51], a[68:71]\n\t"
"ds_read_b128 v[100:103], v1 offset:10240\n\t"
"v_mfma_f32_16x16x32_bf16 a[72:75], v[72:75], v[48:51], a[72:75]\n\t"
"ds_read_b128 v[104:107], v1 offset:12288\n\t"
"v_mfma_f32_16x16x32_bf16 a[76:79], v[76:79], v[48:51], a[76:79]\n\t"
"buffer_load_dwordx4 v12, s[56:59], s81 offen offset:2048 lds\n\t"
"v_mfma_f32_16x16x32_bf16 a[80:83], v[64:67], v[52:55], a[80:83]\n\t"
"ds_read_b128 v[108:111], v1 offset:14336\n\t"
"v_mfma_f32_16x16x32_bf16 a[84:87], v[68:71], v[52:55], a[84:87]\n\t"
"v_add_u32 v0, 49152, v0\n\t"
"v_mfma_f32_16x16x32_bf16 a[88:91], v[72:75], v[52:55], a[88:91]\n\t"
"v_add_u32 v1, 49152, v1\n\t"
"v_mfma_f32_16x16x32_bf16 a[92:95], v[76:79], v[52:55], a[92:95]\n\t"
"v_add_u32 v2, 49152, v2\n\t"
"v_mfma_f32_16x16x32_bf16 a[96:99], v[64:67], v[56:59], a[96:99]\n\t"
"buffer_load_dwordx4 v13, s[56:59], s81 offen offset:3072 lds\n\t"
"v_mfma_f32_16x16x32_bf16 a[100:103], v[68:71], v[56:59], a[100:103]\n\t"
"v_add_u32 v3, 49152, v3\n\t"
"v_mfma_f32_16x16x32_bf16 a[104:107], v[72:75], v[56:59], a[104:107]\n\t"
"v_add_u32 v4, 49152, v4\n\t"
"v_mfma_f32_16x16x32_bf16 a[108:111], v[76:79], v[56:59], a[108:111]\n\t"
"v_add_u32 v5, 49152, v5\n\t"
"v_mfma_f32_16x16x32_bf16 a[112:115], v[64:67], v[60:63], a[112:115]\n\t"
"v_mfma_f32_16x16x32_bf16 a[116:119], v[68:71], v[60:63], a[116:119]\n\t"
"v_mfma_f32_16x16x32_bf16 a[120:123], v[72:75], v[60:63], a[120:123]\n\t"
"v_mfma_f32_16x16x32_bf16 a[124:127], v[76:79], v[60:63], a[124:127]\n\t"
"s_waitcnt vmcnt(13) lgkmcnt(0)\n\t"
"s_barrier\n\t"
"s_cmp_lg_u32 s39, 0\n\t"
"s_cbranch_scc1 LW05_%=\n\t"
"s_lshl_b32 s82, s47, 8\n\t"
"s_add_u32 s82, s82, 24576\n\t"
"s_lshl_b32 s83, s46, 3\n\t"
"s_add_u32 s82, s82, s83\n\t"
"v_mov_b32 v204, s82\n\t"
"v_mov_b32 v206, s26\n\t"
"v_mov_b32 v207, s27\n\t"
"s_mov_b64 s[86:87], exec\n\t"
"s_mov_b64 exec, 1\n\t"
"s_nop 1\n\t"
"global_store_dwordx2 v204, v[206:207], s[42:43] sc1\n\t"
"s_mov_b64 exec, s[86:87]\n\t"
"s_nop 1\n\t"
"LW05_%=:\n\t"
"s_mov_b32 m0, 147456\n\t"
"s_nop 0\n\t"
"buffer_load_dwordx4 v14, s[28:31], s97 offen offset:1024 lds\n\t"
"v_mfma_f32_16x16x32_bf16 a[0:3], v[112:115], v[80:83], a[0:3]\n\t"
"ds_read_b128 v[32:35], v0 offset:0\n\t"
"v_mfma_f32_16x16x32_bf16 a[4:7], v[116:119], v[80:83], a[4:7]\n\t"
"s_add_u32 m0, s79, 0\n\t"
"s_add_u32 s81, s76, 640\n\t"
"buffer_load_dwordx4 v6, s[48:51], s81 offen offset:0 lds\n\t"
"v_mfma_f32_16x16x32_bf16 a[8:11], v[120:123], v[80:83], a[8:11]\n\t"
"ds_read_b64_tr_b16 v[64:65], v2 offset:0\n\t"
"v_mfma_f32_16x16x32_bf16 a[12:15], v[124:127], v[80:83], a[12:15]\n\t"
"ds_read_b64_tr_b16 v[66:67], v2 offset:1024\n\t"
"v_mfma_f32_16x16x32_bf16 a[16:19], v[112:115], v[84:87], a[16:19]\n\t"
"v_mfma_f32_16x16x32_bf16 a[20:23], v[116:119], v[84:87], a[20:23]\n\t"
"buffer_load_dwordx4 v7, s[48:51], s81 offen offset:1024 lds\n\t"
"v_mfma_f32_16x16x32_bf16 a[24:27], v[120:123], v[84:87], a[24:27]\n\t"
"ds_read_b64_tr_b16 v[68:69], v3 offset:0\n\t"
"v_mfma_f32_16x16x32_bf16 a[28:31], v[124:127], v[84:87], a[28:31]\n\t"
"ds_read_b64_tr_b16 v[70:71], v3 offset:1024\n\t"
"v_mfma_f32_16x16x32_bf16 a[32:35], v[112:115], v[88:91], a[32:35]\n\t"
"ds_read_b64_tr_b16 v[72:73], v4 offset:0\n\t"
"v_mfma_f32_16x16x32_bf16 a[36:39], v[116:119], v[88:91], a[36:39]\n\t"
"v_mfma_f32_16x16x32_bf16 a[40:43], v[120:123], v[88:91], a[40:43]\n\t"
"buffer_load_dwordx4 v8, s[48:51], s81 offen offset:2048 lds\n\t"
"v_mfma_f32_16x16x32_bf16 a[44:47], v[124:127], v[88:91], a[44:47]\n\t"
"ds_read_b64_tr_b16 v[74:75], v4 offset:1024\n\t"
"v_mfma_f32_16x16x32_bf16 a[48:51], v[112:115], v[92:95], a[48:51]\n\t"
"ds_read_b64_tr_b16 v[76:77], v5 offset:0\n\t"
"v_mfma_f32_16x16x32_bf16 a[52:55], v[116:119], v[92:95], a[52:55]\n\t"
"v_mfma_f32_16x16x32_bf16 a[56:59], v[120:123], v[92:95], a[56:59]\n\t"
"ds_read_b64_tr_b16 v[78:79], v5 offset:1024\n\t"
"v_mfma_f32_16x16x32_bf16 a[60:63], v[124:127], v[92:95], a[60:63]\n\t"
"buffer_load_dwordx4 v9, s[48:51], s81 offen offset:3072 lds\n\t"
"v_mfma_f32_16x16x32_bf16 a[64:67], v[112:115], v[96:99], a[64:67]\n\t"
"ds_read_b128 v[36:39], v0 offset:2048\n\t"
"v_mfma_f32_16x16x32_bf16 a[68:71], v[116:119], v[96:99], a[68:71]\n\t"
"ds_read_b128 v[40:43], v0 offset:4096\n\t"
"v_mfma_f32_16x16x32_bf16 a[72:75], v[120:123], v[96:99], a[72:75]\n\t"
"v_mfma_f32_16x16x32_bf16 a[76:79], v[124:127], v[96:99], a[76:79]\n\t"
"s_add_u32 m0, s79, 4096\n\t"
"s_add_u32 s81, s76, 33408\n\t"
"buffer_load_dwordx4 v6, s[48:51], s81 offen offset:0 lds\n\t"
"v_mfma_f32_16x16x32_bf16 a[80:83], v[112:115], v[100:103], a[80:83]\n\t"
"ds_read_b128 v[44:47], v0 offset:6144\n\t"
"v_mfma_f32_16x16x32_bf16 a[84:87], v[116:119], v[100:103], a[84:87]\n\t"
"ds_read_b128 v[48:51], v0 offset:8192\n\t"
"v_mfma_f32_16x16x32_bf16 a[88:91], v[120:123], v[100:103], a[88:91]\n\t"
"ds_read_b128 v[52:55], v0 offset:10240\n\t"
"v_mfma_f32_16x16x32_bf16 a[92:95], v[124:127], v[100:103], a[92:95]\n\t"
"v_mfma_f32_16x16x32_bf16 a[96:99], v[112:115], v[104:107], a[96:99]\n\t"
"buffer_load_dwordx4 v7, s[48:51], s81 offen offset:1024 lds\n\t"
"v_mfma_f32_16x16x32_bf16 a[100:103], v[116:119], v[104:107], a[100:103]\n\t"
"ds_read_b128 v[56:59], v0 offset:12288\n\t"
"v_mfma_f32_16x16x32_bf16 a[104:107], v[120:123], v[104:107], a[104:107]\n\t"
"ds_read_b128 v[60:63], v0 offset:14336\n\t"
"s_waitcnt lgkmcnt(5)\n\t"
"v_mfma_f32_16x16x32_bf16 a[108:111], v[124:127], v[104:107], a[108:111]\n\t"
"v_mfma_f32_16x16x32_bf16 a[112:115], v[112:115], v[108:111], a[112:115]\n\t"
"v_mfma_f32_16x16x32_bf16 a[116:119], v[116:119], v[108:111], a[116:119]\n\t"
"v_mfma_f32_16x16x32_bf16 a[120:123], v[120:123], v[108:111], a[120:123]\n\t"
"v_mfma_f32_16x16x32_bf16 a[124:127], v[124:127], v[108:111], a[124:127]\n\t"
"v_mfma_f32_16x16x32_bf16 a[0:3], v[64:67], v[32:35], a[0:3]\n\t"
"ds_read_b128 v[80:83], v1 offset:0\n\t"
"v_mfma_f32_16x16x32_bf16 a[4:7], v[68:71], v[32:35], a[4:7]\n\t"
"buffer_load_dwordx4 v8, s[48:51], s81 offen offset:2048 lds\n\t"
"v_mfma_f32_16x16x32_bf16 a[8:11], v[72:75], v[32:35], a[8:11]\n\t"
"ds_read_b64_tr_b16 v[112:113], v2 offset:8192\n\t"
"v_mfma_f32_16x16x32_bf16 a[12:15], v[76:79], v[32:35], a[12:15]\n\t"
"ds_read_b64_tr_b16 v[114:115], v2 offset:9216\n\t"
"v_mfma_f32_16x16x32_bf16 a[16:19], v[64:67], v[36:39], a[16:19]\n\t"
"ds_read_b64_tr_b16 v[116:117], v3 offset:8192\n\t"
"v_mfma_f32_16x16x32_bf16 a[20:23], v[68:71], v[36:39], a[20:23]\n\t"
"buffer_load_dwordx4 v9, s[48:51], s81 offen offset:3072 lds\n\t"
"v_mfma_f32_16x16x32_bf16 a[24:27], v[72:75], v[36:39], a[24:27]\n\t"
"ds_read_b64_tr_b16 v[118:119], v3 offset:9216\n\t"
"v_mfma_f32_16x16x32_bf16 a[28:31], v[76:79], v[36:39], a[28:31]\n\t"
"ds_read_b64_tr_b16 v[120:121], v4 offset:8192\n\t"
"v_mfma_f32_16x16x32_bf16 a[32:35], v[64:67], v[40:43], a[32:35]\n\t"
"ds_read_b64_tr_b16 v[122:123], v4 offset:9216\n\t"
"v_mfma_f32_16x16x32_bf16 a[36:39], v[68:71], v[40:43], a[36:39]\n\t"
"ds_read_b64_tr_b16 v[124:125], v5 offset:8192\n\t"
"v_mfma_f32_16x16x32_bf16 a[40:43], v[72:75], v[40:43], a[40:43]\n\t"
"s_add_u32 m0, s80, 0\n\t"
"s_add_u32 s81, s78, 2621440\n\t"
"buffer_load_dwordx4 v10, s[56:59], s81 offen offset:0 lds\n\t"
"v_mfma_f32_16x16x32_bf16 a[44:47], v[76:79], v[40:43], a[44:47]\n\t"
"ds_read_b64_tr_b16 v[126:127], v5 offset:9216\n\t"
"s_waitcnt lgkmcnt(5)\n\t"
"v_mfma_f32_16x16x32_bf16 a[48:51], v[64:67], v[44:47], a[48:51]\n\t"
"ds_read_b128 v[84:87], v1 offset:2048\n\t"
"v_mfma_f32_16x16x32_bf16 a[52:55], v[68:71], v[44:47], a[52:55]\n\t"
"ds_read_b128 v[88:91], v1 offset:4096\n\t"
"v_mfma_f32_16x16x32_bf16 a[56:59], v[72:75], v[44:47], a[56:59]\n\t"
"ds_read_b128 v[92:95], v1 offset:6144\n\t"
"v_mfma_f32_16x16x32_bf16 a[60:63], v[76:79], v[44:47], a[60:63]\n\t"
"buffer_load_dwordx4 v11, s[56:59], s81 offen offset:1024 lds\n\t"
"v_mfma_f32_16x16x32_bf16 a[64:67], v[64:67], v[48:51], a[64:67]\n\t"
"ds_read_b128 v[96:99], v1 offset:8192\n\t"
"v_mfma_f32_16x16x32_bf16 a[68:71], v[68:71], v[48:51], a[68:71]\n\t"
"ds_read_b128 v[100:103], v1 offset:10240\n\t"
"v_mfma_f32_16x16x32_bf16 a[72:75], v[72:75], v[48:51], a[72:75]\n\t"
"ds_read_b128 v[104:107], v1 offset:12288\n\t"
"v_mfma_f32_16x16x32_bf16 a[76:79], v[76:79], v[48:51], a[76:79]\n\t"
"buffer_load_dwordx4 v12, s[56:59], s81 offen offset:2048 lds\n\t"
"v_mfma_f32_16x16x32_bf16 a[80:83], v[64:67], v[52:55], a[80:83]\n\t"
"ds_read_b128 v[108:111], v1 offset:14336\n\t"
"v_mfma_f32_16x16x32_bf16 a[84:87], v[68:71], v[52:55], a[84:87]\n\t"
"v_add_u32 v0, 49152, v0\n\t"
"v_mfma_f32_16x16x32_bf16 a[88:91], v[72:75], v[52:55], a[88:91]\n\t"
"v_add_u32 v1, 49152, v1\n\t"
"v_mfma_f32_16x16x32_bf16 a[92:95], v[76:79], v[52:55], a[92:95]\n\t"
"v_add_u32 v2, 49152, v2\n\t"
"v_mfma_f32_16x16x32_bf16 a[96:99], v[64:67], v[56:59], a[96:99]\n\t"
"buffer_load_dwordx4 v13, s[56:59], s81 offen offset:3072 lds\n\t"
"v_mfma_f32_16x16x32_bf16 a[100:103], v[68:71], v[56:59], a[100:103]\n\t"
"v_add_u32 v3, 49152, v3\n\t"
"v_mfma_f32_16x16x32_bf16 a[104:107], v[72:75], v[56:59], a[104:107]\n\t"
"v_add_u32 v4, 49152, v4\n\t"
"v_mfma_f32_16x16x32_bf16 a[108:111], v[76:79], v[56:59], a[108:111]\n\t"
"v_add_u32 v5, 49152, v5\n\t"
"v_mfma_f32_16x16x32_bf16 a[112:115], v[64:67], v[60:63], a[112:115]\n\t"
"v_mfma_f32_16x16x32_bf16 a[116:119], v[68:71], v[60:63], a[116:119]\n\t"
"v_mfma_f32_16x16x32_bf16 a[120:123], v[72:75], v[60:63], a[120:123]\n\t"
"v_mfma_f32_16x16x32_bf16 a[124:127], v[76:79], v[60:63], a[124:127]\n\t"
"s_waitcnt vmcnt(13) lgkmcnt(0)\n\t"
"s_barrier\n\t"
"s_mov_b32 m0, 147456\n\t"
"s_nop 0\n\t"
"buffer_load_dwordx4 v14, s[28:31], s97 offen offset:2048 lds\n\t"
"v_mfma_f32_16x16x32_bf16 a[0:3], v[112:115], v[80:83], a[0:3]\n\t"
"ds_read_b128 v[32:35], v0 offset:0\n\t"
"v_mfma_f32_16x16x32_bf16 a[4:7], v[116:119], v[80:83], a[4:7]\n\t"
"s_add_u32 m0, s79, 49152\n\t"
"s_add_u32 s81, s76, 768\n\t"
"buffer_load_dwordx4 v6, s[48:51], s81 offen offset:0 lds\n\t"
"v_mfma_f32_16x16x32_bf16 a[8:11], v[120:123], v[80:83], a[8:11]\n\t"
"ds_read_b64_tr_b16 v[64:65], v2 offset:0\n\t"
"v_mfma_f32_16x16x32_bf16 a[12:15], v[124:127], v[80:83], a[12:15]\n\t"
"ds_read_b64_tr_b16 v[66:67], v2 offset:1024\n\t"
"v_mfma_f32_16x16x32_bf16 a[16:19], v[112:115], v[84:87], a[16:19]\n\t"
"v_mfma_f32_16x16x32_bf16 a[20:23], v[116:119], v[84:87], a[20:23]\n\t"
"buffer_load_dwordx4 v7, s[48:51], s81 offen offset:1024 lds\n\t"
"v_mfma_f32_16x16x32_bf16 a[24:27], v[120:123], v[84:87], a[24:27]\n\t"
"ds_read_b64_tr_b16 v[68:69], v3 offset:0\n\t"
"v_mfma_f32_16x16x32_bf16 a[28:31], v[124:127], v[84:87], a[28:31]\n\t"
"ds_read_b64_tr_b16 v[70:71], v3 offset:1024\n\t"
"v_mfma_f32_16x16x32_bf16 a[32:35], v[112:115], v[88:91], a[32:35]\n\t"
"ds_read_b64_tr_b16 v[72:73], v4 offset:0\n\t"
"v_mfma_f32_16x16x32_bf16 a[36:39], v[116:119], v[88:91], a[36:39]\n\t"
"v_mfma_f32_16x16x32_bf16 a[40:43], v[120:123], v[88:91], a[40:43]\n\t"
"buffer_load_dwordx4 v8, s[48:51], s81 offen offset:2048 lds\n\t"
"v_mfma_f32_16x16x32_bf16 a[44:47], v[124:127], v[88:91], a[44:47]\n\t"
"ds_read_b64_tr_b16 v[74:75], v4 offset:1024\n\t"
"v_mfma_f32_16x16x32_bf16 a[48:51], v[112:115], v[92:95], a[48:51]\n\t"
"ds_read_b64_tr_b16 v[76:77], v5 offset:0\n\t"
"v_mfma_f32_16x16x32_bf16 a[52:55], v[116:119], v[92:95], a[52:55]\n\t"
"v_mfma_f32_16x16x32_bf16 a[56:59], v[120:123], v[92:95], a[56:59]\n\t"
"ds_read_b64_tr_b16 v[78:79], v5 offset:1024\n\t"
"v_mfma_f32_16x16x32_bf16 a[60:63], v[124:127], v[92:95], a[60:63]\n\t"
"buffer_load_dwordx4 v9, s[48:51], s81 offen offset:3072 lds\n\t"
"v_mfma_f32_16x16x32_bf16 a[64:67], v[112:115], v[96:99], a[64:67]\n\t"
"ds_read_b128 v[36:39], v0 offset:2048\n\t"
"v_mfma_f32_16x16x32_bf16 a[68:71], v[116:119], v[96:99], a[68:71]\n\t"
"ds_read_b128 v[40:43], v0 offset:4096\n\t"
"v_mfma_f32_16x16x32_bf16 a[72:75], v[120:123], v[96:99], a[72:75]\n\t"
"v_mfma_f32_16x16x32_bf16 a[76:79], v[124:127], v[96:99], a[76:79]\n\t"
"s_add_u32 m0, s79, 53248\n\t"
"s_add_u32 s81, s76, 33536\n\t"
"buffer_load_dwordx4 v6, s[48:51], s81 offen offset:0 lds\n\t"
"v_mfma_f32_16x16x32_bf16 a[80:83], v[112:115], v[100:103], a[80:83]\n\t"
"ds_read_b128 v[44:47], v0 offset:6144\n\t"
"v_mfma_f32_16x16x32_bf16 a[84:87], v[116:119], v[100:103], a[84:87]\n\t"
"ds_read_b128 v[48:51], v0 offset:8192\n\t"
"v_mfma_f32_16x16x32_bf16 a[88:91], v[120:123], v[100:103], a[88:91]\n\t"
"ds_read_b128 v[52:55], v0 offset:10240\n\t"
"v_mfma_f32_16x16x32_bf16 a[92:95], v[124:127], v[100:103], a[92:95]\n\t"
"v_mfma_f32_16x16x32_bf16 a[96:99], v[112:115], v[104:107], a[96:99]\n\t"
"buffer_load_dwordx4 v7, s[48:51], s81 offen offset:1024 lds\n\t"
"v_mfma_f32_16x16x32_bf16 a[100:103], v[116:119], v[104:107], a[100:103]\n\t"
"ds_read_b128 v[56:59], v0 offset:12288\n\t"
"v_mfma_f32_16x16x32_bf16 a[104:107], v[120:123], v[104:107], a[104:107]\n\t"
"ds_read_b128 v[60:63], v0 offset:14336\n\t"
"s_waitcnt lgkmcnt(5)\n\t"
"v_mfma_f32_16x16x32_bf16 a[108:111], v[124:127], v[104:107], a[108:111]\n\t"
"v_mfma_f32_16x16x32_bf16 a[112:115], v[112:115], v[108:111], a[112:115]\n\t"
"v_mfma_f32_16x16x32_bf16 a[116:119], v[116:119], v[108:111], a[116:119]\n\t"
"v_mfma_f32_16x16x32_bf16 a[120:123], v[120:123], v[108:111], a[120:123]\n\t"
"v_mfma_f32_16x16x32_bf16 a[124:127], v[124:127], v[108:111], a[124:127]\n\t"
"s_lshl_b32 s82, s47, 8\n\t"
"s_add_u32 s82, s82, 24576\n\t"
"v_mbcnt_lo_u32_b32 v200, -1, 0\n\t"
"v_mbcnt_hi_u32_b32 v200, -1, v200\n\t"
"v_min_u32 v200, 31, v200\n\t"
"v_lshl_add_u32 v200, v200, 3, s82\n\t"
"global_load_dwordx2 v[202:203], v200, s[42:43] sc1\n\t"
"v_mfma_f32_16x16x32_bf16 a[0:3], v[64:67], v[32:35], a[0:3]\n\t"
"ds_read_b128 v[80:83], v1 offset:0\n\t"
"v_mfma_f32_16x16x32_bf16 a[4:7], v[68:71], v[32:35], a[4:7]\n\t"
"buffer_load_dwordx4 v8, s[48:51], s81 offen offset:2048 lds\n\t"
"v_mfma_f32_16x16x32_bf16 a[8:11], v[72:75], v[32:35], a[8:11]\n\t"
"ds_read_b64_tr_b16 v[112:113], v2 offset:8192\n\t"
"v_mfma_f32_16x16x32_bf16 a[12:15], v[76:79], v[32:35], a[12:15]\n\t"
"ds_read_b64_tr_b16 v[114:115], v2 offset:9216\n\t"
"v_mfma_f32_16x16x32_bf16 a[16:19], v[64:67], v[36:39], a[16:19]\n\t"
"ds_read_b64_tr_b16 v[116:117], v3 offset:8192\n\t"
"v_mfma_f32_16x16x32_bf16 a[20:23], v[68:71], v[36:39], a[20:23]\n\t"
"buffer_load_dwordx4 v9, s[48:51], s81 offen offset:3072 lds\n\t"
"v_mfma_f32_16x16x32_bf16 a[24:27], v[72:75], v[36:39], a[24:27]\n\t"
"ds_read_b64_tr_b16 v[118:119], v3 offset:9216\n\t"
"v_mfma_f32_16x16x32_bf16 a[28:31], v[76:79], v[36:39], a[28:31]\n\t"
"ds_read_b64_tr_b16 v[120:121], v4 offset:8192\n\t"
"v_mfma_f32_16x16x32_bf16 a[32:35], v[64:67], v[40:43], a[32:35]\n\t"
"ds_read_b64_tr_b16 v[122:123], v4 offset:9216\n\t"
"v_mfma_f32_16x16x32_bf16 a[36:39], v[68:71], v[40:43], a[36:39]\n\t"
"ds_read_b64_tr_b16 v[124:125], v5 offset:8192\n\t"
"v_mfma_f32_16x16x32_bf16 a[40:43], v[72:75], v[40:43], a[40:43]\n\t"
"s_add_u32 m0, s80, 49152\n\t"
"s_add_u32 s81, s78, 3145728\n\t"
"buffer_load_dwordx4 v10, s[56:59], s81 offen offset:0 lds\n\t"
"v_mfma_f32_16x16x32_bf16 a[44:47], v[76:79], v[40:43], a[44:47]\n\t"
"ds_read_b64_tr_b16 v[126:127], v5 offset:9216\n\t"
"s_waitcnt lgkmcnt(5)\n\t"
"v_mfma_f32_16x16x32_bf16 a[48:51], v[64:67], v[44:47], a[48:51]\n\t"
"ds_read_b128 v[84:87], v1 offset:2048\n\t"
"v_mfma_f32_16x16x32_bf16 a[52:55], v[68:71], v[44:47], a[52:55]\n\t"
"ds_read_b128 v[88:91], v1 offset:4096\n\t"
"v_mfma_f32_16x16x32_bf16 a[56:59], v[72:75], v[44:47], a[56:59]\n\t"
"ds_read_b128 v[92:95], v1 offset:6144\n\t"
"v_mfma_f32_16x16x32_bf16 a[60:63], v[76:79], v[44:47], a[60:63]\n\t"
"buffer_load_dwordx4 v11, s[56:59], s81 offen offset:1024 lds\n\t"
"v_mfma_f32_16x16x32_bf16 a[64:67], v[64:67], v[48:51], a[64:67]\n\t"
"ds_read_b128 v[96:99], v1 offset:8192\n\t"
"v_mfma_f32_16x16x32_bf16 a[68:71], v[68:71], v[48:51], a[68:71]\n\t"
"ds_read_b128 v[100:103], v1 offset:10240\n\t"
"v_mfma_f32_16x16x32_bf16 a[72:75], v[72:75], v[48:51], a[72:75]\n\t"
"ds_read_b128 v[104:107], v1 offset:12288\n\t"
"v_mfma_f32_16x16x32_bf16 a[76:79], v[76:79], v[48:51], a[76:79]\n\t"
"buffer_load_dwordx4 v12, s[56:59], s81 offen offset:2048 lds\n\t"
"v_mfma_f32_16x16x32_bf16 a[80:83], v[64:67], v[52:55], a[80:83]\n\t"
"ds_read_b128 v[108:111], v1 offset:14336\n\t"
"v_mfma_f32_16x16x32_bf16 a[84:87], v[68:71], v[52:55], a[84:87]\n\t"
"v_add_u32 v0, -98304, v0\n\t"
"v_mfma_f32_16x16x32_bf16 a[88:91], v[72:75], v[52:55], a[88:91]\n\t"
"v_add_u32 v1, -98304, v1\n\t"
"v_mfma_f32_16x16x32_bf16 a[92:95], v[76:79], v[52:55], a[92:95]\n\t"
"v_add_u32 v2, -98304, v2\n\t"
"v_mfma_f32_16x16x32_bf16 a[96:99], v[64:67], v[56:59], a[96:99]\n\t"
"buffer_load_dwordx4 v13, s[56:59], s81 offen offset:3072 lds\n\t"
"v_mfma_f32_16x16x32_bf16 a[100:103], v[68:71], v[56:59], a[100:103]\n\t"
"v_add_u32 v3, -98304, v3\n\t"
"v_mfma_f32_16x16x32_bf16 a[104:107], v[72:75], v[56:59], a[104:107]\n\t"
"v_add_u32 v4, -98304, v4\n\t"
"v_mfma_f32_16x16x32_bf16 a[108:111], v[76:79], v[56:59], a[108:111]\n\t"
"v_add_u32 v5, -98304, v5\n\t"
"v_mfma_f32_16x16x32_bf16 a[112:115], v[64:67], v[60:63], a[112:115]\n\t"
"v_mfma_f32_16x16x32_bf16 a[116:119], v[68:71], v[60:63], a[116:119]\n\t"
"v_mfma_f32_16x16x32_bf16 a[120:123], v[72:75], v[60:63], a[120:123]\n\t"
"v_mfma_f32_16x16x32_bf16 a[124:127], v[76:79], v[60:63], a[124:127]\n\t"
"s_waitcnt vmcnt(14) lgkmcnt(0)\n\t"
"s_barrier\n\t"
"s_mov_b32 m0, 147456\n\t"
"s_nop 0\n\t"
"buffer_load_dwordx4 v14, s[28:31], s97 offen offset:3072 lds\n\t"
"v_mfma_f32_16x16x32_bf16 a[0:3], v[112:115], v[80:83], a[0:3]\n\t"
"ds_read_b128 v[32:35], v0 offset:0\n\t"
"v_mfma_f32_16x16x32_bf16 a[4:7], v[116:119], v[80:83], a[4:7]\n\t"
"s_add_u32 m0, s79, 98304\n\t"
"s_add_u32 s81, s76, 896\n\t"
"buffer_load_dwordx4 v6, s[48:51], s81 offen offset:0 lds\n\t"
"v_mfma_f32_16x16x32_bf16 a[8:11], v[120:123], v[80:83], a[8:11]\n\t"
"ds_read_b64_tr_b16 v[64:65], v2 offset:0\n\t"
"v_mfma_f32_16x16x32_bf16 a[12:15], v[124:127], v[80:83], a[12:15]\n\t"
"ds_read_b64_tr_b16 v[66:67], v2 offset:1024\n\t"
"v_mfma_f32_16x16x32_bf16 a[16:19], v[112:115], v[84:87], a[16:19]\n\t"
"v_mfma_f32_16x16x32_bf16 a[20:23], v[116:119], v[84:87], a[20:23]\n\t"
"buffer_load_dwordx4 v7, s[48:51], s81 offen offset:1024 lds\n\t"
"v_mfma_f32_16x16x32_bf16 a[24:27], v[120:123], v[84:87], a[24:27]\n\t"
"ds_read_b64_tr_b16 v[68:69], v3 offset:0\n\t"
"v_mfma_f32_16x16x32_bf16 a[28:31], v[124:127], v[84:87], a[28:31]\n\t"
"ds_read_b64_tr_b16 v[70:71], v3 offset:1024\n\t"
"v_mfma_f32_16x16x32_bf16 a[32:35], v[112:115], v[88:91], a[32:35]\n\t"
"ds_read_b64_tr_b16 v[72:73], v4 offset:0\n\t"
"v_mfma_f32_16x16x32_bf16 a[36:39], v[116:119], v[88:91], a[36:39]\n\t"
"v_mfma_f32_16x16x32_bf16 a[40:43], v[120:123], v[88:91], a[40:43]\n\t"
"buffer_load_dwordx4 v8, s[48:51], s81 offen offset:2048 lds\n\t"
"v_mfma_f32_16x16x32_bf16 a[44:47], v[124:127], v[88:91], a[44:47]\n\t"
"ds_read_b64_tr_b16 v[74:75], v4 offset:1024\n\t"
"v_mfma_f32_16x16x32_bf16 a[48:51], v[112:115], v[92:95], a[48:51]\n\t"
"ds_read_b64_tr_b16 v[76:77], v5 offset:0\n\t"
"v_mfma_f32_16x16x32_bf16 a[52:55], v[116:119], v[92:95], a[52:55]\n\t"
"v_mfma_f32_16x16x32_bf16 a[56:59], v[120:123], v[92:95], a[56:59]\n\t"
"ds_read_b64_tr_b16 v[78:79], v5 offset:1024\n\t"
"v_mfma_f32_16x16x32_bf16 a[60:63], v[124:127], v[92:95], a[60:63]\n\t"
"buffer_load_dwordx4 v9, s[48:51], s81 offen offset:3072 lds\n\t"
"v_mfma_f32_16x16x32_bf16 a[64:67], v[112:115], v[96:99], a[64:67]\n\t"
"ds_read_b128 v[36:39], v0 offset:2048\n\t"
"v_mfma_f32_16x16x32_bf16 a[68:71], v[116:119], v[96:99], a[68:71]\n\t"
"ds_read_b128 v[40:43], v0 offset:4096\n\t"
"v_mfma_f32_16x16x32_bf16 a[72:75], v[120:123], v[96:99], a[72:75]\n\t"
"v_mfma_f32_16x16x32_bf16 a[76:79], v[124:127], v[96:99], a[76:79]\n\t"
"s_add_u32 m0, s79, 102400\n\t"
"s_add_u32 s81, s76, 33664\n\t"
"buffer_load_dwordx4 v6, s[48:51], s81 offen offset:0 lds\n\t"
"v_mfma_f32_16x16x32_bf16 a[80:83], v[112:115], v[100:103], a[80:83]\n\t"
"ds_read_b128 v[44:47], v0 offset:6144\n\t"
"v_mfma_f32_16x16x32_bf16 a[84:87], v[116:119], v[100:103], a[84:87]\n\t"
"ds_read_b128 v[48:51], v0 offset:8192\n\t"
"v_mfma_f32_16x16x32_bf16 a[88:91], v[120:123], v[100:103], a[88:91]\n\t"
"ds_read_b128 v[52:55], v0 offset:10240\n\t"
"v_mfma_f32_16x16x32_bf16 a[92:95], v[124:127], v[100:103], a[92:95]\n\t"
"v_mfma_f32_16x16x32_bf16 a[96:99], v[112:115], v[104:107], a[96:99]\n\t"
"buffer_load_dwordx4 v7, s[48:51], s81 offen offset:1024 lds\n\t"
"v_mfma_f32_16x16x32_bf16 a[100:103], v[116:119], v[104:107], a[100:103]\n\t"
"ds_read_b128 v[56:59], v0 offset:12288\n\t"
"v_mfma_f32_16x16x32_bf16 a[104:107], v[120:123], v[104:107], a[104:107]\n\t"
"ds_read_b128 v[60:63], v0 offset:14336\n\t"
"s_waitcnt lgkmcnt(5)\n\t"
"v_mfma_f32_16x16x32_bf16 a[108:111], v[124:127], v[104:107], a[108:111]\n\t"
"v_mfma_f32_16x16x32_bf16 a[112:115], v[112:115], v[108:111], a[112:115]\n\t"
"v_mfma_f32_16x16x32_bf16 a[116:119], v[116:119], v[108:111], a[116:119]\n\t"
"v_mfma_f32_16x16x32_bf16 a[120:123], v[120:123], v[108:111], a[120:123]\n\t"
"v_mfma_f32_16x16x32_bf16 a[124:127], v[124:127], v[108:111], a[124:127]\n\t"
"s_mov_b32 s82, 0\n\t"
"s_waitcnt vmcnt(13)\n\t"
"LFP6_%=:\n\t"
"v_cmp_eq_u64 vcc, s[26:27], v[202:203]\n\t"
"s_nop 1\n\t"
"s_cmp_eq_u64 vcc, exec\n\t"
"s_cbranch_scc1 LFO7_%=\n\t"
"s_sleep 1\n\t"
"global_load_dwordx2 v[202:203], v200, s[42:43] sc1\n\t"
"s_waitcnt vmcnt(0)\n\t"
"s_add_u32 s82, s82, 1\n\t"
"s_cmp_lt_u32 s82, 0x40000\n\t"
"s_cbranch_scc1 LFP6_%=\n\t"
"LFO7_%=:\n\t"
"s_add_u32 s88, s42, 131072\n\t"
"s_addc_u32 s89, s43, 0\n\t"
"s_and_b32 s89, s89, 0xffff\n\t"
"s_mov_b32 s90, 131072\n\t"
"s_mov_b32 s91, 0x00020000\n\t"
"s_sub_u32 s82, s46, 1\n\t"
"s_lshl_b32 s82, s82, 12\n\t"
"s_lshl_b32 s83, s47, 9\n\t"
"s_add_u32 s82, s82, s83\n\t"
"s_lshl_b32 s83, s39, 7\n\t"
"s_add_u32 s82, s82, s83\n\t"
"v_and_b32 v22, 15, v15\n\t"
"v_lshlrev_b32 v22, 3, v22\n\t"
"v_add_u32 v22, s82, v22\n\t"
"v_lshrrev_b32 v23, 4, v15\n\t"
"v_lshlrev_b32 v23, 15, v23\n\t"
"v_sub_u32 v22, v22, v23\n\t"
"buffer_load_dwordx2 v[128:129], v22, s[88:91], 0 offen sc1\n\t"
"v_subrev_u32 v22, 4096, v22\n\t"
"buffer_load_dwordx2 v[130:131], v22, s[88:91], 0 offen sc1\n\t"
"v_subrev_u32 v22, 4096, v22\n\t"
"buffer_load_dwordx2 v[132:133], v22, s[88:91], 0 offen sc1\n\t"
"v_subrev_u32 v22, 4096, v22\n\t"
"buffer_load_dwordx2 v[134:135], v22, s[88:91], 0 offen sc1\n\t"
"v_subrev_u32 v22, 4096, v22\n\t"
"buffer_load_dwordx2 v[136:137], v22, s[88:91], 0 offen sc1\n\t"
"v_subrev_u32 v22, 4096, v22\n\t"
"buffer_load_dwordx2 v[138:139], v22, s[88:91], 0 offen sc1\n\t"
"v_subrev_u32 v22, 4096, v22\n\t"
"buffer_load_dwordx2 v[140:141], v22, s[88:91], 0 offen sc1\n\t"
"v_subrev_u32 v22, 4096, v22\n\t"
"buffer_load_dwordx2 v[142:143], v22, s[88:91], 0 offen sc1\n\t"
"v_mfma_f32_16x16x32_bf16 a[0:3], v[64:67], v[32:35], a[0:3]\n\t"
"ds_read_b128 v[80:83], v1 offset:0\n\t"
"v_mfma_f32_16x16x32_bf16 a[4:7], v[68:71], v[32:35], a[4:7]\n\t"
"buffer_load_dwordx4 v8, s[48:51], s81 offen offset:2048 lds\n\t"
"v_mfma_f32_16x16x32_bf16 a[8:11], v[72:75], v[32:35], a[8:11]\n\t"
"ds_read_b64_tr_b16 v[112:113], v2 offset:8192\n\t"
"v_mfma_f32_16x16x32_bf16 a[12:15], v[76:79], v[32:35], a[12:15]\n\t"
"ds_read_b64_tr_b16 v[114:115], v2 offset:9216\n\t"
"v_mfma_f32_16x16x32_bf16 a[16:19], v[64:67], v[36:39], a[16:19]\n\t"
"ds_read_b64_tr_b16 v[116:117], v3 offset:8192\n\t"
"v_mfma_f32_16x16x32_bf16 a[20:23], v[68:71], v[36:39], a[20:23]\n\t"
"buffer_load_dwordx4 v9, s[48:51], s81 offen offset:3072 lds\n\t"
"v_mfma_f32_16x16x32_bf16 a[24:27], v[72:75], v[36:39], a[24:27]\n\t"
"ds_read_b64_tr_b16 v[118:119], v3 offset:9216\n\t"
"v_mfma_f32_16x16x32_bf16 a[28:31], v[76:79], v[36:39], a[28:31]\n\t"
"ds_read_b64_tr_b16 v[120:121], v4 offset:8192\n\t"
"v_mfma_f32_16x16x32_bf16 a[32:35], v[64:67], v[40:43], a[32:35]\n\t"
"ds_read_b64_tr_b16 v[122:123], v4 offset:9216\n\t"
"v_mfma_f32_16x16x32_bf16 a[36:39], v[68:71], v[40:43], a[36:39]\n\t"
"ds_read_b64_tr_b16 v[124:125], v5 offset:8192\n\t"
"v_mfma_f32_16x16x32_bf16 a[40:43], v[72:75], v[40:43], a[40:43]\n\t"
"s_add_u32 m0, s80, 98304\n\t"
"s_add_u32 s81, s78, 3670016\n\t"
"buffer_load_dwordx4 v10, s[56:59], s81 offen offset:0 lds\n\t"
"v_mfma_f32_16x16x32_bf16 a[44:47], v[76:79], v[40:43], a[44:47]\n\t"
"ds_read_b64_tr_b16 v[126:127], v5 offset:9216\n\t"
"s_waitcnt lgkmcnt(5)\n\t"
"v_mfma_f32_16x16x32_bf16 a[48:51], v[64:67], v[44:47], a[48:51]\n\t"
"ds_read_b128 v[84:87], v1 offset:2048\n\t"
"v_mfma_f32_16x16x32_bf16 a[52:55], v[68:71], v[44:47], a[52:55]\n\t"
"ds_read_b128 v[88:91], v1 offset:4096\n\t"
"v_mfma_f32_16x16x32_bf16 a[56:59], v[72:75], v[44:47], a[56:59]\n\t"
"ds_read_b128 v[92:95], v1 offset:6144\n\t"
"v_mfma_f32_16x16x32_bf16 a[60:63], v[76:79], v[44:47], a[60:63]\n\t"
"buffer_load_dwordx4 v11, s[56:59], s81 offen offset:1024 lds\n\t"
"v_mfma_f32_16x16x32_bf16 a[64:67], v[64:67], v[48:51], a[64:67]\n\t"
"ds_read_b128 v[96:99], v1 offset:8192\n\t"
"v_mfma_f32_16x16x32_bf16 a[68:71], v[68:71], v[48:51], a[68:71]\n\t"
"ds_read_b128 v[100:103], v1 offset:10240\n\t"
"v_mfma_f32_16x16x32_bf16 a[72:75], v[72:75], v[48:51], a[72:75]\n\t"
"ds_read_b128 v[104:107], v1 offset:12288\n\t"
"v_mfma_f32_16x16x32_bf16 a[76:79], v[76:79], v[48:51], a[76:79]\n\t"
"buffer_load_dwordx4 v12, s[56:59], s81 offen offset:2048 lds\n\t"
"v_mfma_f32_16x16x32_bf16 a[80:83], v[64:67], v[52:55], a[80:83]\n\t"
"ds_read_b128 v[108:111], v1 offset:14336\n\t"
"v_mfma_f32_16x16x32_bf16 a[84:87], v[68:71], v[52:55], a[84:87]\n\t"
"v_add_u32 v0, 49152, v0\n\t"
"v_mfma_f32_16x16x32_bf16 a[88:91], v[72:75], v[52:55], a[88:91]\n\t"
"v_add_u32 v1, 49152, v1\n\t"
"v_mfma_f32_16x16x32_bf16 a[92:95], v[76:79], v[52:55], a[92:95]\n\t"
"v_add_u32 v2, 49152, v2\n\t"
"v_mfma_f32_16x16x32_bf16 a[96:99], v[64:67], v[56:59], a[96:99]\n\t"
"buffer_load_dwordx4 v13, s[56:59], s81 offen offset:3072 lds\n\t"
"v_mfma_f32_16x16x32_bf16 a[100:103], v[68:71], v[56:59], a[100:103]\n\t"
"v_add_u32 v3, 49152, v3\n\t"
"v_mfma_f32_16x16x32_bf16 a[104:107], v[72:75], v[56:59], a[104:107]\n\t"
"v_add_u32 v4, 49152, v4\n\t"
"v_mfma_f32_16x16x32_bf16 a[108:111], v[76:79], v[56:59], a[108:111]\n\t"
"v_add_u32 v5, 49152, v5\n\t"
"v_mfma_f32_16x16x32_bf16 a[112:115], v[64:67], v[60:63], a[112:115]\n\t"
"v_mfma_f32_16x16x32_bf16 a[116:119], v[68:71], v[60:63], a[116:119]\n\t"
"v_mfma_f32_16x16x32_bf16 a[120:123], v[72:75], v[60:63], a[120:123]\n\t"
"v_mfma_f32_16x16x32_bf16 a[124:127], v[76:79], v[60:63], a[124:127]\n\t"
"s_waitcnt vmcnt(21) lgkmcnt(0)\n\t"
"s_barrier\n\t"
"s_load_dwordx2 s[28:29], s[36:37], 16\n\t"
"s_waitcnt lgkmcnt(0)\n\t"
"s_and_b32 s29, s29, 0xffff\n\t"
"s_mov_b32 s30, 4194304\n\t"
"s_mov_b32 s31, 0x00020000\n\t"
"s_lshl_b32 s98, s38, 2\n\t"
"s_add_u32 s98, s98, s39\n\t"
"s_mul_i32 s97, s98, 4096\n\t"
"s_mov_b32 m0, 147456\n\t"
"s_nop 0\n\t"
"buffer_load_dwordx4 v14, s[28:31], s97 offen offset:0 lds\n\t"
"v_mfma_f32_16x16x32_bf16 a[0:3], v[112:115], v[80:83], a[0:3]\n\t"
"ds_read_b128 v[32:35], v0 offset:0\n\t"
"v_mfma_f32_16x16x32_bf16 a[4:7], v[116:119], v[80:83], a[4:7]\n\t"
"s_add_u32 m0, s79, 0\n\t"
"s_add_u32 s81, s76, 0\n\t"
"buffer_load_dwordx4 v6, s[48:51], s81 offen offset:0 lds\n\t"
"v_mfma_f32_16x16x32_bf16 a[8:11], v[120:123], v[80:83], a[8:11]\n\t"
"ds_read_b64_tr_b16 v[64:65], v2 offset:0\n\t"
"v_mfma_f32_16x16x32_bf16 a[12:15], v[124:127], v[80:83], a[12:15]\n\t"
"ds_read_b64_tr_b16 v[66:67], v2 offset:1024\n\t"
"v_mfma_f32_16x16x32_bf16 a[16:19], v[112:115], v[84:87], a[16:19]\n\t"
"v_mfma_f32_16x16x32_bf16 a[20:23], v[116:119], v[84:87], a[20:23]\n\t"
"buffer_load_dwordx4 v7, s[48:51], s81 offen offset:1024 lds\n\t"
"v_mfma_f32_16x16x32_bf16 a[24:27], v[120:123], v[84:87], a[24:27]\n\t"
"ds_read_b64_tr_b16 v[68:69], v3 offset:0\n\t"
"v_mfma_f32_16x16x32_bf16 a[28:31], v[124:127], v[84:87], a[28:31]\n\t"
"ds_read_b64_tr_b16 v[70:71], v3 offset:1024\n\t"
"v_mfma_f32_16x16x32_bf16 a[32:35], v[112:115], v[88:91], a[32:35]\n\t"
"ds_read_b64_tr_b16 v[72:73], v4 offset:0\n\t"
"v_mfma_f32_16x16x32_bf16 a[36:39], v[116:119], v[88:91], a[36:39]\n\t"
"v_mfma_f32_16x16x32_bf16 a[40:43], v[120:123], v[88:91], a[40:43]\n\t"
"buffer_load_dwordx4 v8, s[48:51], s81 offen offset:2048 lds\n\t"
"v_mfma_f32_16x16x32_bf16 a[44:47], v[124:127], v[88:91], a[44:47]\n\t"
"ds_read_b64_tr_b16 v[74:75], v4 offset:1024\n\t"
"v_mfma_f32_16x16x32_bf16 a[48:51], v[112:115], v[92:95], a[48:51]\n\t"
"ds_read_b64_tr_b16 v[76:77], v5 offset:0\n\t"
"v_mfma_f32_16x16x32_bf16 a[52:55], v[116:119], v[92:95], a[52:55]\n\t"
"v_mfma_f32_16x16x32_bf16 a[56:59], v[120:123], v[92:95], a[56:59]\n\t"
"ds_read_b64_tr_b16 v[78:79], v5 offset:1024\n\t"
"v_mfma_f32_16x16x32_bf16 a[60:63], v[124:127], v[92:95], a[60:63]\n\t"
"buffer_load_dwordx4 v9, s[48:51], s81 offen offset:3072 lds\n\t"
"v_mfma_f32_16x16x32_bf16 a[64:67], v[112:115], v[96:99], a[64:67]\n\t"
"ds_read_b128 v[36:39], v0 offset:2048\n\t"
"v_mfma_f32_16x16x32_bf16 a[68:71], v[116:119], v[96:99], a[68:71]\n\t"
"ds_read_b128 v[40:43], v0 offset:4096\n\t"
"v_mfma_f32_16x16x32_bf16 a[72:75], v[120:123], v[96:99], a[72:75]\n\t"
"v_mfma_f32_16x16x32_bf16 a[76:79], v[124:127], v[96:99], a[76:79]\n\t"
"s_add_u32 m0, s79, 4096\n\t"
"s_add_u32 s81, s76, 32768\n\t"
"buffer_load_dwordx4 v6, s[48:51], s81 offen offset:0 lds\n\t"
"v_mfma_f32_16x16x32_bf16 a[80:83], v[112:115], v[100:103], a[80:83]\n\t"
"ds_read_b128 v[44:47], v0 offset:6144\n\t"
"v_mfma_f32_16x16x32_bf16 a[84:87], v[116:119], v[100:103], a[84:87]\n\t"
"ds_read_b128 v[48:51], v0 offset:8192\n\t"
"v_mfma_f32_16x16x32_bf16 a[88:91], v[120:123], v[100:103], a[88:91]\n\t"
"ds_read_b128 v[52:55], v0 offset:10240\n\t"
"v_mfma_f32_16x16x32_bf16 a[92:95], v[124:127], v[100:103], a[92:95]\n\t"
"v_mfma_f32_16x16x32_bf16 a[96:99], v[112:115], v[104:107], a[96:99]\n\t"
"buffer_load_dwordx4 v7, s[48:51], s81 offen offset:1024 lds\n\t"
"v_mfma_f32_16x16x32_bf16 a[100:103], v[116:119], v[104:107], a[100:103]\n\t"
"ds_read_b128 v[56:59], v0 offset:12288\n\t"
"v_mfma_f32_16x16x32_bf16 a[104:107], v[120:123], v[104:107], a[104:107]\n\t"
"ds_read_b128 v[60:63], v0 offset:14336\n\t"
"s_waitcnt lgkmcnt(5)\n\t"
"v_mfma_f32_16x16x32_bf16 a[108:111], v[124:127], v[104:107], a[108:111]\n\t"
"v_mfma_f32_16x16x32_bf16 a[112:115], v[112:115], v[108:111], a[112:115]\n\t"
"v_mfma_f32_16x16x32_bf16 a[116:119], v[116:119], v[108:111], a[116:119]\n\t"
"v_mfma_f32_16x16x32_bf16 a[120:123], v[120:123], v[108:111], a[120:123]\n\t"
"v_mfma_f32_16x16x32_bf16 a[124:127], v[124:127], v[108:111], a[124:127]\n\t"
"s_waitcnt vmcnt(13)\n\t"
"v_mfma_f32_16x16x32_bf16 a[0:3], v[64:67], v[32:35], a[0:3]\n\t"
"ds_read_b128 v[80:83], v1 offset:0\n\t"
"v_fmac_f32 v140, v248, v142\n\t"
"v_fmac_f32 v141, v248, v143\n\t"
"v_mfma_f32_16x16x32_bf16 a[4:7], v[68:71], v[32:35], a[4:7]\n\t"
"buffer_load_dwordx4 v8, s[48:51], s81 offen offset:2048 lds\n\t"
"v_fma_f32 v140, -v249, v143, v140\n\t"
"v_fmac_f32 v141, v249, v142\n\t"
"v_mfma_f32_16x16x32_bf16 a[8:11], v[72:75], v[32:35], a[8:11]\n\t"
"ds_read_b64_tr_b16 v[112:113], v2 offset:8192\n\t"
"v_fmac_f32 v138, v248, v140\n\t"
"v_fmac_f32 v139, v248, v141\n\t"
"v_mfma_f32_16x16x32_bf16 a[12:15], v[76:79], v[32:35], a[12:15]\n\t"
"ds_read_b64_tr_b16 v[114:115], v2 offset:9216\n\t"
"v_fma_f32 v138, -v249, v141, v138\n\t"
"v_fmac_f32 v139, v249, v140\n\t"
"v_mfma_f32_16x16x32_bf16 a[16:19], v[64:67], v[36:39], a[16:19]\n\t"
"ds_read_b64_tr_b16 v[116:117], v3 offset:8192\n\t"
"v_fmac_f32 v136, v248, v138\n\t"
"v_fmac_f32 v137, v248, v139\n\t"
"v_mfma_f32_16x16x32_bf16 a[20:23], v[68:71], v[36:39], a[20:23]\n\t"
"buffer_load_dwordx4 v9, s[48:51], s81 offen offset:3072 lds\n\t"
"v_fma_f32 v136, -v249, v139, v136\n\t"
"v_fmac_f32 v137, v249, v138\n\t"
"v_mfma_f32_16x16x32_bf16 a[24:27], v[72:75], v[36:39], a[24:27]\n\t"
"ds_read_b64_tr_b16 v[118:119], v3 offset:9216\n\t"
"v_fmac_f32 v134, v248, v136\n\t"
"v_mfma_f32_16x16x32_bf16 a[28:31], v[76:79], v[36:39], a[28:31]\n\t"
"ds_read_b64_tr_b16 v[120:121], v4 offset:8192\n\t"
"v_fmac_f32 v135, v248, v137\n\t"
"v_fma_f32 v134, -v249, v137, v134\n\t"
"v_mfma_f32_16x16x32_bf16 a[32:35], v[64:67], v[40:43], a[32:35]\n\t"
"ds_read_b64_tr_b16 v[122:123], v4 offset:9216\n\t"
"v_fmac_f32 v135, v249, v136\n\t"
"v_fmac_f32 v132, v248, v134\n\t"
"v_mfma_f32_16x16x32_bf16 a[36:39], v[68:71], v[40:43], a[36:39]\n\t"
"ds_read_b64_tr_b16 v[124:125], v5 offset:8192\n\t"
"v_fmac_f32 v133, v248, v135\n\t"
"v_fma_f32 v132, -v249, v135, v132\n\t"
"v_mfma_f32_16x16x32_bf16 a[40:43], v[72:75], v[40:43], a[40:43]\n\t"
"s_add_u32 m0, s80, 0\n\t"
"s_add_u32 s81, s78, 0\n\t"
"buffer_load_dwordx4 v10, s[60:63], s81 offen offset:0 lds\n\t"
"v_fmac_f32 v133, v249, v134\n\t"
"v_fmac_f32 v130, v248, v132\n\t"
"v_mfma_f32_16x16x32_bf16 a[44:47], v[76:79], v[40:43], a[44:47]\n\t"
"ds_read_b64_tr_b16 v[126:127], v5 offset:9216\n\t"
"v_fmac_f32 v131, v248, v133\n\t"
"v_fma_f32 v130, -v249, v133, v130\n\t"
"s_waitcnt lgkmcnt(4)\n\t"
"v_mfma_f32_16x16x32_bf16 a[48:51], v[64:67], v[44:47], a[48:51]\n\t"
"ds_read_b128 v[84:87], v1 offset:2048\n\t"
"v_fmac_f32 v131, v249, v132\n\t"
"v_fmac_f32 v128, v248, v130\n\t"
"v_mfma_f32_16x16x32_bf16 a[52:55], v[68:71], v[44:47], a[52:55]\n\t"
"ds_read_b128 v[88:91], v1 offset:4096\n\t"
"v_fmac_f32 v129, v248, v131\n\t"
"v_mfma_f32_16x16x32_bf16 a[56:59], v[72:75], v[44:47], a[56:59]\n\t"
"ds_read_b128 v[92:95], v1 offset:6144\n\t"
"v_fma_f32 v128, -v249, v131, v128\n\t"
"v_fmac_f32 v129, v249, v130\n\t"
"v_mfma_f32_16x16x32_bf16 a[60:63], v[76:79], v[44:47], a[60:63]\n\t"
"buffer_load_dwordx4 v11, s[60:63], s81 offen offset:1024 lds\n\t"
"v_mul_f32 v144, v250, v128\n\t"
"v_mul_f32 v145, v250, v129\n\t"
"v_mfma_f32_16x16x32_bf16 a[64:67], v[64:67], v[48:51], a[64:67]\n\t"
"ds_read_b128 v[96:99], v1 offset:8192\n\t"
"v_fma_f32 v144, -v251, v129, v144\n\t"
"v_fmac_f32 v145, v251, v128\n\t"
"v_mfma_f32_16x16x32_bf16 a[68:71], v[68:71], v[48:51], a[68:71]\n\t"
"ds_read_b128 v[100:103], v1 offset:10240\n\t"
"v_mov_b32 v146, v144\n\t"
"v_mov_b32 v147, v145\n\t"
"v_mfma_f32_16x16x32_bf16 a[72:75], v[72:75], v[48:51], a[72:75]\n\t"
"ds_read_b128 v[104:107], v1 offset:12288\n\t"
"v_permlane16_swap_b32 v144, v146\n\t"
"v_permlane16_swap_b32 v145, v147\n\t"
"v_mfma_f32_16x16x32_bf16 a[76:79], v[76:79], v[48:51], a[76:79]\n\t"
"buffer_load_dwordx4 v12, s[60:63], s81 offen offset:2048 lds\n\t"
"v_add_f32 v144, v144, v146\n\t"
"v_add_f32 v145, v145, v147\n\t"
"v_mfma_f32_16x16x32_bf16 a[80:83], v[64:67], v[52:55], a[80:83]\n\t"
"ds_read_b128 v[108:111], v1 offset:14336\n\t"
"v_mov_b32 v146, v144\n\t"
"v_mfma_f32_16x16x32_bf16 a[84:87], v[68:71], v[52:55], a[84:87]\n\t"
"v_add_u32 v0, 49152, v0\n\t"
"v_mov_b32 v147, v145\n\t"
"v_permlane32_swap_b32 v144, v146\n\t"
"v_mfma_f32_16x16x32_bf16 a[88:91], v[72:75], v[52:55], a[88:91]\n\t"
"v_add_u32 v1, 49152, v1\n\t"
"v_permlane32_swap_b32 v145, v147\n\t"
"v_add_f32 v144, v144, v146\n\t"
"v_mfma_f32_16x16x32_bf16 a[92:95], v[76:79], v[52:55], a[92:95]\n\t"
"v_add_u32 v2, 49152, v2\n\t"
"v_add_f32 v145, v145, v147\n\t"
"v_fmac_f32 v244, v252, v144\n\t"
"v_mfma_f32_16x16x32_bf16 a[96:99], v[64:67], v[56:59], a[96:99]\n\t"
"buffer_load_dwordx4 v13, s[60:63], s81 offen offset:3072 lds\n\t"
"v_fmac_f32 v245, v252, v145\n\t"
"v_fma_f32 v244, -v253, v145, v244\n\t"
"v_mfma_f32_16x16x32_bf16 a[100:103], v[68:71], v[56:59], a[100:103]\n\t"
"v_add_u32 v3, 49152, v3\n\t"
"v_fmac_f32 v245, v253, v144\n\t"
"v_mul_f32 v148, v246, v244\n\t"
"v_mfma_f32_16x16x32_bf16 a[104:107], v[72:75], v[56:59], a[104:107]\n\t"
"v_add_u32 v4, 49152, v4\n\t"
"v_mul_f32 v149, v246, v245\n\t"
"v_fma_f32 v148, -v247, v245, v148\n\t"
"v_mfma_f32_16x16x32_bf16 a[108:111], v[76:79], v[56:59], a[108:111]\n\t"
"v_add_u32 v5, 49152, v5\n\t"
"v_fmac_f32 v149, v247, v244\n\t"
"v_mfma_f32_16x16x32_bf16 a[112:115], v[64:67], v[60:63], a[112:115]\n\t"
"v_mfma_f32_16x16x32_bf16 a[116:119], v[68:71], v[60:63], a[116:119]\n\t"
"v_mfma_f32_16x16x32_bf16 a[120:123], v[72:75], v[60:63], a[120:123]\n\t"
"v_mfma_f32_16x16x32_bf16 a[124:127], v[76:79], v[60:63], a[124:127]\n\t"
"s_waitcnt vmcnt(13) lgkmcnt(0)\n\t"
"s_barrier\n\t"
"s_mov_b32 m0, 147456\n\t"
"s_nop 0\n\t"
"buffer_load_dwordx4 v14, s[28:31], s97 offen offset:1024 lds\n\t"
"v_mfma_f32_16x16x32_bf16 a[0:3], v[112:115], v[80:83], a[0:3]\n\t"
"ds_read_b128 v[32:35], v0 offset:0\n\t"
"v_add_f32 v212, v212, v148\n\t"
"v_add_f32 v228, v228, v149\n\t"
"v_mfma_f32_16x16x32_bf16 a[4:7], v[116:119], v[80:83], a[4:7]\n\t"
"v_mul_f32 v212, 2.0, v212\n\t"
"s_add_u32 m0, s79, 49152\n\t"
"s_add_u32 s81, s76, 128\n\t"
"buffer_load_dwordx4 v6, s[48:51], s81 offen offset:0 lds\n\t"
"v_mul_f32 v228, -2.0, v228\n\t"
"v_mfma_f32_16x16x32_bf16 a[8:11], v[120:123], v[80:83], a[8:11]\n\t"
"v_mul_f32 v150, v246, v148\n\t"
"ds_read_b64_tr_b16 v[64:65], v2 offset:0\n\t"
"v_mul_f32 v151, v246, v149\n\t"
"v_mfma_f32_16x16x32_bf16 a[12:15], v[124:127], v[80:83], a[12:15]\n\t"
"v_fma_f32 v150, -v247, v149, v150\n\t"
"v_fmac_f32 v151, v247, v148\n\t"
"v_mfma_f32_16x16x32_bf16 a[16:19], v[112:115], v[84:87], a[16:19]\n\t"
"ds_read_b64_tr_b16 v[66:67], v2 offset:1024\n\t"
"v_add_f32 v213, v213, v150\n\t"
"v_add_f32 v229, v229, v151\n\t"
"v_mfma_f32_16x16x32_bf16 a[20:23], v[116:119], v[84:87], a[20:23]\n\t"
"buffer_load_dwordx4 v7, s[48:51], s81 offen offset:1024 lds\n\t"
"v_mul_f32 v213, 2.0, v213\n\t"
"v_mul_f32 v229, -2.0, v229\n\t"
"v_mfma_f32_16x16x32_bf16 a[24:27], v[120:123], v[84:87], a[24:27]\n\t"
"v_cvt_pk_bf16_f32 v152, v212, v213\n\t"
"ds_read_b64_tr_b16 v[68:69], v3 offset:0\n\t"
"v_mfma_f32_16x16x32_bf16 a[28:31], v[124:127], v[84:87], a[28:31]\n\t"
"v_cvt_pk_bf16_f32 v160, v228, v229\n\t"
"v_mul_f32 v148, v246, v150\n\t"
"ds_read_b64_tr_b16 v[70:71], v3 offset:1024\n\t"
"v_mfma_f32_16x16x32_bf16 a[32:35], v[112:115], v[88:91], a[32:35]\n\t"
"v_mul_f32 v149, v246, v151\n\t"
"v_fma_f32 v148, -v247, v151, v148\n\t"
"ds_read_b64_tr_b16 v[72:73], v4 offset:0\n\t"
"v_mfma_f32_16x16x32_bf16 a[36:39], v[116:119], v[88:91], a[36:39]\n\t"
"v_fmac_f32 v149, v247, v150\n\t"
"v_add_f32 v214, v214, v148\n\t"
"v_mfma_f32_16x16x32_bf16 a[40:43], v[120:123], v[88:91], a[40:43]\n\t"
"v_add_f32 v230, v230, v149\n\t"
"buffer_load_dwordx4 v8, s[48:51], s81 offen offset:2048 lds\n\t"
"v_mul_f32 v214, 2.0, v214\n\t"
"v_mfma_f32_16x16x32_bf16 a[44:47], v[124:127], v[88:91], a[44:47]\n\t"
"v_mul_f32 v230, -2.0, v230\n\t"
"ds_read_b64_tr_b16 v[74:75], v4 offset:1024\n\t"
"v_mul_f32 v150, v246, v148\n\t"
"v_mfma_f32_16x16x32_bf16 a[48:51], v[112:115], v[92:95], a[48:51]\n\t"
"v_mul_f32 v151, v246, v149\n\t"
"v_fma_f32 v150, -v247, v149, v150\n\t"
"v_mfma_f32_16x16x32_bf16 a[52:55], v[116:119], v[92:95], a[52:55]\n\t"
"ds_read_b64_tr_b16 v[76:77], v5 offset:0\n\t"
"v_fmac_f32 v151, v247, v148\n\t"
"v_add_f32 v215, v215, v150\n\t"
"v_mfma_f32_16x16x32_bf16 a[56:59], v[120:123], v[92:95], a[56:59]\n\t"
"ds_read_b64_tr_b16 v[78:79], v5 offset:1024\n\t"
"v_add_f32 v231, v231, v151\n\t"
"v_mul_f32 v215, 2.0, v215\n\t"
"v_mfma_f32_16x16x32_bf16 a[60:63], v[124:127], v[92:95], a[60:63]\n\t"
"buffer_load_dwordx4 v9, s[48:51], s81 offen offset:3072 lds\n\t"
"v_mul_f32 v231, -2.0, v231\n\t"
"v_mfma_f32_16x16x32_bf16 a[64:67], v[112:115], v[96:99], a[64:67]\n\t"
"v_cvt_pk_bf16_f32 v153, v214, v215\n\t"
"v_cvt_pk_bf16_f32 v161, v230, v231\n\t"
"ds_read_b128 v[36:39], v0 offset:2048\n\t"
"v_mfma_f32_16x16x32_bf16 a[68:71], v[116:119], v[96:99], a[68:71]\n\t"
"v_mul_f32 v148, v246, v150\n\t"
"v_mul_f32 v149, v246, v151\n\t"
"ds_read_b128 v[40:43], v0 offset:4096\n\t"
"v_mfma_f32_16x16x32_bf16 a[72:75], v[120:123], v[96:99], a[72:75]\n\t"
"v_fma_f32 v148, -v247, v151, v148\n\t"
"v_fmac_f32 v149, v247, v150\n\t"
"v_mfma_f32_16x16x32_bf16 a[76:79], v[124:127], v[96:99], a[76:79]\n\t"
"v_add_f32 v216, v216, v148\n\t"
"s_add_u32 m0, s79, 53248\n\t"
"s_add_u32 s81, s76, 32896\n\t"
"buffer_load_dwordx4 v6, s[48:51], s81 offen offset:0 lds\n\t"
"v_add_f32 v232, v232, v149\n\t"
"v_mfma_f32_16x16x32_bf16 a[80:83], v[112:115], v[100:103], a[80:83]\n\t"
"v_mul_f32 v216, 2.0, v216\n\t"
"ds_read_b128 v[44:47], v0 offset:6144\n\t"
"v_mul_f32 v232, -2.0, v232\n\t"
"v_mfma_f32_16x16x32_bf16 a[84:87], v[116:119], v[100:103], a[84:87]\n\t"
"v_mul_f32 v150, v246, v148\n\t"
"ds_read_b128 v[48:51], v0 offset:8192\n\t"
"v_mfma_f32_16x16x32_bf16 a[88:91], v[120:123], v[100:103], a[88:91]\n\t"
"v_mul_f32 v151, v246, v149\n\t"
"v_fma_f32 v150, -v247, v149, v150\n\t"
"v_fmac_f32 v151, v247, v148\n\t"
"v_mfma_f32_16x16x32_bf16 a[92:95], v[124:127], v[100:103], a[92:95]\n\t"
"ds_read_b128 v[52:55], v0 offset:10240\n\t"
"v_add_f32 v217, v217, v150\n\t"
"v_add_f32 v233, v233, v151\n\t"
"v_mfma_f32_16x16x32_bf16 a[96:99], v[112:115], v[104:107], a[96:99]\n\t"
"buffer_load_dwordx4 v7, s[48:51], s81 offen offset:1024 lds\n\t"
"v_mul_f32 v217, 2.0, v217\n\t"
"v_mfma_f32_16x16x32_bf16 a[100:103], v[116:119], v[104:107], a[100:103]\n\t"
"v_mul_f32 v233, -2.0, v233\n\t"
"v_cvt_pk_bf16_f32 v154, v216, v217\n\t"
"ds_read_b128 v[56:59], v0 offset:12288\n\t"
"v_mfma_f32_16x16x32_bf16 a[104:107], v[120:123], v[104:107], a[104:107]\n\t"
"v_cvt_pk_bf16_f32 v162, v232, v233\n\t"
"v_mul_f32 v148, v246, v150\n\t"
"ds_read_b128 v[60:63], v0 offset:14336\n\t"
"s_waitcnt lgkmcnt(4)\n\t"
"v_mfma_f32_16x16x32_bf16 a[108:111], v[124:127], v[104:107], a[108:111]\n\t"
"v_mul_f32 v149, v246, v151\n\t"
"v_fma_f32 v148, -v247, v151, v148\n\t"
"v_mfma_f32_16x16x32_bf16 a[112:115], v[112:115], v[108:111], a[112:115]\n\t"
"v_mfma_f32_16x16x32_bf16 a[116:119], v[116:119], v[108:111], a[116:119]\n\t"
"v_mfma_f32_16x16x32_bf16 a[120:123], v[120:123], v[108:111], a[120:123]\n\t"
"v_mfma_f32_16x16x32_bf16 a[124:127], v[124:127], v[108:111], a[124:127]\n\t"
"v_mfma_f32_16x16x32_bf16 a[0:3], v[64:67], v[32:35], a[0:3]\n\t"
"ds_read_b128 v[80:83], v1 offset:0\n\t"
"v_fmac_f32 v149, v247, v150\n\t"
"v_add_f32 v218, v218, v148\n\t"
"v_mfma_f32_16x16x32_bf16 a[4:7], v[68:71], v[32:35], a[4:7]\n\t"
"buffer_load_dwordx4 v8, s[48:51], s81 offen offset:2048 lds\n\t"
"v_add_f32 v234, v234, v149\n\t"
"v_mul_f32 v218, 2.0, v218\n\t"
"v_mfma_f32_16x16x32_bf16 a[8:11], v[72:75], v[32:35], a[8:11]\n\t"
"ds_read_b64_tr_b16 v[112:113], v2 offset:8192\n\t"
"v_mul_f32 v234, -2.0, v234\n\t"
"v_mul_f32 v150, v246, v148\n\t"
"v_mfma_f32_16x16x32_bf16 a[12:15], v[76:79], v[32:35], a[12:15]\n\t"
"ds_read_b64_tr_b16 v[114:115], v2 offset:9216\n\t"
"v_mul_f32 v151, v246, v149\n\t"
"v_fma_f32 v150, -v247, v149, v150\n\t"
"v_mfma_f32_16x16x32_bf16 a[16:19], v[64:67], v[36:39], a[16:19]\n\t"
"ds_read_b64_tr_b16 v[116:117], v3 offset:8192\n\t"
"v_fmac_f32 v151, v247, v148\n\t"
"v_add_f32 v219, v219, v150\n\t"
"v_mfma_f32_16x16x32_bf16 a[20:23], v[68:71], v[36:39], a[20:23]\n\t"
"buffer_load_dwordx4 v9, s[48:51], s81 offen offset:3072 lds\n\t"
"v_add_f32 v235, v235, v151\n\t"
"v_mul_f32 v219, 2.0, v219\n\t"
"v_mfma_f32_16x16x32_bf16 a[24:27], v[72:75], v[36:39], a[24:27]\n\t"
"ds_read_b64_tr_b16 v[118:119], v3 offset:9216\n\t"
"v_mul_f32 v235, -2.0, v235\n\t"
"v_cvt_pk_bf16_f32 v155, v218, v219\n\t"
"v_mfma_f32_16x16x32_bf16 a[28:31], v[76:79], v[36:39], a[28:31]\n\t"
"ds_read_b64_tr_b16 v[120:121], v4 offset:8192\n\t"
"v_cvt_pk_bf16_f32 v163, v234, v235\n\t"
"v_mul_f32 v148, v246, v150\n\t"
"v_mfma_f32_16x16x32_bf16 a[32:35], v[64:67], v[40:43], a[32:35]\n\t"
"ds_read_b64_tr_b16 v[122:123], v4 offset:9216\n\t"
"v_mul_f32 v149, v246, v151\n\t"
"v_fma_f32 v148, -v247, v151, v148\n\t"
"v_mfma_f32_16x16x32_bf16 a[36:39], v[68:71], v[40:43], a[36:39]\n\t"
"ds_read_b64_tr_b16 v[124:125], v5 offset:8192\n\t"
"v_fmac_f32 v149, v247, v150\n\t"
"v_mfma_f32_16x16x32_bf16 a[40:43], v[72:75], v[40:43], a[40:43]\n\t"
"s_add_u32 m0, s80, 49152\n\t"
"s_add_u32 s81, s78, 524288\n\t"
"buffer_load_dwordx4 v10, s[60:63], s81 offen offset:0 lds\n\t"
"v_add_f32 v220, v220, v148\n\t"
"v_add_f32 v236, v236, v149\n\t"
"v_mfma_f32_16x16x32_bf16 a[44:47], v[76:79], v[40:43], a[44:47]\n\t"
"ds_read_b64_tr_b16 v[126:127], v5 offset:9216\n\t"
"v_mul_f32 v220, 2.0, v220\n\t"
"v_mul_f32 v236, -2.0, v236\n\t"
"v_mfma_f32_16x16x32_bf16 a[48:51], v[64:67], v[44:47], a[48:51]\n\t"
"ds_read_b128 v[84:87], v1 offset:2048\n\t"
"v_mul_f32 v150, v246, v148\n\t"
"v_mul_f32 v151, v246, v149\n\t"
"v_mfma_f32_16x16x32_bf16 a[52:55], v[68:71], v[44:47], a[52:55]\n\t"
"ds_read_b128 v[88:91], v1 offset:4096\n\t"
"v_fma_f32 v150, -v247, v149, v150\n\t"
"v_fmac_f32 v151, v247, v148\n\t"
"v_mfma_f32_16x16x32_bf16 a[56:59], v[72:75], v[44:47], a[56:59]\n\t"
"ds_read_b128 v[92:95], v1 offset:6144\n\t"
"s_waitcnt lgkmcnt(5)\n\t"
"v_add_f32 v221, v221, v150\n\t"
"v_add_f32 v237, v237, v151\n\t"
"v_mfma_f32_16x16x32_bf16 a[60:63], v[76:79], v[44:47], a[60:63]\n\t"
"buffer_load_dwordx4 v11, s[60:63], s81 offen offset:1024 lds\n\t"
"v_mul_f32 v221, 2.0, v221\n\t"
"v_mul_f32 v237, -2.0, v237\n\t"
"v_mfma_f32_16x16x32_bf16 a[64:67], v[64:67], v[48:51], a[64:67]\n\t"
"ds_read_b128 v[96:99], v1 offset:8192\n\t"
"v_cvt_pk_bf16_f32 v156, v220, v221\n\t"
"v_cvt_pk_bf16_f32 v164, v236, v237\n\t"
"v_mfma_f32_16x16x32_bf16 a[68:71], v[68:71], v[48:51], a[68:71]\n\t"
"ds_read_b128 v[100:103], v1 offset:10240\n\t"
"v_mul_f32 v148, v246, v150\n\t"
"v_mul_f32 v149, v246, v151\n\t"
"v_mfma_f32_16x16x32_bf16 a[72:75], v[72:75], v[48:51], a[72:75]\n\t"
"ds_read_b128 v[104:107], v1 offset:12288\n\t"
"v_fma_f32 v148, -v247, v151, v148\n\t"
"v_mfma_f32_16x16x32_bf16 a[76:79], v[76:79], v[48:51], a[76:79]\n\t"
"buffer_load_dwordx4 v12, s[60:63], s81 offen offset:2048 lds\n\t"
"v_fmac_f32 v149, v247, v150\n\t"
"v_add_f32 v222, v222, v148\n\t"
"v_mfma_f32_16x16x32_bf16 a[80:83], v[64:67], v[52:55], a[80:83]\n\t"
"ds_read_b128 v[108:111], v1 offset:14336\n\t"
"v_add_f32 v238, v238, v149\n\t"
"v_mul_f32 v222, 2.0, v222\n\t"
"v_mfma_f32_16x16x32_bf16 a[84:87], v[68:71], v[52:55], a[84:87]\n\t"
"v_add_u32 v0, -98304, v0\n\t"
"v_mul_f32 v238, -2.0, v238\n\t"
"v_mul_f32 v150, v246, v148\n\t"
"v_mfma_f32_16x16x32_bf16 a[88:91], v[72:75], v[52:55], a[88:91]\n\t"
"v_add_u32 v1, -98304, v1\n\t"
"v_mul_f32 v151, v246, v149\n\t"
"v_fma_f32 v150, -v247, v149, v150\n\t"
"v_mfma_f32_16x16x32_bf16 a[92:95], v[76:79], v[52:55], a[92:95]\n\t"
"v_add_u32 v2, -98304, v2\n\t"
"v_fmac_f32 v151, v247, v148\n\t"
"v_add_f32 v223, v223, v150\n\t"
"v_mfma_f32_16x16x32_bf16 a[96:99], v[64:67], v[56:59], a[96:99]\n\t"
"buffer_load_dwordx4 v13, s[60:63], s81 offen offset:3072 lds\n\t"
"v_add_f32 v239, v239, v151\n\t"
"v_mul_f32 v223, 2.0, v223\n\t"
"v_mfma_f32_16x16x32_bf16 a[100:103], v[68:71], v[56:59], a[100:103]\n\t"
"v_add_u32 v3, -98304, v3\n\t"
"v_mul_f32 v239, -2.0, v239\n\t"
"v_cvt_pk_bf16_f32 v157, v222, v223\n\t"
"v_mfma_f32_16x16x32_bf16 a[104:107], v[72:75], v[56:59], a[104:107]\n\t"
"v_add_u32 v4, -98304, v4\n\t"
"v_cvt_pk_bf16_f32 v165, v238, v239\n\t"
"v_mul_f32 v148, v246, v150\n\t"
"v_mfma_f32_16x16x32_bf16 a[108:111], v[76:79], v[56:59], a[108:111]\n\t"
"v_add_u32 v5, -98304, v5\n\t"
"v_mul_f32 v149, v246, v151\n\t"
"v_mfma_f32_16x16x32_bf16 a[112:115], v[64:67], v[60:63], a[112:115]\n\t"
"v_mfma_f32_16x16x32_bf16 a[116:119], v[68:71], v[60:63], a[116:119]\n\t"
"v_mfma_f32_16x16x32_bf16 a[120:123], v[72:75], v[60:63], a[120:123]\n\t"
"v_mfma_f32_16x16x32_bf16 a[124:127], v[76:79], v[60:63], a[124:127]\n\t"
"s_waitcnt vmcnt(13) lgkmcnt(0)\n\t"
"s_barrier\n\t"
"s_mov_b32 m0, 147456\n\t"
"s_nop 0\n\t"
"buffer_load_dwordx4 v14, s[28:31], s97 offen offset:2048 lds\n\t"
"v_mfma_f32_16x16x32_bf16 a[0:3], v[112:115], v[80:83], a[0:3]\n\t"
"ds_read_b128 v[32:35], v0 offset:0\n\t"
"v_fma_f32 v148, -v247, v151, v148\n\t"
"v_fmac_f32 v149, v247, v150\n\t"
"v_mfma_f32_16x16x32_bf16 a[4:7], v[116:119], v[80:83], a[4:7]\n\t"
"v_add_f32 v224, v224, v148\n\t"
"s_add_u32 m0, s79, 98304\n\t"
"s_add_u32 s81, s76, 256\n\t"
"buffer_load_dwordx4 v6, s[48:51], s81 offen offset:0 lds\n\t"
"v_add_f32 v240, v240, v149\n\t"
"v_mfma_f32_16x16x32_bf16 a[8:11], v[120:123], v[80:83], a[8:11]\n\t"
"v_mul_f32 v224, 2.0, v224\n\t"
"ds_read_b64_tr_b16 v[64:65], v2 offset:0\n\t"
"v_mul_f32 v240, -2.0, v240\n\t"
"v_mfma_f32_16x16x32_bf16 a[12:15], v[124:127], v[80:83], a[12:15]\n\t"
"v_mul_f32 v150, v246, v148\n\t"
"v_mul_f32 v151, v246, v149\n\t"
"v_mfma_f32_16x16x32_bf16 a[16:19], v[112:115], v[84:87], a[16:19]\n\t"
"ds_read_b64_tr_b16 v[66:67], v2 offset:1024\n\t"
"v_fma_f32 v150, -v247, v149, v150\n\t"
"v_fmac_f32 v151, v247, v148\n\t"
"v_mfma_f32_16x16x32_bf16 a[20:23], v[116:119], v[84:87], a[20:23]\n\t"
"buffer_load_dwordx4 v7, s[48:51], s81 offen offset:1024 lds\n\t"
"v_add_f32 v225, v225, v150\n\t"
"v_add_f32 v241, v241, v151\n\t"
"v_mfma_f32_16x16x32_bf16 a[24:27], v[120:123], v[84:87], a[24:27]\n\t"
"v_mul_f32 v225, 2.0, v225\n\t"
"ds_read_b64_tr_b16 v[68:69], v3 offset:0\n\t"
"v_mfma_f32_16x16x32_bf16 a[28:31], v[124:127], v[84:87], a[28:31]\n\t"
"v_mul_f32 v241, -2.0, v241\n\t"
"v_cvt_pk_bf16_f32 v158, v224, v225\n\t"
"ds_read_b64_tr_b16 v[70:71], v3 offset:1024\n\t"
"v_mfma_f32_16x16x32_bf16 a[32:35], v[112:115], v[88:91], a[32:35]\n\t"
"v_cvt_pk_bf16_f32 v166, v240, v241\n\t"
"v_mul_f32 v148, v246, v150\n\t"
"ds_read_b64_tr_b16 v[72:73], v4 offset:0\n\t"
"v_mfma_f32_16x16x32_bf16 a[36:39], v[116:119], v[88:91], a[36:39]\n\t"
"v_mul_f32 v149, v246, v151\n\t"
"v_fma_f32 v148, -v247, v151, v148\n\t"
"v_mfma_f32_16x16x32_bf16 a[40:43], v[120:123], v[88:91], a[40:43]\n\t"
"v_fmac_f32 v149, v247, v150\n\t"
"buffer_load_dwordx4 v8, s[48:51], s81 offen offset:2048 lds\n\t"
"v_add_f32 v226, v226, v148\n\t"
"v_mfma_f32_16x16x32_bf16 a[44:47], v[124:127], v[88:91], a[44:47]\n\t"
"v_add_f32 v242, v242, v149\n\t"
"ds_read_b64_tr_b16 v[74:75], v4 offset:1024\n\t"
"v_mul_f32 v226, 2.0, v226\n\t"
"v_mfma_f32_16x16x32_bf16 a[48:51], v[112:115], v[92:95], a[48:51]\n\t"
"v_mul_f32 v242, -2.0, v242\n\t"
"v_mul_f32 v150, v246, v148\n\t"
"v_mfma_f32_16x16x32_bf16 a[52:55], v[116:119], v[92:95], a[52:55]\n\t"
"ds_read_b64_tr_b16 v[76:77], v5 offset:0\n\t"
"v_mul_f32 v151, v246, v149\n\t"
"v_fma_f32 v150, -v247, v149, v150\n\t"
"v_mfma_f32_16x16x32_bf16 a[56:59], v[120:123], v[92:95], a[56:59]\n\t"
"ds_read_b64_tr_b16 v[78:79], v5 offset:1024\n\t"
"v_fmac_f32 v151, v247, v148\n\t"
"v_add_f32 v227, v227, v150\n\t"
"v_mfma_f32_16x16x32_bf16 a[60:63], v[124:127], v[92:95], a[60:63]\n\t"
"buffer_load_dwordx4 v9, s[48:51], s81 offen offset:3072 lds\n\t"
"v_add_f32 v243, v243, v151\n\t"
"v_mfma_f32_16x16x32_bf16 a[64:67], v[112:115], v[96:99], a[64:67]\n\t"
"v_mul_f32 v227, 2.0, v227\n\t"
"v_mul_f32 v243, -2.0, v243\n\t"
"ds_read_b128 v[36:39], v0 offset:2048\n\t"
"v_mfma_f32_16x16x32_bf16 a[68:71], v[116:119], v[96:99], a[68:71]\n\t"
"v_cvt_pk_bf16_f32 v159, v226, v227\n\t"
"v_cvt_pk_bf16_f32 v167, v242, v243\n\t"
"ds_read_b128 v[40:43], v0 offset:4096\n\t"
"v_mfma_f32_16x16x32_bf16 a[72:75], v[120:123], v[96:99], a[72:75]\n\t"
"s_add_u32 s88, s42, 1048576\n\t"
"s_addc_u32 s89, s43, 0\n\t"
"v_mfma_f32_16x16x32_bf16 a[76:79], v[124:127], v[96:99], a[76:79]\n\t"
"s_and_b32 s89, s89, 0xffff\n\t"
"s_add_u32 m0, s79, 102400\n\t"
"s_add_u32 s81, s76, 33024\n\t"
"buffer_load_dwordx4 v6, s[48:51], s81 offen offset:0 lds\n\t"
"s_mov_b32 s90, 4194304\n\t"
"v_mfma_f32_16x16x32_bf16 a[80:83], v[112:115], v[100:103], a[80:83]\n\t"
"s_mov_b32 s91, 0x00020000\n\t"
"ds_read_b128 v[44:47], v0 offset:6144\n\t"
"v_and_b32 v168, 15, v15\n\t"
"v_mfma_f32_16x16x32_bf16 a[84:87], v[116:119], v[100:103], a[84:87]\n\t"
"v_lshlrev_b32 v168, 6, v168\n\t"
"ds_read_b128 v[48:51], v0 offset:8192\n\t"
"v_mfma_f32_16x16x32_bf16 a[88:91], v[120:123], v[100:103], a[88:91]\n\t"
"v_lshrrev_b32 v169, 4, v15\n\t"
"v_lshl_add_u32 v168, v169, 4, v168\n\t"
"s_lshl_b32 s82, s46, 17\n\t"
"v_mfma_f32_16x16x32_bf16 a[92:95], v[124:127], v[100:103], a[92:95]\n\t"
"ds_read_b128 v[52:55], v0 offset:10240\n\t"
"s_lshl_b32 s83, s47, 13\n\t"
"s_add_u32 s82, s82, s83\n\t"
"v_mfma_f32_16x16x32_bf16 a[96:99], v[112:115], v[104:107], a[96:99]\n\t"
"buffer_load_dwordx4 v7, s[48:51], s81 offen offset:1024 lds\n\t"
"s_lshl_b32 s83, s39, 11\n\t"
"v_mfma_f32_16x16x32_bf16 a[100:103], v[116:119], v[104:107], a[100:103]\n\t"
"s_add_u32 s82, s82, s83\n\t"
"s_add_u32 s83, s82, 65536\n\t"
"ds_read_b128 v[56:59], v0 offset:12288\n\t"
"v_mfma_f32_16x16x32_bf16 a[104:107], v[120:123], v[104:107], a[104:107]\n\t"
"buffer_store_dwordx4 v[152:155], v168, s[88:91], s82 offen offset:0 sc1\n\t"
"buffer_store_dwordx4 v[156:159], v168, s[88:91], s82 offen offset:1024 sc1\n\t"
"ds_read_b128 v[60:63], v0 offset:14336\n\t"
"s_waitcnt lgkmcnt(4)\n\t"
"v_mfma_f32_16x16x32_bf16 a[108:111], v[124:127], v[104:107], a[108:111]\n\t"
"buffer_store_dwordx4 v[160:163], v168, s[88:91], s83 offen offset:0 sc1\n\t"
"buffer_store_dwordx4 v[164:167], v168, s[88:91], s83 offen offset:1024 sc1\n\t"
"v_mfma_f32_16x16x32_bf16 a[112:115], v[112:115], v[108:111], a[112:115]\n\t"
"v_mfma_f32_16x16x32_bf16 a[116:119], v[116:119], v[108:111], a[116:119]\n\t"
"v_mfma_f32_16x16x32_bf16 a[120:123], v[120:123], v[108:111], a[120:123]\n\t"
"v_mfma_f32_16x16x32_bf16 a[124:127], v[124:127], v[108:111], a[124:127]\n\t"
"v_mfma_f32_16x16x32_bf16 a[128:131], v[32:35], v[64:67], 0\n\t"
"ds_read_b128 v[80:83], v1 offset:0\n\t"
"v_accvgpr_read_b32 v128, a0\n\t"
"v_mfma_f32_16x16x32_bf16 a[132:135], v[32:35], v[68:71], 0\n\t"
"buffer_load_dwordx4 v8, s[48:51], s81 offen offset:2048 lds\n\t"
"v_mfma_f32_16x16x32_bf16 a[136:139], v[32:35], v[72:75], 0\n\t"
"ds_read_b64_tr_b16 v[112:113], v2 offset:8192\n\t"
"v_mfma_f32_16x16x32_bf16 a[140:143], v[32:35], v[76:79], 0\n\t"
"ds_read_b64_tr_b16 v[114:115], v2 offset:9216\n\t"
"v_accvgpr_read_b32 v129, a1\n\t"
"v_mfma_f32_16x16x32_bf16 a[144:147], v[36:39], v[64:67], 0\n\t"
"ds_read_b64_tr_b16 v[116:117], v3 offset:8192\n\t"
"v_mfma_f32_16x16x32_bf16 a[148:151], v[36:39], v[68:71], 0\n\t"
"buffer_load_dwordx4 v9, s[48:51], s81 offen offset:3072 lds\n\t"
"v_mfma_f32_16x16x32_bf16 a[152:155], v[36:39], v[72:75], 0\n\t"
"ds_read_b64_tr_b16 v[118:119], v3 offset:9216\n\t"
"v_mfma_f32_16x16x32_bf16 a[156:159], v[36:39], v[76:79], 0\n\t"
"ds_read_b64_tr_b16 v[120:121], v4 offset:8192\n\t"
"v_accvgpr_read_b32 v130, a2\n\t"
"v_mfma_f32_16x16x32_bf16 a[160:163], v[40:43], v[64:67], 0\n\t"
"ds_read_b64_tr_b16 v[122:123], v4 offset:9216\n\t"
"v_mfma_f32_16x16x32_bf16 a[164:167], v[40:43], v[68:71], 0\n\t"
"ds_read_b64_tr_b16 v[124:125], v5 offset:8192\n\t"
"v_mfma_f32_16x16x32_bf16 a[168:171], v[40:43], v[72:75], 0\n\t"
"s_add_u32 m0, s80, 98304\n\t"
"s_add_u32 s81, s78, 1048576\n\t"
"buffer_load_dwordx4 v10, s[60:63], s81 offen offset:0 lds\n\t"
"v_accvgpr_read_b32 v131, a3\n\t"
"v_mfma_f32_16x16x32_bf16 a[172:175], v[40:43], v[76:79], 0\n\t"
"ds_read_b64_tr_b16 v[126:127], v5 offset:9216\n\t"
"v_mfma_f32_16x16x32_bf16 a[176:179], v[44:47], v[64:67], 0\n\t"
"ds_read_b128 v[84:87], v1 offset:2048\n\t"
"v_mfma_f32_16x16x32_bf16 a[180:183], v[44:47], v[68:71], 0\n\t"
"ds_read_b128 v[88:91], v1 offset:4096\n\t"
"v_mfma_f32_16x16x32_bf16 a[184:187], v[44:47], v[72:75], 0\n\t"
"ds_read_b128 v[92:95], v1 offset:6144\n\t"
"s_waitcnt lgkmcnt(7)\n\t"
"v_accvgpr_read_b32 v132, a4\n\t"
"v_mfma_f32_16x16x32_bf16 a[188:191], v[44:47], v[76:79], 0\n\t"
"buffer_load_dwordx4 v11, s[60:63], s81 offen offset:1024 lds\n\t"
"v_mfma_f32_16x16x32_bf16 a[192:195], v[48:51], v[64:67], 0\n\t"
"ds_read_b128 v[96:99], v1 offset:8192\n\t"
"v_mfma_f32_16x16x32_bf16 a[196:199], v[48:51], v[68:71], 0\n\t"
"ds_read_b128 v[100:103], v1 offset:10240\n\t"
"v_accvgpr_read_b32 v133, a5\n\t"
"v_mfma_f32_16x16x32_bf16 a[200:203], v[48:51], v[72:75], 0\n\t"
"ds_read_b128 v[104:107], v1 offset:12288\n\t"
"v_mfma_f32_16x16x32_bf16 a[204:207], v[48:51], v[76:79], 0\n\t"
"buffer_load_dwordx4 v12, s[60:63], s81 offen offset:2048 lds\n\t"
"v_mfma_f32_16x16x32_bf16 a[208:211], v[52:55], v[64:67], 0\n\t"
"ds_read_b128 v[108:111], v1 offset:14336\n\t"
"v_mfma_f32_16x16x32_bf16 a[212:215], v[52:55], v[68:71], 0\n\t"
"v_add_u32 v0, 49152, v0\n\t"
"v_accvgpr_read_b32 v134, a6\n\t"
"v_mfma_f32_16x16x32_bf16 a[216:219], v[52:55], v[72:75], 0\n\t"
"v_add_u32 v1, 49152, v1\n\t"
"v_mfma_f32_16x16x32_bf16 a[220:223], v[52:55], v[76:79], 0\n\t"
"v_add_u32 v2, 49152, v2\n\t"
"v_mfma_f32_16x16x32_bf16 a[224:227], v[56:59], v[64:67], 0\n\t"
"buffer_load_dwordx4 v13, s[60:63], s81 offen offset:3072 lds\n\t"
"v_accvgpr_read_b32 v135, a7\n\t"
"v_mfma_f32_16x16x32_bf16 a[228:231], v[56:59], v[68:71], 0\n\t"
"v_add_u32 v3, 49152, v3\n\t"
"v_mfma_f32_16x16x32_bf16 a[232:235], v[56:59], v[72:75], 0\n\t"
"v_add_u32 v4, 49152, v4\n\t"
"v_mfma_f32_16x16x32_bf16 a[236:239], v[56:59], v[76:79], 0\n\t"
"v_add_u32 v5, 49152, v5\n\t"
"v_mfma_f32_16x16x32_bf16 a[240:243], v[60:63], v[64:67], 0\n\t"
"v_mfma_f32_16x16x32_bf16 a[244:247], v[60:63], v[68:71], 0\n\t"
"v_mfma_f32_16x16x32_bf16 a[248:251], v[60:63], v[72:75], 0\n\t"
"v_mfma_f32_16x16x32_bf16 a[252:255], v[60:63], v[76:79], 0\n\t"
"s_waitcnt vmcnt(17) lgkmcnt(0)\n\t"
"s_barrier\n\t"
"s_mov_b32 m0, 147456\n\t"
"s_nop 0\n\t"
"buffer_load_dwordx4 v14, s[28:31], s97 offen offset:3072 lds\n\t"
"v_mfma_f32_16x16x32_bf16 a[128:131], v[80:83], v[112:115], a[128:131]\n\t"
"ds_read_b128 v[32:35], v0 offset:0\n\t"
"v_cvt_pk_bf16_f32 v136, v128, v129\n\t"
"v_mfma_f32_16x16x32_bf16 a[132:135], v[80:83], v[116:119], a[132:135]\n\t"
"s_add_u32 m0, s79, 0\n\t"
"s_add_u32 s81, s76, 384\n\t"
"buffer_load_dwordx4 v6, s[48:51], s81 offen offset:0 lds\n\t"
"v_mfma_f32_16x16x32_bf16 a[136:139], v[80:83], v[120:123], a[136:139]\n\t"
"v_cvt_pk_bf16_f32 v137, v130, v131\n\t"
"ds_read_b64_tr_b16 v[64:65], v2 offset:0\n\t"
"v_mfma_f32_16x16x32_bf16 a[140:143], v[80:83], v[124:127], a[140:143]\n\t"
"v_cvt_pk_bf16_f32 v138, v132, v133\n\t"
"v_mfma_f32_16x16x32_bf16 a[144:147], v[84:87], v[112:115], a[144:147]\n\t"
"ds_read_b64_tr_b16 v[66:67], v2 offset:1024\n\t"
"v_cvt_pk_bf16_f32 v139, v134, v135\n\t"
"v_mfma_f32_16x16x32_bf16 a[148:151], v[84:87], v[116:119], a[148:151]\n\t"
"buffer_load_dwordx4 v7, s[48:51], s81 offen offset:1024 lds\n\t"
"v_mfma_f32_16x16x32_bf16 a[152:155], v[84:87], v[120:123], a[152:155]\n\t"
"s_add_u32 s82, s84, 0\n\t"
"buffer_store_dwordx4 v[136:139], v14, s[68:71], s82 offen offset:0 sc1\n\t"
"v_mfma_f32_16x16x32_bf16 a[156:159], v[84:87], v[124:127], a[156:159]\n\t"
"ds_read_b64_tr_b16 v[68:69], v3 offset:0\n\t"
"ds_read_b64_tr_b16 v[70:71], v3 offset:1024\n\t"
"v_mfma_f32_16x16x32_bf16 a[160:163], v[88:91], v[112:115], a[160:163]\n\t"
"v_accvgpr_read_b32 v140, a8\n\t"
"v_mfma_f32_16x16x32_bf16 a[164:167], v[88:91], v[116:119], a[164:167]\n\t"
"ds_read_b64_tr_b16 v[72:73], v4 offset:0\n\t"
"v_accvgpr_read_b32 v141, a9\n\t"
"v_mfma_f32_16x16x32_bf16 a[168:171], v[88:91], v[120:123], a[168:171]\n\t"
"buffer_load_dwordx4 v8, s[48:51], s81 offen offset:2048 lds\n\t"
"v_mfma_f32_16x16x32_bf16 a[172:175], v[88:91], v[124:127], a[172:175]\n\t"
"v_accvgpr_read_b32 v142, a10\n\t"
"ds_read_b64_tr_b16 v[74:75], v4 offset:1024\n\t"
"v_mfma_f32_16x16x32_bf16 a[176:179], v[92:95], v[112:115], a[176:179]\n\t"
"v_accvgpr_read_b32 v143, a11\n\t"
"v_mfma_f32_16x16x32_bf16 a[180:183], v[92:95], v[116:119], a[180:183]\n\t"
"ds_read_b64_tr_b16 v[76:77], v5 offset:0\n\t"
"v_mfma_f32_16x16x32_bf16 a[184:187], v[92:95], v[120:123], a[184:187]\n\t"
"ds_read_b64_tr_b16 v[78:79], v5 offset:1024\n\t"
"v_accvgpr_read_b32 v144, a12\n\t"
"v_mfma_f32_16x16x32_bf16 a[188:191], v[92:95], v[124:127], a[188:191]\n\t"
"buffer_load_dwordx4 v9, s[48:51], s81 offen offset:3072 lds\n\t"
"v_mfma_f32_16x16x32_bf16 a[192:195], v[96:99], v[112:115], a[192:195]\n\t"
"v_accvgpr_read_b32 v145, a13\n\t"
"ds_read_b128 v[36:39], v0 offset:2048\n\t"
"v_mfma_f32_16x16x32_bf16 a[196:199], v[96:99], v[116:119], a[196:199]\n\t"
"v_accvgpr_read_b32 v146, a14\n\t"
"v_mfma_f32_16x16x32_bf16 a[200:203], v[96:99], v[120:123], a[200:203]\n\t"
"ds_read_b128 v[40:43], v0 offset:4096\n\t"
"v_accvgpr_read_b32 v147, a15\n\t"
"v_mfma_f32_16x16x32_bf16 a[204:207], v[96:99], v[124:127], a[204:207]\n\t"
"s_add_u32 m0, s79, 4096\n\t"
"s_add_u32 s81, s76, 33152\n\t"
"buffer_load_dwordx4 v6, s[48:51], s81 offen offset:0 lds\n\t"
"v_mfma_f32_16x16x32_bf16 a[208:211], v[100:103], v[112:115], a[208:211]\n\t"
"v_cvt_pk_bf16_f32 v148, v140, v141\n\t"
"v_mfma_f32_16x16x32_bf16 a[212:215], v[100:103], v[116:119], a[212:215]\n\t"
"ds_read_b128 v[44:47], v0 offset:6144\n\t"
"ds_read_b128 v[48:51], v0 offset:8192\n\t"
"v_mfma_f32_16x16x32_bf16 a[216:219], v[100:103], v[120:123], a[216:219]\n\t"
"v_cvt_pk_bf16_f32 v149, v142, v143\n\t"
"v_mfma_f32_16x16x32_bf16 a[220:223], v[100:103], v[124:127], a[220:223]\n\t"
"ds_read_b128 v[52:55], v0 offset:10240\n\t"
"v_cvt_pk_bf16_f32 v150, v144, v145\n\t"
"v_mfma_f32_16x16x32_bf16 a[224:227], v[104:107], v[112:115], a[224:227]\n\t"
"buffer_load_dwordx4 v7, s[48:51], s81 offen offset:1024 lds\n\t"
"v_mfma_f32_16x16x32_bf16 a[228:231], v[104:107], v[116:119], a[228:231]\n\t"
"v_cvt_pk_bf16_f32 v151, v146, v147\n\t"
"ds_read_b128 v[56:59], v0 offset:12288\n\t"
"v_mfma_f32_16x16x32_bf16 a[232:235], v[104:107], v[120:123], a[232:235]\n\t"
"s_add_u32 s82, s84, 0\n\t"
"buffer_store_dwordx4 v[148:151], v14, s[68:71], s82 offen offset:1024 sc1\n\t"
"v_mfma_f32_16x16x32_bf16 a[236:239], v[104:107], v[124:127], a[236:239]\n\t"
"ds_read_b128 v[60:63], v0 offset:14336\n\t"
"s_waitcnt lgkmcnt(5)\n\t"
"v_mfma_f32_16x16x32_bf16 a[240:243], v[108:111], v[112:115], a[240:243]\n\t"
"v_mfma_f32_16x16x32_bf16 a[244:247], v[108:111], v[116:119], a[244:247]\n\t"
"v_mfma_f32_16x16x32_bf16 a[248:251], v[108:111], v[120:123], a[248:251]\n\t"
"v_mfma_f32_16x16x32_bf16 a[252:255], v[108:111], v[124:127], a[252:255]\n\t"
"v_mfma_f32_16x16x32_bf16 a[128:131], v[32:35], v[64:67], a[128:131]\n\t"
"ds_read_b128 v[80:83], v1 offset:0\n\t"
"v_accvgpr_read_b32 v152, a16\n\t"
"v_mfma_f32_16x16x32_bf16 a[132:135], v[32:35], v[68:71], a[132:135]\n\t"
"buffer_load_dwordx4 v8, s[48:51], s81 offen offset:2048 lds\n\t"
"v_accvgpr_read_b32 v153, a17\n\t"
"v_mfma_f32_16x16x32_bf16 a[136:139], v[32:35], v[72:75], a[136:139]\n\t"
"ds_read_b64_tr_b16 v[112:113], v2 offset:8192\n\t"
"v_accvgpr_read_b32 v154, a18\n\t"
"v_mfma_f32_16x16x32_bf16 a[140:143], v[32:35], v[76:79], a[140:143]\n\t"
"ds_read_b64_tr_b16 v[114:115], v2 offset:9216\n\t"
"v_mfma_f32_16x16x32_bf16 a[144:147], v[36:39], v[64:67], a[144:147]\n\t"
"ds_read_b64_tr_b16 v[116:117], v3 offset:8192\n\t"
"v_accvgpr_read_b32 v155, a19\n\t"
"v_mfma_f32_16x16x32_bf16 a[148:151], v[36:39], v[68:71], a[148:151]\n\t"
"buffer_load_dwordx4 v9, s[48:51], s81 offen offset:3072 lds\n\t"
"v_accvgpr_read_b32 v156, a20\n\t"
"v_mfma_f32_16x16x32_bf16 a[152:155], v[36:39], v[72:75], a[152:155]\n\t"
"ds_read_b64_tr_b16 v[118:119], v3 offset:9216\n\t"
"v_mfma_f32_16x16x32_bf16 a[156:159], v[36:39], v[76:79], a[156:159]\n\t"
"ds_read_b64_tr_b16 v[120:121], v4 offset:8192\n\t"
"v_accvgpr_read_b32 v157, a21\n\t"
"v_mfma_f32_16x16x32_bf16 a[160:163], v[40:43], v[64:67], a[160:163]\n\t"
"ds_read_b64_tr_b16 v[122:123], v4 offset:9216\n\t"
"v_accvgpr_read_b32 v158, a22\n\t"
"v_mfma_f32_16x16x32_bf16 a[164:167], v[40:43], v[68:71], a[164:167]\n\t"
"ds_read_b64_tr_b16 v[124:125], v5 offset:8192\n\t"
"v_mfma_f32_16x16x32_bf16 a[168:171], v[40:43], v[72:75], a[168:171]\n\t"
"s_add_u32 m0, s80, 0\n\t"
"s_add_u32 s81, s78, 1572864\n\t"
"buffer_load_dwordx4 v10, s[60:63], s81 offen offset:0 lds\n\t"
"v_accvgpr_read_b32 v159, a23\n\t"
"v_mfma_f32_16x16x32_bf16 a[172:175], v[40:43], v[76:79], a[172:175]\n\t"
"ds_read_b64_tr_b16 v[126:127], v5 offset:9216\n\t"
"v_cvt_pk_bf16_f32 v160, v152, v153\n\t"
"s_waitcnt lgkmcnt(5)\n\t"
"v_mfma_f32_16x16x32_bf16 a[176:179], v[44:47], v[64:67], a[176:179]\n\t"
"ds_read_b128 v[84:87], v1 offset:2048\n\t"
"v_mfma_f32_16x16x32_bf16 a[180:183], v[44:47], v[68:71], a[180:183]\n\t"
"ds_read_b128 v[88:91], v1 offset:4096\n\t"
"v_cvt_pk_bf16_f32 v161, v154, v155\n\t"
"v_mfma_f32_16x16x32_bf16 a[184:187], v[44:47], v[72:75], a[184:187]\n\t"
"ds_read_b128 v[92:95], v1 offset:6144\n\t"
"v_cvt_pk_bf16_f32 v162, v156, v157\n\t"
"v_mfma_f32_16x16x32_bf16 a[188:191], v[44:47], v[76:79], a[188:191]\n\t"
"buffer_load_dwordx4 v11, s[60:63], s81 offen offset:1024 lds\n\t"
"v_mfma_f32_16x16x32_bf16 a[192:195], v[48:51], v[64:67], a[192:195]\n\t"
"ds_read_b128 v[96:99], v1 offset:8192\n\t"
"v_cvt_pk_bf16_f32 v163, v158, v159\n\t"
"v_mfma_f32_16x16x32_bf16 a[196:199], v[48:51], v[68:71], a[196:199]\n\t"
"ds_read_b128 v[100:103], v1 offset:10240\n\t"
"s_add_u32 s82, s84, 4096\n\t"
"buffer_store_dwordx4 v[160:163], v14, s[68:71], s82 offen offset:0 sc1\n\t"
"v_mfma_f32_16x16x32_bf16 a[200:203], v[48:51], v[72:75], a[200:203]\n\t"
"ds_read_b128 v[104:107], v1 offset:12288\n\t"
"v_mfma_f32_16x16x32_bf16 a[204:207], v[48:51], v[76:79], a[204:207]\n\t"
"buffer_load_dwordx4 v12, s[60:63], s81 offen offset:2048 lds\n\t"
"v_accvgpr_read_b32 v164, a24\n\t"
"v_mfma_f32_16x16x32_bf16 a[208:211], v[52:55], v[64:67], a[208:211]\n\t"
"ds_read_b128 v[108:111], v1 offset:14336\n\t"
"v_accvgpr_read_b32 v165, a25\n\t"
"v_mfma_f32_16x16x32_bf16 a[212:215], v[52:55], v[68:71], a[212:215]\n\t"
"v_add_u32 v0, 49152, v0\n\t"
"v_mfma_f32_16x16x32_bf16 a[216:219], v[52:55], v[72:75], a[216:219]\n\t"
"v_add_u32 v1, 49152, v1\n\t"
"v_accvgpr_read_b32 v166, a26\n\t"
"v_mfma_f32_16x16x32_bf16 a[220:223], v[52:55], v[76:79], a[220:223]\n\t"
"v_add_u32 v2, 49152, v2\n\t"
"v_accvgpr_read_b32 v167, a27\n\t"
"v_mfma_f32_16x16x32_bf16 a[224:227], v[56:59], v[64:67], a[224:227]\n\t"
"buffer_load_dwordx4 v13, s[60:63], s81 offen offset:3072 lds\n\t"
"v_mfma_f32_16x16x32_bf16 a[228:231], v[56:59], v[68:71], a[228:231]\n\t"
"v_add_u32 v3, 49152, v3\n\t"
"v_accvgpr_read_b32 v168, a28\n\t"
"v_mfma_f32_16x16x32_bf16 a[232:235], v[56:59], v[72:75], a[232:235]\n\t"
"v_add_u32 v4, 49152, v4\n\t"
"v_accvgpr_read_b32 v169, a29\n\t"
"v_mfma_f32_16x16x32_bf16 a[236:239], v[56:59], v[76:79], a[236:239]\n\t"
"v_add_u32 v5, 49152, v5\n\t"
"v_mfma_f32_16x16x32_bf16 a[240:243], v[60:63], v[64:67], a[240:243]\n\t"
"v_mfma_f32_16x16x32_bf16 a[244:247], v[60:63], v[68:71], a[244:247]\n\t"
"v_mfma_f32_16x16x32_bf16 a[248:251], v[60:63], v[72:75], a[248:251]\n\t"
"v_mfma_f32_16x16x32_bf16 a[252:255], v[60:63], v[76:79], a[252:255]\n\t"
"s_waitcnt vmcnt(16) lgkmcnt(0)\n\t"
"s_barrier\n\t"
"s_load_dwordx2 s[28:29], s[36:37], 0\n\t"
"s_waitcnt lgkmcnt(0)\n\t"
"s_and_b32 s29, s29, 0xffff\n\t"
"s_mov_b32 s30, 2097152\n\t"
"s_mov_b32 s31, 0x00020000\n\t"
"s_lshl_b32 s98, s38, 2\n\t"
"s_add_u32 s98, s98, s39\n\t"
"s_mul_i32 s97, s98, 2048\n\t"
"s_mov_b32 m0, 147456\n\t"
"s_nop 0\n\t"
"buffer_load_dwordx4 v14, s[28:31], s97 offen offset:0 lds\n\t"
"v_mfma_f32_16x16x32_bf16 a[128:131], v[80:83], v[112:115], a[128:131]\n\t"
"ds_read_b128 v[32:35], v0 offset:0\n\t"
"v_accvgpr_read_b32 v170, a30\n\t"
"v_mfma_f32_16x16x32_bf16 a[132:135], v[80:83], v[116:119], a[132:135]\n\t"
"s_add_u32 m0, s79, 49152\n\t"
"s_add_u32 s81, s76, 512\n\t"
"buffer_load_dwordx4 v6, s[48:51], s81 offen offset:0 lds\n\t"
"v_mfma_f32_16x16x32_bf16 a[136:139], v[80:83], v[120:123], a[136:139]\n\t"
"v_accvgpr_read_b32 v171, a31\n\t"
"ds_read_b64_tr_b16 v[64:65], v2 offset:0\n\t"
"v_mfma_f32_16x16x32_bf16 a[140:143], v[80:83], v[124:127], a[140:143]\n\t"
"v_cvt_pk_bf16_f32 v172, v164, v165\n\t"
"v_mfma_f32_16x16x32_bf16 a[144:147], v[84:87], v[112:115], a[144:147]\n\t"
"ds_read_b64_tr_b16 v[66:67], v2 offset:1024\n\t"
"v_cvt_pk_bf16_f32 v173, v166, v167\n\t"
"v_mfma_f32_16x16x32_bf16 a[148:151], v[84:87], v[116:119], a[148:151]\n\t"
"buffer_load_dwordx4 v7, s[48:51], s81 offen offset:1024 lds\n\t"
"v_mfma_f32_16x16x32_bf16 a[152:155], v[84:87], v[120:123], a[152:155]\n\t"
"v_cvt_pk_bf16_f32 v174, v168, v169\n\t"
"ds_read_b64_tr_b16 v[68:69], v3 offset:0\n\t"
"v_mfma_f32_16x16x32_bf16 a[156:159], v[84:87], v[124:127], a[156:159]\n\t"
"v_cvt_pk_bf16_f32 v175, v170, v171\n\t"
"v_mfma_f32_16x16x32_bf16 a[160:163], v[88:91], v[112:115], a[160:163]\n\t"
"ds_read_b64_tr_b16 v[70:71], v3 offset:1024\n\t"
"s_add_u32 s82, s84, 4096\n\t"
"buffer_store_dwordx4 v[172:175], v14, s[68:71], s82 offen offset:1024 sc1\n\t"
"v_mfma_f32_16x16x32_bf16 a[164:167], v[88:91], v[116:119], a[164:167]\n\t"
"ds_read_b64_tr_b16 v[72:73], v4 offset:0\n\t"
"v_mfma_f32_16x16x32_bf16 a[168:171], v[88:91], v[120:123], a[168:171]\n\t"
"v_accvgpr_read_b32 v176, a32\n\t"
"buffer_load_dwordx4 v8, s[48:51], s81 offen offset:2048 lds\n\t"
"v_mfma_f32_16x16x32_bf16 a[172:175], v[88:91], v[124:127], a[172:175]\n\t"
"v_accvgpr_read_b32 v177, a33\n\t"
"v_mfma_f32_16x16x32_bf16 a[176:179], v[92:95], v[112:115], a[176:179]\n\t"
"ds_read_b64_tr_b16 v[74:75], v4 offset:1024\n\t"
"v_accvgpr_read_b32 v178, a34\n\t"
"v_mfma_f32_16x16x32_bf16 a[180:183], v[92:95], v[116:119], a[180:183]\n\t"
"ds_read_b64_tr_b16 v[76:77], v5 offset:0\n\t"
"v_mfma_f32_16x16x32_bf16 a[184:187], v[92:95], v[120:123], a[184:187]\n\t"
"ds_read_b64_tr_b16 v[78:79], v5 offset:1024\n\t"
"v_accvgpr_read_b32 v179, a35\n\t"
"v_mfma_f32_16x16x32_bf16 a[188:191], v[92:95], v[124:127], a[188:191]\n\t"
"buffer_load_dwordx4 v9, s[48:51], s81 offen offset:3072 lds\n\t"
"v_mfma_f32_16x16x32_bf16 a[192:195], v[96:99], v[112:115], a[192:195]\n\t"
"v_accvgpr_read_b32 v180, a36\n\t"
"ds_read_b128 v[36:39], v0 offset:2048\n\t"
"v_mfma_f32_16x16x32_bf16 a[196:199], v[96:99], v[116:119], a[196:199]\n\t"
"v_accvgpr_read_b32 v181, a37\n\t"
"v_mfma_f32_16x16x32_bf16 a[200:203], v[96:99], v[120:123], a[200:203]\n\t"
"ds_read_b128 v[40:43], v0 offset:4096\n\t"
"v_accvgpr_read_b32 v182, a38\n\t"
"v_mfma_f32_16x16x32_bf16 a[204:207], v[96:99], v[124:127], a[204:207]\n\t"
"s_add_u32 m0, s79, 53248\n\t"
"s_add_u32 s81, s76, 33280\n\t"
"buffer_load_dwordx4 v6, s[48:51], s81 offen offset:0 lds\n\t"
"v_mfma_f32_16x16x32_bf16 a[208:211], v[100:103], v[112:115], a[208:211]\n\t"
"v_accvgpr_read_b32 v183, a39\n\t"
"ds_read_b128 v[44:47], v0 offset:6144\n\t"
"v_mfma_f32_16x16x32_bf16 a[212:215], v[100:103], v[116:119], a[212:215]\n\t"
"v_cvt_pk_bf16_f32 v184, v176, v177\n\t"
"v_mfma_f32_16x16x32_bf16 a[216:219], v[100:103], v[120:123], a[216:219]\n\t"
"ds_read_b128 v[48:51], v0 offset:8192\n\t"
"v_cvt_pk_bf16_f32 v185, v178, v179\n\t"
"v_mfma_f32_16x16x32_bf16 a[220:223], v[100:103], v[124:127], a[220:223]\n\t"
"ds_read_b128 v[52:55], v0 offset:10240\n\t"
"v_mfma_f32_16x16x32_bf16 a[224:227], v[104:107], v[112:115], a[224:227]\n\t"
"v_cvt_pk_bf16_f32 v186, v180, v181\n\t"
"buffer_load_dwordx4 v7, s[48:51], s81 offen offset:1024 lds\n\t"
"v_mfma_f32_16x16x32_bf16 a[228:231], v[104:107], v[116:119], a[228:231]\n\t"
"v_cvt_pk_bf16_f32 v187, v182, v183\n\t"
"v_mfma_f32_16x16x32_bf16 a[232:235], v[104:107], v[120:123], a[232:235]\n\t"
"ds_read_b128 v[56:59], v0 offset:12288\n\t"
"s_add_u32 s82, s84, 8192\n\t"
"buffer_store_dwordx4 v[184:187], v14, s[68:71], s82 offen offset:0 sc1\n\t"
"v_mfma_f32_16x16x32_bf16 a[236:239], v[104:107], v[124:127], a[236:239]\n\t"
"ds_read_b128 v[60:63], v0 offset:14336\n\t"
"s_waitcnt lgkmcnt(5)\n\t"
"v_mfma_f32_16x16x32_bf16 a[240:243], v[108:111], v[112:115], a[240:243]\n\t"
"v_mfma_f32_16x16x32_bf16 a[244:247], v[108:111], v[116:119], a[244:247]\n\t"
"v_mfma_f32_16x16x32_bf16 a[248:251], v[108:111], v[120:123], a[248:251]\n\t"
"v_mfma_f32_16x16x32_bf16 a[252:255], v[108:111], v[124:127], a[252:255]\n\t"
"v_mfma_f32_16x16x32_bf16 a[128:131], v[32:35], v[64:67], a[128:131]\n\t"
"ds_read_b128 v[80:83], v1 offset:0\n\t"
"v_accvgpr_read_b32 v188, a40\n\t"
"v_mfma_f32_16x16x32_bf16 a[132:135], v[32:35], v[68:71], a[132:135]\n\t"
"buffer_load_dwordx4 v8, s[48:51], s81 offen offset:2048 lds\n\t"
"v_accvgpr_read_b32 v189, a41\n\t"
"v_mfma_f32_16x16x32_bf16 a[136:139], v[32:35], v[72:75], a[136:139]\n\t"
"ds_read_b64_tr_b16 v[112:113], v2 offset:8192\n\t"
"v_accvgpr_read_b32 v190, a42\n\t"
"v_mfma_f32_16x16x32_bf16 a[140:143], v[32:35], v[76:79], a[140:143]\n\t"
"ds_read_b64_tr_b16 v[114:115], v2 offset:9216\n\t"
"v_mfma_f32_16x16x32_bf16 a[144:147], v[36:39], v[64:67], a[144:147]\n\t"
"ds_read_b64_tr_b16 v[116:117], v3 offset:8192\n\t"
"v_accvgpr_read_b32 v191, a43\n\t"
"v_mfma_f32_16x16x32_bf16 a[148:151], v[36:39], v[68:71], a[148:151]\n\t"
"buffer_load_dwordx4 v9, s[48:51], s81 offen offset:3072 lds\n\t"
"v_accvgpr_read_b32 v192, a44\n\t"
"v_mfma_f32_16x16x32_bf16 a[152:155], v[36:39], v[72:75], a[152:155]\n\t"
"ds_read_b64_tr_b16 v[118:119], v3 offset:9216\n\t"
"v_mfma_f32_16x16x32_bf16 a[156:159], v[36:39], v[76:79], a[156:159]\n\t"
"ds_read_b64_tr_b16 v[120:121], v4 offset:8192\n\t"
"v_accvgpr_read_b32 v193, a45\n\t"
"v_mfma_f32_16x16x32_bf16 a[160:163], v[40:43], v[64:67], a[160:163]\n\t"
"ds_read_b64_tr_b16 v[122:123], v4 offset:9216\n\t"
"v_accvgpr_read_b32 v194, a46\n\t"
"v_mfma_f32_16x16x32_bf16 a[164:167], v[40:43], v[68:71], a[164:167]\n\t"
"ds_read_b64_tr_b16 v[124:125], v5 offset:8192\n\t"
"v_mfma_f32_16x16x32_bf16 a[168:171], v[40:43], v[72:75], a[168:171]\n\t"
"s_add_u32 m0, s80, 49152\n\t"
"s_add_u32 s81, s78, 2097152\n\t"
"buffer_load_dwordx4 v10, s[60:63], s81 offen offset:0 lds\n\t"
"v_accvgpr_read_b32 v195, a47\n\t"
"v_mfma_f32_16x16x32_bf16 a[172:175], v[40:43], v[76:79], a[172:175]\n\t"
"ds_read_b64_tr_b16 v[126:127], v5 offset:9216\n\t"
"v_cvt_pk_bf16_f32 v196, v188, v189\n\t"
"s_waitcnt lgkmcnt(5)\n\t"
"v_mfma_f32_16x16x32_bf16 a[176:179], v[44:47], v[64:67], a[176:179]\n\t"
"ds_read_b128 v[84:87], v1 offset:2048\n\t"
"v_mfma_f32_16x16x32_bf16 a[180:183], v[44:47], v[68:71], a[180:183]\n\t"
"ds_read_b128 v[88:91], v1 offset:4096\n\t"
"v_cvt_pk_bf16_f32 v197, v190, v191\n\t"
"v_mfma_f32_16x16x32_bf16 a[184:187], v[44:47], v[72:75], a[184:187]\n\t"
"ds_read_b128 v[92:95], v1 offset:6144\n\t"
"v_cvt_pk_bf16_f32 v198, v192, v193\n\t"
"v_mfma_f32_16x16x32_bf16 a[188:191], v[44:47], v[76:79], a[188:191]\n\t"
"buffer_load_dwordx4 v11, s[60:63], s81 offen offset:1024 lds\n\t"
"v_mfma_f32_16x16x32_bf16 a[192:195], v[48:51], v[64:67], a[192:195]\n\t"
"ds_read_b128 v[96:99], v1 offset:8192\n\t"
"v_cvt_pk_bf16_f32 v199, v194, v195\n\t"
"v_mfma_f32_16x16x32_bf16 a[196:199], v[48:51], v[68:71], a[196:199]\n\t"
"ds_read_b128 v[100:103], v1 offset:10240\n\t"
"s_add_u32 s82, s84, 8192\n\t"
"buffer_store_dwordx4 v[196:199], v14, s[68:71], s82 offen offset:1024 sc1\n\t"
"v_mfma_f32_16x16x32_bf16 a[200:203], v[48:51], v[72:75], a[200:203]\n\t"
"ds_read_b128 v[104:107], v1 offset:12288\n\t"
"v_mfma_f32_16x16x32_bf16 a[204:207], v[48:51], v[76:79], a[204:207]\n\t"
"buffer_load_dwordx4 v12, s[60:63], s81 offen offset:2048 lds\n\t"
"v_accvgpr_read_b32 v200, a48\n\t"
"v_mfma_f32_16x16x32_bf16 a[208:211], v[52:55], v[64:67], a[208:211]\n\t"
"ds_read_b128 v[108:111], v1 offset:14336\n\t"
"v_accvgpr_read_b32 v201, a49\n\t"
"v_mfma_f32_16x16x32_bf16 a[212:215], v[52:55], v[68:71], a[212:215]\n\t"
"v_add_u32 v0, -98304, v0\n\t"
"v_mfma_f32_16x16x32_bf16 a[216:219], v[52:55], v[72:75], a[216:219]\n\t"
"v_add_u32 v1, -98304, v1\n\t"
"v_accvgpr_read_b32 v202, a50\n\t"
"v_mfma_f32_16x16x32_bf16 a[220:223], v[52:55], v[76:79], a[220:223]\n\t"
"v_add_u32 v2, -98304, v2\n\t"
"v_accvgpr_read_b32 v203, a51\n\t"
"v_mfma_f32_16x16x32_bf16 a[224:227], v[56:59], v[64:67], a[224:227]\n\t"
"buffer_load_dwordx4 v13, s[60:63], s81 offen offset:3072 lds\n\t"
"v_mfma_f32_16x16x32_bf16 a[228:231], v[56:59], v[68:71], a[228:231]\n\t"
"v_add_u32 v3, -98304, v3\n\t"
"v_accvgpr_read_b32 v204, a52\n\t"
"v_mfma_f32_16x16x32_bf16 a[232:235], v[56:59], v[72:75], a[232:235]\n\t"
"v_add_u32 v4, -98304, v4\n\t"
"v_accvgpr_read_b32 v205, a53\n\t"
"v_mfma_f32_16x16x32_bf16 a[236:239], v[56:59], v[76:79], a[236:239]\n\t"
"v_add_u32 v5, -98304, v5\n\t"
"v_mfma_f32_16x16x32_bf16 a[240:243], v[60:63], v[64:67], a[240:243]\n\t"
"v_mfma_f32_16x16x32_bf16 a[244:247], v[60:63], v[68:71], a[244:247]\n\t"
"v_mfma_f32_16x16x32_bf16 a[248:251], v[60:63], v[72:75], a[248:251]\n\t"
"v_mfma_f32_16x16x32_bf16 a[252:255], v[60:63], v[76:79], a[252:255]\n\t"
"s_waitcnt vmcnt(16) lgkmcnt(0)\n\t"
"s_barrier\n\t"
"s_mov_b32 m0, 147456\n\t"
"s_nop 0\n\t"
"buffer_load_dwordx4 v14, s[28:31], s97 offen offset:1024 lds\n\t"
"s_lshl_b32 s82, s46, 8\n\t"
"s_add_u32 s82, s82, 26624\n\t"
"s_lshl_b32 s83, s47, 5\n\t"
"s_add_u32 s82, s82, s83\n\t"
"s_lshl_b32 s83, s39, 3\n\t"
"s_add_u32 s82, s82, s83\n\t"
"v_mov_b32 v216, s82\n\t"
"v_mov_b32 v218, s26\n\t"
"v_mov_b32 v219, s27\n\t"
"s_mov_b64 s[86:87], exec\n\t"
"s_mov_b64 exec, 1\n\t"
"s_nop 1\n\t"
"global_store_dwordx2 v216, v[218:219], s[42:43] sc1\n\t"
"s_mov_b64 exec, s[86:87]\n\t"
"s_nop 1\n\t"
"v_mfma_f32_16x16x32_bf16 a[128:131], v[80:83], v[112:115], a[128:131]\n\t"
"ds_read_b128 v[32:35], v0 offset:0\n\t"
"v_accvgpr_read_b32 v206, a54\n\t"
"v_mfma_f32_16x16x32_bf16 a[132:135], v[80:83], v[116:119], a[132:135]\n\t"
"s_add_u32 m0, s79, 98304\n\t"
"s_add_u32 s81, s76, 640\n\t"
"buffer_load_dwordx4 v6, s[48:51], s81 offen offset:0 lds\n\t"
"v_mfma_f32_16x16x32_bf16 a[136:139], v[80:83], v[120:123], a[136:139]\n\t"
"v_accvgpr_read_b32 v207, a55\n\t"
"ds_read_b64_tr_b16 v[64:65], v2 offset:0\n\t"
"v_mfma_f32_16x16x32_bf16 a[140:143], v[80:83], v[124:127], a[140:143]\n\t"
"v_cvt_pk_bf16_f32 v208, v200, v201\n\t"
"v_mfma_f32_16x16x32_bf16 a[144:147], v[84:87], v[112:115], a[144:147]\n\t"
"ds_read_b64_tr_b16 v[66:67], v2 offset:1024\n\t"
"v_cvt_pk_bf16_f32 v209, v202, v203\n\t"
"v_mfma_f32_16x16x32_bf16 a[148:151], v[84:87], v[116:119], a[148:151]\n\t"
"buffer_load_dwordx4 v7, s[48:51], s81 offen offset:1024 lds\n\t"
"v_mfma_f32_16x16x32_bf16 a[152:155], v[84:87], v[120:123], a[152:155]\n\t"
"v_cvt_pk_bf16_f32 v210, v204, v205\n\t"
"ds_read_b64_tr_b16 v[68:69], v3 offset:0\n\t"
"v_mfma_f32_16x16x32_bf16 a[156:159], v[84:87], v[124:127], a[156:159]\n\t"
"v_cvt_pk_bf16_f32 v211, v206, v207\n\t"
"v_mfma_f32_16x16x32_bf16 a[160:163], v[88:91], v[112:115], a[160:163]\n\t"
"ds_read_b64_tr_b16 v[70:71], v3 offset:1024\n\t"
"s_add_u32 s82, s84, 12288\n\t"
"buffer_store_dwordx4 v[208:211], v14, s[68:71], s82 offen offset:0 sc1\n\t"
"v_mfma_f32_16x16x32_bf16 a[164:167], v[88:91], v[116:119], a[164:167]\n\t"
"ds_read_b64_tr_b16 v[72:73], v4 offset:0\n\t"
"v_mfma_f32_16x16x32_bf16 a[168:171], v[88:91], v[120:123], a[168:171]\n\t"
"v_accvgpr_read_b32 v128, a56\n\t"
"buffer_load_dwordx4 v8, s[48:51], s81 offen offset:2048 lds\n\t"
"v_mfma_f32_16x16x32_bf16 a[172:175], v[88:91], v[124:127], a[172:175]\n\t"
"v_accvgpr_read_b32 v129, a57\n\t"
"v_mfma_f32_16x16x32_bf16 a[176:179], v[92:95], v[112:115], a[176:179]\n\t"
"ds_read_b64_tr_b16 v[74:75], v4 offset:1024\n\t"
"v_accvgpr_read_b32 v130, a58\n\t"
"v_mfma_f32_16x16x32_bf16 a[180:183], v[92:95], v[116:119], a[180:183]\n\t"
"ds_read_b64_tr_b16 v[76:77], v5 offset:0\n\t"
"v_mfma_f32_16x16x32_bf16 a[184:187], v[92:95], v[120:123], a[184:187]\n\t"
"ds_read_b64_tr_b16 v[78:79], v5 offset:1024\n\t"
"v_accvgpr_read_b32 v131, a59\n\t"
"v_mfma_f32_16x16x32_bf16 a[188:191], v[92:95], v[124:127], a[188:191]\n\t"
"buffer_load_dwordx4 v9, s[48:51], s81 offen offset:3072 lds\n\t"
"v_mfma_f32_16x16x32_bf16 a[192:195], v[96:99], v[112:115], a[192:195]\n\t"
"v_accvgpr_read_b32 v132, a60\n\t"
"ds_read_b128 v[36:39], v0 offset:2048\n\t"
"v_mfma_f32_16x16x32_bf16 a[196:199], v[96:99], v[116:119], a[196:199]\n\t"
"v_accvgpr_read_b32 v133, a61\n\t"
"v_mfma_f32_16x16x32_bf16 a[200:203], v[96:99], v[120:123], a[200:203]\n\t"
"ds_read_b128 v[40:43], v0 offset:4096\n\t"
"v_accvgpr_read_b32 v134, a62\n\t"
"v_mfma_f32_16x16x32_bf16 a[204:207], v[96:99], v[124:127], a[204:207]\n\t"
"s_add_u32 m0, s79, 102400\n\t"
"s_add_u32 s81, s76, 33408\n\t"
"buffer_load_dwordx4 v6, s[48:51], s81 offen offset:0 lds\n\t"
"v_mfma_f32_16x16x32_bf16 a[208:211], v[100:103], v[112:115], a[208:211]\n\t"
"v_accvgpr_read_b32 v135, a63\n\t"
"ds_read_b128 v[44:47], v0 offset:6144\n\t"
"v_mfma_f32_16x16x32_bf16 a[212:215], v[100:103], v[116:119], a[212:215]\n\t"
"v_cvt_pk_bf16_f32 v136, v128, v129\n\t"
"v_mfma_f32_16x16x32_bf16 a[216:219], v[100:103], v[120:123], a[216:219]\n\t"
"ds_read_b128 v[48:51], v0 offset:8192\n\t"
"v_cvt_pk_bf16_f32 v137, v130, v131\n\t"
"v_mfma_f32_16x16x32_bf16 a[220:223], v[100:103], v[124:127], a[220:223]\n\t"
"ds_read_b128 v[52:55], v0 offset:10240\n\t"
"v_mfma_f32_16x16x32_bf16 a[224:227], v[104:107], v[112:115], a[224:227]\n\t"
"v_cvt_pk_bf16_f32 v138, v132, v133\n\t"
"buffer_load_dwordx4 v7, s[48:51], s81 offen offset:1024 lds\n\t"
"v_mfma_f32_16x16x32_bf16 a[228:231], v[104:107], v[116:119], a[228:231]\n\t"
"v_cvt_pk_bf16_f32 v139, v134, v135\n\t"
"v_mfma_f32_16x16x32_bf16 a[232:235], v[104:107], v[120:123], a[232:235]\n\t"
"ds_read_b128 v[56:59], v0 offset:12288\n\t"
"s_add_u32 s82, s84, 12288\n\t"
"buffer_store_dwordx4 v[136:139], v14, s[68:71], s82 offen offset:1024 sc1\n\t"
"v_mfma_f32_16x16x32_bf16 a[236:239], v[104:107], v[124:127], a[236:239]\n\t"
"ds_read_b128 v[60:63], v0 offset:14336\n\t"
"s_waitcnt lgkmcnt(5)\n\t"
"v_mfma_f32_16x16x32_bf16 a[240:243], v[108:111], v[112:115], a[240:243]\n\t"
"v_mfma_f32_16x16x32_bf16 a[244:247], v[108:111], v[116:119], a[244:247]\n\t"
"v_mfma_f32_16x16x32_bf16 a[248:251], v[108:111], v[120:123], a[248:251]\n\t"
"v_mfma_f32_16x16x32_bf16 a[252:255], v[108:111], v[124:127], a[252:255]\n\t"
"v_mfma_f32_16x16x32_bf16 a[128:131], v[32:35], v[64:67], a[128:131]\n\t"
"ds_read_b128 v[80:83], v1 offset:0\n\t"
"v_accvgpr_read_b32 v140, a64\n\t"
"v_mfma_f32_16x16x32_bf16 a[132:135], v[32:35], v[68:71], a[132:135]\n\t"
"buffer_load_dwordx4 v8, s[48:51], s81 offen offset:2048 lds\n\t"
"v_mfma_f32_16x16x32_bf16 a[136:139], v[32:35], v[72:75], a[136:139]\n\t"
"ds_read_b64_tr_b16 v[112:113], v2 offset:8192\n\t"
"v_accvgpr_read_b32 v141, a65\n\t"
"v_mfma_f32_16x16x32_bf16 a[140:143], v[32:35], v[76:79], a[140:143]\n\t"
"ds_read_b64_tr_b16 v[114:115], v2 offset:9216\n\t"
"v_mfma_f32_16x16x32_bf16 a[144:147], v[36:39], v[64:67], a[144:147]\n\t"
"ds_read_b64_tr_b16 v[116:117], v3 offset:8192\n\t"
"v_accvgpr_read_b32 v142, a66\n\t"
"v_mfma_f32_16x16x32_bf16 a[148:151], v[36:39], v[68:71], a[148:151]\n\t"
"buffer_load_dwordx4 v9, s[48:51], s81 offen offset:3072 lds\n\t"
"v_mfma_f32_16x16x32_bf16 a[152:155], v[36:39], v[72:75], a[152:155]\n\t"
"ds_read_b64_tr_b16 v[118:119], v3 offset:9216\n\t"
"v_accvgpr_read_b32 v143, a67\n\t"
"v_mfma_f32_16x16x32_bf16 a[156:159], v[36:39], v[76:79], a[156:159]\n\t"
"ds_read_b64_tr_b16 v[120:121], v4 offset:8192\n\t"
"v_mfma_f32_16x16x32_bf16 a[160:163], v[40:43], v[64:67], a[160:163]\n\t"
"ds_read_b64_tr_b16 v[122:123], v4 offset:9216\n\t"
"v_accvgpr_read_b32 v144, a68\n\t"
"v_mfma_f32_16x16x32_bf16 a[164:167], v[40:43], v[68:71], a[164:167]\n\t"
"ds_read_b64_tr_b16 v[124:125], v5 offset:8192\n\t"
"v_mfma_f32_16x16x32_bf16 a[168:171], v[40:43], v[72:75], a[168:171]\n\t"
"s_add_u32 m0, s80, 98304\n\t"
"s_add_u32 s81, s78, 2621440\n\t"
"buffer_load_dwordx4 v10, s[60:63], s81 offen offset:0 lds\n\t"
"v_accvgpr_read_b32 v145, a69\n\t"
"v_mfma_f32_16x16x32_bf16 a[172:175], v[40:43], v[76:79], a[172:175]\n\t"
"ds_read_b64_tr_b16 v[126:127], v5 offset:9216\n\t"
"s_waitcnt lgkmcnt(5)\n\t"
"v_mfma_f32_16x16x32_bf16 a[176:179], v[44:47], v[64:67], a[176:179]\n\t"
"ds_read_b128 v[84:87], v1 offset:2048\n\t"
"v_accvgpr_read_b32 v146, a70\n\t"
"v_mfma_f32_16x16x32_bf16 a[180:183], v[44:47], v[68:71], a[180:183]\n\t"
"ds_read_b128 v[88:91], v1 offset:4096\n\t"
"v_mfma_f32_16x16x32_bf16 a[184:187], v[44:47], v[72:75], a[184:187]\n\t"
"ds_read_b128 v[92:95], v1 offset:6144\n\t"
"v_mfma_f32_16x16x32_bf16 a[188:191], v[44:47], v[76:79], a[188:191]\n\t"
"buffer_load_dwordx4 v11, s[60:63], s81 offen offset:1024 lds\n\t"
"v_accvgpr_read_b32 v147, a71\n\t"
"v_mfma_f32_16x16x32_bf16 a[192:195], v[48:51], v[64:67], a[192:195]\n\t"
"ds_read_b128 v[96:99], v1 offset:8192\n\t"
"v_mfma_f32_16x16x32_bf16 a[196:199], v[48:51], v[68:71], a[196:199]\n\t"
"ds_read_b128 v[100:103], v1 offset:10240\n\t"
"v_cvt_pk_bf16_f32 v148, v140, v141\n\t"
"v_mfma_f32_16x16x32_bf16 a[200:203], v[48:51], v[72:75], a[200:203]\n\t"
"ds_read_b128 v[104:107], v1 offset:12288\n\t"
"v_mfma_f32_16x16x32_bf16 a[204:207], v[48:51], v[76:79], a[204:207]\n\t"
"buffer_load_dwordx4 v12, s[60:63], s81 offen offset:2048 lds\n\t"
"v_cvt_pk_bf16_f32 v149, v142, v143\n\t"
"v_mfma_f32_16x16x32_bf16 a[208:211], v[52:55], v[64:67], a[208:211]\n\t"
"ds_read_b128 v[108:111], v1 offset:14336\n\t"
"v_mfma_f32_16x16x32_bf16 a[212:215], v[52:55], v[68:71], a[212:215]\n\t"
"v_add_u32 v0, 49152, v0\n\t"
"v_cvt_pk_bf16_f32 v150, v144, v145\n\t"
"v_mfma_f32_16x16x32_bf16 a[216:219], v[52:55], v[72:75], a[216:219]\n\t"
"v_add_u32 v1, 49152, v1\n\t"
"v_mfma_f32_16x16x32_bf16 a[220:223], v[52:55], v[76:79], a[220:223]\n\t"
"v_add_u32 v2, 49152, v2\n\t"
"v_cvt_pk_bf16_f32 v151, v146, v147\n\t"
"v_mfma_f32_16x16x32_bf16 a[224:227], v[56:59], v[64:67], a[224:227]\n\t"
"buffer_load_dwordx4 v13, s[60:63], s81 offen offset:3072 lds\n\t"
"v_mfma_f32_16x16x32_bf16 a[228:231], v[56:59], v[68:71], a[228:231]\n\t"
"v_add_u32 v3, 49152, v3\n\t"
"s_add_u32 s82, s84, 16384\n\t"
"buffer_store_dwordx4 v[148:151], v14, s[68:71], s82 offen offset:0 sc1\n\t"
"v_mfma_f32_16x16x32_bf16 a[232:235], v[56:59], v[72:75], a[232:235]\n\t"
"v_add_u32 v4, 49152, v4\n\t"
"v_mfma_f32_16x16x32_bf16 a[236:239], v[56:59], v[76:79], a[236:239]\n\t"
"v_add_u32 v5, 49152, v5\n\t"
"v_mfma_f32_16x16x32_bf16 a[240:243], v[60:63], v[64:67], a[240:243]\n\t"
"v_mfma_f32_16x16x32_bf16 a[244:247], v[60:63], v[68:71], a[244:247]\n\t"
"v_mfma_f32_16x16x32_bf16 a[248:251], v[60:63], v[72:75], a[248:251]\n\t"
"v_mfma_f32_16x16x32_bf16 a[252:255], v[60:63], v[76:79], a[252:255]\n\t"
"s_waitcnt vmcnt(17) lgkmcnt(0)\n\t"
"s_barrier\n\t"
"s_load_dwordx2 s[28:29], s[36:37], 88\n\t"
"s_waitcnt lgkmcnt(0)\n\t"
"s_and_b32 s29, s29, 0xffff\n\t"
"s_mov_b32 s30, 524288\n\t"
"s_mov_b32 s31, 0x00020000\n\t"
"s_lshl_b32 s98, s38, 2\n\t"
"s_add_u32 s98, s98, s39\n\t"
"s_mul_i32 s97, s98, 1024\n\t"
"s_mov_b32 m0, 147456\n\t"
"s_nop 0\n\t"
"buffer_load_dwordx4 v14, s[28:31], s97 offen offset:0 lds\n\t"
"v_mfma_f32_16x16x32_bf16 a[128:131], v[80:83], v[112:115], a[128:131]\n\t"
"ds_read_b128 v[32:35], v0 offset:0\n\t"
"v_accvgpr_read_b32 v152, a72\n\t"
"v_mfma_f32_16x16x32_bf16 a[132:135], v[80:83], v[116:119], a[132:135]\n\t"
"s_add_u32 m0, s79, 0\n\t"
"s_add_u32 s81, s76, 768\n\t"
"buffer_load_dwordx4 v6, s[48:51], s81 offen offset:0 lds\n\t"
"v_mfma_f32_16x16x32_bf16 a[136:139], v[80:83], v[120:123], a[136:139]\n\t"
"v_accvgpr_read_b32 v153, a73\n\t"
"v_mfma_f32_16x16x32_bf16 a[140:143], v[80:83], v[124:127], a[140:143]\n\t"
"ds_read_b64_tr_b16 v[64:65], v2 offset:0\n\t"
"v_mfma_f32_16x16x32_bf16 a[144:147], v[84:87], v[112:115], a[144:147]\n\t"
"ds_read_b64_tr_b16 v[66:67], v2 offset:1024\n\t"
"v_accvgpr_read_b32 v154, a74\n\t"
"v_mfma_f32_16x16x32_bf16 a[148:151], v[84:87], v[116:119], a[148:151]\n\t"
"buffer_load_dwordx4 v7, s[48:51], s81 offen offset:1024 lds\n\t"
"v_mfma_f32_16x16x32_bf16 a[152:155], v[84:87], v[120:123], a[152:155]\n\t"
"ds_read_b64_tr_b16 v[68:69], v3 offset:0\n\t"
"v_mfma_f32_16x16x32_bf16 a[156:159], v[84:87], v[124:127], a[156:159]\n\t"
"v_accvgpr_read_b32 v155, a75\n\t"
"v_mfma_f32_16x16x32_bf16 a[160:163], v[88:91], v[112:115], a[160:163]\n\t"
"ds_read_b64_tr_b16 v[70:71], v3 offset:1024\n\t"
"v_accvgpr_read_b32 v156, a76\n\t"
"v_mfma_f32_16x16x32_bf16 a[164:167], v[88:91], v[116:119], a[164:167]\n\t"
"ds_read_b64_tr_b16 v[72:73], v4 offset:0\n\t"
"v_mfma_f32_16x16x32_bf16 a[168:171], v[88:91], v[120:123], a[168:171]\n\t"
"buffer_load_dwordx4 v8, s[48:51], s81 offen offset:2048 lds\n\t"
"v_mfma_f32_16x16x32_bf16 a[172:175], v[88:91], v[124:127], a[172:175]\n\t"
"v_accvgpr_read_b32 v157, a77\n\t"
"v_mfma_f32_16x16x32_bf16 a[176:179], v[92:95], v[112:115], a[176:179]\n\t"
"ds_read_b64_tr_b16 v[74:75], v4 offset:1024\n\t"
"ds_read_b64_tr_b16 v[76:77], v5 offset:0\n\t"
"v_mfma_f32_16x16x32_bf16 a[180:183], v[92:95], v[116:119], a[180:183]\n\t"
"v_accvgpr_read_b32 v158, a78\n\t"
"v_mfma_f32_16x16x32_bf16 a[184:187], v[92:95], v[120:123], a[184:187]\n\t"
"ds_read_b64_tr_b16 v[78:79], v5 offset:1024\n\t"
"v_mfma_f32_16x16x32_bf16 a[188:191], v[92:95], v[124:127], a[188:191]\n\t"
"v_accvgpr_read_b32 v159, a79\n\t"
"v_mfma_f32_16x16x32_bf16 a[192:195], v[96:99], v[112:115], a[192:195]\n\t"
"buffer_load_dwordx4 v9, s[48:51], s81 offen offset:3072 lds\n\t"
"ds_read_b128 v[36:39], v0 offset:2048\n\t"
"v_mfma_f32_16x16x32_bf16 a[196:199], v[96:99], v[116:119], a[196:199]\n\t"
"v_cvt_pk_bf16_f32 v160, v152, v153\n\t"
"v_mfma_f32_16x16x32_bf16 a[200:203], v[96:99], v[120:123], a[200:203]\n\t"
"ds_read_b128 v[40:43], v0 offset:4096\n\t"
"v_mfma_f32_16x16x32_bf16 a[204:207], v[96:99], v[124:127], a[204:207]\n\t"
"s_add_u32 m0, s79, 4096\n\t"
"s_add_u32 s81, s76, 33536\n\t"
"buffer_load_dwordx4 v6, s[48:51], s81 offen offset:0 lds\n\t"
"v_mfma_f32_16x16x32_bf16 a[208:211], v[100:103], v[112:115], a[208:211]\n\t"
"v_cvt_pk_bf16_f32 v161, v154, v155\n\t"
"ds_read_b128 v[44:47], v0 offset:6144\n\t"
"v_mfma_f32_16x16x32_bf16 a[212:215], v[100:103], v[116:119], a[212:215]\n\t"
"v_cvt_pk_bf16_f32 v162, v156, v157\n\t"
"v_mfma_f32_16x16x32_bf16 a[216:219], v[100:103], v[120:123], a[216:219]\n\t"
"ds_read_b128 v[48:51], v0 offset:8192\n\t"
"v_mfma_f32_16x16x32_bf16 a[220:223], v[100:103], v[124:127], a[220:223]\n\t"
"ds_read_b128 v[52:55], v0 offset:10240\n\t"
"v_mfma_f32_16x16x32_bf16 a[224:227], v[104:107], v[112:115], a[224:227]\n\t"
"v_cvt_pk_bf16_f32 v163, v158, v159\n\t"
"buffer_load_dwordx4 v7, s[48:51], s81 offen offset:1024 lds\n\t"
"v_mfma_f32_16x16x32_bf16 a[228:231], v[104:107], v[116:119], a[228:231]\n\t"
"ds_read_b128 v[56:59], v0 offset:12288\n\t"
"v_mfma_f32_16x16x32_bf16 a[232:235], v[104:107], v[120:123], a[232:235]\n\t"
"s_add_u32 s82, s84, 16384\n\t"
"buffer_store_dwordx4 v[160:163], v14, s[68:71], s82 offen offset:1024 sc1\n\t"
"v_mfma_f32_16x16x32_bf16 a[236:239], v[104:107], v[124:127], a[236:239]\n\t"
"ds_read_b128 v[60:63], v0 offset:14336\n\t"
"s_waitcnt lgkmcnt(5)\n\t"
"v_mfma_f32_16x16x32_bf16 a[240:243], v[108:111], v[112:115], a[240:243]\n\t"
"v_mfma_f32_16x16x32_bf16 a[244:247], v[108:111], v[116:119], a[244:247]\n\t"
"v_mfma_f32_16x16x32_bf16 a[248:251], v[108:111], v[120:123], a[248:251]\n\t"
"v_mfma_f32_16x16x32_bf16 a[252:255], v[108:111], v[124:127], a[252:255]\n\t"
"v_mfma_f32_16x16x32_bf16 a[128:131], v[32:35], v[64:67], a[128:131]\n\t"
"ds_read_b128 v[80:83], v1 offset:0\n\t"
"v_accvgpr_read_b32 v164, a80\n\t"
"v_mfma_f32_16x16x32_bf16 a[132:135], v[32:35], v[68:71], a[132:135]\n\t"
"buffer_load_dwordx4 v8, s[48:51], s81 offen offset:2048 lds\n\t"
"v_accvgpr_read_b32 v165, a81\n\t"
"v_mfma_f32_16x16x32_bf16 a[136:139], v[32:35], v[72:75], a[136:139]\n\t"
"ds_read_b64_tr_b16 v[112:113], v2 offset:8192\n\t"
"v_accvgpr_read_b32 v166, a82\n\t"
"v_mfma_f32_16x16x32_bf16 a[140:143], v[32:35], v[76:79], a[140:143]\n\t"
"ds_read_b64_tr_b16 v[114:115], v2 offset:9216\n\t"
"v_mfma_f32_16x16x32_bf16 a[144:147], v[36:39], v[64:67], a[144:147]\n\t"
"ds_read_b64_tr_b16 v[116:117], v3 offset:8192\n\t"
"v_accvgpr_read_b32 v167, a83\n\t"
"v_mfma_f32_16x16x32_bf16 a[148:151], v[36:39], v[68:71], a[148:151]\n\t"
"buffer_load_dwordx4 v9, s[48:51], s81 offen offset:3072 lds\n\t"
"v_accvgpr_read_b32 v168, a84\n\t"
"v_mfma_f32_16x16x32_bf16 a[152:155], v[36:39], v[72:75], a[152:155]\n\t"
"ds_read_b64_tr_b16 v[118:119], v3 offset:9216\n\t"
"v_mfma_f32_16x16x32_bf16 a[156:159], v[36:39], v[76:79], a[156:159]\n\t"
"ds_read_b64_tr_b16 v[120:121], v4 offset:8192\n\t"
"v_accvgpr_read_b32 v169, a85\n\t"
"v_mfma_f32_16x16x32_bf16 a[160:163], v[40:43], v[64:67], a[160:163]\n\t"
"ds_read_b64_tr_b16 v[122:123], v4 offset:9216\n\t"
"v_accvgpr_read_b32 v170, a86\n\t"
"v_mfma_f32_16x16x32_bf16 a[164:167], v[40:43], v[68:71], a[164:167]\n\t"
"ds_read_b64_tr_b16 v[124:125], v5 offset:8192\n\t"
"v_mfma_f32_16x16x32_bf16 a[168:171], v[40:43], v[72:75], a[168:171]\n\t"
"s_add_u32 m0, s80, 0\n\t"
"s_add_u32 s81, s78, 3145728\n\t"
"buffer_load_dwordx4 v10, s[60:63], s81 offen offset:0 lds\n\t"
"v_accvgpr_read_b32 v171, a87\n\t"
"v_mfma_f32_16x16x32_bf16 a[172:175], v[40:43], v[76:79], a[172:175]\n\t"
"ds_read_b64_tr_b16 v[126:127], v5 offset:9216\n\t"
"v_cvt_pk_bf16_f32 v172, v164, v165\n\t"
"s_waitcnt lgkmcnt(5)\n\t"
"v_mfma_f32_16x16x32_bf16 a[176:179], v[44:47], v[64:67], a[176:179]\n\t"
"ds_read_b128 v[84:87], v1 offset:2048\n\t"
"v_mfma_f32_16x16x32_bf16 a[180:183], v[44:47], v[68:71], a[180:183]\n\t"
"ds_read_b128 v[88:91], v1 offset:4096\n\t"
"v_cvt_pk_bf16_f32 v173, v166, v167\n\t"
"v_mfma_f32_16x16x32_bf16 a[184:187], v[44:47], v[72:75], a[184:187]\n\t"
"ds_read_b128 v[92:95], v1 offset:6144\n\t"
"v_cvt_pk_bf16_f32 v174, v168, v169\n\t"
"v_mfma_f32_16x16x32_bf16 a[188:191], v[44:47], v[76:79], a[188:191]\n\t"
"buffer_load_dwordx4 v11, s[60:63], s81 offen offset:1024 lds\n\t"
"v_mfma_f32_16x16x32_bf16 a[192:195], v[48:51], v[64:67], a[192:195]\n\t"
"ds_read_b128 v[96:99], v1 offset:8192\n\t"
"v_cvt_pk_bf16_f32 v175, v170, v171\n\t"
"v_mfma_f32_16x16x32_bf16 a[196:199], v[48:51], v[68:71], a[196:199]\n\t"
"ds_read_b128 v[100:103], v1 offset:10240\n\t"
"s_add_u32 s82, s84, 20480\n\t"
"buffer_store_dwordx4 v[172:175], v14, s[68:71], s82 offen offset:0 sc1\n\t"
"v_mfma_f32_16x16x32_bf16 a[200:203], v[48:51], v[72:75], a[200:203]\n\t"
"ds_read_b128 v[104:107], v1 offset:12288\n\t"
"v_mfma_f32_16x16x32_bf16 a[204:207], v[48:51], v[76:79], a[204:207]\n\t"
"buffer_load_dwordx4 v12, s[60:63], s81 offen offset:2048 lds\n\t"
"v_accvgpr_read_b32 v176, a88\n\t"
"v_mfma_f32_16x16x32_bf16 a[208:211], v[52:55], v[64:67], a[208:211]\n\t"
"ds_read_b128 v[108:111], v1 offset:14336\n\t"
"v_accvgpr_read_b32 v177, a89\n\t"
"v_mfma_f32_16x16x32_bf16 a[212:215], v[52:55], v[68:71], a[212:215]\n\t"
"v_add_u32 v0, 49152, v0\n\t"
"v_mfma_f32_16x16x32_bf16 a[216:219], v[52:55], v[72:75], a[216:219]\n\t"
"v_add_u32 v1, 49152, v1\n\t"
"v_accvgpr_read_b32 v178, a90\n\t"
"v_mfma_f32_16x16x32_bf16 a[220:223], v[52:55], v[76:79], a[220:223]\n\t"
"v_add_u32 v2, 49152, v2\n\t"
"v_accvgpr_read_b32 v179, a91\n\t"
"v_mfma_f32_16x16x32_bf16 a[224:227], v[56:59], v[64:67], a[224:227]\n\t"
"buffer_load_dwordx4 v13, s[60:63], s81 offen offset:3072 lds\n\t"
"v_mfma_f32_16x16x32_bf16 a[228:231], v[56:59], v[68:71], a[228:231]\n\t"
"v_add_u32 v3, 49152, v3\n\t"
"v_accvgpr_read_b32 v180, a92\n\t"
"v_mfma_f32_16x16x32_bf16 a[232:235], v[56:59], v[72:75], a[232:235]\n\t"
"v_add_u32 v4, 49152, v4\n\t"
"v_accvgpr_read_b32 v181, a93\n\t"
"v_mfma_f32_16x16x32_bf16 a[236:239], v[56:59], v[76:79], a[236:239]\n\t"
"v_add_u32 v5, 49152, v5\n\t"
"v_mfma_f32_16x16x32_bf16 a[240:243], v[60:63], v[64:67], a[240:243]\n\t"
"v_mfma_f32_16x16x32_bf16 a[244:247], v[60:63], v[68:71], a[244:247]\n\t"
"v_mfma_f32_16x16x32_bf16 a[248:251], v[60:63], v[72:75], a[248:251]\n\t"
"v_mfma_f32_16x16x32_bf16 a[252:255], v[60:63], v[76:79], a[252:255]\n\t"
"s_waitcnt vmcnt(16) lgkmcnt(0)\n\t"
"s_barrier\n\t"
"s_load_dwordx2 s[28:29], s[36:37], 96\n\t"
"s_waitcnt lgkmcnt(0)\n\t"
"s_and_b32 s29, s29, 0xffff\n\t"
"s_mov_b32 s30, 524288\n\t"
"s_mov_b32 s31, 0x00020000\n\t"
"s_lshl_b32 s98, s38, 2\n\t"
"s_add_u32 s98, s98, s39\n\t"
"s_mul_i32 s97, s98, 1024\n\t"
"s_mov_b32 m0, 147456\n\t"
"s_nop 0\n\t"
"buffer_load_dwordx4 v14, s[28:31], s97 offen offset:0 lds\n\t"
"v_mfma_f32_16x16x32_bf16 a[128:131], v[80:83], v[112:115], a[128:131]\n\t"
"ds_read_b128 v[32:35], v0 offset:0\n\t"
"v_accvgpr_read_b32 v182, a94\n\t"
"v_mfma_f32_16x16x32_bf16 a[132:135], v[80:83], v[116:119], a[132:135]\n\t"
"s_add_u32 m0, s79, 49152\n\t"
"s_add_u32 s81, s76, 896\n\t"
"buffer_load_dwordx4 v6, s[48:51], s81 offen offset:0 lds\n\t"
"v_mfma_f32_16x16x32_bf16 a[136:139], v[80:83], v[120:123], a[136:139]\n\t"
"v_accvgpr_read_b32 v183, a95\n\t"
"ds_read_b64_tr_b16 v[64:65], v2 offset:0\n\t"
"v_mfma_f32_16x16x32_bf16 a[140:143], v[80:83], v[124:127], a[140:143]\n\t"
"v_cvt_pk_bf16_f32 v184, v176, v177\n\t"
"v_mfma_f32_16x16x32_bf16 a[144:147], v[84:87], v[112:115], a[144:147]\n\t"
"ds_read_b64_tr_b16 v[66:67], v2 offset:1024\n\t"
"v_cvt_pk_bf16_f32 v185, v178, v179\n\t"
"v_mfma_f32_16x16x32_bf16 a[148:151], v[84:87], v[116:119], a[148:151]\n\t"
"buffer_load_dwordx4 v7, s[48:51], s81 offen offset:1024 lds\n\t"
"v_mfma_f32_16x16x32_bf16 a[152:155], v[84:87], v[120:123], a[152:155]\n\t"
"v_cvt_pk_bf16_f32 v186, v180, v181\n\t"
"ds_read_b64_tr_b16 v[68:69], v3 offset:0\n\t"
"v_mfma_f32_16x16x32_bf16 a[156:159], v[84:87], v[124:127], a[156:159]\n\t"
"v_cvt_pk_bf16_f32 v187, v182, v183\n\t"
"v_mfma_f32_16x16x32_bf16 a[160:163], v[88:91], v[112:115], a[160:163]\n\t"
"ds_read_b64_tr_b16 v[70:71], v3 offset:1024\n\t"
"s_add_u32 s82, s84, 20480\n\t"
"buffer_store_dwordx4 v[184:187], v14, s[68:71], s82 offen offset:1024 sc1\n\t"
"v_mfma_f32_16x16x32_bf16 a[164:167], v[88:91], v[116:119], a[164:167]\n\t"
"ds_read_b64_tr_b16 v[72:73], v4 offset:0\n\t"
"v_mfma_f32_16x16x32_bf16 a[168:171], v[88:91], v[120:123], a[168:171]\n\t"
"v_accvgpr_read_b32 v188, a96\n\t"
"buffer_load_dwordx4 v8, s[48:51], s81 offen offset:2048 lds\n\t"
"v_mfma_f32_16x16x32_bf16 a[172:175], v[88:91], v[124:127], a[172:175]\n\t"
"v_accvgpr_read_b32 v189, a97\n\t"
"v_mfma_f32_16x16x32_bf16 a[176:179], v[92:95], v[112:115], a[176:179]\n\t"
"ds_read_b64_tr_b16 v[74:75], v4 offset:1024\n\t"
"v_accvgpr_read_b32 v190, a98\n\t"
"v_mfma_f32_16x16x32_bf16 a[180:183], v[92:95], v[116:119], a[180:183]\n\t"
"ds_read_b64_tr_b16 v[76:77], v5 offset:0\n\t"
"v_mfma_f32_16x16x32_bf16 a[184:187], v[92:95], v[120:123], a[184:187]\n\t"
"ds_read_b64_tr_b16 v[78:79], v5 offset:1024\n\t"
"v_accvgpr_read_b32 v191, a99\n\t"
"v_mfma_f32_16x16x32_bf16 a[188:191], v[92:95], v[124:127], a[188:191]\n\t"
"buffer_load_dwordx4 v9, s[48:51], s81 offen offset:3072 lds\n\t"
"v_mfma_f32_16x16x32_bf16 a[192:195], v[96:99], v[112:115], a[192:195]\n\t"
"v_accvgpr_read_b32 v192, a100\n\t"
"ds_read_b128 v[36:39], v0 offset:2048\n\t"
"v_mfma_f32_16x16x32_bf16 a[196:199], v[96:99], v[116:119], a[196:199]\n\t"
"v_accvgpr_read_b32 v193, a101\n\t"
"v_mfma_f32_16x16x32_bf16 a[200:203], v[96:99], v[120:123], a[200:203]\n\t"
"ds_read_b128 v[40:43], v0 offset:4096\n\t"
"v_accvgpr_read_b32 v194, a102\n\t"
"v_mfma_f32_16x16x32_bf16 a[204:207], v[96:99], v[124:127], a[204:207]\n\t"
"s_add_u32 m0, s79, 53248\n\t"
"s_add_u32 s81, s76, 33664\n\t"
"buffer_load_dwordx4 v6, s[48:51], s81 offen offset:0 lds\n\t"
"v_mfma_f32_16x16x32_bf16 a[208:211], v[100:103], v[112:115], a[208:211]\n\t"
"v_accvgpr_read_b32 v195, a103\n\t"
"ds_read_b128 v[44:47], v0 offset:6144\n\t"
"v_mfma_f32_16x16x32_bf16 a[212:215], v[100:103], v[116:119], a[212:215]\n\t"
"v_cvt_pk_bf16_f32 v196, v188, v189\n\t"
"v_mfma_f32_16x16x32_bf16 a[216:219], v[100:103], v[120:123], a[216:219]\n\t"
"ds_read_b128 v[48:51], v0 offset:8192\n\t"
"v_cvt_pk_bf16_f32 v197, v190, v191\n\t"
"v_mfma_f32_16x16x32_bf16 a[220:223], v[100:103], v[124:127], a[220:223]\n\t"
"ds_read_b128 v[52:55], v0 offset:10240\n\t"
"v_mfma_f32_16x16x32_bf16 a[224:227], v[104:107], v[112:115], a[224:227]\n\t"
"v_cvt_pk_bf16_f32 v198, v192, v193\n\t"
"buffer_load_dwordx4 v7, s[48:51], s81 offen offset:1024 lds\n\t"
"v_mfma_f32_16x16x32_bf16 a[228:231], v[104:107], v[116:119], a[228:231]\n\t"
"v_cvt_pk_bf16_f32 v199, v194, v195\n\t"
"v_mfma_f32_16x16x32_bf16 a[232:235], v[104:107], v[120:123], a[232:235]\n\t"
"ds_read_b128 v[56:59], v0 offset:12288\n\t"
"s_add_u32 s82, s84, 24576\n\t"
"buffer_store_dwordx4 v[196:199], v14, s[68:71], s82 offen offset:0 sc1\n\t"
"v_mfma_f32_16x16x32_bf16 a[236:239], v[104:107], v[124:127], a[236:239]\n\t"
"ds_read_b128 v[60:63], v0 offset:14336\n\t"
"s_waitcnt lgkmcnt(5)\n\t"
"v_mfma_f32_16x16x32_bf16 a[240:243], v[108:111], v[112:115], a[240:243]\n\t"
"v_mfma_f32_16x16x32_bf16 a[244:247], v[108:111], v[116:119], a[244:247]\n\t"
"v_mfma_f32_16x16x32_bf16 a[248:251], v[108:111], v[120:123], a[248:251]\n\t"
"v_mfma_f32_16x16x32_bf16 a[252:255], v[108:111], v[124:127], a[252:255]\n\t"
"v_mfma_f32_16x16x32_bf16 a[128:131], v[32:35], v[64:67], a[128:131]\n\t"
"ds_read_b128 v[80:83], v1 offset:0\n\t"
"v_accvgpr_read_b32 v200, a104\n\t"
"v_mfma_f32_16x16x32_bf16 a[132:135], v[32:35], v[68:71], a[132:135]\n\t"
"buffer_load_dwordx4 v8, s[48:51], s81 offen offset:2048 lds\n\t"
"v_accvgpr_read_b32 v201, a105\n\t"
"v_mfma_f32_16x16x32_bf16 a[136:139], v[32:35], v[72:75], a[136:139]\n\t"
"ds_read_b64_tr_b16 v[112:113], v2 offset:8192\n\t"
"v_accvgpr_read_b32 v202, a106\n\t"
"v_mfma_f32_16x16x32_bf16 a[140:143], v[32:35], v[76:79], a[140:143]\n\t"
"ds_read_b64_tr_b16 v[114:115], v2 offset:9216\n\t"
"v_mfma_f32_16x16x32_bf16 a[144:147], v[36:39], v[64:67], a[144:147]\n\t"
"ds_read_b64_tr_b16 v[116:117], v3 offset:8192\n\t"
"v_accvgpr_read_b32 v203, a107\n\t"
"v_mfma_f32_16x16x32_bf16 a[148:151], v[36:39], v[68:71], a[148:151]\n\t"
"buffer_load_dwordx4 v9, s[48:51], s81 offen offset:3072 lds\n\t"
"v_accvgpr_read_b32 v204, a108\n\t"
"v_mfma_f32_16x16x32_bf16 a[152:155], v[36:39], v[72:75], a[152:155]\n\t"
"ds_read_b64_tr_b16 v[118:119], v3 offset:9216\n\t"
"v_mfma_f32_16x16x32_bf16 a[156:159], v[36:39], v[76:79], a[156:159]\n\t"
"ds_read_b64_tr_b16 v[120:121], v4 offset:8192\n\t"
"v_accvgpr_read_b32 v205, a109\n\t"
"v_mfma_f32_16x16x32_bf16 a[160:163], v[40:43], v[64:67], a[160:163]\n\t"
"ds_read_b64_tr_b16 v[122:123], v4 offset:9216\n\t"
"v_accvgpr_read_b32 v206, a110\n\t"
"v_mfma_f32_16x16x32_bf16 a[164:167], v[40:43], v[68:71], a[164:167]\n\t"
"ds_read_b64_tr_b16 v[124:125], v5 offset:8192\n\t"
"v_mfma_f32_16x16x32_bf16 a[168:171], v[40:43], v[72:75], a[168:171]\n\t"
"s_add_u32 m0, s80, 49152\n\t"
"s_add_u32 s81, s78, 3670016\n\t"
"buffer_load_dwordx4 v10, s[60:63], s81 offen offset:0 lds\n\t"
"v_accvgpr_read_b32 v207, a111\n\t"
"v_mfma_f32_16x16x32_bf16 a[172:175], v[40:43], v[76:79], a[172:175]\n\t"
"ds_read_b64_tr_b16 v[126:127], v5 offset:9216\n\t"
"v_cvt_pk_bf16_f32 v208, v200, v201\n\t"
"s_waitcnt lgkmcnt(5)\n\t"
"v_mfma_f32_16x16x32_bf16 a[176:179], v[44:47], v[64:67], a[176:179]\n\t"
"ds_read_b128 v[84:87], v1 offset:2048\n\t"
"v_mfma_f32_16x16x32_bf16 a[180:183], v[44:47], v[68:71], a[180:183]\n\t"
"ds_read_b128 v[88:91], v1 offset:4096\n\t"
"v_cvt_pk_bf16_f32 v209, v202, v203\n\t"
"v_mfma_f32_16x16x32_bf16 a[184:187], v[44:47], v[72:75], a[184:187]\n\t"
"ds_read_b128 v[92:95], v1 offset:6144\n\t"
"v_cvt_pk_bf16_f32 v210, v204, v205\n\t"
"v_mfma_f32_16x16x32_bf16 a[188:191], v[44:47], v[76:79], a[188:191]\n\t"
"buffer_load_dwordx4 v11, s[60:63], s81 offen offset:1024 lds\n\t"
"v_mfma_f32_16x16x32_bf16 a[192:195], v[48:51], v[64:67], a[192:195]\n\t"
"ds_read_b128 v[96:99], v1 offset:8192\n\t"
"v_cvt_pk_bf16_f32 v211, v206, v207\n\t"
"v_mfma_f32_16x16x32_bf16 a[196:199], v[48:51], v[68:71], a[196:199]\n\t"
"ds_read_b128 v[100:103], v1 offset:10240\n\t"
"s_add_u32 s82, s84, 24576\n\t"
"buffer_store_dwordx4 v[208:211], v14, s[68:71], s82 offen offset:1024 sc1\n\t"
"v_mfma_f32_16x16x32_bf16 a[200:203], v[48:51], v[72:75], a[200:203]\n\t"
"ds_read_b128 v[104:107], v1 offset:12288\n\t"
"v_mfma_f32_16x16x32_bf16 a[204:207], v[48:51], v[76:79], a[204:207]\n\t"
"buffer_load_dwordx4 v12, s[60:63], s81 offen offset:2048 lds\n\t"
"v_accvgpr_read_b32 v128, a112\n\t"
"v_mfma_f32_16x16x32_bf16 a[208:211], v[52:55], v[64:67], a[208:211]\n\t"
"ds_read_b128 v[108:111], v1 offset:14336\n\t"
"v_accvgpr_read_b32 v129, a113\n\t"
"v_mfma_f32_16x16x32_bf16 a[212:215], v[52:55], v[68:71], a[212:215]\n\t"
"v_add_u32 v0, -98304, v0\n\t"
"v_mfma_f32_16x16x32_bf16 a[216:219], v[52:55], v[72:75], a[216:219]\n\t"
"v_add_u32 v1, -98304, v1\n\t"
"v_accvgpr_read_b32 v130, a114\n\t"
"v_mfma_f32_16x16x32_bf16 a[220:223], v[52:55], v[76:79], a[220:223]\n\t"
"v_add_u32 v2, -98304, v2\n\t"
"v_accvgpr_read_b32 v131, a115\n\t"
"v_mfma_f32_16x16x32_bf16 a[224:227], v[56:59], v[64:67], a[224:227]\n\t"
"buffer_load_dwordx4 v13, s[60:63], s81 offen offset:3072 lds\n\t"
"v_mfma_f32_16x16x32_bf16 a[228:231], v[56:59], v[68:71], a[228:231]\n\t"
"v_add_u32 v3, -98304, v3\n\t"
"v_accvgpr_read_b32 v132, a116\n\t"
"v_mfma_f32_16x16x32_bf16 a[232:235], v[56:59], v[72:75], a[232:235]\n\t"
"v_add_u32 v4, -98304, v4\n\t"
"v_accvgpr_read_b32 v133, a117\n\t"
"v_mfma_f32_16x16x32_bf16 a[236:239], v[56:59], v[76:79], a[236:239]\n\t"
"v_add_u32 v5, -98304, v5\n\t"
"v_mfma_f32_16x16x32_bf16 a[240:243], v[60:63], v[64:67], a[240:243]\n\t"
"v_mfma_f32_16x16x32_bf16 a[244:247], v[60:63], v[68:71], a[244:247]\n\t"
"v_mfma_f32_16x16x32_bf16 a[248:251], v[60:63], v[72:75], a[248:251]\n\t"
"v_mfma_f32_16x16x32_bf16 a[252:255], v[60:63], v[76:79], a[252:255]\n\t"
"s_waitcnt vmcnt(16) lgkmcnt(0)\n\t"
"s_barrier\n\t"
"s_load_dwordx2 s[28:29], s[36:37], 40\n\t"
"s_waitcnt lgkmcnt(0)\n\t"
"s_and_b32 s29, s29, 0xffff\n\t"
"s_mov_b32 s30, 4194304\n\t"
"s_mov_b32 s31, 0x00020000\n\t"
"s_lshl_b32 s98, s38, 2\n\t"
"s_add_u32 s98, s98, s39\n\t"
"s_mul_i32 s97, s98, 4096\n\t"
"s_mov_b32 m0, 147456\n\t"
"s_nop 0\n\t"
"buffer_load_dwordx4 v14, s[28:31], s97 offen offset:0 lds\n\t"
"v_mfma_f32_16x16x32_bf16 a[128:131], v[80:83], v[112:115], a[128:131]\n\t"
"ds_read_b128 v[32:35], v0 offset:0\n\t"
"v_accvgpr_read_b32 v134, a118\n\t"
"v_mfma_f32_16x16x32_bf16 a[132:135], v[80:83], v[116:119], a[132:135]\n\t"
"s_add_u32 m0, s79, 98304\n\t"
"s_add_u32 s81, s77, 0\n\t"
"buffer_load_dwordx4 v6, s[52:55], s81 offen offset:0 lds\n\t"
"v_mfma_f32_16x16x32_bf16 a[136:139], v[80:83], v[120:123], a[136:139]\n\t"
"v_accvgpr_read_b32 v135, a119\n\t"
"ds_read_b64_tr_b16 v[64:65], v2 offset:0\n\t"
"v_mfma_f32_16x16x32_bf16 a[140:143], v[80:83], v[124:127], a[140:143]\n\t"
"v_cvt_pk_bf16_f32 v136, v128, v129\n\t"
"v_mfma_f32_16x16x32_bf16 a[144:147], v[84:87], v[112:115], a[144:147]\n\t"
"ds_read_b64_tr_b16 v[66:67], v2 offset:1024\n\t"
"v_cvt_pk_bf16_f32 v137, v130, v131\n\t"
"v_mfma_f32_16x16x32_bf16 a[148:151], v[84:87], v[116:119], a[148:151]\n\t"
"buffer_load_dwordx4 v7, s[52:55], s81 offen offset:1024 lds\n\t"
"v_mfma_f32_16x16x32_bf16 a[152:155], v[84:87], v[120:123], a[152:155]\n\t"
"v_cvt_pk_bf16_f32 v138, v132, v133\n\t"
"ds_read_b64_tr_b16 v[68:69], v3 offset:0\n\t"
"v_mfma_f32_16x16x32_bf16 a[156:159], v[84:87], v[124:127], a[156:159]\n\t"
"v_cvt_pk_bf16_f32 v139, v134, v135\n\t"
"v_mfma_f32_16x16x32_bf16 a[160:163], v[88:91], v[112:115], a[160:163]\n\t"
"ds_read_b64_tr_b16 v[70:71], v3 offset:1024\n\t"
"s_add_u32 s82, s84, 28672\n\t"
"buffer_store_dwordx4 v[136:139], v14, s[68:71], s82 offen offset:0 sc1\n\t"
"v_mfma_f32_16x16x32_bf16 a[164:167], v[88:91], v[116:119], a[164:167]\n\t"
"ds_read_b64_tr_b16 v[72:73], v4 offset:0\n\t"
"v_mfma_f32_16x16x32_bf16 a[168:171], v[88:91], v[120:123], a[168:171]\n\t"
"v_accvgpr_read_b32 v140, a120\n\t"
"buffer_load_dwordx4 v8, s[52:55], s81 offen offset:2048 lds\n\t"
"v_mfma_f32_16x16x32_bf16 a[172:175], v[88:91], v[124:127], a[172:175]\n\t"
"v_accvgpr_read_b32 v141, a121\n\t"
"v_mfma_f32_16x16x32_bf16 a[176:179], v[92:95], v[112:115], a[176:179]\n\t"
"ds_read_b64_tr_b16 v[74:75], v4 offset:1024\n\t"
"v_accvgpr_read_b32 v142, a122\n\t"
"v_mfma_f32_16x16x32_bf16 a[180:183], v[92:95], v[116:119], a[180:183]\n\t"
"ds_read_b64_tr_b16 v[76:77], v5 offset:0\n\t"
"v_mfma_f32_16x16x32_bf16 a[184:187], v[92:95], v[120:123], a[184:187]\n\t"
"ds_read_b64_tr_b16 v[78:79], v5 offset:1024\n\t"
"v_accvgpr_read_b32 v143, a123\n\t"
"v_mfma_f32_16x16x32_bf16 a[188:191], v[92:95], v[124:127], a[188:191]\n\t"
"buffer_load_dwordx4 v9, s[52:55], s81 offen offset:3072 lds\n\t"
"v_mfma_f32_16x16x32_bf16 a[192:195], v[96:99], v[112:115], a[192:195]\n\t"
"v_accvgpr_read_b32 v144, a124\n\t"
"ds_read_b128 v[36:39], v0 offset:2048\n\t"
"v_mfma_f32_16x16x32_bf16 a[196:199], v[96:99], v[116:119], a[196:199]\n\t"
"v_accvgpr_read_b32 v145, a125\n\t"
"v_mfma_f32_16x16x32_bf16 a[200:203], v[96:99], v[120:123], a[200:203]\n\t"
"ds_read_b128 v[40:43], v0 offset:4096\n\t"
"v_accvgpr_read_b32 v146, a126\n\t"
"v_mfma_f32_16x16x32_bf16 a[204:207], v[96:99], v[124:127], a[204:207]\n\t"
"s_add_u32 m0, s79, 102400\n\t"
"s_add_u32 s81, s77, 32768\n\t"
"buffer_load_dwordx4 v6, s[52:55], s81 offen offset:0 lds\n\t"
"v_mfma_f32_16x16x32_bf16 a[208:211], v[100:103], v[112:115], a[208:211]\n\t"
"v_accvgpr_read_b32 v147, a127\n\t"
"ds_read_b128 v[44:47], v0 offset:6144\n\t"
"v_mfma_f32_16x16x32_bf16 a[212:215], v[100:103], v[116:119], a[212:215]\n\t"
"v_cvt_pk_bf16_f32 v148, v140, v141\n\t"
"v_mfma_f32_16x16x32_bf16 a[216:219], v[100:103], v[120:123], a[216:219]\n\t"
"ds_read_b128 v[48:51], v0 offset:8192\n\t"
"v_cvt_pk_bf16_f32 v149, v142, v143\n\t"
"v_mfma_f32_16x16x32_bf16 a[220:223], v[100:103], v[124:127], a[220:223]\n\t"
"ds_read_b128 v[52:55], v0 offset:10240\n\t"
"v_mfma_f32_16x16x32_bf16 a[224:227], v[104:107], v[112:115], a[224:227]\n\t"
"v_cvt_pk_bf16_f32 v150, v144, v145\n\t"
"buffer_load_dwordx4 v7, s[52:55], s81 offen offset:1024 lds\n\t"
"v_mfma_f32_16x16x32_bf16 a[228:231], v[104:107], v[116:119], a[228:231]\n\t"
"v_cvt_pk_bf16_f32 v151, v146, v147\n\t"
"v_mfma_f32_16x16x32_bf16 a[232:235], v[104:107], v[120:123], a[232:235]\n\t"
"ds_read_b128 v[56:59], v0 offset:12288\n\t"
"s_add_u32 s82, s84, 28672\n\t"
"buffer_store_dwordx4 v[148:151], v14, s[68:71], s82 offen offset:1024 sc1\n\t"
"v_mfma_f32_16x16x32_bf16 a[236:239], v[104:107], v[124:127], a[236:239]\n\t"
"ds_read_b128 v[60:63], v0 offset:14336\n\t"
"s_waitcnt lgkmcnt(5)\n\t"
"v_mfma_f32_16x16x32_bf16 a[240:243], v[108:111], v[112:115], a[240:243]\n\t"
"v_mfma_f32_16x16x32_bf16 a[244:247], v[108:111], v[116:119], a[244:247]\n\t"
"v_mfma_f32_16x16x32_bf16 a[248:251], v[108:111], v[120:123], a[248:251]\n\t"
"v_mfma_f32_16x16x32_bf16 a[252:255], v[108:111], v[124:127], a[252:255]\n\t"
"v_mfma_f32_16x16x32_bf16 a[128:131], v[32:35], v[64:67], a[128:131]\n\t"
"ds_read_b128 v[80:83], v1 offset:0\n\t"
"v_mfma_f32_16x16x32_bf16 a[132:135], v[32:35], v[68:71], a[132:135]\n\t"
"buffer_load_dwordx4 v8, s[52:55], s81 offen offset:2048 lds\n\t"
"v_mfma_f32_16x16x32_bf16 a[136:139], v[32:35], v[72:75], a[136:139]\n\t"
"ds_read_b64_tr_b16 v[112:113], v2 offset:8192\n\t"
"v_mfma_f32_16x16x32_bf16 a[140:143], v[32:35], v[76:79], a[140:143]\n\t"
"ds_read_b64_tr_b16 v[114:115], v2 offset:9216\n\t"
"v_mfma_f32_16x16x32_bf16 a[144:147], v[36:39], v[64:67], a[144:147]\n\t"
"ds_read_b64_tr_b16 v[116:117], v3 offset:8192\n\t"
"v_mfma_f32_16x16x32_bf16 a[148:151], v[36:39], v[68:71], a[148:151]\n\t"
"buffer_load_dwordx4 v9, s[52:55], s81 offen offset:3072 lds\n\t"
"v_mfma_f32_16x16x32_bf16 a[152:155], v[36:39], v[72:75], a[152:155]\n\t"
"ds_read_b64_tr_b16 v[118:119], v3 offset:9216\n\t"
"v_mfma_f32_16x16x32_bf16 a[156:159], v[36:39], v[76:79], a[156:159]\n\t"
"ds_read_b64_tr_b16 v[120:121], v4 offset:8192\n\t"
"v_mfma_f32_16x16x32_bf16 a[160:163], v[40:43], v[64:67], a[160:163]\n\t"
"ds_read_b64_tr_b16 v[122:123], v4 offset:9216\n\t"
"v_mfma_f32_16x16x32_bf16 a[164:167], v[40:43], v[68:71], a[164:167]\n\t"
"ds_read_b64_tr_b16 v[124:125], v5 offset:8192\n\t"
"v_mfma_f32_16x16x32_bf16 a[168:171], v[40:43], v[72:75], a[168:171]\n\t"
"s_add_u32 m0, s80, 98304\n\t"
"s_add_u32 s81, s78, 0\n\t"
"buffer_load_dwordx4 v10, s[64:67], s81 offen offset:0 lds\n\t"
"v_mfma_f32_16x16x32_bf16 a[172:175], v[40:43], v[76:79], a[172:175]\n\t"
"ds_read_b64_tr_b16 v[126:127], v5 offset:9216\n\t"
"s_waitcnt lgkmcnt(5)\n\t"
"v_mfma_f32_16x16x32_bf16 a[176:179], v[44:47], v[64:67], a[176:179]\n\t"
"ds_read_b128 v[84:87], v1 offset:2048\n\t"
"v_mfma_f32_16x16x32_bf16 a[180:183], v[44:47], v[68:71], a[180:183]\n\t"
"ds_read_b128 v[88:91], v1 offset:4096\n\t"
"v_mfma_f32_16x16x32_bf16 a[184:187], v[44:47], v[72:75], a[184:187]\n\t"
"ds_read_b128 v[92:95], v1 offset:6144\n\t"
"v_mfma_f32_16x16x32_bf16 a[188:191], v[44:47], v[76:79], a[188:191]\n\t"
"buffer_load_dwordx4 v11, s[64:67], s81 offen offset:1024 lds\n\t"
"v_mfma_f32_16x16x32_bf16 a[192:195], v[48:51], v[64:67], a[192:195]\n\t"
"ds_read_b128 v[96:99], v1 offset:8192\n\t"
"v_mfma_f32_16x16x32_bf16 a[196:199], v[48:51], v[68:71], a[196:199]\n\t"
"ds_read_b128 v[100:103], v1 offset:10240\n\t"
"v_mfma_f32_16x16x32_bf16 a[200:203], v[48:51], v[72:75], a[200:203]\n\t"
"ds_read_b128 v[104:107], v1 offset:12288\n\t"
"v_mfma_f32_16x16x32_bf16 a[204:207], v[48:51], v[76:79], a[204:207]\n\t"
"buffer_load_dwordx4 v12, s[64:67], s81 offen offset:2048 lds\n\t"
"v_mfma_f32_16x16x32_bf16 a[208:211], v[52:55], v[64:67], a[208:211]\n\t"
"ds_read_b128 v[108:111], v1 offset:14336\n\t"
"v_mfma_f32_16x16x32_bf16 a[212:215], v[52:55], v[68:71], a[212:215]\n\t"
"v_add_u32 v0, 49152, v0\n\t"
"v_mfma_f32_16x16x32_bf16 a[216:219], v[52:55], v[72:75], a[216:219]\n\t"
"v_add_u32 v1, 49152, v1\n\t"
"v_mfma_f32_16x16x32_bf16 a[220:223], v[52:55], v[76:79], a[220:223]\n\t"
"v_add_u32 v2, 49152, v2\n\t"
"v_mfma_f32_16x16x32_bf16 a[224:227], v[56:59], v[64:67], a[224:227]\n\t"
"buffer_load_dwordx4 v13, s[64:67], s81 offen offset:3072 lds\n\t"
"v_mfma_f32_16x16x32_bf16 a[228:231], v[56:59], v[68:71], a[228:231]\n\t"
"v_add_u32 v3, 49152, v3\n\t"
"v_mfma_f32_16x16x32_bf16 a[232:235], v[56:59], v[72:75], a[232:235]\n\t"
"v_add_u32 v4, 49152, v4\n\t"
"v_mfma_f32_16x16x32_bf16 a[236:239], v[56:59], v[76:79], a[236:239]\n\t"
"v_add_u32 v5, 49152, v5\n\t"
"v_mfma_f32_16x16x32_bf16 a[240:243], v[60:63], v[64:67], a[240:243]\n\t"
"v_mfma_f32_16x16x32_bf16 a[244:247], v[60:63], v[68:71], a[244:247]\n\t"
"v_mfma_f32_16x16x32_bf16 a[248:251], v[60:63], v[72:75], a[248:251]\n\t"
"v_mfma_f32_16x16x32_bf16 a[252:255], v[60:63], v[76:79], a[252:255]\n\t"
"s_waitcnt vmcnt(15) lgkmcnt(0)\n\t"
"s_barrier\n\t"
"s_mov_b32 m0, 147456\n\t"
"s_nop 0\n\t"
"buffer_load_dwordx4 v14, s[28:31], s97 offen offset:1024 lds\n\t"
"v_mfma_f32_16x16x32_bf16 a[128:131], v[80:83], v[112:115], a[128:131]\n\t"
"ds_read_b128 v[32:35], v0 offset:0\n\t"
"v_mfma_f32_16x16x32_bf16 a[132:135], v[80:83], v[116:119], a[132:135]\n\t"
"s_add_u32 m0, s79, 0\n\t"
"s_add_u32 s81, s77, 128\n\t"
"buffer_load_dwordx4 v6, s[52:55], s81 offen offset:0 lds\n\t"
"v_mfma_f32_16x16x32_bf16 a[136:139], v[80:83], v[120:123], a[136:139]\n\t"
"ds_read_b64_tr_b16 v[64:65], v2 offset:0\n\t"
"v_mfma_f32_16x16x32_bf16 a[140:143], v[80:83], v[124:127], a[140:143]\n\t"
"ds_read_b64_tr_b16 v[66:67], v2 offset:1024\n\t"
"v_mfma_f32_16x16x32_bf16 a[144:147], v[84:87], v[112:115], a[144:147]\n\t"
"v_mfma_f32_16x16x32_bf16 a[148:151], v[84:87], v[116:119], a[148:151]\n\t"
"buffer_load_dwordx4 v7, s[52:55], s81 offen offset:1024 lds\n\t"
"v_mfma_f32_16x16x32_bf16 a[152:155], v[84:87], v[120:123], a[152:155]\n\t"
"ds_read_b64_tr_b16 v[68:69], v3 offset:0\n\t"
"v_mfma_f32_16x16x32_bf16 a[156:159], v[84:87], v[124:127], a[156:159]\n\t"
"ds_read_b64_tr_b16 v[70:71], v3 offset:1024\n\t"
"v_mfma_f32_16x16x32_bf16 a[160:163], v[88:91], v[112:115], a[160:163]\n\t"
"ds_read_b64_tr_b16 v[72:73], v4 offset:0\n\t"
"v_mfma_f32_16x16x32_bf16 a[164:167], v[88:91], v[116:119], a[164:167]\n\t"
"v_mfma_f32_16x16x32_bf16 a[168:171], v[88:91], v[120:123], a[168:171]\n\t"
"buffer_load_dwordx4 v8, s[52:55], s81 offen offset:2048 lds\n\t"
"v_mfma_f32_16x16x32_bf16 a[172:175], v[88:91], v[124:127], a[172:175]\n\t"
"ds_read_b64_tr_b16 v[74:75], v4 offset:1024\n\t"
"v_mfma_f32_16x16x32_bf16 a[176:179], v[92:95], v[112:115], a[176:179]\n\t"
"ds_read_b64_tr_b16 v[76:77], v5 offset:0\n\t"
"v_mfma_f32_16x16x32_bf16 a[180:183], v[92:95], v[116:119], a[180:183]\n\t"
"v_mfma_f32_16x16x32_bf16 a[184:187], v[92:95], v[120:123], a[184:187]\n\t"
"ds_read_b64_tr_b16 v[78:79], v5 offset:1024\n\t"
"v_mfma_f32_16x16x32_bf16 a[188:191], v[92:95], v[124:127], a[188:191]\n\t"
"buffer_load_dwordx4 v9, s[52:55], s81 offen offset:3072 lds\n\t"
"v_mfma_f32_16x16x32_bf16 a[192:195], v[96:99], v[112:115], a[192:195]\n\t"
"ds_read_b128 v[36:39], v0 offset:2048\n\t"
"v_mfma_f32_16x16x32_bf16 a[196:199], v[96:99], v[116:119], a[196:199]\n\t"
"ds_read_b128 v[40:43], v0 offset:4096\n\t"
"v_mfma_f32_16x16x32_bf16 a[200:203], v[96:99], v[120:123], a[200:203]\n\t"
"v_mfma_f32_16x16x32_bf16 a[204:207], v[96:99], v[124:127], a[204:207]\n\t"
"s_add_u32 m0, s79, 4096\n\t"
"s_add_u32 s81, s77, 32896\n\t"
"buffer_load_dwordx4 v6, s[52:55], s81 offen offset:0 lds\n\t"
"v_mfma_f32_16x16x32_bf16 a[208:211], v[100:103], v[112:115], a[208:211]\n\t"
"ds_read_b128 v[44:47], v0 offset:6144\n\t"
"v_mfma_f32_16x16x32_bf16 a[212:215], v[100:103], v[116:119], a[212:215]\n\t"
"ds_read_b128 v[48:51], v0 offset:8192\n\t"
"v_mfma_f32_16x16x32_bf16 a[216:219], v[100:103], v[120:123], a[216:219]\n\t"
"ds_read_b128 v[52:55], v0 offset:10240\n\t"
"v_mfma_f32_16x16x32_bf16 a[220:223], v[100:103], v[124:127], a[220:223]\n\t"
"v_mfma_f32_16x16x32_bf16 a[224:227], v[104:107], v[112:115], a[224:227]\n\t"
"buffer_load_dwordx4 v7, s[52:55], s81 offen offset:1024 lds\n\t"
"v_mfma_f32_16x16x32_bf16 a[228:231], v[104:107], v[116:119], a[228:231]\n\t"
"ds_read_b128 v[56:59], v0 offset:12288\n\t"
"v_mfma_f32_16x16x32_bf16 a[232:235], v[104:107], v[120:123], a[232:235]\n\t"
"ds_read_b128 v[60:63], v0 offset:14336\n\t"
"s_waitcnt lgkmcnt(5)\n\t"
"v_mfma_f32_16x16x32_bf16 a[236:239], v[104:107], v[124:127], a[236:239]\n\t"
"v_mfma_f32_16x16x32_bf16 a[240:243], v[108:111], v[112:115], a[240:243]\n\t"
"v_mfma_f32_16x16x32_bf16 a[244:247], v[108:111], v[116:119], a[244:247]\n\t"
"v_mfma_f32_16x16x32_bf16 a[248:251], v[108:111], v[120:123], a[248:251]\n\t"
"v_mfma_f32_16x16x32_bf16 a[252:255], v[108:111], v[124:127], a[252:255]\n\t"
"v_mfma_f32_16x16x32_bf16 a[128:131], v[32:35], v[64:67], a[128:131]\n\t"
"ds_read_b128 v[80:83], v1 offset:0\n\t"
"v_mfma_f32_16x16x32_bf16 a[132:135], v[32:35], v[68:71], a[132:135]\n\t"
"buffer_load_dwordx4 v8, s[52:55], s81 offen offset:2048 lds\n\t"
"v_mfma_f32_16x16x32_bf16 a[136:139], v[32:35], v[72:75], a[136:139]\n\t"
"ds_read_b64_tr_b16 v[112:113], v2 offset:8192\n\t"
"v_mfma_f32_16x16x32_bf16 a[140:143], v[32:35], v[76:79], a[140:143]\n\t"
"ds_read_b64_tr_b16 v[114:115], v2 offset:9216\n\t"
"v_mfma_f32_16x16x32_bf16 a[144:147], v[36:39], v[64:67], a[144:147]\n\t"
"ds_read_b64_tr_b16 v[116:117], v3 offset:8192\n\t"
"v_mfma_f32_16x16x32_bf16 a[148:151], v[36:39], v[68:71], a[148:151]\n\t"
"buffer_load_dwordx4 v9, s[52:55], s81 offen offset:3072 lds\n\t"
"v_mfma_f32_16x16x32_bf16 a[152:155], v[36:39], v[72:75], a[152:155]\n\t"
"ds_read_b64_tr_b16 v[118:119], v3 offset:9216\n\t"
"v_mfma_f32_16x16x32_bf16 a[156:159], v[36:39], v[76:79], a[156:159]\n\t"
"ds_read_b64_tr_b16 v[120:121], v4 offset:8192\n\t"
"v_mfma_f32_16x16x32_bf16 a[160:163], v[40:43], v[64:67], a[160:163]\n\t"
"ds_read_b64_tr_b16 v[122:123], v4 offset:9216\n\t"
"v_mfma_f32_16x16x32_bf16 a[164:167], v[40:43], v[68:71], a[164:167]\n\t"
"ds_read_b64_tr_b16 v[124:125], v5 offset:8192\n\t"
"v_mfma_f32_16x16x32_bf16 a[168:171], v[40:43], v[72:75], a[168:171]\n\t"
"s_add_u32 m0, s80, 0\n\t"
"s_add_u32 s81, s78, 524288\n\t"
"buffer_load_dwordx4 v10, s[64:67], s81 offen offset:0 lds\n\t"
"v_mfma_f32_16x16x32_bf16 a[172:175], v[40:43], v[76:79], a[172:175]\n\t"
"ds_read_b64_tr_b16 v[126:127], v5 offset:9216\n\t"
"s_waitcnt lgkmcnt(5)\n\t"
"v_mfma_f32_16x16x32_bf16 a[176:179], v[44:47], v[64:67], a[176:179]\n\t"
"ds_read_b128 v[84:87], v1 offset:2048\n\t"
"v_mfma_f32_16x16x32_bf16 a[180:183], v[44:47], v[68:71], a[180:183]\n\t"
"ds_read_b128 v[88:91], v1 offset:4096\n\t"
"v_mfma_f32_16x16x32_bf16 a[184:187], v[44:47], v[72:75], a[184:187]\n\t"
"ds_read_b128 v[92:95], v1 offset:6144\n\t"
"v_mfma_f32_16x16x32_bf16 a[188:191], v[44:47], v[76:79], a[188:191]\n\t"
"buffer_load_dwordx4 v11, s[64:67], s81 offen offset:1024 lds\n\t"
"v_mfma_f32_16x16x32_bf16 a[192:195], v[48:51], v[64:67], a[192:195]\n\t"
"ds_read_b128 v[96:99], v1 offset:8192\n\t"
"v_mfma_f32_16x16x32_bf16 a[196:199], v[48:51], v[68:71], a[196:199]\n\t"
"ds_read_b128 v[100:103], v1 offset:10240\n\t"
"v_mfma_f32_16x16x32_bf16 a[200:203], v[48:51], v[72:75], a[200:203]\n\t"
"ds_read_b128 v[104:107], v1 offset:12288\n\t"
"v_mfma_f32_16x16x32_bf16 a[204:207], v[48:51], v[76:79], a[204:207]\n\t"
"buffer_load_dwordx4 v12, s[64:67], s81 offen offset:2048 lds\n\t"
"v_mfma_f32_16x16x32_bf16 a[208:211], v[52:55], v[64:67], a[208:211]\n\t"
"ds_read_b128 v[108:111], v1 offset:14336\n\t"
"v_mfma_f32_16x16x32_bf16 a[212:215], v[52:55], v[68:71], a[212:215]\n\t"
"v_add_u32 v0, 49152, v0\n\t"
"v_mfma_f32_16x16x32_bf16 a[216:219], v[52:55], v[72:75], a[216:219]\n\t"
"v_add_u32 v1, 49152, v1\n\t"
"v_mfma_f32_16x16x32_bf16 a[220:223], v[52:55], v[76:79], a[220:223]\n\t"
"v_add_u32 v2, 49152, v2\n\t"
"v_mfma_f32_16x16x32_bf16 a[224:227], v[56:59], v[64:67], a[224:227]\n\t"
"buffer_load_dwordx4 v13, s[64:67], s81 offen offset:3072 lds\n\t"
"v_mfma_f32_16x16x32_bf16 a[228:231], v[56:59], v[68:71], a[228:231]\n\t"
"v_add_u32 v3, 49152, v3\n\t"
"v_mfma_f32_16x16x32_bf16 a[232:235], v[56:59], v[72:75], a[232:235]\n\t"
"v_add_u32 v4, 49152, v4\n\t"
"v_mfma_f32_16x16x32_bf16 a[236:239], v[56:59], v[76:79], a[236:239]\n\t"
"v_add_u32 v5, 49152, v5\n\t"
"v_mfma_f32_16x16x32_bf16 a[240:243], v[60:63], v[64:67], a[240:243]\n\t"
"v_mfma_f32_16x16x32_bf16 a[244:247], v[60:63], v[68:71], a[244:247]\n\t"
"v_mfma_f32_16x16x32_bf16 a[248:251], v[60:63], v[72:75], a[248:251]\n\t"
"v_mfma_f32_16x16x32_bf16 a[252:255], v[60:63], v[76:79], a[252:255]\n\t"
"s_waitcnt vmcnt(13) lgkmcnt(0)\n\t"
"s_barrier\n\t"
"s_mov_b32 m0, 147456\n\t"
"s_nop 0\n\t"
"buffer_load_dwordx4 v14, s[28:31], s97 offen offset:2048 lds\n\t"
"v_mfma_f32_16x16x32_bf16 a[128:131], v[80:83], v[112:115], a[128:131]\n\t"
"ds_read_b128 v[32:35], v0 offset:0\n\t"
"v_mfma_f32_16x16x32_bf16 a[132:135], v[80:83], v[116:119], a[132:135]\n\t"
"s_add_u32 m0, s79, 49152\n\t"
"s_add_u32 s81, s77, 256\n\t"
"buffer_load_dwordx4 v6, s[52:55], s81 offen offset:0 lds\n\t"
"v_mfma_f32_16x16x32_bf16 a[136:139], v[80:83], v[120:123], a[136:139]\n\t"
"ds_read_b64_tr_b16 v[64:65], v2 offset:0\n\t"
"v_mfma_f32_16x16x32_bf16 a[140:143], v[80:83], v[124:127], a[140:143]\n\t"
"ds_read_b64_tr_b16 v[66:67], v2 offset:1024\n\t"
"v_mfma_f32_16x16x32_bf16 a[144:147], v[84:87], v[112:115], a[144:147]\n\t"
"v_mfma_f32_16x16x32_bf16 a[148:151], v[84:87], v[116:119], a[148:151]\n\t"
"buffer_load_dwordx4 v7, s[52:55], s81 offen offset:1024 lds\n\t"
"v_mfma_f32_16x16x32_bf16 a[152:155], v[84:87], v[120:123], a[152:155]\n\t"
"ds_read_b64_tr_b16 v[68:69], v3 offset:0\n\t"
"v_mfma_f32_16x16x32_bf16 a[156:159], v[84:87], v[124:127], a[156:159]\n\t"
"ds_read_b64_tr_b16 v[70:71], v3 offset:1024\n\t"
"v_mfma_f32_16x16x32_bf16 a[160:163], v[88:91], v[112:115], a[160:163]\n\t"
"ds_read_b64_tr_b16 v[72:73], v4 offset:0\n\t"
"v_mfma_f32_16x16x32_bf16 a[164:167], v[88:91], v[116:119], a[164:167]\n\t"
"v_mfma_f32_16x16x32_bf16 a[168:171], v[88:91], v[120:123], a[168:171]\n\t"
"buffer_load_dwordx4 v8, s[52:55], s81 offen offset:2048 lds\n\t"
"v_mfma_f32_16x16x32_bf16 a[172:175], v[88:91], v[124:127], a[172:175]\n\t"
"ds_read_b64_tr_b16 v[74:75], v4 offset:1024\n\t"
"v_mfma_f32_16x16x32_bf16 a[176:179], v[92:95], v[112:115], a[176:179]\n\t"
"ds_read_b64_tr_b16 v[76:77], v5 offset:0\n\t"
"v_mfma_f32_16x16x32_bf16 a[180:183], v[92:95], v[116:119], a[180:183]\n\t"
"v_mfma_f32_16x16x32_bf16 a[184:187], v[92:95], v[120:123], a[184:187]\n\t"
"ds_read_b64_tr_b16 v[78:79], v5 offset:1024\n\t"
"v_mfma_f32_16x16x32_bf16 a[188:191], v[92:95], v[124:127], a[188:191]\n\t"
"buffer_load_dwordx4 v9, s[52:55], s81 offen offset:3072 lds\n\t"
"v_mfma_f32_16x16x32_bf16 a[192:195], v[96:99], v[112:115], a[192:195]\n\t"
"ds_read_b128 v[36:39], v0 offset:2048\n\t"
"v_mfma_f32_16x16x32_bf16 a[196:199], v[96:99], v[116:119], a[196:199]\n\t"
"ds_read_b128 v[40:43], v0 offset:4096\n\t"
"v_mfma_f32_16x16x32_bf16 a[200:203], v[96:99], v[120:123], a[200:203]\n\t"
"v_mfma_f32_16x16x32_bf16 a[204:207], v[96:99], v[124:127], a[204:207]\n\t"
"s_add_u32 m0, s79, 53248\n\t"
"s_add_u32 s81, s77, 33024\n\t"
"buffer_load_dwordx4 v6, s[52:55], s81 offen offset:0 lds\n\t"
"v_mfma_f32_16x16x32_bf16 a[208:211], v[100:103], v[112:115], a[208:211]\n\t"
"ds_read_b128 v[44:47], v0 offset:6144\n\t"
"v_mfma_f32_16x16x32_bf16 a[212:215], v[100:103], v[116:119], a[212:215]\n\t"
"ds_read_b128 v[48:51], v0 offset:8192\n\t"
"v_mfma_f32_16x16x32_bf16 a[216:219], v[100:103], v[120:123], a[216:219]\n\t"
"ds_read_b128 v[52:55], v0 offset:10240\n\t"
"v_mfma_f32_16x16x32_bf16 a[220:223], v[100:103], v[124:127], a[220:223]\n\t"
"v_mfma_f32_16x16x32_bf16 a[224:227], v[104:107], v[112:115], a[224:227]\n\t"
"buffer_load_dwordx4 v7, s[52:55], s81 offen offset:1024 lds\n\t"
"v_mfma_f32_16x16x32_bf16 a[228:231], v[104:107], v[116:119], a[228:231]\n\t"
"ds_read_b128 v[56:59], v0 offset:12288\n\t"
"v_mfma_f32_16x16x32_bf16 a[232:235], v[104:107], v[120:123], a[232:235]\n\t"
"ds_read_b128 v[60:63], v0 offset:14336\n\t"
"s_waitcnt lgkmcnt(5)\n\t"
"v_mfma_f32_16x16x32_bf16 a[236:239], v[104:107], v[124:127], a[236:239]\n\t"
"v_mfma_f32_16x16x32_bf16 a[240:243], v[108:111], v[112:115], a[240:243]\n\t"
"v_mfma_f32_16x16x32_bf16 a[244:247], v[108:111], v[116:119], a[244:247]\n\t"
"v_mfma_f32_16x16x32_bf16 a[248:251], v[108:111], v[120:123], a[248:251]\n\t"
"v_mfma_f32_16x16x32_bf16 a[252:255], v[108:111], v[124:127], a[252:255]\n\t"
"v_mfma_f32_16x16x32_bf16 a[0:3], v[64:67], v[32:35], 0\n\t"
"ds_read_b128 v[80:83], v1 offset:0\n\t"
"v_accvgpr_read_b32 v128, a128\n\t"
"v_mfma_f32_16x16x32_bf16 a[4:7], v[68:71], v[32:35], 0\n\t"
"buffer_load_dwordx4 v8, s[52:55], s81 offen offset:2048 lds\n\t"
"v_mfma_f32_16x16x32_bf16 a[8:11], v[72:75], v[32:35], 0\n\t"
"ds_read_b64_tr_b16 v[112:113], v2 offset:8192\n\t"
"v_mfma_f32_16x16x32_bf16 a[12:15], v[76:79], v[32:35], 0\n\t"
"ds_read_b64_tr_b16 v[114:115], v2 offset:9216\n\t"
"v_accvgpr_read_b32 v129, a129\n\t"
"v_mfma_f32_16x16x32_bf16 a[16:19], v[64:67], v[36:39], 0\n\t"
"ds_read_b64_tr_b16 v[116:117], v3 offset:8192\n\t"
"v_mfma_f32_16x16x32_bf16 a[20:23], v[68:71], v[36:39], 0\n\t"
"buffer_load_dwordx4 v9, s[52:55], s81 offen offset:3072 lds\n\t"
"v_mfma_f32_16x16x32_bf16 a[24:27], v[72:75], v[36:39], 0\n\t"
"ds_read_b64_tr_b16 v[118:119], v3 offset:9216\n\t"
"v_mfma_f32_16x16x32_bf16 a[28:31], v[76:79], v[36:39], 0\n\t"
"ds_read_b64_tr_b16 v[120:121], v4 offset:8192\n\t"
"v_accvgpr_read_b32 v130, a130\n\t"
"v_mfma_f32_16x16x32_bf16 a[32:35], v[64:67], v[40:43], 0\n\t"
"ds_read_b64_tr_b16 v[122:123], v4 offset:9216\n\t"
"v_mfma_f32_16x16x32_bf16 a[36:39], v[68:71], v[40:43], 0\n\t"
"ds_read_b64_tr_b16 v[124:125], v5 offset:8192\n\t"
"v_mfma_f32_16x16x32_bf16 a[40:43], v[72:75], v[40:43], 0\n\t"
"s_add_u32 m0, s80, 49152\n\t"
"s_add_u32 s81, s78, 1048576\n\t"
"buffer_load_dwordx4 v10, s[64:67], s81 offen offset:0 lds\n\t"
"v_accvgpr_read_b32 v131, a131\n\t"
"v_mfma_f32_16x16x32_bf16 a[44:47], v[76:79], v[40:43], 0\n\t"
"ds_read_b64_tr_b16 v[126:127], v5 offset:9216\n\t"
"s_waitcnt lgkmcnt(5)\n\t"
"v_mfma_f32_16x16x32_bf16 a[48:51], v[64:67], v[44:47], 0\n\t"
"ds_read_b128 v[84:87], v1 offset:2048\n\t"
"v_mfma_f32_16x16x32_bf16 a[52:55], v[68:71], v[44:47], 0\n\t"
"ds_read_b128 v[88:91], v1 offset:4096\n\t"
"v_mfma_f32_16x16x32_bf16 a[56:59], v[72:75], v[44:47], 0\n\t"
"ds_read_b128 v[92:95], v1 offset:6144\n\t"
"v_accvgpr_read_b32 v132, a144\n\t"
"v_mfma_f32_16x16x32_bf16 a[60:63], v[76:79], v[44:47], 0\n\t"
"buffer_load_dwordx4 v11, s[64:67], s81 offen offset:1024 lds\n\t"
"v_mfma_f32_16x16x32_bf16 a[64:67], v[64:67], v[48:51], 0\n\t"
"ds_read_b128 v[96:99], v1 offset:8192\n\t"
"v_mfma_f32_16x16x32_bf16 a[68:71], v[68:71], v[48:51], 0\n\t"
"ds_read_b128 v[100:103], v1 offset:10240\n\t"
"v_accvgpr_read_b32 v133, a145\n\t"
"v_mfma_f32_16x16x32_bf16 a[72:75], v[72:75], v[48:51], 0\n\t"
"ds_read_b128 v[104:107], v1 offset:12288\n\t"
"v_mfma_f32_16x16x32_bf16 a[76:79], v[76:79], v[48:51], 0\n\t"
"buffer_load_dwordx4 v12, s[64:67], s81 offen offset:2048 lds\n\t"
"v_mfma_f32_16x16x32_bf16 a[80:83], v[64:67], v[52:55], 0\n\t"
"ds_read_b128 v[108:111], v1 offset:14336\n\t"
"v_mfma_f32_16x16x32_bf16 a[84:87], v[68:71], v[52:55], 0\n\t"
"v_add_u32 v0, -98304, v0\n\t"
"v_accvgpr_read_b32 v134, a146\n\t"
"v_mfma_f32_16x16x32_bf16 a[88:91], v[72:75], v[52:55], 0\n\t"
"v_add_u32 v1, -98304, v1\n\t"
"v_mfma_f32_16x16x32_bf16 a[92:95], v[76:79], v[52:55], 0\n\t"
"v_add_u32 v2, -98304, v2\n\t"
"v_mfma_f32_16x16x32_bf16 a[96:99], v[64:67], v[56:59], 0\n\t"
"buffer_load_dwordx4 v13, s[64:67], s81 offen offset:3072 lds\n\t"
"v_accvgpr_read_b32 v135, a147\n\t"
"v_mfma_f32_16x16x32_bf16 a[100:103], v[68:71], v[56:59], 0\n\t"
"v_add_u32 v3, -98304, v3\n\t"
"v_mfma_f32_16x16x32_bf16 a[104:107], v[72:75], v[56:59], 0\n\t"
"v_add_u32 v4, -98304, v4\n\t"
"v_mfma_f32_16x16x32_bf16 a[108:111], v[76:79], v[56:59], 0\n\t"
"v_add_u32 v5, -98304, v5\n\t"
"v_mfma_f32_16x16x32_bf16 a[112:115], v[64:67], v[60:63], 0\n\t"
"v_mfma_f32_16x16x32_bf16 a[116:119], v[68:71], v[60:63], 0\n\t"
"v_mfma_f32_16x16x32_bf16 a[120:123], v[72:75], v[60:63], 0\n\t"
"v_mfma_f32_16x16x32_bf16 a[124:127], v[76:79], v[60:63], 0\n\t"
"s_waitcnt vmcnt(13) lgkmcnt(0)\n\t"
"s_barrier\n\t"
"s_mov_b32 m0, 147456\n\t"
"s_nop 0\n\t"
"buffer_load_dwordx4 v14, s[28:31], s97 offen offset:3072 lds\n\t"
"v_mfma_f32_16x16x32_bf16 a[0:3], v[112:115], v[80:83], a[0:3]\n\t"
"ds_read_b128 v[32:35], v0 offset:0\n\t"
"v_cvt_pk_bf16_f32 v136, v128, v129\n\t"
"v_mfma_f32_16x16x32_bf16 a[4:7], v[116:119], v[80:83], a[4:7]\n\t"
"s_add_u32 m0, s79, 98304\n\t"
"s_add_u32 s81, s77, 384\n\t"
"buffer_load_dwordx4 v6, s[52:55], s81 offen offset:0 lds\n\t"
"v_mfma_f32_16x16x32_bf16 a[8:11], v[120:123], v[80:83], a[8:11]\n\t"
"v_cvt_pk_bf16_f32 v137, v130, v131\n\t"
"ds_read_b64_tr_b16 v[64:65], v2 offset:0\n\t"
"v_mfma_f32_16x16x32_bf16 a[12:15], v[124:127], v[80:83], a[12:15]\n\t"
"v_cvt_pk_bf16_f32 v138, v132, v133\n\t"
"v_mfma_f32_16x16x32_bf16 a[16:19], v[112:115], v[84:87], a[16:19]\n\t"
"ds_read_b64_tr_b16 v[66:67], v2 offset:1024\n\t"
"v_cvt_pk_bf16_f32 v139, v134, v135\n\t"
"v_mfma_f32_16x16x32_bf16 a[20:23], v[116:119], v[84:87], a[20:23]\n\t"
"buffer_load_dwordx4 v7, s[52:55], s81 offen offset:1024 lds\n\t"
"v_mfma_f32_16x16x32_bf16 a[24:27], v[120:123], v[84:87], a[24:27]\n\t"
"s_add_u32 s82, s85, 0\n\t"
"buffer_store_dwordx4 v[136:139], v14, s[72:75], s82 offen offset:0 sc1\n\t"
"v_mfma_f32_16x16x32_bf16 a[28:31], v[124:127], v[84:87], a[28:31]\n\t"
"ds_read_b64_tr_b16 v[68:69], v3 offset:0\n\t"
"ds_read_b64_tr_b16 v[70:71], v3 offset:1024\n\t"
"v_mfma_f32_16x16x32_bf16 a[32:35], v[112:115], v[88:91], a[32:35]\n\t"
"v_accvgpr_read_b32 v140, a132\n\t"
"v_mfma_f32_16x16x32_bf16 a[36:39], v[116:119], v[88:91], a[36:39]\n\t"
"ds_read_b64_tr_b16 v[72:73], v4 offset:0\n\t"
"v_accvgpr_read_b32 v141, a133\n\t"
"v_mfma_f32_16x16x32_bf16 a[40:43], v[120:123], v[88:91], a[40:43]\n\t"
"buffer_load_dwordx4 v8, s[52:55], s81 offen offset:2048 lds\n\t"
"v_mfma_f32_16x16x32_bf16 a[44:47], v[124:127], v[88:91], a[44:47]\n\t"
"v_accvgpr_read_b32 v142, a134\n\t"
"ds_read_b64_tr_b16 v[74:75], v4 offset:1024\n\t"
"v_mfma_f32_16x16x32_bf16 a[48:51], v[112:115], v[92:95], a[48:51]\n\t"
"v_accvgpr_read_b32 v143, a135\n\t"
"v_mfma_f32_16x16x32_bf16 a[52:55], v[116:119], v[92:95], a[52:55]\n\t"
"ds_read_b64_tr_b16 v[76:77], v5 offset:0\n\t"
"v_mfma_f32_16x16x32_bf16 a[56:59], v[120:123], v[92:95], a[56:59]\n\t"
"ds_read_b64_tr_b16 v[78:79], v5 offset:1024\n\t"
"v_accvgpr_read_b32 v144, a148\n\t"
"v_mfma_f32_16x16x32_bf16 a[60:63], v[124:127], v[92:95], a[60:63]\n\t"
"buffer_load_dwordx4 v9, s[52:55], s81 offen offset:3072 lds\n\t"
"v_mfma_f32_16x16x32_bf16 a[64:67], v[112:115], v[96:99], a[64:67]\n\t"
"v_accvgpr_read_b32 v145, a149\n\t"
"ds_read_b128 v[36:39], v0 offset:2048\n\t"
"v_mfma_f32_16x16x32_bf16 a[68:71], v[116:119], v[96:99], a[68:71]\n\t"
"v_accvgpr_read_b32 v146, a150\n\t"
"v_mfma_f32_16x16x32_bf16 a[72:75], v[120:123], v[96:99], a[72:75]\n\t"
"ds_read_b128 v[40:43], v0 offset:4096\n\t"
"v_accvgpr_read_b32 v147, a151\n\t"
"v_mfma_f32_16x16x32_bf16 a[76:79], v[124:127], v[96:99], a[76:79]\n\t"
"s_add_u32 m0, s79, 102400\n\t"
"s_add_u32 s81, s77, 33152\n\t"
"buffer_load_dwordx4 v6, s[52:55], s81 offen offset:0 lds\n\t"
"v_mfma_f32_16x16x32_bf16 a[80:83], v[112:115], v[100:103], a[80:83]\n\t"
"v_cvt_pk_bf16_f32 v148, v140, v141\n\t"
"v_mfma_f32_16x16x32_bf16 a[84:87], v[116:119], v[100:103], a[84:87]\n\t"
"ds_read_b128 v[44:47], v0 offset:6144\n\t"
"ds_read_b128 v[48:51], v0 offset:8192\n\t"
"v_mfma_f32_16x16x32_bf16 a[88:91], v[120:123], v[100:103], a[88:91]\n\t"
"v_cvt_pk_bf16_f32 v149, v142, v143\n\t"
"v_mfma_f32_16x16x32_bf16 a[92:95], v[124:127], v[100:103], a[92:95]\n\t"
"ds_read_b128 v[52:55], v0 offset:10240\n\t"
"v_cvt_pk_bf16_f32 v150, v144, v145\n\t"
"v_mfma_f32_16x16x32_bf16 a[96:99], v[112:115], v[104:107], a[96:99]\n\t"
"buffer_load_dwordx4 v7, s[52:55], s81 offen offset:1024 lds\n\t"
"v_mfma_f32_16x16x32_bf16 a[100:103], v[116:119], v[104:107], a[100:103]\n\t"
"v_cvt_pk_bf16_f32 v151, v146, v147\n\t"
"ds_read_b128 v[56:59], v0 offset:12288\n\t"
"v_mfma_f32_16x16x32_bf16 a[104:107], v[120:123], v[104:107], a[104:107]\n\t"
"s_add_u32 s82, s85, 0\n\t"
"buffer_store_dwordx4 v[148:151], v14, s[72:75], s82 offen offset:1024 sc1\n\t"
"v_mfma_f32_16x16x32_bf16 a[108:111], v[124:127], v[104:107], a[108:111]\n\t"
"ds_read_b128 v[60:63], v0 offset:14336\n\t"
"s_waitcnt lgkmcnt(5)\n\t"
"v_mfma_f32_16x16x32_bf16 a[112:115], v[112:115], v[108:111], a[112:115]\n\t"
"v_mfma_f32_16x16x32_bf16 a[116:119], v[116:119], v[108:111], a[116:119]\n\t"
"v_mfma_f32_16x16x32_bf16 a[120:123], v[120:123], v[108:111], a[120:123]\n\t"
"v_mfma_f32_16x16x32_bf16 a[124:127], v[124:127], v[108:111], a[124:127]\n\t"
"s_load_dwordx2 s[48:49], s[36:37], 88\n\t"
"s_load_dwordx2 s[56:57], s[36:37], 96\n\t"
"s_load_dwordx2 s[68:69], s[36:37], 120\n\t"
"s_waitcnt lgkmcnt(0)\n\t"
"s_and_b32 s49, s49, 0xffff\n\t"
"s_mov_b32 s50, 524288\n\t"
"s_mov_b32 s51, 0x00020000\n\t"
"s_and_b32 s57, s57, 0xffff\n\t"
"s_mov_b32 s58, 524288\n\t"
"s_mov_b32 s59, 0x00020000\n\t"
"s_add_u32 s60, s42, 1044480\n\t"
"s_addc_u32 s61, s43, 0\n\t"
"s_and_b32 s61, s61, 0xffff\n\t"
"s_mov_b32 s62, 4198400\n\t"
"s_mov_b32 s63, 0x00020000\n\t"
"s_and_b32 s69, s69, 0xffff\n\t"
"s_mov_b32 s70, 8388608\n\t"
"s_mov_b32 s71, 0x00020000\n\t"
"s_lshl_b32 s76, s47, 16\n\t"
"s_lshl_b32 s83, s39, 14\n\t"
"s_add_u32 s76, s76, s83\n\t"
"s_lshl_b32 s82, s46, 8\n\t"
"s_add_u32 s82, s82, 26624\n\t"
"v_mbcnt_lo_u32_b32 v212, -1, 0\n\t"
"v_mbcnt_hi_u32_b32 v212, -1, v212\n\t"
"v_min_u32 v212, 31, v212\n\t"
"v_lshl_add_u32 v212, v212, 3, s82\n\t"
"global_load_dwordx2 v[214:215], v212, s[42:43] sc1\n\t"
"v_mfma_f32_16x16x32_bf16 a[0:3], v[64:67], v[32:35], a[0:3]\n\t"
"ds_read_b128 v[80:83], v1 offset:0\n\t"
"v_accvgpr_read_b32 v152, a136\n\t"
"v_mfma_f32_16x16x32_bf16 a[4:7], v[68:71], v[32:35], a[4:7]\n\t"
"buffer_load_dwordx4 v8, s[52:55], s81 offen offset:2048 lds\n\t"
"v_accvgpr_read_b32 v153, a137\n\t"
"v_mfma_f32_16x16x32_bf16 a[8:11], v[72:75], v[32:35], a[8:11]\n\t"
"ds_read_b64_tr_b16 v[112:113], v2 offset:8192\n\t"
"v_accvgpr_read_b32 v154, a138\n\t"
"v_mfma_f32_16x16x32_bf16 a[12:15], v[76:79], v[32:35], a[12:15]\n\t"
"ds_read_b64_tr_b16 v[114:115], v2 offset:9216\n\t"
"v_mfma_f32_16x16x32_bf16 a[16:19], v[64:67], v[36:39], a[16:19]\n\t"
"ds_read_b64_tr_b16 v[116:117], v3 offset:8192\n\t"
"v_accvgpr_read_b32 v155, a139\n\t"
"v_mfma_f32_16x16x32_bf16 a[20:23], v[68:71], v[36:39], a[20:23]\n\t"
"buffer_load_dwordx4 v9, s[52:55], s81 offen offset:3072 lds\n\t"
"v_accvgpr_read_b32 v156, a152\n\t"
"v_mfma_f32_16x16x32_bf16 a[24:27], v[72:75], v[36:39], a[24:27]\n\t"
"ds_read_b64_tr_b16 v[118:119], v3 offset:9216\n\t"
"v_mfma_f32_16x16x32_bf16 a[28:31], v[76:79], v[36:39], a[28:31]\n\t"
"ds_read_b64_tr_b16 v[120:121], v4 offset:8192\n\t"
"v_accvgpr_read_b32 v157, a153\n\t"
"v_mfma_f32_16x16x32_bf16 a[32:35], v[64:67], v[40:43], a[32:35]\n\t"
"ds_read_b64_tr_b16 v[122:123], v4 offset:9216\n\t"
"v_accvgpr_read_b32 v158, a154\n\t"
"v_mfma_f32_16x16x32_bf16 a[36:39], v[68:71], v[40:43], a[36:39]\n\t"
"ds_read_b64_tr_b16 v[124:125], v5 offset:8192\n\t"
"v_mfma_f32_16x16x32_bf16 a[40:43], v[72:75], v[40:43], a[40:43]\n\t"
"s_add_u32 m0, s80, 98304\n\t"
"s_add_u32 s81, s78, 1572864\n\t"
"buffer_load_dwordx4 v10, s[64:67], s81 offen offset:0 lds\n\t"
"v_accvgpr_read_b32 v159, a155\n\t"
"v_mfma_f32_16x16x32_bf16 a[44:47], v[76:79], v[40:43], a[44:47]\n\t"
"ds_read_b64_tr_b16 v[126:127], v5 offset:9216\n\t"
"v_cvt_pk_bf16_f32 v160, v152, v153\n\t"
"v_mfma_f32_16x16x32_bf16 a[48:51], v[64:67], v[44:47], a[48:51]\n\t"
"ds_read_b128 v[84:87], v1 offset:2048\n\t"
"v_mfma_f32_16x16x32_bf16 a[52:55], v[68:71], v[44:47], a[52:55]\n\t"
"ds_read_b128 v[88:91], v1 offset:4096\n\t"
"v_cvt_pk_bf16_f32 v161, v154, v155\n\t"
"v_mfma_f32_16x16x32_bf16 a[56:59], v[72:75], v[44:47], a[56:59]\n\t"
"ds_read_b128 v[92:95], v1 offset:6144\n\t"
"v_cvt_pk_bf16_f32 v162, v156, v157\n\t"
"v_mfma_f32_16x16x32_bf16 a[60:63], v[76:79], v[44:47], a[60:63]\n\t"
"buffer_load_dwordx4 v11, s[64:67], s81 offen offset:1024 lds\n\t"
"v_mfma_f32_16x16x32_bf16 a[64:67], v[64:67], v[48:51], a[64:67]\n\t"
"ds_read_b128 v[96:99], v1 offset:8192\n\t"
"v_cvt_pk_bf16_f32 v163, v158, v159\n\t"
"v_mfma_f32_16x16x32_bf16 a[68:71], v[68:71], v[48:51], a[68:71]\n\t"
"ds_read_b128 v[100:103], v1 offset:10240\n\t"
"s_add_u32 s82, s85, 0\n\t"
"buffer_store_dwordx4 v[160:163], v14, s[72:75], s82 offen offset:2048 sc1\n\t"
"v_mfma_f32_16x16x32_bf16 a[72:75], v[72:75], v[48:51], a[72:75]\n\t"
"ds_read_b128 v[104:107], v1 offset:12288\n\t"
"v_mfma_f32_16x16x32_bf16 a[76:79], v[76:79], v[48:51], a[76:79]\n\t"
"buffer_load_dwordx4 v12, s[64:67], s81 offen offset:2048 lds\n\t"
"v_accvgpr_read_b32 v164, a140\n\t"
"v_mfma_f32_16x16x32_bf16 a[80:83], v[64:67], v[52:55], a[80:83]\n\t"
"ds_read_b128 v[108:111], v1 offset:14336\n\t"
"s_waitcnt lgkmcnt(5)\n\t"
"v_accvgpr_read_b32 v165, a141\n\t"
"v_mfma_f32_16x16x32_bf16 a[84:87], v[68:71], v[52:55], a[84:87]\n\t"
"v_add_u32 v0, 49152, v0\n\t"
"v_mfma_f32_16x16x32_bf16 a[88:91], v[72:75], v[52:55], a[88:91]\n\t"
"v_add_u32 v1, 49152, v1\n\t"
"v_accvgpr_read_b32 v166, a142\n\t"
"v_mfma_f32_16x16x32_bf16 a[92:95], v[76:79], v[52:55], a[92:95]\n\t"
"v_add_u32 v2, 49152, v2\n\t"
"v_accvgpr_read_b32 v167, a143\n\t"
"v_mfma_f32_16x16x32_bf16 a[96:99], v[64:67], v[56:59], a[96:99]\n\t"
"buffer_load_dwordx4 v13, s[64:67], s81 offen offset:3072 lds\n\t"
"v_mfma_f32_16x16x32_bf16 a[100:103], v[68:71], v[56:59], a[100:103]\n\t"
"v_add_u32 v3, 49152, v3\n\t"
"v_accvgpr_read_b32 v168, a156\n\t"
"v_mfma_f32_16x16x32_bf16 a[104:107], v[72:75], v[56:59], a[104:107]\n\t"
"v_add_u32 v4, 49152, v4\n\t"
"v_accvgpr_read_b32 v169, a157\n\t"
"v_mfma_f32_16x16x32_bf16 a[108:111], v[76:79], v[56:59], a[108:111]\n\t"
"v_add_u32 v5, 49152, v5\n\t"
"v_mfma_f32_16x16x32_bf16 a[112:115], v[64:67], v[60:63], a[112:115]\n\t"
"v_mfma_f32_16x16x32_bf16 a[116:119], v[68:71], v[60:63], a[116:119]\n\t"
"v_mfma_f32_16x16x32_bf16 a[120:123], v[72:75], v[60:63], a[120:123]\n\t"
"v_mfma_f32_16x16x32_bf16 a[124:127], v[76:79], v[60:63], a[124:127]\n\t"
"s_waitcnt vmcnt(17) lgkmcnt(0)\n\t"
"s_barrier\n\t"
"v_mfma_f32_16x16x32_bf16 a[0:3], v[112:115], v[80:83], a[0:3]\n\t"
"ds_read_b128 v[32:35], v0 offset:0\n\t"
"v_accvgpr_read_b32 v170, a158\n\t"
"v_mfma_f32_16x16x32_bf16 a[4:7], v[116:119], v[80:83], a[4:7]\n\t"
"s_add_u32 m0, s79, 0\n\t"
"s_add_u32 s81, s77, 512\n\t"
"buffer_load_dwordx4 v6, s[52:55], s81 offen offset:0 lds\n\t"
"v_mfma_f32_16x16x32_bf16 a[8:11], v[120:123], v[80:83], a[8:11]\n\t"
"v_accvgpr_read_b32 v171, a159\n\t"
"ds_read_b64_tr_b16 v[64:65], v2 offset:0\n\t"
"v_mfma_f32_16x16x32_bf16 a[12:15], v[124:127], v[80:83], a[12:15]\n\t"
"v_cvt_pk_bf16_f32 v172, v164, v165\n\t"
"v_mfma_f32_16x16x32_bf16 a[16:19], v[112:115], v[84:87], a[16:19]\n\t"
"ds_read_b64_tr_b16 v[66:67], v2 offset:1024\n\t"
"v_cvt_pk_bf16_f32 v173, v166, v167\n\t"
"v_mfma_f32_16x16x32_bf16 a[20:23], v[116:119], v[84:87], a[20:23]\n\t"
"buffer_load_dwordx4 v7, s[52:55], s81 offen offset:1024 lds\n\t"
"v_mfma_f32_16x16x32_bf16 a[24:27], v[120:123], v[84:87], a[24:27]\n\t"
"v_cvt_pk_bf16_f32 v174, v168, v169\n\t"
"ds_read_b64_tr_b16 v[68:69], v3 offset:0\n\t"
"v_mfma_f32_16x16x32_bf16 a[28:31], v[124:127], v[84:87], a[28:31]\n\t"
"v_cvt_pk_bf16_f32 v175, v170, v171\n\t"
"v_mfma_f32_16x16x32_bf16 a[32:35], v[112:115], v[88:91], a[32:35]\n\t"
"ds_read_b64_tr_b16 v[70:71], v3 offset:1024\n\t"
"s_add_u32 s82, s85, 0\n\t"
"buffer_store_dwordx4 v[172:175], v14, s[72:75], s82 offen offset:3072 sc1\n\t"
"v_mfma_f32_16x16x32_bf16 a[36:39], v[116:119], v[88:91], a[36:39]\n\t"
"ds_read_b64_tr_b16 v[72:73], v4 offset:0\n\t"
"v_mfma_f32_16x16x32_bf16 a[40:43], v[120:123], v[88:91], a[40:43]\n\t"
"v_accvgpr_read_b32 v176, a160\n\t"
"buffer_load_dwordx4 v8, s[52:55], s81 offen offset:2048 lds\n\t"
"v_mfma_f32_16x16x32_bf16 a[44:47], v[124:127], v[88:91], a[44:47]\n\t"
"v_accvgpr_read_b32 v177, a161\n\t"
"v_mfma_f32_16x16x32_bf16 a[48:51], v[112:115], v[92:95], a[48:51]\n\t"
"ds_read_b64_tr_b16 v[74:75], v4 offset:1024\n\t"
"v_accvgpr_read_b32 v178, a162\n\t"
"v_mfma_f32_16x16x32_bf16 a[52:55], v[116:119], v[92:95], a[52:55]\n\t"
"ds_read_b64_tr_b16 v[76:77], v5 offset:0\n\t"
"v_mfma_f32_16x16x32_bf16 a[56:59], v[120:123], v[92:95], a[56:59]\n\t"
"ds_read_b64_tr_b16 v[78:79], v5 offset:1024\n\t"
"v_accvgpr_read_b32 v179, a163\n\t"
"v_mfma_f32_16x16x32_bf16 a[60:63], v[124:127], v[92:95], a[60:63]\n\t"
"buffer_load_dwordx4 v9, s[52:55], s81 offen offset:3072 lds\n\t"
"v_mfma_f32_16x16x32_bf16 a[64:67], v[112:115], v[96:99], a[64:67]\n\t"
"v_accvgpr_read_b32 v180, a176\n\t"
"ds_read_b128 v[36:39], v0 offset:2048\n\t"
"v_mfma_f32_16x16x32_bf16 a[68:71], v[116:119], v[96:99], a[68:71]\n\t"
"v_accvgpr_read_b32 v181, a177\n\t"
"v_mfma_f32_16x16x32_bf16 a[72:75], v[120:123], v[96:99], a[72:75]\n\t"
"ds_read_b128 v[40:43], v0 offset:4096\n\t"
"v_accvgpr_read_b32 v182, a178\n\t"
"v_mfma_f32_16x16x32_bf16 a[76:79], v[124:127], v[96:99], a[76:79]\n\t"
"s_add_u32 m0, s79, 4096\n\t"
"s_add_u32 s81, s77, 33280\n\t"
"buffer_load_dwordx4 v6, s[52:55], s81 offen offset:0 lds\n\t"
"v_mfma_f32_16x16x32_bf16 a[80:83], v[112:115], v[100:103], a[80:83]\n\t"
"v_accvgpr_read_b32 v183, a179\n\t"
"ds_read_b128 v[44:47], v0 offset:6144\n\t"
"v_mfma_f32_16x16x32_bf16 a[84:87], v[116:119], v[100:103], a[84:87]\n\t"
"v_cvt_pk_bf16_f32 v184, v176, v177\n\t"
"v_mfma_f32_16x16x32_bf16 a[88:91], v[120:123], v[100:103], a[88:91]\n\t"
"ds_read_b128 v[48:51], v0 offset:8192\n\t"
"v_cvt_pk_bf16_f32 v185, v178, v179\n\t"
"v_mfma_f32_16x16x32_bf16 a[92:95], v[124:127], v[100:103], a[92:95]\n\t"
"ds_read_b128 v[52:55], v0 offset:10240\n\t"
"v_mfma_f32_16x16x32_bf16 a[96:99], v[112:115], v[104:107], a[96:99]\n\t"
"v_cvt_pk_bf16_f32 v186, v180, v181\n\t"
"buffer_load_dwordx4 v7, s[52:55], s81 offen offset:1024 lds\n\t"
"v_mfma_f32_16x16x32_bf16 a[100:103], v[116:119], v[104:107], a[100:103]\n\t"
"v_cvt_pk_bf16_f32 v187, v182, v183\n\t"
"v_mfma_f32_16x16x32_bf16 a[104:107], v[120:123], v[104:107], a[104:107]\n\t"
"ds_read_b128 v[56:59], v0 offset:12288\n\t"
"s_add_u32 s82, s85, 8192\n\t"
"buffer_store_dwordx4 v[184:187], v14, s[72:75], s82 offen offset:0 sc1\n\t"
"v_mfma_f32_16x16x32_bf16 a[108:111], v[124:127], v[104:107], a[108:111]\n\t"
"ds_read_b128 v[60:63], v0 offset:14336\n\t"
"s_waitcnt lgkmcnt(5)\n\t"
"v_mfma_f32_16x16x32_bf16 a[112:115], v[112:115], v[108:111], a[112:115]\n\t"
"v_mfma_f32_16x16x32_bf16 a[116:119], v[116:119], v[108:111], a[116:119]\n\t"
"v_mfma_f32_16x16x32_bf16 a[120:123], v[120:123], v[108:111], a[120:123]\n\t"
"v_mfma_f32_16x16x32_bf16 a[124:127], v[124:127], v[108:111], a[124:127]\n\t"
"v_mfma_f32_16x16x32_bf16 a[0:3], v[64:67], v[32:35], a[0:3]\n\t"
"ds_read_b128 v[80:83], v1 offset:0\n\t"
"v_accvgpr_read_b32 v188, a164\n\t"
"v_mfma_f32_16x16x32_bf16 a[4:7], v[68:71], v[32:35], a[4:7]\n\t"
"buffer_load_dwordx4 v8, s[52:55], s81 offen offset:2048 lds\n\t"
"v_accvgpr_read_b32 v189, a165\n\t"
"v_mfma_f32_16x16x32_bf16 a[8:11], v[72:75], v[32:35], a[8:11]\n\t"
"ds_read_b64_tr_b16 v[112:113], v2 offset:8192\n\t"
"v_accvgpr_read_b32 v190, a166\n\t"
"v_mfma_f32_16x16x32_bf16 a[12:15], v[76:79], v[32:35], a[12:15]\n\t"
"ds_read_b64_tr_b16 v[114:115], v2 offset:9216\n\t"
"v_mfma_f32_16x16x32_bf16 a[16:19], v[64:67], v[36:39], a[16:19]\n\t"
"ds_read_b64_tr_b16 v[116:117], v3 offset:8192\n\t"
"v_accvgpr_read_b32 v191, a167\n\t"
"v_mfma_f32_16x16x32_bf16 a[20:23], v[68:71], v[36:39], a[20:23]\n\t"
"buffer_load_dwordx4 v9, s[52:55], s81 offen offset:3072 lds\n\t"
"v_accvgpr_read_b32 v192, a180\n\t"
"v_mfma_f32_16x16x32_bf16 a[24:27], v[72:75], v[36:39], a[24:27]\n\t"
"ds_read_b64_tr_b16 v[118:119], v3 offset:9216\n\t"
"v_mfma_f32_16x16x32_bf16 a[28:31], v[76:79], v[36:39], a[28:31]\n\t"
"ds_read_b64_tr_b16 v[120:121], v4 offset:8192\n\t"
"v_accvgpr_read_b32 v193, a181\n\t"
"v_mfma_f32_16x16x32_bf16 a[32:35], v[64:67], v[40:43], a[32:35]\n\t"
"ds_read_b64_tr_b16 v[122:123], v4 offset:9216\n\t"
"v_accvgpr_read_b32 v194, a182\n\t"
"v_mfma_f32_16x16x32_bf16 a[36:39], v[68:71], v[40:43], a[36:39]\n\t"
"ds_read_b64_tr_b16 v[124:125], v5 offset:8192\n\t"
"v_mfma_f32_16x16x32_bf16 a[40:43], v[72:75], v[40:43], a[40:43]\n\t"
"s_add_u32 m0, s80, 0\n\t"
"s_add_u32 s81, s78, 2097152\n\t"
"buffer_load_dwordx4 v10, s[64:67], s81 offen offset:0 lds\n\t"
"v_accvgpr_read_b32 v195, a183\n\t"
"v_mfma_f32_16x16x32_bf16 a[44:47], v[76:79], v[40:43], a[44:47]\n\t"
"ds_read_b64_tr_b16 v[126:127], v5 offset:9216\n\t"
"v_cvt_pk_bf16_f32 v196, v188, v189\n\t"
"s_waitcnt lgkmcnt(5)\n\t"
"v_mfma_f32_16x16x32_bf16 a[48:51], v[64:67], v[44:47], a[48:51]\n\t"
"ds_read_b128 v[84:87], v1 offset:2048\n\t"
"v_mfma_f32_16x16x32_bf16 a[52:55], v[68:71], v[44:47], a[52:55]\n\t"
"ds_read_b128 v[88:91], v1 offset:4096\n\t"
"v_cvt_pk_bf16_f32 v197, v190, v191\n\t"
"v_mfma_f32_16x16x32_bf16 a[56:59], v[72:75], v[44:47], a[56:59]\n\t"
"ds_read_b128 v[92:95], v1 offset:6144\n\t"
"v_cvt_pk_bf16_f32 v198, v192, v193\n\t"
"v_mfma_f32_16x16x32_bf16 a[60:63], v[76:79], v[44:47], a[60:63]\n\t"
"buffer_load_dwordx4 v11, s[64:67], s81 offen offset:1024 lds\n\t"
"v_mfma_f32_16x16x32_bf16 a[64:67], v[64:67], v[48:51], a[64:67]\n\t"
"ds_read_b128 v[96:99], v1 offset:8192\n\t"
"v_cvt_pk_bf16_f32 v199, v194, v195\n\t"
"v_mfma_f32_16x16x32_bf16 a[68:71], v[68:71], v[48:51], a[68:71]\n\t"
"ds_read_b128 v[100:103], v1 offset:10240\n\t"
"s_add_u32 s82, s85, 8192\n\t"
"buffer_store_dwordx4 v[196:199], v14, s[72:75], s82 offen offset:1024 sc1\n\t"
"v_mfma_f32_16x16x32_bf16 a[72:75], v[72:75], v[48:51], a[72:75]\n\t"
"ds_read_b128 v[104:107], v1 offset:12288\n\t"
"v_mfma_f32_16x16x32_bf16 a[76:79], v[76:79], v[48:51], a[76:79]\n\t"
"buffer_load_dwordx4 v12, s[64:67], s81 offen offset:2048 lds\n\t"
"v_accvgpr_read_b32 v200, a168\n\t"
"v_mfma_f32_16x16x32_bf16 a[80:83], v[64:67], v[52:55], a[80:83]\n\t"
"ds_read_b128 v[108:111], v1 offset:14336\n\t"
"v_accvgpr_read_b32 v201, a169\n\t"
"v_mfma_f32_16x16x32_bf16 a[84:87], v[68:71], v[52:55], a[84:87]\n\t"
"v_add_u32 v0, 49152, v0\n\t"
"v_mfma_f32_16x16x32_bf16 a[88:91], v[72:75], v[52:55], a[88:91]\n\t"
"v_add_u32 v1, 49152, v1\n\t"
"v_accvgpr_read_b32 v202, a170\n\t"
"v_mfma_f32_16x16x32_bf16 a[92:95], v[76:79], v[52:55], a[92:95]\n\t"
"v_add_u32 v2, 49152, v2\n\t"
"v_accvgpr_read_b32 v203, a171\n\t"
"v_mfma_f32_16x16x32_bf16 a[96:99], v[64:67], v[56:59], a[96:99]\n\t"
"buffer_load_dwordx4 v13, s[64:67], s81 offen offset:3072 lds\n\t"
"v_mfma_f32_16x16x32_bf16 a[100:103], v[68:71], v[56:59], a[100:103]\n\t"
"v_add_u32 v3, 49152, v3\n\t"
"v_accvgpr_read_b32 v204, a184\n\t"
"v_mfma_f32_16x16x32_bf16 a[104:107], v[72:75], v[56:59], a[104:107]\n\t"
"v_add_u32 v4, 49152, v4\n\t"
"v_accvgpr_read_b32 v205, a185\n\t"
"v_mfma_f32_16x16x32_bf16 a[108:111], v[76:79], v[56:59], a[108:111]\n\t"
"v_add_u32 v5, 49152, v5\n\t"
"v_mfma_f32_16x16x32_bf16 a[112:115], v[64:67], v[60:63], a[112:115]\n\t"
"v_mfma_f32_16x16x32_bf16 a[116:119], v[68:71], v[60:63], a[116:119]\n\t"
"v_mfma_f32_16x16x32_bf16 a[120:123], v[72:75], v[60:63], a[120:123]\n\t"
"v_mfma_f32_16x16x32_bf16 a[124:127], v[76:79], v[60:63], a[124:127]\n\t"
"s_waitcnt vmcnt(15) lgkmcnt(0)\n\t"
"s_barrier\n\t"
"v_mfma_f32_16x16x32_bf16 a[0:3], v[112:115], v[80:83], a[0:3]\n\t"
"ds_read_b128 v[32:35], v0 offset:0\n\t"
"v_accvgpr_read_b32 v206, a186\n\t"
"v_mfma_f32_16x16x32_bf16 a[4:7], v[116:119], v[80:83], a[4:7]\n\t"
"s_add_u32 m0, s79, 49152\n\t"
"s_add_u32 s81, s77, 640\n\t"
"buffer_load_dwordx4 v6, s[52:55], s81 offen offset:0 lds\n\t"
"v_mfma_f32_16x16x32_bf16 a[8:11], v[120:123], v[80:83], a[8:11]\n\t"
"v_accvgpr_read_b32 v207, a187\n\t"
"ds_read_b64_tr_b16 v[64:65], v2 offset:0\n\t"
"v_mfma_f32_16x16x32_bf16 a[12:15], v[124:127], v[80:83], a[12:15]\n\t"
"v_cvt_pk_bf16_f32 v208, v200, v201\n\t"
"v_mfma_f32_16x16x32_bf16 a[16:19], v[112:115], v[84:87], a[16:19]\n\t"
"ds_read_b64_tr_b16 v[66:67], v2 offset:1024\n\t"
"v_cvt_pk_bf16_f32 v209, v202, v203\n\t"
"v_mfma_f32_16x16x32_bf16 a[20:23], v[116:119], v[84:87], a[20:23]\n\t"
"buffer_load_dwordx4 v7, s[52:55], s81 offen offset:1024 lds\n\t"
"v_mfma_f32_16x16x32_bf16 a[24:27], v[120:123], v[84:87], a[24:27]\n\t"
"v_cvt_pk_bf16_f32 v210, v204, v205\n\t"
"ds_read_b64_tr_b16 v[68:69], v3 offset:0\n\t"
"v_mfma_f32_16x16x32_bf16 a[28:31], v[124:127], v[84:87], a[28:31]\n\t"
"v_cvt_pk_bf16_f32 v211, v206, v207\n\t"
"v_mfma_f32_16x16x32_bf16 a[32:35], v[112:115], v[88:91], a[32:35]\n\t"
"ds_read_b64_tr_b16 v[70:71], v3 offset:1024\n\t"
"s_add_u32 s82, s85, 8192\n\t"
"buffer_store_dwordx4 v[208:211], v14, s[72:75], s82 offen offset:2048 sc1\n\t"
"v_mfma_f32_16x16x32_bf16 a[36:39], v[116:119], v[88:91], a[36:39]\n\t"
"ds_read_b64_tr_b16 v[72:73], v4 offset:0\n\t"
"v_mfma_f32_16x16x32_bf16 a[40:43], v[120:123], v[88:91], a[40:43]\n\t"
"v_accvgpr_read_b32 v128, a172\n\t"
"buffer_load_dwordx4 v8, s[52:55], s81 offen offset:2048 lds\n\t"
"v_mfma_f32_16x16x32_bf16 a[44:47], v[124:127], v[88:91], a[44:47]\n\t"
"v_accvgpr_read_b32 v129, a173\n\t"
"v_mfma_f32_16x16x32_bf16 a[48:51], v[112:115], v[92:95], a[48:51]\n\t"
"ds_read_b64_tr_b16 v[74:75], v4 offset:1024\n\t"
"v_accvgpr_read_b32 v130, a174\n\t"
"v_mfma_f32_16x16x32_bf16 a[52:55], v[116:119], v[92:95], a[52:55]\n\t"
"ds_read_b64_tr_b16 v[76:77], v5 offset:0\n\t"
"v_mfma_f32_16x16x32_bf16 a[56:59], v[120:123], v[92:95], a[56:59]\n\t"
"ds_read_b64_tr_b16 v[78:79], v5 offset:1024\n\t"
"v_accvgpr_read_b32 v131, a175\n\t"
"v_mfma_f32_16x16x32_bf16 a[60:63], v[124:127], v[92:95], a[60:63]\n\t"
"buffer_load_dwordx4 v9, s[52:55], s81 offen offset:3072 lds\n\t"
"v_mfma_f32_16x16x32_bf16 a[64:67], v[112:115], v[96:99], a[64:67]\n\t"
"v_accvgpr_read_b32 v132, a188\n\t"
"ds_read_b128 v[36:39], v0 offset:2048\n\t"
"v_mfma_f32_16x16x32_bf16 a[68:71], v[116:119], v[96:99], a[68:71]\n\t"
"v_accvgpr_read_b32 v133, a189\n\t"
"v_mfma_f32_16x16x32_bf16 a[72:75], v[120:123], v[96:99], a[72:75]\n\t"
"ds_read_b128 v[40:43], v0 offset:4096\n\t"
"v_accvgpr_read_b32 v134, a190\n\t"
"v_mfma_f32_16x16x32_bf16 a[76:79], v[124:127], v[96:99], a[76:79]\n\t"
"s_add_u32 m0, s79, 53248\n\t"
"s_add_u32 s81, s77, 33408\n\t"
"buffer_load_dwordx4 v6, s[52:55], s81 offen offset:0 lds\n\t"
"v_mfma_f32_16x16x32_bf16 a[80:83], v[112:115], v[100:103], a[80:83]\n\t"
"v_accvgpr_read_b32 v135, a191\n\t"
"ds_read_b128 v[44:47], v0 offset:6144\n\t"
"v_mfma_f32_16x16x32_bf16 a[84:87], v[116:119], v[100:103], a[84:87]\n\t"
"v_cvt_pk_bf16_f32 v136, v128, v129\n\t"
"v_mfma_f32_16x16x32_bf16 a[88:91], v[120:123], v[100:103], a[88:91]\n\t"
"ds_read_b128 v[48:51], v0 offset:8192\n\t"
"v_cvt_pk_bf16_f32 v137, v130, v131\n\t"
"v_mfma_f32_16x16x32_bf16 a[92:95], v[124:127], v[100:103], a[92:95]\n\t"
"ds_read_b128 v[52:55], v0 offset:10240\n\t"
"v_mfma_f32_16x16x32_bf16 a[96:99], v[112:115], v[104:107], a[96:99]\n\t"
"v_cvt_pk_bf16_f32 v138, v132, v133\n\t"
"buffer_load_dwordx4 v7, s[52:55], s81 offen offset:1024 lds\n\t"
"v_mfma_f32_16x16x32_bf16 a[100:103], v[116:119], v[104:107], a[100:103]\n\t"
"v_cvt_pk_bf16_f32 v139, v134, v135\n\t"
"v_mfma_f32_16x16x32_bf16 a[104:107], v[120:123], v[104:107], a[104:107]\n\t"
"ds_read_b128 v[56:59], v0 offset:12288\n\t"
"s_add_u32 s82, s85, 8192\n\t"
"buffer_store_dwordx4 v[136:139], v14, s[72:75], s82 offen offset:3072 sc1\n\t"
"v_mfma_f32_16x16x32_bf16 a[108:111], v[124:127], v[104:107], a[108:111]\n\t"
"ds_read_b128 v[60:63], v0 offset:14336\n\t"
"s_waitcnt lgkmcnt(5)\n\t"
"v_mfma_f32_16x16x32_bf16 a[112:115], v[112:115], v[108:111], a[112:115]\n\t"
"v_mfma_f32_16x16x32_bf16 a[116:119], v[116:119], v[108:111], a[116:119]\n\t"
"v_mfma_f32_16x16x32_bf16 a[120:123], v[120:123], v[108:111], a[120:123]\n\t"
"v_mfma_f32_16x16x32_bf16 a[124:127], v[124:127], v[108:111], a[124:127]\n\t"
"s_mov_b32 s82, 0\n\t"
"s_waitcnt vmcnt(30)\n\t"
"LFP8_%=:\n\t"
"v_cmp_eq_u64 vcc, s[26:27], v[214:215]\n\t"
"s_nop 1\n\t"
"s_cmp_eq_u64 vcc, exec\n\t"
"s_cbranch_scc1 LFO9_%=\n\t"
"s_sleep 1\n\t"
"global_load_dwordx2 v[214:215], v212, s[42:43] sc1\n\t"
"s_waitcnt vmcnt(0)\n\t"
"s_add_u32 s82, s82, 1\n\t"
"s_cmp_lt_u32 s82, 0x40000\n\t"
"s_cbranch_scc1 LFP8_%=\n\t"
"LFO9_%=:\n\t"
"v_mfma_f32_16x16x32_bf16 a[0:3], v[64:67], v[32:35], a[0:3]\n\t"
"ds_read_b128 v[80:83], v1 offset:0\n\t"
"v_accvgpr_read_b32 v140, a192\n\t"
"v_mfma_f32_16x16x32_bf16 a[4:7], v[68:71], v[32:35], a[4:7]\n\t"
"buffer_load_dwordx4 v8, s[52:55], s81 offen offset:2048 lds\n\t"
"v_mfma_f32_16x16x32_bf16 a[8:11], v[72:75], v[32:35], a[8:11]\n\t"
"ds_read_b64_tr_b16 v[112:113], v2 offset:8192\n\t"
"v_accvgpr_read_b32 v141, a193\n\t"
"v_mfma_f32_16x16x32_bf16 a[12:15], v[76:79], v[32:35], a[12:15]\n\t"
"ds_read_b64_tr_b16 v[114:115], v2 offset:9216\n\t"
"v_mfma_f32_16x16x32_bf16 a[16:19], v[64:67], v[36:39], a[16:19]\n\t"
"ds_read_b64_tr_b16 v[116:117], v3 offset:8192\n\t"
"v_accvgpr_read_b32 v142, a194\n\t"
"v_mfma_f32_16x16x32_bf16 a[20:23], v[68:71], v[36:39], a[20:23]\n\t"
"buffer_load_dwordx4 v9, s[52:55], s81 offen offset:3072 lds\n\t"
"v_mfma_f32_16x16x32_bf16 a[24:27], v[72:75], v[36:39], a[24:27]\n\t"
"ds_read_b64_tr_b16 v[118:119], v3 offset:9216\n\t"
"v_accvgpr_read_b32 v143, a195\n\t"
"v_mfma_f32_16x16x32_bf16 a[28:31], v[76:79], v[36:39], a[28:31]\n\t"
"ds_read_b64_tr_b16 v[120:121], v4 offset:8192\n\t"
"v_mfma_f32_16x16x32_bf16 a[32:35], v[64:67], v[40:43], a[32:35]\n\t"
"ds_read_b64_tr_b16 v[122:123], v4 offset:9216\n\t"
"v_accvgpr_read_b32 v144, a208\n\t"
"v_mfma_f32_16x16x32_bf16 a[36:39], v[68:71], v[40:43], a[36:39]\n\t"
"ds_read_b64_tr_b16 v[124:125], v5 offset:8192\n\t"
"v_mfma_f32_16x16x32_bf16 a[40:43], v[72:75], v[40:43], a[40:43]\n\t"
"s_add_u32 m0, s80, 49152\n\t"
"s_add_u32 s81, s78, 2621440\n\t"
"buffer_load_dwordx4 v10, s[64:67], s81 offen offset:0 lds\n\t"
"v_accvgpr_read_b32 v145, a209\n\t"
"v_mfma_f32_16x16x32_bf16 a[44:47], v[76:79], v[40:43], a[44:47]\n\t"
"ds_read_b64_tr_b16 v[126:127], v5 offset:9216\n\t"
"s_waitcnt lgkmcnt(5)\n\t"
"v_mfma_f32_16x16x32_bf16 a[48:51], v[64:67], v[44:47], a[48:51]\n\t"
"ds_read_b128 v[84:87], v1 offset:2048\n\t"
"v_accvgpr_read_b32 v146, a210\n\t"
"v_mfma_f32_16x16x32_bf16 a[52:55], v[68:71], v[44:47], a[52:55]\n\t"
"ds_read_b128 v[88:91], v1 offset:4096\n\t"
"v_mfma_f32_16x16x32_bf16 a[56:59], v[72:75], v[44:47], a[56:59]\n\t"
"ds_read_b128 v[92:95], v1 offset:6144\n\t"
"v_mfma_f32_16x16x32_bf16 a[60:63], v[76:79], v[44:47], a[60:63]\n\t"
"buffer_load_dwordx4 v11, s[64:67], s81 offen offset:1024 lds\n\t"
"v_accvgpr_read_b32 v147, a211\n\t"
"v_mfma_f32_16x16x32_bf16 a[64:67], v[64:67], v[48:51], a[64:67]\n\t"
"ds_read_b128 v[96:99], v1 offset:8192\n\t"
"v_mfma_f32_16x16x32_bf16 a[68:71], v[68:71], v[48:51], a[68:71]\n\t"
"ds_read_b128 v[100:103], v1 offset:10240\n\t"
"v_cvt_pk_bf16_f32 v148, v140, v141\n\t"
"v_mfma_f32_16x16x32_bf16 a[72:75], v[72:75], v[48:51], a[72:75]\n\t"
"ds_read_b128 v[104:107], v1 offset:12288\n\t"
"v_mfma_f32_16x16x32_bf16 a[76:79], v[76:79], v[48:51], a[76:79]\n\t"
"buffer_load_dwordx4 v12, s[64:67], s81 offen offset:2048 lds\n\t"
"v_cvt_pk_bf16_f32 v149, v142, v143\n\t"
"v_mfma_f32_16x16x32_bf16 a[80:83], v[64:67], v[52:55], a[80:83]\n\t"
"ds_read_b128 v[108:111], v1 offset:14336\n\t"
"v_mfma_f32_16x16x32_bf16 a[84:87], v[68:71], v[52:55], a[84:87]\n\t"
"v_add_u32 v0, -98304, v0\n\t"
"v_cvt_pk_bf16_f32 v150, v144, v145\n\t"
"v_mfma_f32_16x16x32_bf16 a[88:91], v[72:75], v[52:55], a[88:91]\n\t"
"v_add_u32 v1, -98304, v1\n\t"
"v_mfma_f32_16x16x32_bf16 a[92:95], v[76:79], v[52:55], a[92:95]\n\t"
"v_add_u32 v2, -98304, v2\n\t"
"v_cvt_pk_bf16_f32 v151, v146, v147\n\t"
"v_mfma_f32_16x16x32_bf16 a[96:99], v[64:67], v[56:59], a[96:99]\n\t"
"buffer_load_dwordx4 v13, s[64:67], s81 offen offset:3072 lds\n\t"
"v_mfma_f32_16x16x32_bf16 a[100:103], v[68:71], v[56:59], a[100:103]\n\t"
"v_add_u32 v3, -98304, v3\n\t"
"s_add_u32 s82, s85, 16384\n\t"
"buffer_store_dwordx4 v[148:151], v14, s[72:75], s82 offen offset:0 sc1\n\t"
"v_mfma_f32_16x16x32_bf16 a[104:107], v[72:75], v[56:59], a[104:107]\n\t"
"v_add_u32 v4, -98304, v4\n\t"
"v_mfma_f32_16x16x32_bf16 a[108:111], v[76:79], v[56:59], a[108:111]\n\t"
"v_add_u32 v5, -98304, v5\n\t"
"v_mfma_f32_16x16x32_bf16 a[112:115], v[64:67], v[60:63], a[112:115]\n\t"
"v_mfma_f32_16x16x32_bf16 a[116:119], v[68:71], v[60:63], a[116:119]\n\t"
"v_mfma_f32_16x16x32_bf16 a[120:123], v[72:75], v[60:63], a[120:123]\n\t"
"v_mfma_f32_16x16x32_bf16 a[124:127], v[76:79], v[60:63], a[124:127]\n\t"
"s_waitcnt vmcnt(15) lgkmcnt(0)\n\t"
"s_barrier\n\t"
"v_mfma_f32_16x16x32_bf16 a[0:3], v[112:115], v[80:83], a[0:3]\n\t"
"ds_read_b128 v[32:35], v0 offset:0\n\t"
"v_accvgpr_read_b32 v152, a196\n\t"
"v_mfma_f32_16x16x32_bf16 a[4:7], v[116:119], v[80:83], a[4:7]\n\t"
"s_add_u32 m0, s79, 98304\n\t"
"s_add_u32 s81, s77, 768\n\t"
"buffer_load_dwordx4 v6, s[52:55], s81 offen offset:0 lds\n\t"
"v_mfma_f32_16x16x32_bf16 a[8:11], v[120:123], v[80:83], a[8:11]\n\t"
"v_accvgpr_read_b32 v153, a197\n\t"
"v_mfma_f32_16x16x32_bf16 a[12:15], v[124:127], v[80:83], a[12:15]\n\t"
"ds_read_b64_tr_b16 v[64:65], v2 offset:0\n\t"
"v_mfma_f32_16x16x32_bf16 a[16:19], v[112:115], v[84:87], a[16:19]\n\t"
"ds_read_b64_tr_b16 v[66:67], v2 offset:1024\n\t"
"v_accvgpr_read_b32 v154, a198\n\t"
"v_mfma_f32_16x16x32_bf16 a[20:23], v[116:119], v[84:87], a[20:23]\n\t"
"buffer_load_dwordx4 v7, s[52:55], s81 offen offset:1024 lds\n\t"
"v_mfma_f32_16x16x32_bf16 a[24:27], v[120:123], v[84:87], a[24:27]\n\t"
"ds_read_b64_tr_b16 v[68:69], v3 offset:0\n\t"
"v_mfma_f32_16x16x32_bf16 a[28:31], v[124:127], v[84:87], a[28:31]\n\t"
"v_accvgpr_read_b32 v155, a199\n\t"
"v_mfma_f32_16x16x32_bf16 a[32:35], v[112:115], v[88:91], a[32:35]\n\t"
"ds_read_b64_tr_b16 v[70:71], v3 offset:1024\n\t"
"v_accvgpr_read_b32 v156, a212\n\t"
"v_mfma_f32_16x16x32_bf16 a[36:39], v[116:119], v[88:91], a[36:39]\n\t"
"ds_read_b64_tr_b16 v[72:73], v4 offset:0\n\t"
"v_mfma_f32_16x16x32_bf16 a[40:43], v[120:123], v[88:91], a[40:43]\n\t"
"buffer_load_dwordx4 v8, s[52:55], s81 offen offset:2048 lds\n\t"
"v_mfma_f32_16x16x32_bf16 a[44:47], v[124:127], v[88:91], a[44:47]\n\t"
"v_accvgpr_read_b32 v157, a213\n\t"
"v_mfma_f32_16x16x32_bf16 a[48:51], v[112:115], v[92:95], a[48:51]\n\t"
"ds_read_b64_tr_b16 v[74:75], v4 offset:1024\n\t"
"ds_read_b64_tr_b16 v[76:77], v5 offset:0\n\t"
"v_mfma_f32_16x16x32_bf16 a[52:55], v[116:119], v[92:95], a[52:55]\n\t"
"v_accvgpr_read_b32 v158, a214\n\t"
"v_mfma_f32_16x16x32_bf16 a[56:59], v[120:123], v[92:95], a[56:59]\n\t"
"ds_read_b64_tr_b16 v[78:79], v5 offset:1024\n\t"
"v_mfma_f32_16x16x32_bf16 a[60:63], v[124:127], v[92:95], a[60:63]\n\t"
"v_accvgpr_read_b32 v159, a215\n\t"
"v_mfma_f32_16x16x32_bf16 a[64:67], v[112:115], v[96:99], a[64:67]\n\t"
"buffer_load_dwordx4 v9, s[52:55], s81 offen offset:3072 lds\n\t"
"ds_read_b128 v[36:39], v0 offset:2048\n\t"
"v_mfma_f32_16x16x32_bf16 a[68:71], v[116:119], v[96:99], a[68:71]\n\t"
"v_cvt_pk_bf16_f32 v160, v152, v153\n\t"
"v_mfma_f32_16x16x32_bf16 a[72:75], v[120:123], v[96:99], a[72:75]\n\t"
"ds_read_b128 v[40:43], v0 offset:4096\n\t"
"v_mfma_f32_16x16x32_bf16 a[76:79], v[124:127], v[96:99], a[76:79]\n\t"
"s_add_u32 m0, s79, 102400\n\t"
"s_add_u32 s81, s77, 33536\n\t"
"buffer_load_dwordx4 v6, s[52:55], s81 offen offset:0 lds\n\t"
"v_mfma_f32_16x16x32_bf16 a[80:83], v[112:115], v[100:103], a[80:83]\n\t"
"v_cvt_pk_bf16_f32 v161, v154, v155\n\t"
"ds_read_b128 v[44:47], v0 offset:6144\n\t"
"v_mfma_f32_16x16x32_bf16 a[84:87], v[116:119], v[100:103], a[84:87]\n\t"
"v_cvt_pk_bf16_f32 v162, v156, v157\n\t"
"v_mfma_f32_16x16x32_bf16 a[88:91], v[120:123], v[100:103], a[88:91]\n\t"
"ds_read_b128 v[48:51], v0 offset:8192\n\t"
"v_mfma_f32_16x16x32_bf16 a[92:95], v[124:127], v[100:103], a[92:95]\n\t"
"ds_read_b128 v[52:55], v0 offset:10240\n\t"
"v_mfma_f32_16x16x32_bf16 a[96:99], v[112:115], v[104:107], a[96:99]\n\t"
"v_cvt_pk_bf16_f32 v163, v158, v159\n\t"
"buffer_load_dwordx4 v7, s[52:55], s81 offen offset:1024 lds\n\t"
"v_mfma_f32_16x16x32_bf16 a[100:103], v[116:119], v[104:107], a[100:103]\n\t"
"ds_read_b128 v[56:59], v0 offset:12288\n\t"
"v_mfma_f32_16x16x32_bf16 a[104:107], v[120:123], v[104:107], a[104:107]\n\t"
"s_add_u32 s82, s85, 16384\n\t"
"buffer_store_dwordx4 v[160:163], v14, s[72:75], s82 offen offset:1024 sc1\n\t"
"v_mfma_f32_16x16x32_bf16 a[108:111], v[124:127], v[104:107], a[108:111]\n\t"
"ds_read_b128 v[60:63], v0 offset:14336\n\t"
"s_waitcnt lgkmcnt(5)\n\t"
"v_mfma_f32_16x16x32_bf16 a[112:115], v[112:115], v[108:111], a[112:115]\n\t"
"v_mfma_f32_16x16x32_bf16 a[116:119], v[116:119], v[108:111], a[116:119]\n\t"
"v_mfma_f32_16x16x32_bf16 a[120:123], v[120:123], v[108:111], a[120:123]\n\t"
"v_mfma_f32_16x16x32_bf16 a[124:127], v[124:127], v[108:111], a[124:127]\n\t"
"v_mfma_f32_16x16x32_bf16 a[0:3], v[64:67], v[32:35], a[0:3]\n\t"
"ds_read_b128 v[80:83], v1 offset:0\n\t"
"v_accvgpr_read_b32 v164, a200\n\t"
"v_mfma_f32_16x16x32_bf16 a[4:7], v[68:71], v[32:35], a[4:7]\n\t"
"buffer_load_dwordx4 v8, s[52:55], s81 offen offset:2048 lds\n\t"
"v_accvgpr_read_b32 v165, a201\n\t"
"v_mfma_f32_16x16x32_bf16 a[8:11], v[72:75], v[32:35], a[8:11]\n\t"
"ds_read_b64_tr_b16 v[112:113], v2 offset:8192\n\t"
"v_accvgpr_read_b32 v166, a202\n\t"
"v_mfma_f32_16x16x32_bf16 a[12:15], v[76:79], v[32:35], a[12:15]\n\t"
"ds_read_b64_tr_b16 v[114:115], v2 offset:9216\n\t"
"v_mfma_f32_16x16x32_bf16 a[16:19], v[64:67], v[36:39], a[16:19]\n\t"
"ds_read_b64_tr_b16 v[116:117], v3 offset:8192\n\t"
"v_accvgpr_read_b32 v167, a203\n\t"
"v_mfma_f32_16x16x32_bf16 a[20:23], v[68:71], v[36:39], a[20:23]\n\t"
"buffer_load_dwordx4 v9, s[52:55], s81 offen offset:3072 lds\n\t"
"v_accvgpr_read_b32 v168, a216\n\t"
"v_mfma_f32_16x16x32_bf16 a[24:27], v[72:75], v[36:39], a[24:27]\n\t"
"ds_read_b64_tr_b16 v[118:119], v3 offset:9216\n\t"
"v_mfma_f32_16x16x32_bf16 a[28:31], v[76:79], v[36:39], a[28:31]\n\t"
"ds_read_b64_tr_b16 v[120:121], v4 offset:8192\n\t"
"v_accvgpr_read_b32 v169, a217\n\t"
"v_mfma_f32_16x16x32_bf16 a[32:35], v[64:67], v[40:43], a[32:35]\n\t"
"ds_read_b64_tr_b16 v[122:123], v4 offset:9216\n\t"
"v_accvgpr_read_b32 v170, a218\n\t"
"v_mfma_f32_16x16x32_bf16 a[36:39], v[68:71], v[40:43], a[36:39]\n\t"
"ds_read_b64_tr_b16 v[124:125], v5 offset:8192\n\t"
"v_mfma_f32_16x16x32_bf16 a[40:43], v[72:75], v[40:43], a[40:43]\n\t"
"s_add_u32 m0, s80, 98304\n\t"
"s_add_u32 s81, s78, 3145728\n\t"
"buffer_load_dwordx4 v10, s[64:67], s81 offen offset:0 lds\n\t"
"v_accvgpr_read_b32 v171, a219\n\t"
"v_mfma_f32_16x16x32_bf16 a[44:47], v[76:79], v[40:43], a[44:47]\n\t"
"ds_read_b64_tr_b16 v[126:127], v5 offset:9216\n\t"
"v_cvt_pk_bf16_f32 v172, v164, v165\n\t"
"s_waitcnt lgkmcnt(5)\n\t"
"v_mfma_f32_16x16x32_bf16 a[48:51], v[64:67], v[44:47], a[48:51]\n\t"
"ds_read_b128 v[84:87], v1 offset:2048\n\t"
"v_mfma_f32_16x16x32_bf16 a[52:55], v[68:71], v[44:47], a[52:55]\n\t"
"ds_read_b128 v[88:91], v1 offset:4096\n\t"
"v_cvt_pk_bf16_f32 v173, v166, v167\n\t"
"v_mfma_f32_16x16x32_bf16 a[56:59], v[72:75], v[44:47], a[56:59]\n\t"
"ds_read_b128 v[92:95], v1 offset:6144\n\t"
"v_cvt_pk_bf16_f32 v174, v168, v169\n\t"
"v_mfma_f32_16x16x32_bf16 a[60:63], v[76:79], v[44:47], a[60:63]\n\t"
"buffer_load_dwordx4 v11, s[64:67], s81 offen offset:1024 lds\n\t"
"v_mfma_f32_16x16x32_bf16 a[64:67], v[64:67], v[48:51], a[64:67]\n\t"
"ds_read_b128 v[96:99], v1 offset:8192\n\t"
"v_cvt_pk_bf16_f32 v175, v170, v171\n\t"
"v_mfma_f32_16x16x32_bf16 a[68:71], v[68:71], v[48:51], a[68:71]\n\t"
"ds_read_b128 v[100:103], v1 offset:10240\n\t"
"s_add_u32 s82, s85, 16384\n\t"
"buffer_store_dwordx4 v[172:175], v14, s[72:75], s82 offen offset:2048 sc1\n\t"
"v_mfma_f32_16x16x32_bf16 a[72:75], v[72:75], v[48:51], a[72:75]\n\t"
"ds_read_b128 v[104:107], v1 offset:12288\n\t"
"v_mfma_f32_16x16x32_bf16 a[76:79], v[76:79], v[48:51], a[76:79]\n\t"
"buffer_load_dwordx4 v12, s[64:67], s81 offen offset:2048 lds\n\t"
"v_accvgpr_read_b32 v176, a204\n\t"
"v_mfma_f32_16x16x32_bf16 a[80:83], v[64:67], v[52:55], a[80:83]\n\t"
"ds_read_b128 v[108:111], v1 offset:14336\n\t"
"v_accvgpr_read_b32 v177, a205\n\t"
"v_mfma_f32_16x16x32_bf16 a[84:87], v[68:71], v[52:55], a[84:87]\n\t"
"v_add_u32 v0, 49152, v0\n\t"
"v_mfma_f32_16x16x32_bf16 a[88:91], v[72:75], v[52:55], a[88:91]\n\t"
"v_add_u32 v1, 49152, v1\n\t"
"v_accvgpr_read_b32 v178, a206\n\t"
"v_mfma_f32_16x16x32_bf16 a[92:95], v[76:79], v[52:55], a[92:95]\n\t"
"v_add_u32 v2, 49152, v2\n\t"
"v_accvgpr_read_b32 v179, a207\n\t"
"v_mfma_f32_16x16x32_bf16 a[96:99], v[64:67], v[56:59], a[96:99]\n\t"
"buffer_load_dwordx4 v13, s[64:67], s81 offen offset:3072 lds\n\t"
"v_mfma_f32_16x16x32_bf16 a[100:103], v[68:71], v[56:59], a[100:103]\n\t"
"v_add_u32 v3, 49152, v3\n\t"
"v_accvgpr_read_b32 v180, a220\n\t"
"v_mfma_f32_16x16x32_bf16 a[104:107], v[72:75], v[56:59], a[104:107]\n\t"
"v_add_u32 v4, 49152, v4\n\t"
"v_accvgpr_read_b32 v181, a221\n\t"
"v_mfma_f32_16x16x32_bf16 a[108:111], v[76:79], v[56:59], a[108:111]\n\t"
"v_add_u32 v5, 49152, v5\n\t"
"v_mfma_f32_16x16x32_bf16 a[112:115], v[64:67], v[60:63], a[112:115]\n\t"
"v_mfma_f32_16x16x32_bf16 a[116:119], v[68:71], v[60:63], a[116:119]\n\t"
"v_mfma_f32_16x16x32_bf16 a[120:123], v[72:75], v[60:63], a[120:123]\n\t"
"v_mfma_f32_16x16x32_bf16 a[124:127], v[76:79], v[60:63], a[124:127]\n\t"
"s_waitcnt vmcnt(15) lgkmcnt(0)\n\t"
"s_barrier\n\t"
"v_mfma_f32_16x16x32_bf16 a[0:3], v[112:115], v[80:83], a[0:3]\n\t"
"ds_read_b128 v[32:35], v0 offset:0\n\t"
"v_accvgpr_read_b32 v182, a222\n\t"
"v_mfma_f32_16x16x32_bf16 a[4:7], v[116:119], v[80:83], a[4:7]\n\t"
"s_add_u32 m0, s79, 0\n\t"
"s_add_u32 s81, s77, 896\n\t"
"buffer_load_dwordx4 v6, s[52:55], s81 offen offset:0 lds\n\t"
"v_mfma_f32_16x16x32_bf16 a[8:11], v[120:123], v[80:83], a[8:11]\n\t"
"v_accvgpr_read_b32 v183, a223\n\t"
"ds_read_b64_tr_b16 v[64:65], v2 offset:0\n\t"
"v_mfma_f32_16x16x32_bf16 a[12:15], v[124:127], v[80:83], a[12:15]\n\t"
"v_cvt_pk_bf16_f32 v184, v176, v177\n\t"
"v_mfma_f32_16x16x32_bf16 a[16:19], v[112:115], v[84:87], a[16:19]\n\t"
"ds_read_b64_tr_b16 v[66:67], v2 offset:1024\n\t"
"v_cvt_pk_bf16_f32 v185, v178, v179\n\t"
"v_mfma_f32_16x16x32_bf16 a[20:23], v[116:119], v[84:87], a[20:23]\n\t"
"buffer_load_dwordx4 v7, s[52:55], s81 offen offset:1024 lds\n\t"
"v_mfma_f32_16x16x32_bf16 a[24:27], v[120:123], v[84:87], a[24:27]\n\t"
"v_cvt_pk_bf16_f32 v186, v180, v181\n\t"
"ds_read_b64_tr_b16 v[68:69], v3 offset:0\n\t"
"v_mfma_f32_16x16x32_bf16 a[28:31], v[124:127], v[84:87], a[28:31]\n\t"
"v_cvt_pk_bf16_f32 v187, v182, v183\n\t"
"v_mfma_f32_16x16x32_bf16 a[32:35], v[112:115], v[88:91], a[32:35]\n\t"
"ds_read_b64_tr_b16 v[70:71], v3 offset:1024\n\t"
"s_add_u32 s82, s85, 16384\n\t"
"buffer_store_dwordx4 v[184:187], v14, s[72:75], s82 offen offset:3072 sc1\n\t"
"v_mfma_f32_16x16x32_bf16 a[36:39], v[116:119], v[88:91], a[36:39]\n\t"
"ds_read_b64_tr_b16 v[72:73], v4 offset:0\n\t"
"v_mfma_f32_16x16x32_bf16 a[40:43], v[120:123], v[88:91], a[40:43]\n\t"
"v_accvgpr_read_b32 v188, a224\n\t"
"buffer_load_dwordx4 v8, s[52:55], s81 offen offset:2048 lds\n\t"
"v_mfma_f32_16x16x32_bf16 a[44:47], v[124:127], v[88:91], a[44:47]\n\t"
"v_accvgpr_read_b32 v189, a225\n\t"
"v_mfma_f32_16x16x32_bf16 a[48:51], v[112:115], v[92:95], a[48:51]\n\t"
"ds_read_b64_tr_b16 v[74:75], v4 offset:1024\n\t"
"v_accvgpr_read_b32 v190, a226\n\t"
"v_mfma_f32_16x16x32_bf16 a[52:55], v[116:119], v[92:95], a[52:55]\n\t"
"ds_read_b64_tr_b16 v[76:77], v5 offset:0\n\t"
"v_mfma_f32_16x16x32_bf16 a[56:59], v[120:123], v[92:95], a[56:59]\n\t"
"ds_read_b64_tr_b16 v[78:79], v5 offset:1024\n\t"
"v_accvgpr_read_b32 v191, a227\n\t"
"v_mfma_f32_16x16x32_bf16 a[60:63], v[124:127], v[92:95], a[60:63]\n\t"
"buffer_load_dwordx4 v9, s[52:55], s81 offen offset:3072 lds\n\t"
"v_mfma_f32_16x16x32_bf16 a[64:67], v[112:115], v[96:99], a[64:67]\n\t"
"v_accvgpr_read_b32 v192, a240\n\t"
"ds_read_b128 v[36:39], v0 offset:2048\n\t"
"v_mfma_f32_16x16x32_bf16 a[68:71], v[116:119], v[96:99], a[68:71]\n\t"
"v_accvgpr_read_b32 v193, a241\n\t"
"v_mfma_f32_16x16x32_bf16 a[72:75], v[120:123], v[96:99], a[72:75]\n\t"
"ds_read_b128 v[40:43], v0 offset:4096\n\t"
"v_accvgpr_read_b32 v194, a242\n\t"
"v_mfma_f32_16x16x32_bf16 a[76:79], v[124:127], v[96:99], a[76:79]\n\t"
"s_add_u32 m0, s79, 4096\n\t"
"s_add_u32 s81, s77, 33664\n\t"
"buffer_load_dwordx4 v6, s[52:55], s81 offen offset:0 lds\n\t"
"v_mfma_f32_16x16x32_bf16 a[80:83], v[112:115], v[100:103], a[80:83]\n\t"
"v_accvgpr_read_b32 v195, a243\n\t"
"ds_read_b128 v[44:47], v0 offset:6144\n\t"
"v_mfma_f32_16x16x32_bf16 a[84:87], v[116:119], v[100:103], a[84:87]\n\t"
"v_cvt_pk_bf16_f32 v196, v188, v189\n\t"
"v_mfma_f32_16x16x32_bf16 a[88:91], v[120:123], v[100:103], a[88:91]\n\t"
"ds_read_b128 v[48:51], v0 offset:8192\n\t"
"v_cvt_pk_bf16_f32 v197, v190, v191\n\t"
"v_mfma_f32_16x16x32_bf16 a[92:95], v[124:127], v[100:103], a[92:95]\n\t"
"ds_read_b128 v[52:55], v0 offset:10240\n\t"
"v_mfma_f32_16x16x32_bf16 a[96:99], v[112:115], v[104:107], a[96:99]\n\t"
"v_cvt_pk_bf16_f32 v198, v192, v193\n\t"
"buffer_load_dwordx4 v7, s[52:55], s81 offen offset:1024 lds\n\t"
"v_mfma_f32_16x16x32_bf16 a[100:103], v[116:119], v[104:107], a[100:103]\n\t"
"v_cvt_pk_bf16_f32 v199, v194, v195\n\t"
"v_mfma_f32_16x16x32_bf16 a[104:107], v[120:123], v[104:107], a[104:107]\n\t"
"ds_read_b128 v[56:59], v0 offset:12288\n\t"
"s_add_u32 s82, s85, 24576\n\t"
"buffer_store_dwordx4 v[196:199], v14, s[72:75], s82 offen offset:0 sc1\n\t"
"v_mfma_f32_16x16x32_bf16 a[108:111], v[124:127], v[104:107], a[108:111]\n\t"
"ds_read_b128 v[60:63], v0 offset:14336\n\t"
"s_waitcnt lgkmcnt(5)\n\t"
"v_mfma_f32_16x16x32_bf16 a[112:115], v[112:115], v[108:111], a[112:115]\n\t"
"v_mfma_f32_16x16x32_bf16 a[116:119], v[116:119], v[108:111], a[116:119]\n\t"
"v_mfma_f32_16x16x32_bf16 a[120:123], v[120:123], v[108:111], a[120:123]\n\t"
"v_mfma_f32_16x16x32_bf16 a[124:127], v[124:127], v[108:111], a[124:127]\n\t"
"v_mfma_f32_16x16x32_bf16 a[0:3], v[64:67], v[32:35], a[0:3]\n\t"
"ds_read_b128 v[80:83], v1 offset:0\n\t"
"v_accvgpr_read_b32 v200, a228\n\t"
"v_mfma_f32_16x16x32_bf16 a[4:7], v[68:71], v[32:35], a[4:7]\n\t"
"buffer_load_dwordx4 v8, s[52:55], s81 offen offset:2048 lds\n\t"
"v_accvgpr_read_b32 v201, a229\n\t"
"v_mfma_f32_16x16x32_bf16 a[8:11], v[72:75], v[32:35], a[8:11]\n\t"
"ds_read_b64_tr_b16 v[112:113], v2 offset:8192\n\t"
"v_accvgpr_read_b32 v202, a230\n\t"
"v_mfma_f32_16x16x32_bf16 a[12:15], v[76:79], v[32:35], a[12:15]\n\t"
"ds_read_b64_tr_b16 v[114:115], v2 offset:9216\n\t"
"v_mfma_f32_16x16x32_bf16 a[16:19], v[64:67], v[36:39], a[16:19]\n\t"
"ds_read_b64_tr_b16 v[116:117], v3 offset:8192\n\t"
"v_accvgpr_read_b32 v203, a231\n\t"
"v_mfma_f32_16x16x32_bf16 a[20:23], v[68:71], v[36:39], a[20:23]\n\t"
"buffer_load_dwordx4 v9, s[52:55], s81 offen offset:3072 lds\n\t"
"v_accvgpr_read_b32 v204, a244\n\t"
"v_mfma_f32_16x16x32_bf16 a[24:27], v[72:75], v[36:39], a[24:27]\n\t"
"ds_read_b64_tr_b16 v[118:119], v3 offset:9216\n\t"
"v_mfma_f32_16x16x32_bf16 a[28:31], v[76:79], v[36:39], a[28:31]\n\t"
"ds_read_b64_tr_b16 v[120:121], v4 offset:8192\n\t"
"v_accvgpr_read_b32 v205, a245\n\t"
"v_mfma_f32_16x16x32_bf16 a[32:35], v[64:67], v[40:43], a[32:35]\n\t"
"ds_read_b64_tr_b16 v[122:123], v4 offset:9216\n\t"
"v_accvgpr_read_b32 v206, a246\n\t"
"v_mfma_f32_16x16x32_bf16 a[36:39], v[68:71], v[40:43], a[36:39]\n\t"
"ds_read_b64_tr_b16 v[124:125], v5 offset:8192\n\t"
"v_mfma_f32_16x16x32_bf16 a[40:43], v[72:75], v[40:43], a[40:43]\n\t"
"s_add_u32 m0, s80, 0\n\t"
"s_add_u32 s81, s78, 3670016\n\t"
"buffer_load_dwordx4 v10, s[64:67], s81 offen offset:0 lds\n\t"
"v_accvgpr_read_b32 v207, a247\n\t"
"v_mfma_f32_16x16x32_bf16 a[44:47], v[76:79], v[40:43], a[44:47]\n\t"
"ds_read_b64_tr_b16 v[126:127], v5 offset:9216\n\t"
"v_cvt_pk_bf16_f32 v208, v200, v201\n\t"
"s_waitcnt lgkmcnt(5)\n\t"
"v_mfma_f32_16x16x32_bf16 a[48:51], v[64:67], v[44:47], a[48:51]\n\t"
"ds_read_b128 v[84:87], v1 offset:2048\n\t"
"v_mfma_f32_16x16x32_bf16 a[52:55], v[68:71], v[44:47], a[52:55]\n\t"
"ds_read_b128 v[88:91], v1 offset:4096\n\t"
"v_cvt_pk_bf16_f32 v209, v202, v203\n\t"
"v_mfma_f32_16x16x32_bf16 a[56:59], v[72:75], v[44:47], a[56:59]\n\t"
"ds_read_b128 v[92:95], v1 offset:6144\n\t"
"v_cvt_pk_bf16_f32 v210, v204, v205\n\t"
"v_mfma_f32_16x16x32_bf16 a[60:63], v[76:79], v[44:47], a[60:63]\n\t"
"buffer_load_dwordx4 v11, s[64:67], s81 offen offset:1024 lds\n\t"
"v_mfma_f32_16x16x32_bf16 a[64:67], v[64:67], v[48:51], a[64:67]\n\t"
"ds_read_b128 v[96:99], v1 offset:8192\n\t"
"v_cvt_pk_bf16_f32 v211, v206, v207\n\t"
"v_mfma_f32_16x16x32_bf16 a[68:71], v[68:71], v[48:51], a[68:71]\n\t"
"ds_read_b128 v[100:103], v1 offset:10240\n\t"
"s_add_u32 s82, s85, 24576\n\t"
"buffer_store_dwordx4 v[208:211], v14, s[72:75], s82 offen offset:1024 sc1\n\t"
"v_mfma_f32_16x16x32_bf16 a[72:75], v[72:75], v[48:51], a[72:75]\n\t"
"ds_read_b128 v[104:107], v1 offset:12288\n\t"
"v_mfma_f32_16x16x32_bf16 a[76:79], v[76:79], v[48:51], a[76:79]\n\t"
"buffer_load_dwordx4 v12, s[64:67], s81 offen offset:2048 lds\n\t"
"v_accvgpr_read_b32 v128, a232\n\t"
"v_mfma_f32_16x16x32_bf16 a[80:83], v[64:67], v[52:55], a[80:83]\n\t"
"ds_read_b128 v[108:111], v1 offset:14336\n\t"
"v_accvgpr_read_b32 v129, a233\n\t"
"v_mfma_f32_16x16x32_bf16 a[84:87], v[68:71], v[52:55], a[84:87]\n\t"
"v_add_u32 v0, 49152, v0\n\t"
"v_mfma_f32_16x16x32_bf16 a[88:91], v[72:75], v[52:55], a[88:91]\n\t"
"v_add_u32 v1, 49152, v1\n\t"
"v_accvgpr_read_b32 v130, a234\n\t"
"v_mfma_f32_16x16x32_bf16 a[92:95], v[76:79], v[52:55], a[92:95]\n\t"
"v_add_u32 v2, 49152, v2\n\t"
"v_accvgpr_read_b32 v131, a235\n\t"
"v_mfma_f32_16x16x32_bf16 a[96:99], v[64:67], v[56:59], a[96:99]\n\t"
"buffer_load_dwordx4 v13, s[64:67], s81 offen offset:3072 lds\n\t"
"v_mfma_f32_16x16x32_bf16 a[100:103], v[68:71], v[56:59], a[100:103]\n\t"
"v_add_u32 v3, 49152, v3\n\t"
"v_accvgpr_read_b32 v132, a248\n\t"
"v_mfma_f32_16x16x32_bf16 a[104:107], v[72:75], v[56:59], a[104:107]\n\t"
"v_add_u32 v4, 49152, v4\n\t"
"v_accvgpr_read_b32 v133, a249\n\t"
"v_mfma_f32_16x16x32_bf16 a[108:111], v[76:79], v[56:59], a[108:111]\n\t"
"v_add_u32 v5, 49152, v5\n\t"
"v_mfma_f32_16x16x32_bf16 a[112:115], v[64:67], v[60:63], a[112:115]\n\t"
"v_mfma_f32_16x16x32_bf16 a[116:119], v[68:71], v[60:63], a[116:119]\n\t"
"v_mfma_f32_16x16x32_bf16 a[120:123], v[72:75], v[60:63], a[120:123]\n\t"
"v_mfma_f32_16x16x32_bf16 a[124:127], v[76:79], v[60:63], a[124:127]\n\t"
"s_waitcnt vmcnt(15) lgkmcnt(0)\n\t"
"s_barrier\n\t"
"s_lshl_b32 s78, s46, 17\n\t"
"s_lshl_b32 s83, s39, 12\n\t"
"s_add_u32 s78, s78, s83\n\t"
"s_lshl_b32 s79, s39, 11\n\t"
"v_bfe_u32 v22, v15, 1, 2\n\t"
"v_bfe_u32 v23, v15, 4, 2\n\t"
"v_xor_b32 v22, v22, v23\n\t"
"v_lshlrev_b32 v22, 4, v22\n\t"
"v_lshrrev_b32 v23, 5, v15\n\t"
"v_lshl_add_u32 v22, v23, 9, v22\n\t"
"v_bfe_u32 v23, v15, 3, 2\n\t"
"v_lshl_add_u32 v22, v23, 6, v22\n\t"
"v_and_b32 v23, 1, v15\n\t"
"v_lshl_add_u32 v10, v23, 10, v22\n\t"
"v_mfma_f32_16x16x32_bf16 a[0:3], v[112:115], v[80:83], a[0:3]\n\t"
"ds_read_b128 v[32:35], v0 offset:0\n\t"
"v_accvgpr_read_b32 v134, a250\n\t"
"v_mfma_f32_16x16x32_bf16 a[4:7], v[116:119], v[80:83], a[4:7]\n\t"
"s_add_u32 m0, s79, 65536\n\t"
"s_add_u32 s81, s76, 0\n\t"
"buffer_load_dwordx4 v6, s[48:51], s81 offen offset:0 lds\n\t"
"v_accvgpr_read_b32 v135, a251\n\t"
"v_mfma_f32_16x16x32_bf16 a[8:11], v[120:123], v[80:83], a[8:11]\n\t"
"ds_read_b64_tr_b16 v[64:65], v2 offset:0\n\t"
"v_mfma_f32_16x16x32_bf16 a[12:15], v[124:127], v[80:83], a[12:15]\n\t"
"v_cvt_pk_bf16_f32 v136, v128, v129\n\t"
"ds_read_b64_tr_b16 v[66:67], v2 offset:1024\n\t"
"v_mfma_f32_16x16x32_bf16 a[16:19], v[112:115], v[84:87], a[16:19]\n\t"
"v_cvt_pk_bf16_f32 v137, v130, v131\n\t"
"v_mfma_f32_16x16x32_bf16 a[20:23], v[116:119], v[84:87], a[20:23]\n\t"
"buffer_load_dwordx4 v7, s[48:51], s81 offen offset:1024 lds\n\t"
"v_cvt_pk_bf16_f32 v138, v132, v133\n\t"
"v_mfma_f32_16x16x32_bf16 a[24:27], v[120:123], v[84:87], a[24:27]\n\t"
"ds_read_b64_tr_b16 v[68:69], v3 offset:0\n\t"
"v_mfma_f32_16x16x32_bf16 a[28:31], v[124:127], v[84:87], a[28:31]\n\t"
"ds_read_b64_tr_b16 v[70:71], v3 offset:1024\n\t"
"v_cvt_pk_bf16_f32 v139, v134, v135\n\t"
"v_mfma_f32_16x16x32_bf16 a[32:35], v[112:115], v[88:91], a[32:35]\n\t"
"s_add_u32 m0, s79, 73728\n\t"
"s_add_u32 s81, s76, 128\n\t"
"buffer_load_dwordx4 v6, s[48:51], s81 offen offset:0 lds\n\t"
"s_add_u32 s82, s85, 24576\n\t"
"buffer_store_dwordx4 v[136:139], v14, s[72:75], s82 offen offset:2048 sc1\n\t"
"v_mfma_f32_16x16x32_bf16 a[36:39], v[116:119], v[88:91], a[36:39]\n\t"
"ds_read_b64_tr_b16 v[72:73], v4 offset:0\n\t"
"v_mfma_f32_16x16x32_bf16 a[40:43], v[120:123], v[88:91], a[40:43]\n\t"
"v_accvgpr_read_b32 v140, a236\n\t"
"ds_read_b64_tr_b16 v[74:75], v4 offset:1024\n\t"
"v_mfma_f32_16x16x32_bf16 a[44:47], v[124:127], v[88:91], a[44:47]\n\t"
"v_accvgpr_read_b32 v141, a237\n\t"
"v_mfma_f32_16x16x32_bf16 a[48:51], v[112:115], v[92:95], a[48:51]\n\t"
"buffer_load_dwordx4 v7, s[48:51], s81 offen offset:1024 lds\n\t"
"v_accvgpr_read_b32 v142, a238\n\t"
"v_mfma_f32_16x16x32_bf16 a[52:55], v[116:119], v[92:95], a[52:55]\n\t"
"ds_read_b64_tr_b16 v[76:77], v5 offset:0\n\t"
"v_mfma_f32_16x16x32_bf16 a[56:59], v[120:123], v[92:95], a[56:59]\n\t"
"ds_read_b64_tr_b16 v[78:79], v5 offset:1024\n\t"
"v_accvgpr_read_b32 v143, a239\n\t"
"v_mfma_f32_16x16x32_bf16 a[60:63], v[124:127], v[92:95], a[60:63]\n\t"
"s_lshl_b32 s81, s79, 1\n\t"
"s_add_u32 m0, s81, 81920\n\t"
"s_add_u32 s81, s78, 4096\n\t"
"buffer_load_dwordx4 v10, s[60:63], s81 offen offset:0 sc1 lds\n\t"
"v_accvgpr_read_b32 v144, a252\n\t"
"v_mfma_f32_16x16x32_bf16 a[64:67], v[112:115], v[96:99], a[64:67]\n\t"
"ds_read_b128 v[36:39], v0 offset:2048\n\t"
"v_mfma_f32_16x16x32_bf16 a[68:71], v[116:119], v[96:99], a[68:71]\n\t"
"v_accvgpr_read_b32 v145, a253\n\t"
"ds_read_b128 v[40:43], v0 offset:4096\n\t"
"v_mfma_f32_16x16x32_bf16 a[72:75], v[120:123], v[96:99], a[72:75]\n\t"
"v_accvgpr_read_b32 v146, a254\n\t"
"v_mfma_f32_16x16x32_bf16 a[76:79], v[124:127], v[96:99], a[76:79]\n\t"
"s_add_u32 s81, s78, 5120\n\t"
"buffer_load_dwordx4 v10, s[60:63], s81 offen offset:1024 sc1 lds\n\t"
"v_accvgpr_read_b32 v147, a255\n\t"
"v_mfma_f32_16x16x32_bf16 a[80:83], v[112:115], v[100:103], a[80:83]\n\t"
"ds_read_b128 v[44:47], v0 offset:6144\n\t"
"v_mfma_f32_16x16x32_bf16 a[84:87], v[116:119], v[100:103], a[84:87]\n\t"
"ds_read_b128 v[48:51], v0 offset:8192\n\t"
"v_cvt_pk_bf16_f32 v148, v140, v141\n\t"
"v_mfma_f32_16x16x32_bf16 a[88:91], v[120:123], v[100:103], a[88:91]\n\t"
"s_add_u32 s81, s78, 2304\n\t"
"buffer_load_dwordx4 v10, s[60:63], s81 offen offset:2048 sc1 lds\n\t"
"v_cvt_pk_bf16_f32 v149, v142, v143\n\t"
"v_mfma_f32_16x16x32_bf16 a[92:95], v[124:127], v[100:103], a[92:95]\n\t"
"ds_read_b128 v[52:55], v0 offset:10240\n\t"
"v_mfma_f32_16x16x32_bf16 a[96:99], v[112:115], v[104:107], a[96:99]\n\t"
"v_cvt_pk_bf16_f32 v150, v144, v145\n\t"
"ds_read_b128 v[56:59], v0 offset:12288\n\t"
"v_mfma_f32_16x16x32_bf16 a[100:103], v[116:119], v[104:107], a[100:103]\n\t"
"v_cvt_pk_bf16_f32 v151, v146, v147\n\t"
"v_mfma_f32_16x16x32_bf16 a[104:107], v[120:123], v[104:107], a[104:107]\n\t"
"s_add_u32 s81, s78, 3328\n\t"
"buffer_load_dwordx4 v10, s[60:63], s81 offen offset:3072 sc1 lds\n\t"
"s_add_u32 s82, s85, 24576\n\t"
"buffer_store_dwordx4 v[148:151], v14, s[72:75], s82 offen offset:3072 sc1\n\t"
"v_mfma_f32_16x16x32_bf16 a[108:111], v[124:127], v[104:107], a[108:111]\n\t"
"ds_read_b128 v[60:63], v0 offset:14336\n\t"
"s_waitcnt lgkmcnt(4)\n\t"
"v_mfma_f32_16x16x32_bf16 a[112:115], v[112:115], v[108:111], a[112:115]\n\t"
"v_mfma_f32_16x16x32_bf16 a[116:119], v[116:119], v[108:111], a[116:119]\n\t"
"v_mfma_f32_16x16x32_bf16 a[120:123], v[120:123], v[108:111], a[120:123]\n\t"
"v_mfma_f32_16x16x32_bf16 a[124:127], v[124:127], v[108:111], a[124:127]\n\t"
"v_mfma_f32_16x16x32_bf16 a[0:3], v[64:67], v[32:35], a[0:3]\n\t"
"ds_read_b128 v[80:83], v1 offset:0\n\t"
"v_mfma_f32_16x16x32_bf16 a[4:7], v[68:71], v[32:35], a[4:7]\n\t"
"ds_read_b64_tr_b16 v[112:113], v2 offset:8192\n\t"
"v_mfma_f32_16x16x32_bf16 a[8:11], v[72:75], v[32:35], a[8:11]\n\t"
"ds_read_b64_tr_b16 v[114:115], v2 offset:9216\n\t"
"v_mfma_f32_16x16x32_bf16 a[12:15], v[76:79], v[32:35], a[12:15]\n\t"
"ds_read_b64_tr_b16 v[116:117], v3 offset:8192\n\t"
"v_mfma_f32_16x16x32_bf16 a[16:19], v[64:67], v[36:39], a[16:19]\n\t"
"v_mfma_f32_16x16x32_bf16 a[20:23], v[68:71], v[36:39], a[20:23]\n\t"
"ds_read_b64_tr_b16 v[118:119], v3 offset:9216\n\t"
"v_mfma_f32_16x16x32_bf16 a[24:27], v[72:75], v[36:39], a[24:27]\n\t"
"ds_read_b64_tr_b16 v[120:121], v4 offset:8192\n\t"
"v_mfma_f32_16x16x32_bf16 a[28:31], v[76:79], v[36:39], a[28:31]\n\t"
"ds_read_b64_tr_b16 v[122:123], v4 offset:9216\n\t"
"v_mfma_f32_16x16x32_bf16 a[32:35], v[64:67], v[40:43], a[32:35]\n\t"
"ds_read_b64_tr_b16 v[124:125], v5 offset:8192\n\t"
"v_mfma_f32_16x16x32_bf16 a[36:39], v[68:71], v[40:43], a[36:39]\n\t"
"v_mfma_f32_16x16x32_bf16 a[40:43], v[72:75], v[40:43], a[40:43]\n\t"
"ds_read_b64_tr_b16 v[126:127], v5 offset:9216\n\t"
"v_mfma_f32_16x16x32_bf16 a[44:47], v[76:79], v[40:43], a[44:47]\n\t"
"ds_read_b128 v[84:87], v1 offset:2048\n\t"
"v_mfma_f32_16x16x32_bf16 a[48:51], v[64:67], v[44:47], a[48:51]\n\t"
"ds_read_b128 v[88:91], v1 offset:4096\n\t"
"v_mfma_f32_16x16x32_bf16 a[52:55], v[68:71], v[44:47], a[52:55]\n\t"
"v_mfma_f32_16x16x32_bf16 a[56:59], v[72:75], v[44:47], a[56:59]\n\t"
"ds_read_b128 v[92:95], v1 offset:6144\n\t"
"s_waitcnt lgkmcnt(7)\n\t"
"v_mfma_f32_16x16x32_bf16 a[60:63], v[76:79], v[44:47], a[60:63]\n\t"
"ds_read_b128 v[96:99], v1 offset:8192\n\t"
"v_mfma_f32_16x16x32_bf16 a[64:67], v[64:67], v[48:51], a[64:67]\n\t"
"ds_read_b128 v[100:103], v1 offset:10240\n\t"
"v_mfma_f32_16x16x32_bf16 a[68:71], v[68:71], v[48:51], a[68:71]\n\t"
"ds_read_b128 v[104:107], v1 offset:12288\n\t"
"v_mfma_f32_16x16x32_bf16 a[72:75], v[72:75], v[48:51], a[72:75]\n\t"
"v_mfma_f32_16x16x32_bf16 a[76:79], v[76:79], v[48:51], a[76:79]\n\t"
"ds_read_b128 v[108:111], v1 offset:14336\n\t"
"v_mfma_f32_16x16x32_bf16 a[80:83], v[64:67], v[52:55], a[80:83]\n\t"
"v_add_u32 v0, -98304, v0\n\t"
"v_mfma_f32_16x16x32_bf16 a[84:87], v[68:71], v[52:55], a[84:87]\n\t"
"v_add_u32 v1, -98304, v1\n\t"
"v_mfma_f32_16x16x32_bf16 a[88:91], v[72:75], v[52:55], a[88:91]\n\t"
"v_add_u32 v2, -98304, v2\n\t"
"v_mfma_f32_16x16x32_bf16 a[92:95], v[76:79], v[52:55], a[92:95]\n\t"
"v_mfma_f32_16x16x32_bf16 a[96:99], v[64:67], v[56:59], a[96:99]\n\t"
"v_add_u32 v3, -98304, v3\n\t"
"v_mfma_f32_16x16x32_bf16 a[100:103], v[68:71], v[56:59], a[100:103]\n\t"
"v_add_u32 v4, -98304, v4\n\t"
"v_mfma_f32_16x16x32_bf16 a[104:107], v[72:75], v[56:59], a[104:107]\n\t"
"v_add_u32 v5, -98304, v5\n\t"
"v_mfma_f32_16x16x32_bf16 a[108:111], v[76:79], v[56:59], a[108:111]\n\t"
"v_mfma_f32_16x16x32_bf16 a[112:115], v[64:67], v[60:63], a[112:115]\n\t"
"v_mfma_f32_16x16x32_bf16 a[116:119], v[68:71], v[60:63], a[116:119]\n\t"
"v_mfma_f32_16x16x32_bf16 a[120:123], v[72:75], v[60:63], a[120:123]\n\t"
"v_mfma_f32_16x16x32_bf16 a[124:127], v[76:79], v[60:63], a[124:127]\n\t"
"s_waitcnt vmcnt(10) lgkmcnt(0)\n\t"
"s_barrier\n\t"
"v_mfma_f32_16x16x32_bf16 a[0:3], v[112:115], v[80:83], a[0:3]\n\t"
"ds_read_b128 v[32:35], v0 offset:0\n\t"
"v_mfma_f32_16x16x32_bf16 a[4:7], v[116:119], v[80:83], a[4:7]\n\t"
"ds_read_b64_tr_b16 v[64:65], v2 offset:0\n\t"
"v_mfma_f32_16x16x32_bf16 a[8:11], v[120:123], v[80:83], a[8:11]\n\t"
"v_mfma_f32_16x16x32_bf16 a[12:15], v[124:127], v[80:83], a[12:15]\n\t"
"ds_read_b64_tr_b16 v[66:67], v2 offset:1024\n\t"
"v_mfma_f32_16x16x32_bf16 a[16:19], v[112:115], v[84:87], a[16:19]\n\t"
"v_mfma_f32_16x16x32_bf16 a[20:23], v[116:119], v[84:87], a[20:23]\n\t"
"ds_read_b64_tr_b16 v[68:69], v3 offset:0\n\t"
"v_mfma_f32_16x16x32_bf16 a[24:27], v[120:123], v[84:87], a[24:27]\n\t"
"v_mfma_f32_16x16x32_bf16 a[28:31], v[124:127], v[84:87], a[28:31]\n\t"
"ds_read_b64_tr_b16 v[70:71], v3 offset:1024\n\t"
"v_mfma_f32_16x16x32_bf16 a[32:35], v[112:115], v[88:91], a[32:35]\n\t"
"ds_read_b64_tr_b16 v[72:73], v4 offset:0\n\t"
"v_mfma_f32_16x16x32_bf16 a[36:39], v[116:119], v[88:91], a[36:39]\n\t"
"v_mfma_f32_16x16x32_bf16 a[40:43], v[120:123], v[88:91], a[40:43]\n\t"
"ds_read_b64_tr_b16 v[74:75], v4 offset:1024\n\t"
"v_mfma_f32_16x16x32_bf16 a[44:47], v[124:127], v[88:91], a[44:47]\n\t"
"v_mfma_f32_16x16x32_bf16 a[48:51], v[112:115], v[92:95], a[48:51]\n\t"
"ds_read_b64_tr_b16 v[76:77], v5 offset:0\n\t"
"v_mfma_f32_16x16x32_bf16 a[52:55], v[116:119], v[92:95], a[52:55]\n\t"
"v_mfma_f32_16x16x32_bf16 a[56:59], v[120:123], v[92:95], a[56:59]\n\t"
"ds_read_b64_tr_b16 v[78:79], v5 offset:1024\n\t"
"v_mfma_f32_16x16x32_bf16 a[60:63], v[124:127], v[92:95], a[60:63]\n\t"
"ds_read_b128 v[36:39], v0 offset:2048\n\t"
"v_mfma_f32_16x16x32_bf16 a[64:67], v[112:115], v[96:99], a[64:67]\n\t"
"v_mfma_f32_16x16x32_bf16 a[68:71], v[116:119], v[96:99], a[68:71]\n\t"
"ds_read_b128 v[40:43], v0 offset:4096\n\t"
"v_mfma_f32_16x16x32_bf16 a[72:75], v[120:123], v[96:99], a[72:75]\n\t"
"v_mfma_f32_16x16x32_bf16 a[76:79], v[124:127], v[96:99], a[76:79]\n\t"
"ds_read_b128 v[44:47], v0 offset:6144\n\t"
"v_mfma_f32_16x16x32_bf16 a[80:83], v[112:115], v[100:103], a[80:83]\n\t"
"v_mfma_f32_16x16x32_bf16 a[84:87], v[116:119], v[100:103], a[84:87]\n\t"
"ds_read_b128 v[48:51], v0 offset:8192\n\t"
"v_mfma_f32_16x16x32_bf16 a[88:91], v[120:123], v[100:103], a[88:91]\n\t"
"ds_read_b128 v[52:55], v0 offset:10240\n\t"
"v_mfma_f32_16x16x32_bf16 a[92:95], v[124:127], v[100:103], a[92:95]\n\t"
"v_mfma_f32_16x16x32_bf16 a[96:99], v[112:115], v[104:107], a[96:99]\n\t"
"ds_read_b128 v[56:59], v0 offset:12288\n\t"
"v_mfma_f32_16x16x32_bf16 a[100:103], v[116:119], v[104:107], a[100:103]\n\t"
"v_mfma_f32_16x16x32_bf16 a[104:107], v[120:123], v[104:107], a[104:107]\n\t"
"ds_read_b128 v[60:63], v0 offset:14336\n\t"
"s_waitcnt lgkmcnt(5)\n\t"
"v_mfma_f32_16x16x32_bf16 a[108:111], v[124:127], v[104:107], a[108:111]\n\t"
"v_mfma_f32_16x16x32_bf16 a[112:115], v[112:115], v[108:111], a[112:115]\n\t"
"v_mfma_f32_16x16x32_bf16 a[116:119], v[116:119], v[108:111], a[116:119]\n\t"
"v_mfma_f32_16x16x32_bf16 a[120:123], v[120:123], v[108:111], a[120:123]\n\t"
"v_mfma_f32_16x16x32_bf16 a[124:127], v[124:127], v[108:111], a[124:127]\n\t"
"v_mfma_f32_16x16x32_bf16 a[0:3], v[64:67], v[32:35], a[0:3]\n\t"
"ds_read_b128 v[80:83], v1 offset:0\n\t"
"v_mfma_f32_16x16x32_bf16 a[4:7], v[68:71], v[32:35], a[4:7]\n\t"
"ds_read_b64_tr_b16 v[112:113], v2 offset:8192\n\t"
"v_mfma_f32_16x16x32_bf16 a[8:11], v[72:75], v[32:35], a[8:11]\n\t"
"ds_read_b64_tr_b16 v[114:115], v2 offset:9216\n\t"
"v_mfma_f32_16x16x32_bf16 a[12:15], v[76:79], v[32:35], a[12:15]\n\t"
"ds_read_b64_tr_b16 v[116:117], v3 offset:8192\n\t"
"v_mfma_f32_16x16x32_bf16 a[16:19], v[64:67], v[36:39], a[16:19]\n\t"
"v_mfma_f32_16x16x32_bf16 a[20:23], v[68:71], v[36:39], a[20:23]\n\t"
"ds_read_b64_tr_b16 v[118:119], v3 offset:9216\n\t"
"v_mfma_f32_16x16x32_bf16 a[24:27], v[72:75], v[36:39], a[24:27]\n\t"
"ds_read_b64_tr_b16 v[120:121], v4 offset:8192\n\t"
"v_mfma_f32_16x16x32_bf16 a[28:31], v[76:79], v[36:39], a[28:31]\n\t"
"ds_read_b64_tr_b16 v[122:123], v4 offset:9216\n\t"
"v_mfma_f32_16x16x32_bf16 a[32:35], v[64:67], v[40:43], a[32:35]\n\t"
"ds_read_b64_tr_b16 v[124:125], v5 offset:8192\n\t"
"v_mfma_f32_16x16x32_bf16 a[36:39], v[68:71], v[40:43], a[36:39]\n\t"
"v_mfma_f32_16x16x32_bf16 a[40:43], v[72:75], v[40:43], a[40:43]\n\t"
"ds_read_b64_tr_b16 v[126:127], v5 offset:9216\n\t"
"v_mfma_f32_16x16x32_bf16 a[44:47], v[76:79], v[40:43], a[44:47]\n\t"
"ds_read_b128 v[84:87], v1 offset:2048\n\t"
"s_waitcnt lgkmcnt(6)\n\t"
"v_mfma_f32_16x16x32_bf16 a[48:51], v[64:67], v[44:47], a[48:51]\n\t"
"ds_read_b128 v[88:91], v1 offset:4096\n\t"
"v_mfma_f32_16x16x32_bf16 a[52:55], v[68:71], v[44:47], a[52:55]\n\t"
"v_mfma_f32_16x16x32_bf16 a[56:59], v[72:75], v[44:47], a[56:59]\n\t"
"ds_read_b128 v[92:95], v1 offset:6144\n\t"
"v_mfma_f32_16x16x32_bf16 a[60:63], v[76:79], v[44:47], a[60:63]\n\t"
"ds_read_b128 v[96:99], v1 offset:8192\n\t"
"v_mfma_f32_16x16x32_bf16 a[64:67], v[64:67], v[48:51], a[64:67]\n\t"
"ds_read_b128 v[100:103], v1 offset:10240\n\t"
"v_mfma_f32_16x16x32_bf16 a[68:71], v[68:71], v[48:51], a[68:71]\n\t"
"ds_read_b128 v[104:107], v1 offset:12288\n\t"
"v_mfma_f32_16x16x32_bf16 a[72:75], v[72:75], v[48:51], a[72:75]\n\t"
"v_mfma_f32_16x16x32_bf16 a[76:79], v[76:79], v[48:51], a[76:79]\n\t"
"ds_read_b128 v[108:111], v1 offset:14336\n\t"
"v_mfma_f32_16x16x32_bf16 a[80:83], v[64:67], v[52:55], a[80:83]\n\t"
"v_add_u32 v0, 49152, v0\n\t"
"v_mfma_f32_16x16x32_bf16 a[84:87], v[68:71], v[52:55], a[84:87]\n\t"
"v_add_u32 v1, 49152, v1\n\t"
"v_mfma_f32_16x16x32_bf16 a[88:91], v[72:75], v[52:55], a[88:91]\n\t"
"v_add_u32 v2, 49152, v2\n\t"
"v_mfma_f32_16x16x32_bf16 a[92:95], v[76:79], v[52:55], a[92:95]\n\t"
"v_mfma_f32_16x16x32_bf16 a[96:99], v[64:67], v[56:59], a[96:99]\n\t"
"v_add_u32 v3, 49152, v3\n\t"
"v_mfma_f32_16x16x32_bf16 a[100:103], v[68:71], v[56:59], a[100:103]\n\t"
"v_add_u32 v4, 49152, v4\n\t"
"v_mfma_f32_16x16x32_bf16 a[104:107], v[72:75], v[56:59], a[104:107]\n\t"
"v_add_u32 v5, 49152, v5\n\t"
"v_mfma_f32_16x16x32_bf16 a[108:111], v[76:79], v[56:59], a[108:111]\n\t"
"v_mfma_f32_16x16x32_bf16 a[112:115], v[64:67], v[60:63], a[112:115]\n\t"
"v_mfma_f32_16x16x32_bf16 a[116:119], v[68:71], v[60:63], a[116:119]\n\t"
"v_mfma_f32_16x16x32_bf16 a[120:123], v[72:75], v[60:63], a[120:123]\n\t"
"v_mfma_f32_16x16x32_bf16 a[124:127], v[76:79], v[60:63], a[124:127]\n\t"
"s_waitcnt lgkmcnt(0)\n\t"
"s_barrier\n\t"
"v_mfma_f32_16x16x32_bf16 a[0:3], v[112:115], v[80:83], a[0:3]\n\t"
"s_add_u32 m0, s79, 0\n\t"
"s_add_u32 s81, s76, 256\n\t"
"buffer_load_dwordx4 v6, s[48:51], s81 offen offset:0 lds\n\t"
"v_and_b32 v20, 15, v15\n\t"
"v_mfma_f32_16x16x32_bf16 a[4:7], v[116:119], v[80:83], a[4:7]\n\t"
"v_lshrrev_b32 v21, 4, v15\n\t"
"v_mfma_f32_16x16x32_bf16 a[8:11], v[120:123], v[80:83], a[8:11]\n\t"
"buffer_load_dwordx4 v7, s[48:51], s81 offen offset:1024 lds\n\t"
"v_mfma_f32_16x16x32_bf16 a[12:15], v[124:127], v[80:83], a[12:15]\n\t"
"v_bfe_u32 v22, v15, 1, 3\n\t"
"s_add_u32 m0, s79, 8192\n\t"
"s_add_u32 s81, s76, 384\n\t"
"buffer_load_dwordx4 v6, s[48:51], s81 offen offset:0 lds\n\t"
"v_mfma_f32_16x16x32_bf16 a[16:19], v[112:115], v[84:87], a[16:19]\n\t"
"v_xor_b32 v22, v22, v21\n\t"
"v_mfma_f32_16x16x32_bf16 a[20:23], v[116:119], v[84:87], a[20:23]\n\t"
"s_lshl_b32 s83, s45, 12\n\t"
"v_mfma_f32_16x16x32_bf16 a[24:27], v[120:123], v[84:87], a[24:27]\n\t"
"buffer_load_dwordx4 v7, s[48:51], s81 offen offset:1024 lds\n\t"
"s_add_u32 s83, s83, 65536\n\t"
"v_mfma_f32_16x16x32_bf16 a[28:31], v[124:127], v[84:87], a[28:31]\n\t"
"s_lshl_b32 s81, s79, 1\n\t"
"s_add_u32 m0, s81, 16384\n\t"
"s_add_u32 s81, s78, 20480\n\t"
"buffer_load_dwordx4 v10, s[60:63], s81 offen offset:0 sc1 lds\n\t"
"v_mfma_f32_16x16x32_bf16 a[32:35], v[112:115], v[88:91], a[32:35]\n\t"
"v_lshlrev_b32 v20, 7, v20\n\t"
"v_mfma_f32_16x16x32_bf16 a[36:39], v[116:119], v[88:91], a[36:39]\n\t"
"v_add_u32 v20, s83, v20\n\t"
"s_add_u32 s81, s78, 21504\n\t"
"buffer_load_dwordx4 v10, s[60:63], s81 offen offset:1024 sc1 lds\n\t"
"v_mfma_f32_16x16x32_bf16 a[40:43], v[120:123], v[88:91], a[40:43]\n\t"
"v_xor_b32 v23, 0, v22\n\t"
"v_mfma_f32_16x16x32_bf16 a[44:47], v[124:127], v[88:91], a[44:47]\n\t"
"s_add_u32 s81, s78, 18688\n\t"
"buffer_load_dwordx4 v10, s[60:63], s81 offen offset:2048 sc1 lds\n\t"
"v_mfma_f32_16x16x32_bf16 a[48:51], v[112:115], v[92:95], a[48:51]\n\t"
"v_lshl_add_u32 v2, v23, 4, v20\n\t"
"v_xor_b32 v23, 4, v22\n\t"
"v_mfma_f32_16x16x32_bf16 a[52:55], v[116:119], v[92:95], a[52:55]\n\t"
"s_add_u32 s81, s78, 19712\n\t"
"buffer_load_dwordx4 v10, s[60:63], s81 offen offset:3072 sc1 lds\n\t"
"v_mfma_f32_16x16x32_bf16 a[56:59], v[120:123], v[92:95], a[56:59]\n\t"
"v_lshl_add_u32 v3, v23, 4, v20\n\t"
"v_mfma_f32_16x16x32_bf16 a[60:63], v[124:127], v[92:95], a[60:63]\n\t"
"s_add_u32 m0, s79, 32768\n\t"
"s_add_u32 s81, s76, 512\n\t"
"buffer_load_dwordx4 v6, s[48:51], s81 offen offset:0 lds\n\t"
"v_lshrrev_b32 v20, 2, v15\n\t"
"v_mfma_f32_16x16x32_bf16 a[64:67], v[112:115], v[96:99], a[64:67]\n\t"
"v_lshlrev_b32 v20, 7, v20\n\t"
"v_mfma_f32_16x16x32_bf16 a[68:71], v[116:119], v[96:99], a[68:71]\n\t"
"buffer_load_dwordx4 v7, s[48:51], s81 offen offset:1024 lds\n\t"
"v_mfma_f32_16x16x32_bf16 a[72:75], v[120:123], v[96:99], a[72:75]\n\t"
"v_and_b32 v21, 3, v15\n\t"
"s_add_u32 m0, s79, 40960\n\t"
"s_add_u32 s81, s76, 640\n\t"
"buffer_load_dwordx4 v6, s[48:51], s81 offen offset:0 lds\n\t"
"v_mfma_f32_16x16x32_bf16 a[76:79], v[124:127], v[96:99], a[76:79]\n\t"
"v_lshl_add_u32 v20, v21, 3, v20\n\t"
"v_mfma_f32_16x16x32_bf16 a[80:83], v[112:115], v[100:103], a[80:83]\n\t"
"v_add_u32 v20, 81920, v20\n\t"
"v_mfma_f32_16x16x32_bf16 a[84:87], v[116:119], v[100:103], a[84:87]\n\t"
"buffer_load_dwordx4 v7, s[48:51], s81 offen offset:1024 lds\n\t"
"s_lshl_b32 s83, s44, 1\n\t"
"v_mfma_f32_16x16x32_bf16 a[88:91], v[120:123], v[100:103], a[88:91]\n\t"
"s_lshl_b32 s81, s79, 1\n\t"
"s_add_u32 m0, s81, 98304\n\t"
"s_add_u32 s81, s78, 36864\n\t"
"buffer_load_dwordx4 v10, s[60:63], s81 offen offset:0 sc1 lds\n\t"
"v_mfma_f32_16x16x32_bf16 a[92:95], v[124:127], v[100:103], a[92:95]\n\t"
"v_bfe_u32 v21, v15, 3, 2\n\t"
"v_mfma_f32_16x16x32_bf16 a[96:99], v[112:115], v[104:107], a[96:99]\n\t"
"v_xor_b32 v21, s83, v21\n\t"
"s_add_u32 s81, s78, 37888\n\t"
"buffer_load_dwordx4 v10, s[60:63], s81 offen offset:1024 sc1 lds\n\t"
"v_mfma_f32_16x16x32_bf16 a[100:103], v[116:119], v[104:107], a[100:103]\n\t"
"v_xor_b32 v22, 0, v21\n\t"
"v_mfma_f32_16x16x32_bf16 a[104:107], v[120:123], v[104:107], a[104:107]\n\t"
"s_add_u32 s81, s78, 35072\n\t"
"buffer_load_dwordx4 v10, s[60:63], s81 offen offset:2048 sc1 lds\n\t"
"v_mfma_f32_16x16x32_bf16 a[108:111], v[124:127], v[104:107], a[108:111]\n\t"
"v_lshl_add_u32 v4, v22, 5, v20\n\t"
"v_xor_b32 v22, 1, v21\n\t"
"v_mfma_f32_16x16x32_bf16 a[112:115], v[112:115], v[108:111], a[112:115]\n\t"
"s_add_u32 s81, s78, 36096\n\t"
"buffer_load_dwordx4 v10, s[60:63], s81 offen offset:3072 sc1 lds\n\t"
"v_mfma_f32_16x16x32_bf16 a[116:119], v[116:119], v[108:111], a[116:119]\n\t"
"v_lshl_add_u32 v5, v22, 5, v20\n\t"
"v_mfma_f32_16x16x32_bf16 a[120:123], v[120:123], v[108:111], a[120:123]\n\t"
"v_mfma_f32_16x16x32_bf16 a[124:127], v[124:127], v[108:111], a[124:127]\n\t"
"s_mul_i32 s82, s45, 81920\n\t"
"s_add_u32 s82, s82, 49152\n\t"
"s_lshl_b32 s83, s44, 11\n\t"
"s_add_u32 s82, s82, s83\n\t"
"v_add_u32 v16, s82, v14\n\t"
"s_mul_i32 s82, s45, 49152\n\t"
"s_sub_u32 s82, 65536, s82\n\t"
"v_add_u32 v17, s82, v16\n\t"
"v_mov_b32 v18, 0x3e0293ee\n\t"
"s_waitcnt vmcnt(17)\n\t"
"s_barrier\n\t"
"ds_read_b128 v[48:51], v2 offset:0\n\t"
"ds_read_b128 v[52:55], v2 offset:2048\n\t"
"ds_read_b64_tr_b16 v[56:57], v4 offset:0\n\t"
"ds_read_b64_tr_b16 v[58:59], v4 offset:2048\n\t"
"ds_read_b64_tr_b16 v[60:61], v5 offset:0\n\t"
"ds_read_b64_tr_b16 v[62:63], v5 offset:2048\n\t"
"ds_read_b128 v[64:67], v3 offset:0\n\t"
"ds_read_b128 v[68:71], v3 offset:2048\n\t"
"ds_read_b64_tr_b16 v[72:73], v4 offset:4096\n\t"
"ds_read_b64_tr_b16 v[74:75], v4 offset:6144\n\t"
"ds_read_b64_tr_b16 v[76:77], v5 offset:4096\n\t"
"ds_read_b64_tr_b16 v[78:79], v5 offset:6144\n\t"
"s_load_dwordx2 s[52:53], s[36:37], 48\n\t"
"s_load_dwordx2 s[64:65], s[36:37], 112\n\t"
"s_waitcnt lgkmcnt(0)\n\t"
"s_and_b32 s53, s53, 0xffff\n\t"
"s_mov_b32 s54, 2097152\n\t"
"s_mov_b32 s55, 0x00020000\n\t"
"s_and_b32 s65, s65, 0xffff\n\t"
"s_mov_b32 s66, 1024\n\t"
"s_mov_b32 s67, 0x00020000\n\t"
"v_lshrrev_b32 v21, 4, v15\n\t"
"v_lshlrev_b32 v21, 3, v21\n\t"
"v_and_b32 v20, 15, v15\n\t"
"v_lshl_add_u32 v20, v20, 10, v21\n\t"
"s_lshl_b32 s83, s47, 7\n\t"
"s_lshl_b32 s82, s45, 6\n\t"
"s_add_u32 s83, s83, s82\n\t"
"s_lshl_b32 s82, s46, 16\n\t"
"s_add_u32 s82, s82, s83\n\t"
"s_lshl_b32 s81, s44, 15\n\t"
"s_add_u32 s82, s82, s81\n\t"
"s_add_u32 s81, s82, 16384\n\t"
"buffer_load_dwordx2 v[120:121], v21, s[64:67], s83 offen offset:0\n\t"
"buffer_load_dwordx2 v[112:113], v20, s[52:55], s82 offen offset:0\n\t"
"buffer_load_dwordx2 v[114:115], v20, s[52:55], s81 offen offset:0\n\t"
"buffer_load_dwordx2 v[122:123], v21, s[64:67], s83 offen offset:32\n\t"
"buffer_load_dwordx2 v[116:117], v20, s[52:55], s82 offen offset:32\n\t"
"buffer_load_dwordx2 v[118:119], v20, s[52:55], s81 offen offset:32\n\t"
"v_mfma_f32_16x16x32_bf16 v[32:35], v[48:51], v[56:59], 0\n\t"
"ds_read_b128 v[80:83], v2 offset:8192\n\t"
"v_accvgpr_read_b32 v128, a0\n\t"
"v_accvgpr_read_b32 v129, a1\n\t"
"ds_read_b128 v[84:87], v2 offset:10240\n\t"
"v_accvgpr_read_b32 v130, a2\n\t"
"v_mfma_f32_16x16x32_bf16 v[36:39], v[48:51], v[60:63], 0\n\t"
"ds_read_b64_tr_b16 v[88:89], v4 offset:8192\n\t"
"v_accvgpr_read_b32 v131, a3\n\t"
"ds_read_b64_tr_b16 v[90:91], v4 offset:10240\n\t"
"v_accvgpr_read_b32 v132, a4\n\t"
"v_accvgpr_read_b32 v133, a5\n\t"
"v_mfma_f32_16x16x32_bf16 v[40:43], v[52:55], v[56:59], 0\n\t"
"ds_read_b64_tr_b16 v[92:93], v5 offset:8192\n\t"
"v_accvgpr_read_b32 v134, a6\n\t"
"ds_read_b64_tr_b16 v[94:95], v5 offset:10240\n\t"
"v_accvgpr_read_b32 v135, a7\n\t"
"v_mfma_f32_16x16x32_bf16 v[44:47], v[52:55], v[60:63], 0\n\t"
"ds_read_b128 v[96:99], v3 offset:8192\n\t"
"v_mul_f32 v128, v128, v18\n\t"
"v_mul_f32 v129, v129, v18\n\t"
"ds_read_b128 v[100:103], v3 offset:10240\n\t"
"v_mul_f32 v130, v130, v18\n\t"
"v_mfma_f32_16x16x32_bf16 v[32:35], v[64:67], v[72:75], v[32:35]\n\t"
"ds_read_b64_tr_b16 v[104:105], v4 offset:12288\n\t"
"v_mul_f32 v131, v131, v18\n\t"
"ds_read_b64_tr_b16 v[106:107], v4 offset:14336\n\t"
"v_mul_f32 v132, v132, v18\n\t"
"v_mul_f32 v133, v133, v18\n\t"
"v_mfma_f32_16x16x32_bf16 v[36:39], v[64:67], v[76:79], v[36:39]\n\t"
"ds_read_b64_tr_b16 v[108:109], v5 offset:12288\n\t"
"v_mul_f32 v134, v134, v18\n\t"
"ds_read_b64_tr_b16 v[110:111], v5 offset:14336\n\t"
"v_mul_f32 v135, v135, v18\n\t"
"v_mfma_f32_16x16x32_bf16 v[40:43], v[68:71], v[72:75], v[40:43]\n\t"
"v_add_u32 v2, -65536, v2\n\t"
"v_cvt_pk_bf16_f32 v136, v128, v129\n\t"
"v_cvt_pk_bf16_f32 v137, v130, v131\n\t"
"v_add_u32 v3, -65536, v3\n\t"
"v_cvt_pk_bf16_f32 v138, v132, v133\n\t"
"v_mfma_f32_16x16x32_bf16 v[44:47], v[68:71], v[76:79], v[44:47]\n\t"
"v_add_u32 v4, -65536, v4\n\t"
"v_cvt_pk_bf16_f32 v139, v134, v135\n\t"
"v_add_u32 v5, -65536, v5\n\t"
"ds_write_b128 v16, v[136:139] offset:0\n\t"
"s_waitcnt vmcnt(14) lgkmcnt(0)\n\t"
"s_barrier\n\t"
"v_mfma_f32_16x16x32_bf16 v[32:35], v[80:83], v[88:91], v[32:35]\n\t"
"ds_read_b128 v[48:51], v2 offset:0\n\t"
"v_accvgpr_read_b32 v140, a8\n\t"
"v_accvgpr_read_b32 v141, a9\n\t"
"s_add_u32 m0, s79, 65536\n\t"
"s_add_u32 s81, s76, 768\n\t"
"buffer_load_dwordx4 v6, s[48:51], s81 offen offset:0 lds\n\t"
"v_accvgpr_read_b32 v142, a10\n\t"
"v_mfma_f32_16x16x32_bf16 v[36:39], v[80:83], v[92:95], v[36:39]\n\t"
"ds_read_b128 v[52:55], v2 offset:2048\n\t"
"v_accvgpr_read_b32 v143, a11\n\t"
"ds_read_b64_tr_b16 v[56:57], v4 offset:0\n\t"
"v_accvgpr_read_b32 v144, a12\n\t"
"v_accvgpr_read_b32 v145, a13\n\t"
"v_mfma_f32_16x16x32_bf16 v[40:43], v[84:87], v[88:91], v[40:43]\n\t"
"ds_read_b64_tr_b16 v[58:59], v4 offset:2048\n\t"
"v_accvgpr_read_b32 v146, a14\n\t"
"buffer_load_dwordx4 v7, s[48:51], s81 offen offset:1024 lds\n\t"
"v_accvgpr_read_b32 v147, a15\n\t"
"v_mfma_f32_16x16x32_bf16 v[44:47], v[84:87], v[92:95], v[44:47]\n\t"
"ds_read_b64_tr_b16 v[60:61], v5 offset:0\n\t"
"v_mul_f32 v140, v140, v18\n\t"
"v_mul_f32 v141, v141, v18\n\t"
"ds_read_b64_tr_b16 v[62:63], v5 offset:2048\n\t"
"v_mul_f32 v142, v142, v18\n\t"
"v_mfma_f32_16x16x32_bf16 v[32:35], v[96:99], v[104:107], v[32:35]\n\t"
"ds_read_b128 v[64:67], v3 offset:0\n\t"
"v_mul_f32 v143, v143, v18\n\t"
"s_add_u32 m0, s79, 73728\n\t"
"s_add_u32 s81, s76, 896\n\t"
"buffer_load_dwordx4 v6, s[48:51], s81 offen offset:0 lds\n\t"
"v_mul_f32 v144, v144, v18\n\t"
"v_mul_f32 v145, v145, v18\n\t"
"v_mfma_f32_16x16x32_bf16 v[36:39], v[96:99], v[108:111], v[36:39]\n\t"
"ds_read_b128 v[68:71], v3 offset:2048\n\t"
"v_mul_f32 v146, v146, v18\n\t"
"ds_read_b64_tr_b16 v[72:73], v4 offset:4096\n\t"
"v_mul_f32 v147, v147, v18\n\t"
"v_mfma_f32_16x16x32_bf16 v[40:43], v[100:103], v[104:107], v[40:43]\n\t"
"ds_read_b64_tr_b16 v[74:75], v4 offset:6144\n\t"
"v_cvt_pk_bf16_f32 v148, v140, v141\n\t"
"v_cvt_pk_bf16_f32 v149, v142, v143\n\t"
"buffer_load_dwordx4 v7, s[48:51], s81 offen offset:1024 lds\n\t"
"v_cvt_pk_bf16_f32 v150, v144, v145\n\t"
"v_mfma_f32_16x16x32_bf16 v[44:47], v[100:103], v[108:111], v[44:47]\n\t"
"ds_read_b64_tr_b16 v[76:77], v5 offset:4096\n\t"
"v_cvt_pk_bf16_f32 v151, v146, v147\n\t"
"ds_read_b64_tr_b16 v[78:79], v5 offset:6144\n\t"
"ds_write_b128 v16, v[148:151] offset:1024\n\t"
"s_waitcnt vmcnt(26)\n\t"
"s_lshl_b32 s82, s40, 8\n\t"
"s_lshl_b32 s83, s41, 5\n\t"
"s_add_u32 s82, s82, s83\n\t"
"s_lshl_b32 s83, s39, 3\n\t"
"s_add_u32 s82, s82, s83\n\t"
"v_mov_b32 v216, s82\n\t"
"v_mov_b32 v218, s26\n\t"
"v_mov_b32 v219, s27\n\t"
"s_mov_b64 s[86:87], exec\n\t"
"s_mov_b64 exec, 1\n\t"
"s_nop 1\n\t"
"global_store_dwordx2 v216, v[218:219], s[42:43] sc1\n\t"
"s_mov_b64 exec, s[86:87]\n\t"
"s_nop 1\n\t"
"s_waitcnt lgkmcnt(6)\n\t"
"v_mfma_f32_16x16x32_bf16 v[32:35], v[48:51], v[56:59], v[32:35]\n\t"
"ds_read_b128 v[80:83], v2 offset:8192\n\t"
"v_accvgpr_read_b32 v152, a16\n\t"
"v_accvgpr_read_b32 v153, a17\n\t"
"s_lshl_b32 s81, s79, 1\n\t"
"s_add_u32 m0, s81, 81920\n\t"
"s_add_u32 s81, s78, 53248\n\t"
"buffer_load_dwordx4 v10, s[60:63], s81 offen offset:0 sc1 lds\n\t"
"v_accvgpr_read_b32 v154, a18\n\t"
"ds_read_b128 v[84:87], v2 offset:10240\n\t"
"v_mfma_f32_16x16x32_bf16 v[36:39], v[48:51], v[60:63], v[36:39]\n\t"
"v_accvgpr_read_b32 v155, a19\n\t"
"ds_read_b64_tr_b16 v[88:89], v4 offset:8192\n\t"
"v_accvgpr_read_b32 v156, a20\n\t"
"ds_read_b64_tr_b16 v[90:91], v4 offset:10240\n\t"
"v_accvgpr_read_b32 v157, a21\n\t"
"v_mfma_f32_16x16x32_bf16 v[40:43], v[52:55], v[56:59], v[40:43]\n\t"
"ds_read_b64_tr_b16 v[92:93], v5 offset:8192\n\t"
"v_accvgpr_read_b32 v158, a22\n\t"
"s_add_u32 s81, s78, 54272\n\t"
"buffer_load_dwordx4 v10, s[60:63], s81 offen offset:1024 sc1 lds\n\t"
"v_accvgpr_read_b32 v159, a23\n\t"
"ds_read_b64_tr_b16 v[94:95], v5 offset:10240\n\t"
"v_mfma_f32_16x16x32_bf16 v[44:47], v[52:55], v[60:63], v[44:47]\n\t"
"v_mul_f32 v152, v152, v18\n\t"
"ds_read_b128 v[96:99], v3 offset:8192\n\t"
"v_mul_f32 v153, v153, v18\n\t"
"ds_read_b128 v[100:103], v3 offset:10240\n\t"
"v_mul_f32 v154, v154, v18\n\t"
"s_waitcnt lgkmcnt(7)\n\t"
"v_mfma_f32_16x16x32_bf16 v[32:35], v[64:67], v[72:75], v[32:35]\n\t"
"ds_read_b64_tr_b16 v[104:105], v4 offset:12288\n\t"
"v_mul_f32 v155, v155, v18\n\t"
"s_add_u32 s81, s78, 51456\n\t"
"buffer_load_dwordx4 v10, s[60:63], s81 offen offset:2048 sc1 lds\n\t"
"v_mul_f32 v156, v156, v18\n\t"
"ds_read_b64_tr_b16 v[106:107], v4 offset:14336\n\t"
"v_mfma_f32_16x16x32_bf16 v[36:39], v[64:67], v[76:79], v[36:39]\n\t"
"v_mul_f32 v157, v157, v18\n\t"
"ds_read_b64_tr_b16 v[108:109], v5 offset:12288\n\t"
"v_mul_f32 v158, v158, v18\n\t"
"ds_read_b64_tr_b16 v[110:111], v5 offset:14336\n\t"
"v_mul_f32 v159, v159, v18\n\t"
"v_mfma_f32_16x16x32_bf16 v[40:43], v[68:71], v[72:75], v[40:43]\n\t"
"v_add_u32 v2, 32768, v2\n\t"
"v_cvt_pk_bf16_f32 v160, v152, v153\n\t"
"s_add_u32 s81, s78, 52480\n\t"
"buffer_load_dwordx4 v10, s[60:63], s81 offen offset:3072 sc1 lds\n\t"
"v_cvt_pk_bf16_f32 v161, v154, v155\n\t"
"v_add_u32 v3, 32768, v3\n\t"
"v_mfma_f32_16x16x32_bf16 v[44:47], v[68:71], v[76:79], v[44:47]\n\t"
"v_cvt_pk_bf16_f32 v162, v156, v157\n\t"
"v_add_u32 v4, 81920, v4\n\t"
"v_cvt_pk_bf16_f32 v163, v158, v159\n\t"
"v_add_u32 v5, 81920, v5\n\t"
"ds_write_b128 v16, v[160:163] offset:4096\n\t"
"s_waitcnt vmcnt(15) lgkmcnt(0)\n\t"
"s_barrier\n\t"
"v_mfma_f32_16x16x32_bf16 v[32:35], v[80:83], v[88:91], v[32:35]\n\t"
"ds_read_b128 v[48:51], v2 offset:0\n\t"
"v_accvgpr_read_b32 v164, a24\n\t"
"v_accvgpr_read_b32 v165, a25\n\t"
"s_add_u32 m0, s79, 0\n\t"
"s_add_u32 s81, s76, 0\n\t"
"buffer_load_dwordx4 v6, s[56:59], s81 offen offset:0 lds\n\t"
"v_accvgpr_read_b32 v166, a26\n\t"
"v_mfma_f32_16x16x32_bf16 v[36:39], v[80:83], v[92:95], v[36:39]\n\t"
"ds_read_b128 v[52:55], v2 offset:2048\n\t"
"v_accvgpr_read_b32 v167, a27\n\t"
"ds_read_b64_tr_b16 v[56:57], v4 offset:0\n\t"
"v_accvgpr_read_b32 v168, a28\n\t"
"v_accvgpr_read_b32 v169, a29\n\t"
"v_mfma_f32_16x16x32_bf16 v[40:43], v[84:87], v[88:91], v[40:43]\n\t"
"ds_read_b64_tr_b16 v[58:59], v4 offset:2048\n\t"
"v_accvgpr_read_b32 v170, a30\n\t"
"buffer_load_dwordx4 v7, s[56:59], s81 offen offset:1024 lds\n\t"
"v_accvgpr_read_b32 v171, a31\n\t"
"v_mfma_f32_16x16x32_bf16 v[44:47], v[84:87], v[92:95], v[44:47]\n\t"
"ds_read_b64_tr_b16 v[60:61], v5 offset:0\n\t"
"v_mul_f32 v164, v164, v18\n\t"
"v_mul_f32 v165, v165, v18\n\t"
"ds_read_b64_tr_b16 v[62:63], v5 offset:2048\n\t"
"v_mul_f32 v166, v166, v18\n\t"
"v_mfma_f32_16x16x32_bf16 v[32:35], v[96:99], v[104:107], v[32:35]\n\t"
"ds_read_b128 v[64:67], v3 offset:0\n\t"
"v_mul_f32 v167, v167, v18\n\t"
"s_add_u32 m0, s79, 8192\n\t"
"s_add_u32 s81, s76, 128\n\t"
"buffer_load_dwordx4 v6, s[56:59], s81 offen offset:0 lds\n\t"
"v_mul_f32 v168, v168, v18\n\t"
"v_mul_f32 v169, v169, v18\n\t"
"v_mfma_f32_16x16x32_bf16 v[36:39], v[96:99], v[108:111], v[36:39]\n\t"
"ds_read_b128 v[68:71], v3 offset:2048\n\t"
"v_mul_f32 v170, v170, v18\n\t"
"ds_read_b64_tr_b16 v[72:73], v4 offset:4096\n\t"
"v_mul_f32 v171, v171, v18\n\t"
"v_mfma_f32_16x16x32_bf16 v[40:43], v[100:103], v[104:107], v[40:43]\n\t"
"ds_read_b64_tr_b16 v[74:75], v4 offset:6144\n\t"
"v_cvt_pk_bf16_f32 v172, v164, v165\n\t"
"v_cvt_pk_bf16_f32 v173, v166, v167\n\t"
"buffer_load_dwordx4 v7, s[56:59], s81 offen offset:1024 lds\n\t"
"v_cvt_pk_bf16_f32 v174, v168, v169\n\t"
"v_mfma_f32_16x16x32_bf16 v[44:47], v[100:103], v[108:111], v[44:47]\n\t"
"ds_read_b64_tr_b16 v[76:77], v5 offset:4096\n\t"
"v_cvt_pk_bf16_f32 v175, v170, v171\n\t"
"ds_read_b64_tr_b16 v[78:79], v5 offset:6144\n\t"
"ds_write_b128 v16, v[172:175] offset:5120\n\t"
"s_waitcnt lgkmcnt(7)\n\t"
"v_mfma_f32_16x16x32_bf16 v[32:35], v[48:51], v[56:59], v[32:35]\n\t"
"ds_read_b128 v[80:83], v2 offset:8192\n\t"
"v_accvgpr_read_b32 v176, a32\n\t"
"v_accvgpr_read_b32 v177, a33\n\t"
"s_lshl_b32 s81, s79, 1\n\t"
"s_add_u32 m0, s81, 16384\n\t"
"s_add_u32 s81, s78, 69632\n\t"
"buffer_load_dwordx4 v10, s[60:63], s81 offen offset:0 sc1 lds\n\t"
"v_accvgpr_read_b32 v178, a34\n\t"
"ds_read_b128 v[84:87], v2 offset:10240\n\t"
"v_mfma_f32_16x16x32_bf16 v[36:39], v[48:51], v[60:63], v[36:39]\n\t"
"v_accvgpr_read_b32 v179, a35\n\t"
"ds_read_b64_tr_b16 v[88:89], v4 offset:8192\n\t"
"v_accvgpr_read_b32 v180, a36\n\t"
"ds_read_b64_tr_b16 v[90:91], v4 offset:10240\n\t"
"v_accvgpr_read_b32 v181, a37\n\t"
"v_mfma_f32_16x16x32_bf16 v[40:43], v[52:55], v[56:59], v[40:43]\n\t"
"ds_read_b64_tr_b16 v[92:93], v5 offset:8192\n\t"
"v_accvgpr_read_b32 v182, a38\n\t"
"s_add_u32 s81, s78, 70656\n\t"
"buffer_load_dwordx4 v10, s[60:63], s81 offen offset:1024 sc1 lds\n\t"
"v_accvgpr_read_b32 v183, a39\n\t"
"ds_read_b64_tr_b16 v[94:95], v5 offset:10240\n\t"
"v_mfma_f32_16x16x32_bf16 v[44:47], v[52:55], v[60:63], v[44:47]\n\t"
"v_mul_f32 v176, v176, v18\n\t"
"ds_read_b128 v[96:99], v3 offset:8192\n\t"
"v_mul_f32 v177, v177, v18\n\t"
"ds_read_b128 v[100:103], v3 offset:10240\n\t"
"v_mul_f32 v178, v178, v18\n\t"
"s_waitcnt lgkmcnt(7)\n\t"
"v_mfma_f32_16x16x32_bf16 v[32:35], v[64:67], v[72:75], v[32:35]\n\t"
"ds_read_b64_tr_b16 v[104:105], v4 offset:12288\n\t"
"v_mul_f32 v179, v179, v18\n\t"
"s_add_u32 s81, s78, 67840\n\t"
"buffer_load_dwordx4 v10, s[60:63], s81 offen offset:2048 sc1 lds\n\t"
"v_mul_f32 v180, v180, v18\n\t"
"ds_read_b64_tr_b16 v[106:107], v4 offset:14336\n\t"
"v_mfma_f32_16x16x32_bf16 v[36:39], v[64:67], v[76:79], v[36:39]\n\t"
"v_mul_f32 v181, v181, v18\n\t"
"ds_read_b64_tr_b16 v[108:109], v5 offset:12288\n\t"
"v_mul_f32 v182, v182, v18\n\t"
"ds_read_b64_tr_b16 v[110:111], v5 offset:14336\n\t"
"v_mul_f32 v183, v183, v18\n\t"
"v_mfma_f32_16x16x32_bf16 v[40:43], v[68:71], v[72:75], v[40:43]\n\t"
"v_add_u32 v2, 32768, v2\n\t"
"v_cvt_pk_bf16_f32 v184, v176, v177\n\t"
"s_add_u32 s81, s78, 68864\n\t"
"buffer_load_dwordx4 v10, s[60:63], s81 offen offset:3072 sc1 lds\n\t"
"v_cvt_pk_bf16_f32 v185, v178, v179\n\t"
"v_add_u32 v3, 32768, v3\n\t"
"v_mfma_f32_16x16x32_bf16 v[44:47], v[68:71], v[76:79], v[44:47]\n\t"
"v_cvt_pk_bf16_f32 v186, v180, v181\n\t"
"v_add_u32 v4, -16384, v4\n\t"
"v_cvt_pk_bf16_f32 v187, v182, v183\n\t"
"v_add_u32 v5, -16384, v5\n\t"
"ds_write_b128 v16, v[184:187] offset:8192\n\t"
"s_waitcnt vmcnt(8) lgkmcnt(0)\n\t"
"s_barrier\n\t"
"v_mfma_f32_16x16x32_bf16 v[32:35], v[80:83], v[88:91], v[32:35]\n\t"
"ds_read_b128 v[48:51], v2 offset:0\n\t"
"v_accvgpr_read_b32 v188, a40\n\t"
"v_accvgpr_read_b32 v189, a41\n\t"
"s_add_u32 m0, s79, 32768\n\t"
"s_add_u32 s81, s76, 256\n\t"
"buffer_load_dwordx4 v6, s[56:59], s81 offen offset:0 lds\n\t"
"v_accvgpr_read_b32 v190, a42\n\t"
"v_mfma_f32_16x16x32_bf16 v[36:39], v[80:83], v[92:95], v[36:39]\n\t"
"ds_read_b128 v[52:55], v2 offset:2048\n\t"
"v_accvgpr_read_b32 v191, a43\n\t"
"ds_read_b64_tr_b16 v[56:57], v4 offset:0\n\t"
"v_accvgpr_read_b32 v192, a44\n\t"
"v_accvgpr_read_b32 v193, a45\n\t"
"v_mfma_f32_16x16x32_bf16 v[40:43], v[84:87], v[88:91], v[40:43]\n\t"
"ds_read_b64_tr_b16 v[58:59], v4 offset:2048\n\t"
"v_accvgpr_read_b32 v194, a46\n\t"
"buffer_load_dwordx4 v7, s[56:59], s81 offen offset:1024 lds\n\t"
"v_accvgpr_read_b32 v195, a47\n\t"
"v_mfma_f32_16x16x32_bf16 v[44:47], v[84:87], v[92:95], v[44:47]\n\t"
"ds_read_b64_tr_b16 v[60:61], v5 offset:0\n\t"
"v_mul_f32 v188, v188, v18\n\t"
"v_mul_f32 v189, v189, v18\n\t"
"ds_read_b64_tr_b16 v[62:63], v5 offset:2048\n\t"
"v_mul_f32 v190, v190, v18\n\t"
"v_mfma_f32_16x16x32_bf16 v[32:35], v[96:99], v[104:107], v[32:35]\n\t"
"ds_read_b128 v[64:67], v3 offset:0\n\t"
"v_mul_f32 v191, v191, v18\n\t"
"s_add_u32 m0, s79, 40960\n\t"
"s_add_u32 s81, s76, 384\n\t"
"buffer_load_dwordx4 v6, s[56:59], s81 offen offset:0 lds\n\t"
"v_mul_f32 v192, v192, v18\n\t"
"v_mul_f32 v193, v193, v18\n\t"
"v_mfma_f32_16x16x32_bf16 v[36:39], v[96:99], v[108:111], v[36:39]\n\t"
"ds_read_b128 v[68:71], v3 offset:2048\n\t"
"v_mul_f32 v194, v194, v18\n\t"
"ds_read_b64_tr_b16 v[72:73], v4 offset:4096\n\t"
"v_mul_f32 v195, v195, v18\n\t"
"v_mfma_f32_16x16x32_bf16 v[40:43], v[100:103], v[104:107], v[40:43]\n\t"
"ds_read_b64_tr_b16 v[74:75], v4 offset:6144\n\t"
"v_cvt_pk_bf16_f32 v196, v188, v189\n\t"
"v_cvt_pk_bf16_f32 v197, v190, v191\n\t"
"buffer_load_dwordx4 v7, s[56:59], s81 offen offset:1024 lds\n\t"
"v_cvt_pk_bf16_f32 v198, v192, v193\n\t"
"v_mfma_f32_16x16x32_bf16 v[44:47], v[100:103], v[108:111], v[44:47]\n\t"
"ds_read_b64_tr_b16 v[76:77], v5 offset:4096\n\t"
"v_cvt_pk_bf16_f32 v199, v194, v195\n\t"
"ds_read_b64_tr_b16 v[78:79], v5 offset:6144\n\t"
"ds_write_b128 v16, v[196:199] offset:9216\n\t"
"s_lshl_b32 s82, s40, 8\n\t"
"v_mbcnt_lo_u32_b32 v212, -1, 0\n\t"
"v_mbcnt_hi_u32_b32 v212, -1, v212\n\t"
"v_min_u32 v212, 31, v212\n\t"
"v_lshl_add_u32 v212, v212, 3, s82\n\t"
"global_load_dwordx2 v[214:215], v212, s[42:43] sc1\n\t"
"s_waitcnt lgkmcnt(6)\n\t"
"v_mfma_f32_16x16x32_bf16 v[32:35], v[48:51], v[56:59], v[32:35]\n\t"
"ds_read_b128 v[80:83], v2 offset:8192\n\t"
"v_accvgpr_read_b32 v200, a48\n\t"
"v_accvgpr_read_b32 v201, a49\n\t"
"s_lshl_b32 s81, s79, 1\n\t"
"s_add_u32 m0, s81, 98304\n\t"
"s_add_u32 s81, s78, 86016\n\t"
"buffer_load_dwordx4 v10, s[60:63], s81 offen offset:0 sc1 lds\n\t"
"v_accvgpr_read_b32 v202, a50\n\t"
"ds_read_b128 v[84:87], v2 offset:10240\n\t"
"v_mfma_f32_16x16x32_bf16 v[36:39], v[48:51], v[60:63], v[36:39]\n\t"
"v_accvgpr_read_b32 v203, a51\n\t"
"ds_read_b64_tr_b16 v[88:89], v4 offset:8192\n\t"
"v_accvgpr_read_b32 v204, a52\n\t"
"ds_read_b64_tr_b16 v[90:91], v4 offset:10240\n\t"
"v_accvgpr_read_b32 v205, a53\n\t"
"v_mfma_f32_16x16x32_bf16 v[40:43], v[52:55], v[56:59], v[40:43]\n\t"
"ds_read_b64_tr_b16 v[92:93], v5 offset:8192\n\t"
"v_accvgpr_read_b32 v206, a54\n\t"
"s_add_u32 s81, s78, 87040\n\t"
"buffer_load_dwordx4 v10, s[60:63], s81 offen offset:1024 sc1 lds\n\t"
"v_accvgpr_read_b32 v207, a55\n\t"
"ds_read_b64_tr_b16 v[94:95], v5 offset:10240\n\t"
"v_mfma_f32_16x16x32_bf16 v[44:47], v[52:55], v[60:63], v[44:47]\n\t"
"v_mul_f32 v200, v200, v18\n\t"
"ds_read_b128 v[96:99], v3 offset:8192\n\t"
"v_mul_f32 v201, v201, v18\n\t"
"ds_read_b128 v[100:103], v3 offset:10240\n\t"
"v_mul_f32 v202, v202, v18\n\t"
"s_waitcnt lgkmcnt(7)\n\t"
"v_mfma_f32_16x16x32_bf16 v[32:35], v[64:67], v[72:75], v[32:35]\n\t"
"ds_read_b64_tr_b16 v[104:105], v4 offset:12288\n\t"
"v_mul_f32 v203, v203, v18\n\t"
"s_add_u32 s81, s78, 84224\n\t"
"buffer_load_dwordx4 v10, s[60:63], s81 offen offset:2048 sc1 lds\n\t"
"v_mul_f32 v204, v204, v18\n\t"
"ds_read_b64_tr_b16 v[106:107], v4 offset:14336\n\t"
"v_mfma_f32_16x16x32_bf16 v[36:39], v[64:67], v[76:79], v[36:39]\n\t"
"v_mul_f32 v205, v205, v18\n\t"
"ds_read_b64_tr_b16 v[108:109], v5 offset:12288\n\t"
"v_mul_f32 v206, v206, v18\n\t"
"ds_read_b64_tr_b16 v[110:111], v5 offset:14336\n\t"
"v_mul_f32 v207, v207, v18\n\t"
"v_mfma_f32_16x16x32_bf16 v[40:43], v[68:71], v[72:75], v[40:43]\n\t"
"v_add_u32 v2, -65536, v2\n\t"
"v_cvt_pk_bf16_f32 v208, v200, v201\n\t"
"s_add_u32 s81, s78, 85248\n\t"
"buffer_load_dwordx4 v10, s[60:63], s81 offen offset:3072 sc1 lds\n\t"
"v_cvt_pk_bf16_f32 v209, v202, v203\n\t"
"v_add_u32 v3, -65536, v3\n\t"
"v_mfma_f32_16x16x32_bf16 v[44:47], v[68:71], v[76:79], v[44:47]\n\t"
"v_cvt_pk_bf16_f32 v210, v204, v205\n\t"
"v_add_u32 v4, -65536, v4\n\t"
"v_cvt_pk_bf16_f32 v211, v206, v207\n\t"
"v_add_u32 v5, -65536, v5\n\t"
"ds_write_b128 v16, v[208:211] offset:12288\n\t"
"s_waitcnt vmcnt(9) lgkmcnt(0)\n\t"
"s_barrier\n\t"
"v_mfma_f32_16x16x32_bf16 v[32:35], v[80:83], v[88:91], v[32:35]\n\t"
"ds_read_b128 v[48:51], v2 offset:0\n\t"
"v_accvgpr_read_b32 v128, a56\n\t"
"v_accvgpr_read_b32 v129, a57\n\t"
"s_add_u32 m0, s79, 65536\n\t"
"s_add_u32 s81, s76, 512\n\t"
"buffer_load_dwordx4 v6, s[56:59], s81 offen offset:0 lds\n\t"
"v_accvgpr_read_b32 v130, a58\n\t"
"v_mfma_f32_16x16x32_bf16 v[36:39], v[80:83], v[92:95], v[36:39]\n\t"
"ds_read_b128 v[52:55], v2 offset:2048\n\t"
"v_accvgpr_read_b32 v131, a59\n\t"
"ds_read_b64_tr_b16 v[56:57], v4 offset:0\n\t"
"v_accvgpr_read_b32 v132, a60\n\t"
"v_accvgpr_read_b32 v133, a61\n\t"
"v_mfma_f32_16x16x32_bf16 v[40:43], v[84:87], v[88:91], v[40:43]\n\t"
"ds_read_b64_tr_b16 v[58:59], v4 offset:2048\n\t"
"v_accvgpr_read_b32 v134, a62\n\t"
"buffer_load_dwordx4 v7, s[56:59], s81 offen offset:1024 lds\n\t"
"v_accvgpr_read_b32 v135, a63\n\t"
"v_mfma_f32_16x16x32_bf16 v[44:47], v[84:87], v[92:95], v[44:47]\n\t"
"ds_read_b64_tr_b16 v[60:61], v5 offset:0\n\t"
"v_mul_f32 v128, v128, v18\n\t"
"v_mul_f32 v129, v129, v18\n\t"
"ds_read_b64_tr_b16 v[62:63], v5 offset:2048\n\t"
"v_mul_f32 v130, v130, v18\n\t"
"v_mfma_f32_16x16x32_bf16 v[32:35], v[96:99], v[104:107], v[32:35]\n\t"
"ds_read_b128 v[64:67], v3 offset:0\n\t"
"v_mul_f32 v131, v131, v18\n\t"
"s_add_u32 m0, s79, 73728\n\t"
"s_add_u32 s81, s76, 640\n\t"
"buffer_load_dwordx4 v6, s[56:59], s81 offen offset:0 lds\n\t"
"v_mul_f32 v132, v132, v18\n\t"
"v_mul_f32 v133, v133, v18\n\t"
"v_mfma_f32_16x16x32_bf16 v[36:39], v[96:99], v[108:111], v[36:39]\n\t"
"ds_read_b128 v[68:71], v3 offset:2048\n\t"
"v_mul_f32 v134, v134, v18\n\t"
"ds_read_b64_tr_b16 v[72:73], v4 offset:4096\n\t"
"v_mul_f32 v135, v135, v18\n\t"
"v_mfma_f32_16x16x32_bf16 v[40:43], v[100:103], v[104:107], v[40:43]\n\t"
"ds_read_b64_tr_b16 v[74:75], v4 offset:6144\n\t"
"v_cvt_pk_bf16_f32 v136, v128, v129\n\t"
"v_cvt_pk_bf16_f32 v137, v130, v131\n\t"
"buffer_load_dwordx4 v7, s[56:59], s81 offen offset:1024 lds\n\t"
"v_cvt_pk_bf16_f32 v138, v132, v133\n\t"
"v_mfma_f32_16x16x32_bf16 v[44:47], v[100:103], v[108:111], v[44:47]\n\t"
"ds_read_b64_tr_b16 v[76:77], v5 offset:4096\n\t"
"v_cvt_pk_bf16_f32 v139, v134, v135\n\t"
"ds_read_b64_tr_b16 v[78:79], v5 offset:6144\n\t"
"ds_write_b128 v16, v[136:139] offset:13312\n\t"
"s_waitcnt lgkmcnt(7)\n\t"
"v_mfma_f32_16x16x32_bf16 v[32:35], v[48:51], v[56:59], v[32:35]\n\t"
"ds_read_b128 v[80:83], v2 offset:8192\n\t"
"v_accvgpr_read_b32 v140, a64\n\t"
"v_accvgpr_read_b32 v141, a65\n\t"
"s_lshl_b32 s81, s79, 1\n\t"
"s_add_u32 m0, s81, 81920\n\t"
"s_add_u32 s81, s78, 102400\n\t"
"buffer_load_dwordx4 v10, s[60:63], s81 offen offset:0 sc1 lds\n\t"
"v_accvgpr_read_b32 v142, a66\n\t"
"ds_read_b128 v[84:87], v2 offset:10240\n\t"
"v_mfma_f32_16x16x32_bf16 v[36:39], v[48:51], v[60:63], v[36:39]\n\t"
"v_accvgpr_read_b32 v143, a67\n\t"
"ds_read_b64_tr_b16 v[88:89], v4 offset:8192\n\t"
"v_accvgpr_read_b32 v144, a68\n\t"
"ds_read_b64_tr_b16 v[90:91], v4 offset:10240\n\t"
"v_accvgpr_read_b32 v145, a69\n\t"
"v_mfma_f32_16x16x32_bf16 v[40:43], v[52:55], v[56:59], v[40:43]\n\t"
"ds_read_b64_tr_b16 v[92:93], v5 offset:8192\n\t"
"v_accvgpr_read_b32 v146, a70\n\t"
"s_add_u32 s81, s78, 103424\n\t"
"buffer_load_dwordx4 v10, s[60:63], s81 offen offset:1024 sc1 lds\n\t"
"v_accvgpr_read_b32 v147, a71\n\t"
"ds_read_b64_tr_b16 v[94:95], v5 offset:10240\n\t"
"v_mfma_f32_16x16x32_bf16 v[44:47], v[52:55], v[60:63], v[44:47]\n\t"
"v_mul_f32 v140, v140, v18\n\t"
"ds_read_b128 v[96:99], v3 offset:8192\n\t"
"v_mul_f32 v141, v141, v18\n\t"
"ds_read_b128 v[100:103], v3 offset:10240\n\t"
"v_mul_f32 v142, v142, v18\n\t"
"s_waitcnt lgkmcnt(7)\n\t"
"v_mfma_f32_16x16x32_bf16 v[32:35], v[64:67], v[72:75], v[32:35]\n\t"
"ds_read_b64_tr_b16 v[104:105], v4 offset:12288\n\t"
"v_mul_f32 v143, v143, v18\n\t"
"s_add_u32 s81, s78, 100608\n\t"
"buffer_load_dwordx4 v10, s[60:63], s81 offen offset:2048 sc1 lds\n\t"
"v_mul_f32 v144, v144, v18\n\t"
"ds_read_b64_tr_b16 v[106:107], v4 offset:14336\n\t"
"v_mfma_f32_16x16x32_bf16 v[36:39], v[64:67], v[76:79], v[36:39]\n\t"
"v_mul_f32 v145, v145, v18\n\t"
"ds_read_b64_tr_b16 v[108:109], v5 offset:12288\n\t"
"v_mul_f32 v146, v146, v18\n\t"
"ds_read_b64_tr_b16 v[110:111], v5 offset:14336\n\t"
"v_mul_f32 v147, v147, v18\n\t"
"v_mfma_f32_16x16x32_bf16 v[40:43], v[68:71], v[72:75], v[40:43]\n\t"
"v_add_u32 v2, 32768, v2\n\t"
"v_cvt_pk_bf16_f32 v148, v140, v141\n\t"
"s_add_u32 s81, s78, 101632\n\t"
"buffer_load_dwordx4 v10, s[60:63], s81 offen offset:3072 sc1 lds\n\t"
"v_cvt_pk_bf16_f32 v149, v142, v143\n\t"
"v_add_u32 v3, 32768, v3\n\t"
"v_mfma_f32_16x16x32_bf16 v[44:47], v[68:71], v[76:79], v[44:47]\n\t"
"v_cvt_pk_bf16_f32 v150, v144, v145\n\t"
"v_add_u32 v4, 81920, v4\n\t"
"v_cvt_pk_bf16_f32 v151, v146, v147\n\t"
"v_add_u32 v5, 81920, v5\n\t"
"ds_write_b128 v17, v[148:151] offset:0\n\t"
"s_waitcnt vmcnt(8) lgkmcnt(0)\n\t"
"s_barrier\n\t"
"v_mfma_f32_16x16x32_bf16 v[32:35], v[80:83], v[88:91], v[32:35]\n\t"
"ds_read_b128 v[48:51], v2 offset:0\n\t"
"v_accvgpr_read_b32 v152, a72\n\t"
"v_accvgpr_read_b32 v153, a73\n\t"
"s_add_u32 m0, s79, 0\n\t"
"s_add_u32 s81, s76, 768\n\t"
"buffer_load_dwordx4 v6, s[56:59], s81 offen offset:0 lds\n\t"
"v_accvgpr_read_b32 v154, a74\n\t"
"v_mfma_f32_16x16x32_bf16 v[36:39], v[80:83], v[92:95], v[36:39]\n\t"
"ds_read_b128 v[52:55], v2 offset:2048\n\t"
"v_accvgpr_read_b32 v155, a75\n\t"
"ds_read_b64_tr_b16 v[56:57], v4 offset:0\n\t"
"v_accvgpr_read_b32 v156, a76\n\t"
"v_accvgpr_read_b32 v157, a77\n\t"
"v_mfma_f32_16x16x32_bf16 v[40:43], v[84:87], v[88:91], v[40:43]\n\t"
"ds_read_b64_tr_b16 v[58:59], v4 offset:2048\n\t"
"v_accvgpr_read_b32 v158, a78\n\t"
"buffer_load_dwordx4 v7, s[56:59], s81 offen offset:1024 lds\n\t"
"v_accvgpr_read_b32 v159, a79\n\t"
"v_mfma_f32_16x16x32_bf16 v[44:47], v[84:87], v[92:95], v[44:47]\n\t"
"ds_read_b64_tr_b16 v[60:61], v5 offset:0\n\t"
"v_mul_f32 v152, v152, v18\n\t"
"v_mul_f32 v153, v153, v18\n\t"
"ds_read_b64_tr_b16 v[62:63], v5 offset:2048\n\t"
"v_mul_f32 v154, v154, v18\n\t"
"v_mfma_f32_16x16x32_bf16 v[32:35], v[96:99], v[104:107], v[32:35]\n\t"
"ds_read_b128 v[64:67], v3 offset:0\n\t"
"v_mul_f32 v155, v155, v18\n\t"
"s_add_u32 m0, s79, 8192\n\t"
"s_add_u32 s81, s76, 896\n\t"
"buffer_load_dwordx4 v6, s[56:59], s81 offen offset:0 lds\n\t"
"v_mul_f32 v156, v156, v18\n\t"
"v_mul_f32 v157, v157, v18\n\t"
"v_mfma_f32_16x16x32_bf16 v[36:39], v[96:99], v[108:111], v[36:39]\n\t"
"ds_read_b128 v[68:71], v3 offset:2048\n\t"
"v_mul_f32 v158, v158, v18\n\t"
"ds_read_b64_tr_b16 v[72:73], v4 offset:4096\n\t"
"v_mul_f32 v159, v159, v18\n\t"
"v_mfma_f32_16x16x32_bf16 v[40:43], v[100:103], v[104:107], v[40:43]\n\t"
"ds_read_b64_tr_b16 v[74:75], v4 offset:6144\n\t"
"v_cvt_pk_bf16_f32 v160, v152, v153\n\t"
"v_cvt_pk_bf16_f32 v161, v154, v155\n\t"
"buffer_load_dwordx4 v7, s[56:59], s81 offen offset:1024 lds\n\t"
"v_cvt_pk_bf16_f32 v162, v156, v157\n\t"
"v_mfma_f32_16x16x32_bf16 v[44:47], v[100:103], v[108:111], v[44:47]\n\t"
"ds_read_b64_tr_b16 v[76:77], v5 offset:4096\n\t"
"v_cvt_pk_bf16_f32 v163, v158, v159\n\t"
"ds_read_b64_tr_b16 v[78:79], v5 offset:6144\n\t"
"ds_write_b128 v17, v[160:163] offset:1024\n\t"
"s_waitcnt lgkmcnt(7)\n\t"
"v_mfma_f32_16x16x32_bf16 v[32:35], v[48:51], v[56:59], v[32:35]\n\t"
"ds_read_b128 v[80:83], v2 offset:8192\n\t"
"v_accvgpr_read_b32 v164, a80\n\t"
"v_accvgpr_read_b32 v165, a81\n\t"
"s_lshl_b32 s81, s79, 1\n\t"
"s_add_u32 m0, s81, 16384\n\t"
"s_add_u32 s81, s78, 118784\n\t"
"buffer_load_dwordx4 v10, s[60:63], s81 offen offset:0 sc1 lds\n\t"
"v_accvgpr_read_b32 v166, a82\n\t"
"ds_read_b128 v[84:87], v2 offset:10240\n\t"
"v_mfma_f32_16x16x32_bf16 v[36:39], v[48:51], v[60:63], v[36:39]\n\t"
"v_accvgpr_read_b32 v167, a83\n\t"
"ds_read_b64_tr_b16 v[88:89], v4 offset:8192\n\t"
"v_accvgpr_read_b32 v168, a84\n\t"
"ds_read_b64_tr_b16 v[90:91], v4 offset:10240\n\t"
"v_accvgpr_read_b32 v169, a85\n\t"
"v_mfma_f32_16x16x32_bf16 v[40:43], v[52:55], v[56:59], v[40:43]\n\t"
"ds_read_b64_tr_b16 v[92:93], v5 offset:8192\n\t"
"v_accvgpr_read_b32 v170, a86\n\t"
"s_add_u32 s81, s78, 119808\n\t"
"buffer_load_dwordx4 v10, s[60:63], s81 offen offset:1024 sc1 lds\n\t"
"v_accvgpr_read_b32 v171, a87\n\t"
"ds_read_b64_tr_b16 v[94:95], v5 offset:10240\n\t"
"v_mfma_f32_16x16x32_bf16 v[44:47], v[52:55], v[60:63], v[44:47]\n\t"
"v_mul_f32 v164, v164, v18\n\t"
"ds_read_b128 v[96:99], v3 offset:8192\n\t"
"v_mul_f32 v165, v165, v18\n\t"
"ds_read_b128 v[100:103], v3 offset:10240\n\t"
"v_mul_f32 v166, v166, v18\n\t"
"s_waitcnt lgkmcnt(7)\n\t"
"v_mfma_f32_16x16x32_bf16 v[32:35], v[64:67], v[72:75], v[32:35]\n\t"
"ds_read_b64_tr_b16 v[104:105], v4 offset:12288\n\t"
"v_mul_f32 v167, v167, v18\n\t"
"s_add_u32 s81, s78, 116992\n\t"
"buffer_load_dwordx4 v10, s[60:63], s81 offen offset:2048 sc1 lds\n\t"
"v_mul_f32 v168, v168, v18\n\t"
"ds_read_b64_tr_b16 v[106:107], v4 offset:14336\n\t"
"v_mfma_f32_16x16x32_bf16 v[36:39], v[64:67], v[76:79], v[36:39]\n\t"
"v_mul_f32 v169, v169, v18\n\t"
"ds_read_b64_tr_b16 v[108:109], v5 offset:12288\n\t"
"v_mul_f32 v170, v170, v18\n\t"
"ds_read_b64_tr_b16 v[110:111], v5 offset:14336\n\t"
"v_mul_f32 v171, v171, v18\n\t"
"v_mfma_f32_16x16x32_bf16 v[40:43], v[68:71], v[72:75], v[40:43]\n\t"
"v_add_u32 v2, 32768, v2\n\t"
"v_cvt_pk_bf16_f32 v172, v164, v165\n\t"
"s_add_u32 s81, s78, 118016\n\t"
"buffer_load_dwordx4 v10, s[60:63], s81 offen offset:3072 sc1 lds\n\t"
"v_cvt_pk_bf16_f32 v173, v166, v167\n\t"
"v_add_u32 v3, 32768, v3\n\t"
"v_mfma_f32_16x16x32_bf16 v[44:47], v[68:71], v[76:79], v[44:47]\n\t"
"v_cvt_pk_bf16_f32 v174, v168, v169\n\t"
"v_add_u32 v4, -16384, v4\n\t"
"v_cvt_pk_bf16_f32 v175, v170, v171\n\t"
"v_add_u32 v5, -16384, v5\n\t"
"ds_write_b128 v17, v[172:175] offset:4096\n\t"
"s_waitcnt vmcnt(8) lgkmcnt(0)\n\t"
"s_barrier\n\t"
"s_mov_b32 s82, 0\n\t"
"s_waitcnt vmcnt(20)\n\t"
"LFP10_%=:\n\t"
"v_cmp_eq_u64 vcc, s[26:27], v[214:215]\n\t"
"s_nop 1\n\t"
"s_cmp_eq_u64 vcc, exec\n\t"
"s_cbranch_scc1 LFO11_%=\n\t"
"s_sleep 1\n\t"
"global_load_dwordx2 v[214:215], v212, s[42:43] sc1\n\t"
"s_waitcnt vmcnt(0)\n\t"
"s_add_u32 s82, s82, 1\n\t"
"s_cmp_lt_u32 s82, 0x40000\n\t"
"s_cbranch_scc1 LFP10_%=\n\t"
"LFO11_%=:\n\t"
"s_mov_b64 s[88:89], s[68:69]\n\t"
"s_mov_b64 s[90:91], s[70:71]\n\t"
"s_lshl_b32 s71, s40, 19\n\t"
"s_add_u32 s48, s42, 8388608\n\t"
"s_addc_u32 s49, s43, 0\n\t"
"s_add_u32 s48, s48, s71\n\t"
"s_addc_u32 s49, s49, 0\n\t"
"s_and_b32 s49, s49, 0xffff\n\t"
"s_mov_b32 s50, 524288\n\t"
"s_mov_b32 s51, 0x00020000\n\t"
"s_add_u32 s52, s42, 25165824\n\t"
"s_addc_u32 s53, s43, 0\n\t"
"s_add_u32 s52, s52, s71\n\t"
"s_addc_u32 s53, s53, 0\n\t"
"s_and_b32 s53, s53, 0xffff\n\t"
"s_mov_b32 s54, 524288\n\t"
"s_mov_b32 s55, 0x00020000\n\t"
"s_add_u32 s56, s42, 41943040\n\t"
"s_addc_u32 s57, s43, 0\n\t"
"s_and_b32 s57, s57, 0xffff\n\t"
"s_mov_b32 s58, 16777216\n\t"
"s_mov_b32 s59, 0x00020000\n\t"
"s_mov_b32 s68, 0x5d800000\n\t"
"s_lshl_b32 s71, s45, 14\n\t"
"s_lshl_b32 s72, s44, 13\n\t"
"s_add_u32 s70, s71, s72\n\t"
"s_mov_b32 s60, s70\n\t"
"s_mov_b32 s61, s70\n\t"
"s_sub_u32 s66, 16, s41\n\t"
"s_mov_b32 s62, s66\n\t"
"s_mov_b32 s63, s66\n\t"
"s_add_u32 s69, s41, 1\n\t"
"s_add_u32 s64, s70, 65536\n\t"
"s_lshl_b32 s71, s45, 16\n\t"
"s_add_u32 s65, s71, s72\n\t"
"s_add_u32 s65, s65, 32768\n\t"
"s_mov_b32 s73, 0\n\t"
"v_mov_b32 v0, v14\n\t"
"v_mfma_f32_16x16x32_bf16 v[32:35], v[80:83], v[88:91], v[32:35]\n\t"
"ds_read_b128 v[48:51], v2 offset:0\n\t"
"v_accvgpr_read_b32 v176, a88\n\t"
"v_accvgpr_read_b32 v177, a89\n\t"
"s_mov_b32 m0, s65\n\t"
"s_add_u32 s71, s61, 4096\n\t"
"buffer_load_dwordx4 v0, s[52:55], s61 offen offset:0 sc1 lds\n\t"
"v_accvgpr_read_b32 v178, a90\n\t"
"ds_read_b128 v[52:55], v2 offset:2048\n\t"
"v_mfma_f32_16x16x32_bf16 v[36:39], v[80:83], v[92:95], v[36:39]\n\t"
"v_accvgpr_read_b32 v179, a91\n\t"
"buffer_load_dwordx4 v0, s[52:55], s61 offen offset:1024 sc1 lds\n\t"
"v_accvgpr_read_b32 v180, a92\n\t"
"ds_read_b64_tr_b16 v[56:57], v4 offset:0\n\t"
"v_accvgpr_read_b32 v181, a93\n\t"
"v_mfma_f32_16x16x32_bf16 v[40:43], v[84:87], v[88:91], v[40:43]\n\t"
"ds_read_b64_tr_b16 v[58:59], v4 offset:2048\n\t"
"v_accvgpr_read_b32 v182, a94\n\t"
"buffer_load_dwordx4 v0, s[52:55], s61 offen offset:2048 sc1 lds\n\t"
"v_accvgpr_read_b32 v183, a95\n\t"
"ds_read_b64_tr_b16 v[60:61], v5 offset:0\n\t"
"v_mfma_f32_16x16x32_bf16 v[44:47], v[84:87], v[92:95], v[44:47]\n\t"
"v_mul_f32 v176, v176, v18\n\t"
"buffer_load_dwordx4 v0, s[52:55], s61 offen offset:3072 sc1 lds\n\t"
"v_mul_f32 v177, v177, v18\n\t"
"ds_read_b64_tr_b16 v[62:63], v5 offset:2048\n\t"
"v_mul_f32 v178, v178, v18\n\t"
"v_mfma_f32_16x16x32_bf16 v[32:35], v[96:99], v[104:107], v[32:35]\n\t"
"ds_read_b128 v[64:67], v3 offset:0\n\t"
"v_mul_f32 v179, v179, v18\n\t"
"s_add_u32 m0, s65, 4096\n\t"
"s_add_u32 s61, s61, 32768\n\t"
"buffer_load_dwordx4 v0, s[52:55], s71 offen offset:0 sc1 lds\n\t"
"v_mul_f32 v180, v180, v18\n\t"
"ds_read_b128 v[68:71], v3 offset:2048\n\t"
"v_mfma_f32_16x16x32_bf16 v[36:39], v[96:99], v[108:111], v[36:39]\n\t"
"v_mul_f32 v181, v181, v18\n\t"
"s_sub_u32 s63, s63, 1\n\t"
"s_cmp_eq_u32 s63, 0\n\t"
"s_cselect_b32 s61, s70, s61\n\t"
"s_cselect_b32 s63, s69, s63\n\t"
"buffer_load_dwordx4 v0, s[52:55], s71 offen offset:1024 sc1 lds\n\t"
"v_mul_f32 v182, v182, v18\n\t"
"ds_read_b64_tr_b16 v[72:73], v4 offset:4096\n\t"
"v_mul_f32 v183, v183, v18\n\t"
"v_mfma_f32_16x16x32_bf16 v[40:43], v[100:103], v[104:107], v[40:43]\n\t"
"ds_read_b64_tr_b16 v[74:75], v4 offset:6144\n\t"
"v_cvt_pk_bf16_f32 v184, v176, v177\n\t"
"s_xor_b32 s65, s65, 16384\n\t"
"buffer_load_dwordx4 v0, s[52:55], s71 offen offset:2048 sc1 lds\n\t"
"v_cvt_pk_bf16_f32 v185, v178, v179\n\t"
"ds_read_b64_tr_b16 v[76:77], v5 offset:4096\n\t"
"v_mfma_f32_16x16x32_bf16 v[44:47], v[100:103], v[108:111], v[44:47]\n\t"
"v_cvt_pk_bf16_f32 v186, v180, v181\n\t"
"buffer_load_dwordx4 v0, s[52:55], s71 offen offset:3072 sc1 lds\n\t"
"v_cvt_pk_bf16_f32 v187, v182, v183\n\t"
"ds_read_b64_tr_b16 v[78:79], v5 offset:6144\n\t"
"ds_write_b128 v17, v[184:187] offset:5120\n\t"
"s_waitcnt lgkmcnt(6)\n\t"
"v_mfma_f32_16x16x32_bf16 v[32:35], v[48:51], v[56:59], v[32:35]\n\t"
"ds_read_b128 v[80:83], v2 offset:8192\n\t"
"v_accvgpr_read_b32 v188, a96\n\t"
"v_accvgpr_read_b32 v189, a97\n\t"
"ds_read_b128 v[84:87], v2 offset:10240\n\t"
"v_accvgpr_read_b32 v190, a98\n\t"
"v_mfma_f32_16x16x32_bf16 v[36:39], v[48:51], v[60:63], v[36:39]\n\t"
"ds_read_b64_tr_b16 v[88:89], v4 offset:8192\n\t"
"v_accvgpr_read_b32 v191, a99\n\t"
"ds_read_b64_tr_b16 v[90:91], v4 offset:10240\n\t"
"v_accvgpr_read_b32 v192, a100\n\t"
"v_accvgpr_read_b32 v193, a101\n\t"
"v_mfma_f32_16x16x32_bf16 v[40:43], v[52:55], v[56:59], v[40:43]\n\t"
"ds_read_b64_tr_b16 v[92:93], v5 offset:8192\n\t"
"v_accvgpr_read_b32 v194, a102\n\t"
"ds_read_b64_tr_b16 v[94:95], v5 offset:10240\n\t"
"v_accvgpr_read_b32 v195, a103\n\t"
"v_mfma_f32_16x16x32_bf16 v[44:47], v[52:55], v[60:63], v[44:47]\n\t"
"ds_read_b128 v[96:99], v3 offset:8192\n\t"
"v_mul_f32 v188, v188, v18\n\t"
"v_mul_f32 v189, v189, v18\n\t"
"ds_read_b128 v[100:103], v3 offset:10240\n\t"
"v_mul_f32 v190, v190, v18\n\t"
"s_waitcnt lgkmcnt(9)\n\t"
"v_mfma_f32_16x16x32_bf16 v[32:35], v[64:67], v[72:75], v[32:35]\n\t"
"ds_read_b64_tr_b16 v[104:105], v4 offset:12288\n\t"
"v_mul_f32 v191, v191, v18\n\t"
"ds_read_b64_tr_b16 v[106:107], v4 offset:14336\n\t"
"v_mul_f32 v192, v192, v18\n\t"
"v_mul_f32 v193, v193, v18\n\t"
"v_mfma_f32_16x16x32_bf16 v[36:39], v[64:67], v[76:79], v[36:39]\n\t"
"ds_read_b64_tr_b16 v[108:109], v5 offset:12288\n\t"
"v_mul_f32 v194, v194, v18\n\t"
"ds_read_b64_tr_b16 v[110:111], v5 offset:14336\n\t"
"v_mul_f32 v195, v195, v18\n\t"
"v_mfma_f32_16x16x32_bf16 v[40:43], v[68:71], v[72:75], v[40:43]\n\t"
"v_add_u32 v2, -65536, v2\n\t"
"v_cvt_pk_bf16_f32 v196, v188, v189\n\t"
"v_cvt_pk_bf16_f32 v197, v190, v191\n\t"
"v_add_u32 v3, -65536, v3\n\t"
"v_cvt_pk_bf16_f32 v198, v192, v193\n\t"
"v_mfma_f32_16x16x32_bf16 v[44:47], v[68:71], v[76:79], v[44:47]\n\t"
"v_add_u32 v4, -65536, v4\n\t"
"v_cvt_pk_bf16_f32 v199, v194, v195\n\t"
"v_add_u32 v5, -65536, v5\n\t"
"ds_write_b128 v17, v[196:199] offset:8192\n\t"
"s_waitcnt vmcnt(8) lgkmcnt(0)\n\t"
"s_barrier\n\t"
"v_mfma_f32_16x16x32_bf16 v[32:35], v[80:83], v[88:91], v[32:35]\n\t"
"ds_read_b128 v[48:51], v2 offset:0\n\t"
"v_accvgpr_read_b32 v200, a104\n\t"
"v_accvgpr_read_b32 v201, a105\n\t"
"s_mov_b32 m0, s64\n\t"
"s_add_u32 s71, s60, 4096\n\t"
"buffer_load_dwordx4 v0, s[48:51], s60 offen offset:0 sc1 lds\n\t"
"v_accvgpr_read_b32 v202, a106\n\t"
"ds_read_b128 v[52:55], v2 offset:2048\n\t"
"v_mfma_f32_16x16x32_bf16 v[36:39], v[80:83], v[92:95], v[36:39]\n\t"
"v_accvgpr_read_b32 v203, a107\n\t"
"buffer_load_dwordx4 v0, s[48:51], s60 offen offset:1024 sc1 lds\n\t"
"v_accvgpr_read_b32 v204, a108\n\t"
"ds_read_b64_tr_b16 v[56:57], v4 offset:0\n\t"
"v_accvgpr_read_b32 v205, a109\n\t"
"v_mfma_f32_16x16x32_bf16 v[40:43], v[84:87], v[88:91], v[40:43]\n\t"
"ds_read_b64_tr_b16 v[58:59], v4 offset:2048\n\t"
"v_accvgpr_read_b32 v206, a110\n\t"
"buffer_load_dwordx4 v0, s[48:51], s60 offen offset:2048 sc1 lds\n\t"
"v_accvgpr_read_b32 v207, a111\n\t"
"ds_read_b64_tr_b16 v[60:61], v5 offset:0\n\t"
"v_mfma_f32_16x16x32_bf16 v[44:47], v[84:87], v[92:95], v[44:47]\n\t"
"v_mul_f32 v200, v200, v18\n\t"
"buffer_load_dwordx4 v0, s[48:51], s60 offen offset:3072 sc1 lds\n\t"
"v_mul_f32 v201, v201, v18\n\t"
"ds_read_b64_tr_b16 v[62:63], v5 offset:2048\n\t"
"v_mul_f32 v202, v202, v18\n\t"
"v_mfma_f32_16x16x32_bf16 v[32:35], v[96:99], v[104:107], v[32:35]\n\t"
"ds_read_b128 v[64:67], v3 offset:0\n\t"
"v_mul_f32 v203, v203, v18\n\t"
"s_add_u32 m0, s64, 4096\n\t"
"s_add_u32 s60, s60, 32768\n\t"
"buffer_load_dwordx4 v0, s[48:51], s71 offen offset:0 sc1 lds\n\t"
"v_mul_f32 v204, v204, v18\n\t"
"ds_read_b128 v[68:71], v3 offset:2048\n\t"
"v_mfma_f32_16x16x32_bf16 v[36:39], v[96:99], v[108:111], v[36:39]\n\t"
"v_mul_f32 v205, v205, v18\n\t"
"s_sub_u32 s62, s62, 1\n\t"
"s_cmp_eq_u32 s62, 0\n\t"
"s_cselect_b32 s60, s70, s60\n\t"
"s_cselect_b32 s62, s69, s62\n\t"
"buffer_load_dwordx4 v0, s[48:51], s71 offen offset:1024 sc1 lds\n\t"
"v_mul_f32 v206, v206, v18\n\t"
"ds_read_b64_tr_b16 v[72:73], v4 offset:4096\n\t"
"v_mul_f32 v207, v207, v18\n\t"
"v_mfma_f32_16x16x32_bf16 v[40:43], v[100:103], v[104:107], v[40:43]\n\t"
"ds_read_b64_tr_b16 v[74:75], v4 offset:6144\n\t"
"v_cvt_pk_bf16_f32 v208, v200, v201\n\t"
"s_xor_b32 s64, s64, 65536\n\t"
"buffer_load_dwordx4 v0, s[48:51], s71 offen offset:2048 sc1 lds\n\t"
"v_cvt_pk_bf16_f32 v209, v202, v203\n\t"
"ds_read_b64_tr_b16 v[76:77], v5 offset:4096\n\t"
"v_mfma_f32_16x16x32_bf16 v[44:47], v[100:103], v[108:111], v[44:47]\n\t"
"v_cvt_pk_bf16_f32 v210, v204, v205\n\t"
"buffer_load_dwordx4 v0, s[48:51], s71 offen offset:3072 sc1 lds\n\t"
"v_cvt_pk_bf16_f32 v211, v206, v207\n\t"
"ds_read_b64_tr_b16 v[78:79], v5 offset:6144\n\t"
"ds_write_b128 v17, v[208:211] offset:9216\n\t"
"s_waitcnt lgkmcnt(6)\n\t"
"v_mfma_f32_16x16x32_bf16 v[32:35], v[48:51], v[56:59], v[32:35]\n\t"
"ds_read_b128 v[80:83], v2 offset:8192\n\t"
"v_accvgpr_read_b32 v128, a112\n\t"
"v_accvgpr_read_b32 v129, a113\n\t"
"ds_read_b128 v[84:87], v2 offset:10240\n\t"
"v_accvgpr_read_b32 v130, a114\n\t"
"v_mfma_f32_16x16x32_bf16 v[36:39], v[48:51], v[60:63], v[36:39]\n\t"
"ds_read_b64_tr_b16 v[88:89], v4 offset:8192\n\t"
"v_accvgpr_read_b32 v131, a115\n\t"
"ds_read_b64_tr_b16 v[90:91], v4 offset:10240\n\t"
"v_accvgpr_read_b32 v132, a116\n\t"
"v_accvgpr_read_b32 v133, a117\n\t"
"v_mfma_f32_16x16x32_bf16 v[40:43], v[52:55], v[56:59], v[40:43]\n\t"
"ds_read_b64_tr_b16 v[92:93], v5 offset:8192\n\t"
"v_accvgpr_read_b32 v134, a118\n\t"
"ds_read_b64_tr_b16 v[94:95], v5 offset:10240\n\t"
"v_accvgpr_read_b32 v135, a119\n\t"
"v_mfma_f32_16x16x32_bf16 v[44:47], v[52:55], v[60:63], v[44:47]\n\t"
"ds_read_b128 v[96:99], v3 offset:8192\n\t"
"v_mul_f32 v128, v128, v18\n\t"
"v_mul_f32 v129, v129, v18\n\t"
"ds_read_b128 v[100:103], v3 offset:10240\n\t"
"v_mul_f32 v130, v130, v18\n\t"
"s_waitcnt lgkmcnt(9)\n\t"
"v_mfma_f32_16x16x32_bf16 v[32:35], v[64:67], v[72:75], v[32:35]\n\t"
"ds_read_b64_tr_b16 v[104:105], v4 offset:12288\n\t"
"v_mul_f32 v131, v131, v18\n\t"
"ds_read_b64_tr_b16 v[106:107], v4 offset:14336\n\t"
"v_mul_f32 v132, v132, v18\n\t"
"v_mul_f32 v133, v133, v18\n\t"
"v_mfma_f32_16x16x32_bf16 v[36:39], v[64:67], v[76:79], v[36:39]\n\t"
"ds_read_b64_tr_b16 v[108:109], v5 offset:12288\n\t"
"v_mul_f32 v134, v134, v18\n\t"
"ds_read_b64_tr_b16 v[110:111], v5 offset:14336\n\t"
"v_mul_f32 v135, v135, v18\n\t"
"v_mfma_f32_16x16x32_bf16 v[40:43], v[68:71], v[72:75], v[40:43]\n\t"
"v_add_u32 v2, 32768, v2\n\t"
"v_cvt_pk_bf16_f32 v136, v128, v129\n\t"
"v_cvt_pk_bf16_f32 v137, v130, v131\n\t"
"v_add_u32 v3, 32768, v3\n\t"
"v_cvt_pk_bf16_f32 v138, v132, v133\n\t"
"v_mfma_f32_16x16x32_bf16 v[44:47], v[68:71], v[76:79], v[44:47]\n\t"
"v_add_u32 v4, 81920, v4\n\t"
"v_cvt_pk_bf16_f32 v139, v134, v135\n\t"
"v_add_u32 v5, 81920, v5\n\t"
"ds_write_b128 v17, v[136:139] offset:12288\n\t"
"s_waitcnt lgkmcnt(0)\n\t"
"s_barrier\n\t"
"v_mfma_f32_16x16x32_bf16 v[32:35], v[80:83], v[88:91], v[32:35]\n\t"
"s_mov_b32 m0, s64\n\t"
"s_add_u32 s71, s60, 4096\n\t"
"buffer_load_dwordx4 v0, s[48:51], s60 offen offset:0 sc1 lds\n\t"
"v_accvgpr_read_b32 v140, a120\n\t"
"v_accvgpr_read_b32 v141, a121\n\t"
"v_accvgpr_read_b32 v142, a122\n\t"
"v_mfma_f32_16x16x32_bf16 v[36:39], v[80:83], v[92:95], v[36:39]\n\t"
"buffer_load_dwordx4 v0, s[48:51], s60 offen offset:1024 sc1 lds\n\t"
"v_accvgpr_read_b32 v143, a123\n\t"
"v_accvgpr_read_b32 v144, a124\n\t"
"v_accvgpr_read_b32 v145, a125\n\t"
"v_mfma_f32_16x16x32_bf16 v[40:43], v[84:87], v[88:91], v[40:43]\n\t"
"buffer_load_dwordx4 v0, s[48:51], s60 offen offset:2048 sc1 lds\n\t"
"v_accvgpr_read_b32 v146, a126\n\t"
"v_accvgpr_read_b32 v147, a127\n\t"
"v_mfma_f32_16x16x32_bf16 v[44:47], v[84:87], v[92:95], v[44:47]\n\t"
"buffer_load_dwordx4 v0, s[48:51], s60 offen offset:3072 sc1 lds\n\t"
"v_mul_f32 v140, v140, v18\n\t"
"v_mul_f32 v141, v141, v18\n\t"
"v_mul_f32 v142, v142, v18\n\t"
"v_mfma_f32_16x16x32_bf16 v[32:35], v[96:99], v[104:107], v[32:35]\n\t"
"s_add_u32 m0, s64, 4096\n\t"
"s_add_u32 s60, s60, 32768\n\t"
"buffer_load_dwordx4 v0, s[48:51], s71 offen offset:0 sc1 lds\n\t"
"v_mul_f32 v143, v143, v18\n\t"
"v_mul_f32 v144, v144, v18\n\t"
"v_mul_f32 v145, v145, v18\n\t"
"v_mfma_f32_16x16x32_bf16 v[36:39], v[96:99], v[108:111], v[36:39]\n\t"
"s_sub_u32 s62, s62, 1\n\t"
"s_cmp_eq_u32 s62, 0\n\t"
"s_cselect_b32 s60, s70, s60\n\t"
"s_cselect_b32 s62, s69, s62\n\t"
"buffer_load_dwordx4 v0, s[48:51], s71 offen offset:1024 sc1 lds\n\t"
"v_mul_f32 v146, v146, v18\n\t"
"v_mul_f32 v147, v147, v18\n\t"
"v_mfma_f32_16x16x32_bf16 v[40:43], v[100:103], v[104:107], v[40:43]\n\t"
"s_xor_b32 s64, s64, 65536\n\t"
"buffer_load_dwordx4 v0, s[48:51], s71 offen offset:2048 sc1 lds\n\t"
"v_cvt_pk_bf16_f32 v148, v140, v141\n\t"
"v_cvt_pk_bf16_f32 v149, v142, v143\n\t"
"v_cvt_pk_bf16_f32 v150, v144, v145\n\t"
"v_mfma_f32_16x16x32_bf16 v[44:47], v[100:103], v[108:111], v[44:47]\n\t"
"buffer_load_dwordx4 v0, s[48:51], s71 offen offset:3072 sc1 lds\n\t"
"v_cvt_pk_bf16_f32 v151, v146, v147\n\t"
"ds_write_b128 v17, v[148:151] offset:13312\n\t"
"v_lshrrev_b32 v21, 4, v15\n\t"
"v_lshlrev_b32 v21, 4, v21\n\t"
"v_and_b32 v20, 15, v15\n\t"
"v_lshl_add_u32 v20, v20, 12, v21\n\t"
"s_lshl_b32 s82, s46, 18\n\t"
"s_lshl_b32 s83, s44, 17\n\t"
"s_add_u32 s82, s82, s83\n\t"
"s_lshl_b32 s83, s47, 8\n\t"
"s_add_u32 s82, s82, s83\n\t"
"s_lshl_b32 s83, s45, 7\n\t"
"s_add_u32 s82, s82, s83\n\t"
"s_add_u32 s83, s82, 65536\n\t"
"v_lshlrev_b32 v48, 16, v112\n\t"
"v_lshlrev_b32 v49, 16, v120\n\t"
"v_fmac_f32 v32, v48, v49\n\t"
"v_and_b32 v50, 0xffff0000, v112\n\t"
"v_and_b32 v51, 0xffff0000, v120\n\t"
"v_fmac_f32 v33, v50, v51\n\t"
"v_lshlrev_b32 v48, 16, v113\n\t"
"v_lshlrev_b32 v49, 16, v121\n\t"
"v_fmac_f32 v34, v48, v49\n\t"
"v_and_b32 v50, 0xffff0000, v113\n\t"
"v_and_b32 v51, 0xffff0000, v121\n\t"
"v_fmac_f32 v35, v50, v51\n\t"
"buffer_store_dwordx4 v[32:35], v20, s[88:91], s82 offen offset:2048\n\t"
"v_lshlrev_b32 v48, 16, v114\n\t"
"v_lshlrev_b32 v49, 16, v120\n\t"
"v_fmac_f32 v36, v48, v49\n\t"
"v_and_b32 v50, 0xffff0000, v114\n\t"
"v_and_b32 v51, 0xffff0000, v120\n\t"
"v_fmac_f32 v37, v50, v51\n\t"
"v_lshlrev_b32 v48, 16, v115\n\t"
"v_lshlrev_b32 v49, 16, v121\n\t"
"v_fmac_f32 v38, v48, v49\n\t"
"v_and_b32 v50, 0xffff0000, v115\n\t"
"v_and_b32 v51, 0xffff0000, v121\n\t"
"v_fmac_f32 v39, v50, v51\n\t"
"buffer_store_dwordx4 v[36:39], v20, s[88:91], s83 offen offset:2048\n\t"
"v_lshlrev_b32 v48, 16, v116\n\t"
"v_lshlrev_b32 v49, 16, v122\n\t"
"v_fmac_f32 v40, v48, v49\n\t"
"v_and_b32 v50, 0xffff0000, v116\n\t"
"v_and_b32 v51, 0xffff0000, v122\n\t"
"v_fmac_f32 v41, v50, v51\n\t"
"v_lshlrev_b32 v48, 16, v117\n\t"
"v_lshlrev_b32 v49, 16, v123\n\t"
"v_fmac_f32 v42, v48, v49\n\t"
"v_and_b32 v50, 0xffff0000, v117\n\t"
"v_and_b32 v51, 0xffff0000, v123\n\t"
"v_fmac_f32 v43, v50, v51\n\t"
"buffer_store_dwordx4 v[40:43], v20, s[88:91], s82 offen offset:2112\n\t"
"v_lshlrev_b32 v48, 16, v118\n\t"
"v_lshlrev_b32 v49, 16, v122\n\t"
"v_fmac_f32 v44, v48, v49\n\t"
"v_and_b32 v50, 0xffff0000, v118\n\t"
"v_and_b32 v51, 0xffff0000, v122\n\t"
"v_fmac_f32 v45, v50, v51\n\t"
"v_lshlrev_b32 v48, 16, v119\n\t"
"v_lshlrev_b32 v49, 16, v123\n\t"
"v_fmac_f32 v46, v48, v49\n\t"
"v_and_b32 v50, 0xffff0000, v119\n\t"
"v_and_b32 v51, 0xffff0000, v123\n\t"
"v_fmac_f32 v47, v50, v51\n\t"
"buffer_store_dwordx4 v[44:47], v20, s[88:91], s83 offen offset:2112\n\t"
"s_waitcnt vmcnt(12) lgkmcnt(0)\n\t"
"s_barrier\n\t"
"v_mbcnt_lo_u32_b32 v224, -1, 0\n\t"
"v_mbcnt_hi_u32_b32 v224, -1, v224\n\t"
"v_lshlrev_b32 v0, 4, v224\n\t"
"s_lshl_b32 s72, s44, 16\n\t"
"s_add_u32 s72, s72, 49152\n\t"
"v_add_u32 v228, s72, v0\n\t"
"ds_read_b128 a[128:131], v228 offset:0\n\t"
"ds_read_b128 a[132:135], v228 offset:1024\n\t"
"ds_read_b128 a[136:139], v228 offset:2048\n\t"
"ds_read_b128 a[140:143], v228 offset:3072\n\t"
"ds_read_b128 a[144:147], v228 offset:4096\n\t"
"ds_read_b128 a[148:151], v228 offset:5120\n\t"
"ds_read_b128 a[152:155], v228 offset:6144\n\t"
"ds_read_b128 a[156:159], v228 offset:7168\n\t"
"ds_read_b128 a[160:163], v228 offset:8192\n\t"
"ds_read_b128 a[164:167], v228 offset:9216\n\t"
"ds_read_b128 a[168:171], v228 offset:10240\n\t"
"ds_read_b128 a[172:175], v228 offset:11264\n\t"
"ds_read_b128 a[176:179], v228 offset:12288\n\t"
"ds_read_b128 a[180:183], v228 offset:13312\n\t"
"ds_read_b128 a[184:187], v228 offset:14336\n\t"
"ds_read_b128 a[188:191], v228 offset:15360\n\t"
"s_waitcnt lgkmcnt(14)\n\t"
"s_sub_u32 s75, 15, s41\n\t"
"s_lshl_b32 s74, s75, 3\n\t"
"s_lshl_b32 s72, s44, 2\n\t"
"s_add_u32 s74, s74, s72\n\t"
"s_lshl_b32 s74, s74, 7\n\t"
"s_lshl_b32 s72, s40, 2\n\t"
"s_add_u32 s74, s74, s72\n\t"
"s_lshl_b32 s72, s45, 1\n\t"
"s_add_u32 s74, s74, s72\n\t"
"s_lshl_b32 s74, s74, 10\n\t"
"s_lshl_b32 s72, s45, 14\n\t"
"s_add_u32 s72, s72, 65536\n\t"
"v_add_u32 v1, s72, v0\n\t"
"s_lshl_b32 s72, s45, 16\n\t"
"s_add_u32 s72, s72, 32768\n\t"
"v_add_u32 v2, s72, v0\n\t"
"v_and_b32 v225, 15, v224\n\t"
"v_lshrrev_b32 v226, 4, v224\n\t"
"v_lshlrev_b32 v226, 2, v226\n\t"
"v_sub_u32 v225, v225, v226\n\t"
"s_sub_i32 s72, s44, s45\n\t"
"s_lshl_b32 s72, s72, 6\n\t"
"v_add_u32 v225, s72, v225\n\t"
"v_cvt_f32_i32 v225, v225\n\t"
"v_add_f32 v225, 0.5, v225\n\t"
"v_mul_f32 v3, 0x7149f2ca, v225\n\t"
"v_mov_b32 v4, 0x3f803f80\n\t"
"v_mov_b32 v5, 0x3f803f80\n\t"
"v_mov_b32 v6, 0x3f803f80\n\t"
"v_mov_b32 v7, 0x3f803f80\n\t"
"v_subrev_f32 v60, 0x00000000, v3\n\t"
"v_subrev_f32 v61, 0x7149f2ca, v3\n\t"
"v_subrev_f32 v62, 0x71c9f2ca, v3\n\t"
"v_subrev_f32 v63, 0x72177617, v3\n\t"
"s_mov_b32 s72, 0x7149f2ca\n\t"
"s_mov_b32 s75, 0xf149f2ca\n\t"
"s_cmp_gt_u32 s44, s45\n\t"
"s_cselect_b32 s77, s72, s75\n\t"
"s_cmp_lt_u32 s44, s45\n\t"
"s_cselect_b32 s80, s75, s72\n\t"
"ds_read_b128 a[192:195], v1 offset:0\n\t"
"ds_read_b128 a[196:199], v1 offset:1024\n\t"
"s_waitcnt lgkmcnt(2)\n\t"
"ds_read_b128 a[200:203], v1 offset:2048\n\t"
"ds_read_b128 a[204:207], v1 offset:3072\n\t"
"ds_read_b128 a[208:211], v1 offset:4096\n\t"
"ds_read_b128 a[212:215], v1 offset:5120\n\t"
"ds_read_b128 a[216:219], v1 offset:6144\n\t"
"ds_read_b128 a[220:223], v1 offset:7168\n\t"
"ds_read_b128 a[224:227], v1 offset:8192\n\t"
"ds_read_b128 a[228:231], v1 offset:9216\n\t"
"ds_read_b128 a[232:235], v1 offset:10240\n\t"
"ds_read_b128 a[236:239], v1 offset:11264\n\t"
"ds_read_b128 a[240:243], v1 offset:12288\n\t"
"ds_read_b128 a[244:247], v1 offset:13312\n\t"
"ds_read_b128 a[248:251], v1 offset:14336\n\t"
"ds_read_b128 a[252:255], v1 offset:15360\n\t"
"s_waitcnt lgkmcnt(14)\n\t"
"v_xor_b32 v1, 65536, v1\n\t"
"s_waitcnt lgkmcnt(0)\n\t"
"LFIRST12_%=:\n\t"
"s_cmp_eq_u32 s66, 1\n\t"
"s_cselect_b32 s67, 1, 0\n\t"
"v_mov_b32 v252, 0\n\t"
"v_mov_b32 v253, 0\n\t"
"v_mov_b32 v254, 0\n\t"
"v_mov_b32 v255, 0\n\t"
"v_mfma_f32_16x16x32_bf16 v[64:67], a[192:195], a[128:131], 0\n\t"
"v_mfma_f32_16x16x32_bf16 v[80:83], a[192:195], a[144:147], 0\n\t"
"v_mfma_f32_16x16x32_bf16 v[64:67], a[196:199], a[132:135], v[64:67]\n\t"
"v_mfma_f32_16x16x32_bf16 v[80:83], a[196:199], a[148:151], v[80:83]\n\t"
"v_mfma_f32_16x16x32_bf16 v[64:67], a[200:203], a[136:139], v[64:67]\n\t"
"v_mfma_f32_16x16x32_bf16 v[80:83], a[200:203], a[152:155], v[80:83]\n\t"
"v_mfma_f32_16x16x32_bf16 v[64:67], a[204:207], a[140:143], v[64:67]\n\t"
"v_mfma_f32_16x16x32_bf16 v[80:83], a[204:207], a[156:159], v[80:83]\n\t"
"v_mfma_f32_16x16x32_bf16 v[68:71], a[208:211], a[128:131], 0\n\t"
"v_mfma_f32_16x16x32_bf16 v[84:87], a[208:211], a[144:147], 0\n\t"
"v_mfma_f32_16x16x32_bf16 v[68:71], a[212:215], a[132:135], v[68:71]\n\t"
"v_mfma_f32_16x16x32_bf16 v[84:87], a[212:215], a[148:151], v[84:87]\n\t"
"v_mfma_f32_16x16x32_bf16 v[68:71], a[216:219], a[136:139], v[68:71]\n\t"
"v_mfma_f32_16x16x32_bf16 v[84:87], a[216:219], a[152:155], v[84:87]\n\t"
"v_mfma_f32_16x16x32_bf16 v[68:71], a[220:223], a[140:143], v[68:71]\n\t"
"v_mfma_f32_16x16x32_bf16 v[84:87], a[220:223], a[156:159], v[84:87]\n\t"
"v_mfma_f32_16x16x32_bf16 v[72:75], a[224:227], a[128:131], 0\n\t"
"v_mfma_f32_16x16x32_bf16 v[88:91], a[224:227], a[144:147], 0\n\t"
"v_mfma_f32_16x16x32_bf16 v[72:75], a[228:231], a[132:135], v[72:75]\n\t"
"v_mfma_f32_16x16x32_bf16 v[88:91], a[228:231], a[148:151], v[88:91]\n\t"
"v_mfma_f32_16x16x32_bf16 v[72:75], a[232:235], a[136:139], v[72:75]\n\t"
"v_mfma_f32_16x16x32_bf16 v[88:91], a[232:235], a[152:155], v[88:91]\n\t"
"v_mfma_f32_16x16x32_bf16 v[72:75], a[236:239], a[140:143], v[72:75]\n\t"
"v_mfma_f32_16x16x32_bf16 v[88:91], a[236:239], a[156:159], v[88:91]\n\t"
"v_mfma_f32_16x16x32_bf16 v[76:79], a[240:243], a[128:131], 0\n\t"
"v_mfma_f32_16x16x32_bf16 v[92:95], a[240:243], a[144:147], 0\n\t"
"v_mfma_f32_16x16x32_bf16 v[76:79], a[244:247], a[132:135], v[76:79]\n\t"
"v_mfma_f32_16x16x32_bf16 v[92:95], a[244:247], a[148:151], v[92:95]\n\t"
"v_mfma_f32_16x16x32_bf16 v[76:79], a[248:251], a[136:139], v[76:79]\n\t"
"v_mfma_f32_16x16x32_bf16 v[92:95], a[248:251], a[152:155], v[92:95]\n\t"
"v_mfma_f32_16x16x32_bf16 v[76:79], a[252:255], a[140:143], v[76:79]\n\t"
"v_mfma_f32_16x16x32_bf16 v[92:95], a[252:255], a[156:159], v[92:95]\n\t"
"s_waitcnt vmcnt(0)\n\t"
"s_barrier\n\t"
"s_cmp_eq_u32 s73, 0\n\t"
"s_cbranch_scc1 LCF19_%=\n\t"
"s_sub_u32 s72, 15, s41\n\t"
"s_and_b32 s75, s38, 7\n\t"
"s_lshl_b32 s75, s75, 4\n\t"
"s_add_u32 s72, s75, s72\n\t"
"s_lshl_b32 s72, s72, 7\n\t"
"s_add_u32 s72, s72, 8192\n\t"
"s_lshr_b32 s76, s38, 6\n\t"
"s_lshl_b32 s75, s76, 5\n\t"
"s_add_u32 s72, s72, s75\n\t"
"s_lshl_b32 s75, s39, 3\n\t"
"s_add_u32 s72, s72, s75\n\t"
"v_mov_b32 v224, s72\n\t"
"v_mov_b32 v226, s26\n\t"
"v_mov_b32 v227, s27\n\t"
"s_mov_b64 s[78:79], exec\n\t"
"s_mov_b64 exec, 1\n\t"
"s_nop 1\n\t"
"global_store_dwordx2 v224, v[226:227], s[42:43] sc1\n\t"
"s_mov_b64 exec, s[78:79]\n\t"
"s_nop 1\n\t"
"LCF19_%=:\n\t"
"s_cmp_lg_u32 s67, 0\n\t"
"s_cbranch_scc1 LMH20_%=\n\t"
"LMR21_%=:\n\t"
"v_mfma_f32_16x16x32_bf16 a[8:11], v[252:255], v[252:255], 0\n\t"
"s_mov_b32 m0, s65\n\t"
"s_add_u32 s71, s61, 4096\n\t"
"buffer_load_dwordx4 v0, s[52:55], s61 offen offset:0 sc1 lds\n\t"
"v_max3_f32 v232, v64, v65, v66\n\t"
"v_max3_f32 v234, v80, v81, v82\n\t"
"v_max3_f32 v233, v67, v68, v69\n\t"
"v_max3_f32 v235, v83, v84, v85\n\t"
"v_max3_f32 v232, v232, v70, v71\n\t"
"v_mfma_f32_16x16x32_bf16 a[12:15], v[252:255], v[252:255], 0\n\t"
"ds_read_b128 v[160:163], v2 offset:0\n\t"
"v_max3_f32 v234, v234, v86, v87\n\t"
"v_max3_f32 v233, v233, v72, v73\n\t"
"v_max3_f32 v235, v235, v88, v89\n\t"
"v_max3_f32 v232, v232, v74, v75\n\t"
"v_mfma_f32_16x16x32_bf16 a[24:27], v[252:255], v[252:255], 0\n\t"
"buffer_load_dwordx4 v0, s[52:55], s61 offen offset:1024 sc1 lds\n\t"
"v_max3_f32 v234, v234, v90, v91\n\t"
"v_max3_f32 v233, v233, v76, v77\n\t"
"v_max3_f32 v235, v235, v92, v93\n\t"
"v_max3_f32 v232, v232, v78, v79\n\t"
"v_max3_f32 v234, v234, v94, v95\n\t"
"v_mfma_f32_16x16x32_bf16 a[28:31], v[252:255], v[252:255], 0\n\t"
"ds_read_b128 v[164:167], v2 offset:1024\n\t"
"v_max_f32 v232, v232, v233\n\t"
"v_max_f32 v234, v234, v235\n\t"
"v_mov_b32 v233, v232\n\t"
"v_mov_b32 v235, v234\n\t"
"v_mfma_f32_16x16x32_bf16 a[40:43], v[252:255], v[252:255], 0\n\t"
"buffer_load_dwordx4 v0, s[52:55], s61 offen offset:2048 sc1 lds\n\t"
"v_permlane32_swap_b32 v232, v233\n\t"
"v_permlane32_swap_b32 v234, v235\n\t"
"v_max_f32 v232, v232, v233\n\t"
"v_max_f32 v234, v234, v235\n\t"
"v_mov_b32 v233, v232\n\t"
"v_mfma_f32_16x16x32_bf16 a[44:47], v[252:255], v[252:255], 0\n\t"
"ds_read_b128 v[168:171], v2 offset:2048\n\t"
"v_mov_b32 v235, v234\n\t"
"v_permlane16_swap_b32 v232, v233\n\t"
"s_nop 0\n\t"
"v_permlane16_swap_b32 v234, v235\n\t"
"v_max_f32 v232, v232, v233\n\t"
"v_mfma_f32_16x16x32_bf16 a[56:59], v[252:255], v[252:255], 0\n\t"
"buffer_load_dwordx4 v0, s[52:55], s61 offen offset:3072 sc1 lds\n\t"
"v_max_f32 v234, v234, v235\n\t"
"v_mov_b32 v24, v232\n\t"
"v_mov_b32 v25, v234\n\t"
"v_mov_b32 v28, 0\n\t"
"v_mov_b32 v29, 0\n\t"
"v_mfma_f32_16x16x32_bf16 a[60:63], v[252:255], v[252:255], 0\n\t"
"ds_read_b128 v[172:175], v2 offset:3072\n\t"
"v_sub_f32 v64, v64, v232\n\t"
"v_sub_f32 v80, v80, v234\n\t"
"v_sub_f32 v65, v65, v232\n\t"
"v_sub_f32 v81, v81, v234\n\t"
"v_mfma_f32_16x16x32_bf16 a[72:75], v[252:255], v[252:255], 0\n\t"
"s_add_u32 m0, s65, 4096\n\t"
"s_add_u32 s61, s61, 32768\n\t"
"buffer_load_dwordx4 v0, s[52:55], s71 offen offset:0 sc1 lds\n\t"
"v_sub_f32 v66, v66, v232\n\t"
"v_sub_f32 v82, v82, v234\n\t"
"v_sub_f32 v67, v67, v232\n\t"
"v_sub_f32 v83, v83, v234\n\t"
"v_sub_f32 v68, v68, v232\n\t"
"v_mfma_f32_16x16x32_bf16 a[76:79], v[252:255], v[252:255], 0\n\t"
"ds_read_b128 v[176:179], v2 offset:4096\n\t"
"v_sub_f32 v84, v84, v234\n\t"
"v_sub_f32 v69, v69, v232\n\t"
"v_sub_f32 v85, v85, v234\n\t"
"v_sub_f32 v70, v70, v232\n\t"
"v_mfma_f32_16x16x32_bf16 a[88:91], v[252:255], v[252:255], 0\n\t"
"s_sub_u32 s63, s63, 1\n\t"
"s_cmp_eq_u32 s63, 0\n\t"
"s_cselect_b32 s61, s70, s61\n\t"
"s_cselect_b32 s63, s69, s63\n\t"
"buffer_load_dwordx4 v0, s[52:55], s71 offen offset:1024 sc1 lds\n\t"
"v_sub_f32 v86, v86, v234\n\t"
"v_sub_f32 v71, v71, v232\n\t"
"v_sub_f32 v87, v87, v234\n\t"
"v_sub_f32 v72, v72, v232\n\t"
"v_sub_f32 v88, v88, v234\n\t"
"v_mfma_f32_16x16x32_bf16 a[92:95], v[252:255], v[252:255], 0\n\t"
"ds_read_b128 v[180:183], v2 offset:5120\n\t"
"v_sub_f32 v73, v73, v232\n\t"
"v_sub_f32 v89, v89, v234\n\t"
"v_sub_f32 v74, v74, v232\n\t"
"v_sub_f32 v90, v90, v234\n\t"
"v_mfma_f32_16x16x32_bf16 a[104:107], v[252:255], v[252:255], 0\n\t"
"s_xor_b32 s65, s65, 16384\n\t"
"buffer_load_dwordx4 v0, s[52:55], s71 offen offset:2048 sc1 lds\n\t"
"v_sub_f32 v75, v75, v232\n\t"
"v_sub_f32 v91, v91, v234\n\t"
"v_sub_f32 v76, v76, v232\n\t"
"v_sub_f32 v92, v92, v234\n\t"
"v_sub_f32 v77, v77, v232\n\t"
"v_mfma_f32_16x16x32_bf16 a[108:111], v[252:255], v[252:255], 0\n\t"
"ds_read_b128 v[184:187], v2 offset:6144\n\t"
"v_sub_f32 v93, v93, v234\n\t"
"v_sub_f32 v78, v78, v232\n\t"
"v_sub_f32 v94, v94, v234\n\t"
"v_sub_f32 v79, v79, v232\n\t"
"v_mfma_f32_16x16x32_bf16 a[120:123], v[252:255], v[252:255], 0\n\t"
"buffer_load_dwordx4 v0, s[52:55], s71 offen offset:3072 sc1 lds\n\t"
"v_sub_f32 v95, v95, v234\n\t"
"v_sub_f32 v32, 0, v232\n\t"
"v_sub_f32 v36, 0, v234\n\t"
"v_sub_f32 v33, 0, v232\n\t"
"v_sub_f32 v37, 0, v234\n\t"
"v_mfma_f32_16x16x32_bf16 a[124:127], v[252:255], v[252:255], 0\n\t"
"ds_read_b128 v[188:191], v2 offset:7168\n\t"
"v_sub_f32 v34, 0, v232\n\t"
"v_sub_f32 v38, 0, v234\n\t"
"v_sub_f32 v35, 0, v232\n\t"
"v_sub_f32 v39, 0, v234\n\t"
"v_exp_f32 v48, v64\n\t"
"v_exp_f32 v49, v65\n\t"
"v_exp_f32 v50, v66\n\t"
"v_cvt_pk_bf16_f32 v128, v48, v49\n\t"
"v_exp_f32 v51, v67\n\t"
"v_exp_f32 v52, v68\n\t"
"v_cvt_pk_bf16_f32 v129, v50, v51\n\t"
"v_exp_f32 v53, v69\n\t"
"v_exp_f32 v54, v70\n\t"
"v_cvt_pk_bf16_f32 v130, v52, v53\n\t"
"v_exp_f32 v55, v71\n\t"
"v_exp_f32 v48, v80\n\t"
"v_cvt_pk_bf16_f32 v131, v54, v55\n\t"
"v_exp_f32 v49, v81\n\t"
"s_nop 0\n\t"
"v_mfma_f32_16x16x32_bf16 v[8:11], v[4:7], v[128:131], 0\n\t"
"v_exp_f32 v50, v82\n\t"
"v_cvt_pk_bf16_f32 v136, v48, v49\n\t"
"v_exp_f32 v51, v83\n\t"
"v_exp_f32 v52, v84\n\t"
"v_cvt_pk_bf16_f32 v137, v50, v51\n\t"
"v_exp_f32 v53, v85\n\t"
"v_exp_f32 v54, v86\n\t"
"v_cvt_pk_bf16_f32 v138, v52, v53\n\t"
"v_exp_f32 v55, v87\n\t"
"v_exp_f32 v48, v72\n\t"
"v_cvt_pk_bf16_f32 v139, v54, v55\n\t"
"v_exp_f32 v49, v73\n\t"
"s_nop 0\n\t"
"v_mfma_f32_16x16x32_bf16 v[12:15], v[4:7], v[136:139], 0\n\t"
"v_exp_f32 v50, v74\n\t"
"v_cvt_pk_bf16_f32 v132, v48, v49\n\t"
"v_exp_f32 v51, v75\n\t"
"v_mfma_f32_16x16x32_bf16 v[96:99], a[192:195], a[160:163], 0\n\t"
"v_exp_f32 v52, v76\n\t"
"v_mfma_f32_16x16x32_bf16 v[112:115], a[192:195], a[176:179], 0\n\t"
"v_cvt_pk_bf16_f32 v133, v50, v51\n\t"
"s_mov_b32 m0, s64\n\t"
"s_add_u32 s71, s60, 4096\n\t"
"buffer_load_dwordx4 v0, s[48:51], s60 offen offset:0 sc1 lds\n\t"
"v_mfma_f32_16x16x32_bf16 v[96:99], a[196:199], a[164:167], v[96:99]\n\t"
"v_exp_f32 v53, v77\n\t"
"v_mfma_f32_16x16x32_bf16 v[112:115], a[196:199], a[180:183], v[112:115]\n\t"
"ds_read_b128 v[192:195], v2 offset:8192\n\t"
"v_mfma_f32_16x16x32_bf16 v[96:99], a[200:203], a[168:171], v[96:99]\n\t"
"v_exp_f32 v54, v78\n\t"
"v_mfma_f32_16x16x32_bf16 v[112:115], a[200:203], a[184:187], v[112:115]\n\t"
"v_cvt_pk_bf16_f32 v134, v52, v53\n\t"
"buffer_load_dwordx4 v0, s[48:51], s60 offen offset:1024 sc1 lds\n\t"
"v_mfma_f32_16x16x32_bf16 v[96:99], a[204:207], a[172:175], v[96:99]\n\t"
"v_exp_f32 v55, v79\n\t"
"v_mfma_f32_16x16x32_bf16 v[112:115], a[204:207], a[188:191], v[112:115]\n\t"
"ds_read_b128 v[196:199], v2 offset:9216\n\t"
"v_mfma_f32_16x16x32_bf16 v[100:103], a[208:211], a[160:163], 0\n\t"
"v_exp_f32 v48, v88\n\t"
"v_mfma_f32_16x16x32_bf16 v[116:119], a[208:211], a[176:179], 0\n\t"
"v_cvt_pk_bf16_f32 v135, v54, v55\n\t"
"buffer_load_dwordx4 v0, s[48:51], s60 offen offset:2048 sc1 lds\n\t"
"v_mfma_f32_16x16x32_bf16 v[100:103], a[212:215], a[164:167], v[100:103]\n\t"
"v_exp_f32 v49, v89\n\t"
"v_mfma_f32_16x16x32_bf16 v[116:119], a[212:215], a[180:183], v[116:119]\n\t"
"v_mfma_f32_16x16x32_bf16 v[8:11], v[4:7], v[132:135], v[8:11]\n\t"
"ds_read_b128 v[200:203], v2 offset:10240\n\t"
"v_mfma_f32_16x16x32_bf16 v[100:103], a[216:219], a[168:171], v[100:103]\n\t"
"v_exp_f32 v50, v90\n\t"
"v_mfma_f32_16x16x32_bf16 v[116:119], a[216:219], a[184:187], v[116:119]\n\t"
"v_cvt_pk_bf16_f32 v140, v48, v49\n\t"
"buffer_load_dwordx4 v0, s[48:51], s60 offen offset:3072 sc1 lds\n\t"
"v_mfma_f32_16x16x32_bf16 v[100:103], a[220:223], a[172:175], v[100:103]\n\t"
"v_exp_f32 v51, v91\n\t"
"v_mfma_f32_16x16x32_bf16 v[116:119], a[220:223], a[188:191], v[116:119]\n\t"
"ds_read_b128 v[204:207], v2 offset:11264\n\t"
"v_mfma_f32_16x16x32_bf16 v[104:107], a[224:227], a[160:163], 0\n\t"
"v_exp_f32 v52, v92\n\t"
"v_mfma_f32_16x16x32_bf16 v[120:123], a[224:227], a[176:179], 0\n\t"
"v_cvt_pk_bf16_f32 v141, v50, v51\n\t"
"s_add_u32 m0, s64, 4096\n\t"
"s_add_u32 s60, s60, 32768\n\t"
"buffer_load_dwordx4 v0, s[48:51], s71 offen offset:0 sc1 lds\n\t"
"v_mfma_f32_16x16x32_bf16 v[104:107], a[228:231], a[164:167], v[104:107]\n\t"
"v_exp_f32 v53, v93\n\t"
"v_mfma_f32_16x16x32_bf16 v[120:123], a[228:231], a[180:183], v[120:123]\n\t"
"ds_read_b128 v[208:211], v2 offset:12288\n\t"
"v_mfma_f32_16x16x32_bf16 v[104:107], a[232:235], a[168:171], v[104:107]\n\t"
"v_exp_f32 v54, v94\n\t"
"v_mfma_f32_16x16x32_bf16 v[120:123], a[232:235], a[184:187], v[120:123]\n\t"
"v_cvt_pk_bf16_f32 v142, v52, v53\n\t"
"s_sub_u32 s62, s62, 1\n\t"
"s_cmp_eq_u32 s62, 0\n\t"
"s_cselect_b32 s60, s70, s60\n\t"
"s_cselect_b32 s62, s69, s62\n\t"
"buffer_load_dwordx4 v0, s[48:51], s71 offen offset:1024 sc1 lds\n\t"
"v_mfma_f32_16x16x32_bf16 v[104:107], a[236:239], a[172:175], v[104:107]\n\t"
"v_exp_f32 v55, v95\n\t"
"v_mfma_f32_16x16x32_bf16 v[120:123], a[236:239], a[188:191], v[120:123]\n\t"
"ds_read_b128 v[212:215], v2 offset:13312\n\t"
"v_mfma_f32_16x16x32_bf16 v[108:111], a[240:243], a[160:163], 0\n\t"
"v_mfma_f32_16x16x32_bf16 v[124:127], a[240:243], a[176:179], 0\n\t"
"v_cvt_pk_bf16_f32 v143, v54, v55\n\t"
"s_xor_b32 s64, s64, 65536\n\t"
"buffer_load_dwordx4 v0, s[48:51], s71 offen offset:2048 sc1 lds\n\t"
"v_mfma_f32_16x16x32_bf16 v[108:111], a[244:247], a[164:167], v[108:111]\n\t"
"v_mfma_f32_16x16x32_bf16 v[124:127], a[244:247], a[180:183], v[124:127]\n\t"
"v_mfma_f32_16x16x32_bf16 v[12:15], v[4:7], v[140:143], v[12:15]\n\t"
"ds_read_b128 v[216:219], v2 offset:14336\n\t"
"v_mfma_f32_16x16x32_bf16 v[108:111], a[248:251], a[168:171], v[108:111]\n\t"
"v_mfma_f32_16x16x32_bf16 v[124:127], a[248:251], a[184:187], v[124:127]\n\t"
"buffer_load_dwordx4 v0, s[48:51], s71 offen offset:3072 sc1 lds\n\t"
"v_mfma_f32_16x16x32_bf16 v[108:111], a[252:255], a[172:175], v[108:111]\n\t"
"v_mfma_f32_16x16x32_bf16 v[124:127], a[252:255], a[188:191], v[124:127]\n\t"
"ds_read_b128 v[220:223], v2 offset:15360\n\t"
"s_waitcnt lgkmcnt(2)\n\t"
"v_xor_b32 v2, 16384, v2\n\t"
"v_add_f32 v56, v8, v12\n\t"
"v_cmp_nge_f32 vcc, s68, v56\n\t"
"s_cbranch_vccnz LSP22_%=\n\t"
"LSR23_%=:\n\t"
"v_add_f32 v28, v28, v8\n\t"
"v_add_f32 v29, v29, v12\n\t"
"s_cmp_lg_u32 s67, 0\n\t"
"s_cbranch_scc1 LMH24_%=\n\t"
"LMR25_%=:\n\t"
"v_mfma_f32_16x16x32_bf16 a[0:3], v[160:163], v[128:131], 0\n\t"
"v_max3_f32 v232, v96, v97, v98\n\t"
"ds_read_b128 a[192:195], v1 offset:0\n\t"
"v_max3_f32 v234, v112, v113, v114\n\t"
"v_max3_f32 v233, v99, v100, v101\n\t"
"v_mfma_f32_16x16x32_bf16 a[4:7], v[160:163], v[136:139], 0\n\t"
"v_max3_f32 v235, v115, v116, v117\n\t"
"v_max3_f32 v232, v232, v102, v103\n\t"
"v_max3_f32 v234, v234, v118, v119\n\t"
"v_max3_f32 v233, v233, v104, v105\n\t"
"v_mfma_f32_16x16x32_bf16 a[16:19], v[164:167], v[128:131], 0\n\t"
"v_max3_f32 v235, v235, v120, v121\n\t"
"v_max3_f32 v232, v232, v106, v107\n\t"
"v_max3_f32 v234, v234, v122, v123\n\t"
"v_mfma_f32_16x16x32_bf16 a[20:23], v[164:167], v[136:139], 0\n\t"
"v_max3_f32 v233, v233, v108, v109\n\t"
"ds_read_b128 a[196:199], v1 offset:1024\n\t"
"v_max3_f32 v235, v235, v124, v125\n\t"
"v_max3_f32 v232, v232, v110, v111\n\t"
"v_mfma_f32_16x16x32_bf16 a[32:35], v[168:171], v[128:131], 0\n\t"
"v_max3_f32 v234, v234, v126, v127\n\t"
"v_max_f32 v232, v232, v233\n\t"
"v_max_f32 v234, v234, v235\n\t"
"v_mfma_f32_16x16x32_bf16 a[36:39], v[168:171], v[136:139], 0\n\t"
"v_mov_b32 v233, v232\n\t"
"v_mov_b32 v235, v234\n\t"
"s_nop 0\n\t"
"v_permlane32_swap_b32 v232, v233\n\t"
"v_permlane32_swap_b32 v234, v235\n\t"
"v_mfma_f32_16x16x32_bf16 a[48:51], v[172:175], v[128:131], 0\n\t"
"v_max_f32 v232, v232, v233\n\t"
"ds_read_b128 a[200:203], v1 offset:2048\n\t"
"v_max_f32 v234, v234, v235\n\t"
"v_mfma_f32_16x16x32_bf16 a[52:55], v[172:175], v[136:139], 0\n\t"
"v_mov_b32 v233, v232\n\t"
"v_mov_b32 v235, v234\n\t"
"s_nop 0\n\t"
"v_permlane16_swap_b32 v232, v233\n\t"
"v_permlane16_swap_b32 v234, v235\n\t"
"v_mfma_f32_16x16x32_bf16 a[64:67], v[176:179], v[128:131], 0\n\t"
"v_max_f32 v232, v232, v233\n\t"
"v_max_f32 v234, v234, v235\n\t"
"v_mov_b32 v26, v232\n\t"
"v_mfma_f32_16x16x32_bf16 a[68:71], v[176:179], v[136:139], 0\n\t"
"v_mov_b32 v27, v234\n\t"
"v_mov_b32 v30, 0\n\t"
"ds_read_b128 a[204:207], v1 offset:3072\n\t"
"v_mov_b32 v31, 0\n\t"
"v_mfma_f32_16x16x32_bf16 a[80:83], v[180:183], v[128:131], 0\n\t"
"v_sub_f32 v96, v96, v232\n\t"
"v_sub_f32 v112, v112, v234\n\t"
"v_sub_f32 v97, v97, v232\n\t"
"v_mfma_f32_16x16x32_bf16 a[84:87], v[180:183], v[136:139], 0\n\t"
"v_sub_f32 v113, v113, v234\n\t"
"v_sub_f32 v98, v98, v232\n\t"
"v_sub_f32 v114, v114, v234\n\t"
"v_sub_f32 v99, v99, v232\n\t"
"v_mfma_f32_16x16x32_bf16 a[96:99], v[184:187], v[128:131], 0\n\t"
"v_sub_f32 v115, v115, v234\n\t"
"v_sub_f32 v100, v100, v232\n\t"
"v_sub_f32 v116, v116, v234\n\t"
"v_mfma_f32_16x16x32_bf16 a[100:103], v[184:187], v[136:139], 0\n\t"
"ds_read_b128 a[208:211], v1 offset:4096\n\t"
"v_sub_f32 v101, v101, v232\n\t"
"v_sub_f32 v117, v117, v234\n\t"
"v_sub_f32 v102, v102, v232\n\t"
"v_mfma_f32_16x16x32_bf16 a[112:115], v[188:191], v[128:131], 0\n\t"
"v_sub_f32 v118, v118, v234\n\t"
"v_sub_f32 v103, v103, v232\n\t"
"v_sub_f32 v119, v119, v234\n\t"
"v_mfma_f32_16x16x32_bf16 a[116:119], v[188:191], v[136:139], 0\n\t"
"v_sub_f32 v104, v104, v232\n\t"
"v_sub_f32 v120, v120, v234\n\t"
"v_sub_f32 v105, v105, v232\n\t"
"v_sub_f32 v121, v121, v234\n\t"
"v_mfma_f32_16x16x32_bf16 a[0:3], v[192:195], v[132:135], a[0:3]\n\t"
"ds_read_b128 a[212:215], v1 offset:5120\n\t"
"v_sub_f32 v106, v106, v232\n\t"
"v_sub_f32 v122, v122, v234\n\t"
"v_sub_f32 v107, v107, v232\n\t"
"v_mfma_f32_16x16x32_bf16 a[4:7], v[192:195], v[140:143], a[4:7]\n\t"
"v_sub_f32 v123, v123, v234\n\t"
"v_sub_f32 v108, v108, v232\n\t"
"v_sub_f32 v124, v124, v234\n\t"
"v_mfma_f32_16x16x32_bf16 a[16:19], v[196:199], v[132:135], a[16:19]\n\t"
"v_sub_f32 v109, v109, v232\n\t"
"v_sub_f32 v125, v125, v234\n\t"
"v_sub_f32 v110, v110, v232\n\t"
"v_sub_f32 v126, v126, v234\n\t"
"v_mfma_f32_16x16x32_bf16 a[20:23], v[196:199], v[140:143], a[20:23]\n\t"
"ds_read_b128 a[216:219], v1 offset:6144\n\t"
"v_sub_f32 v111, v111, v232\n\t"
"v_sub_f32 v127, v127, v234\n\t"
"v_mfma_f32_16x16x32_bf16 a[32:35], v[200:203], v[132:135], a[32:35]\n\t"
"v_sub_f32 v40, 0, v232\n\t"
"v_sub_f32 v44, 0, v234\n\t"
"v_sub_f32 v41, 0, v232\n\t"
"v_sub_f32 v45, 0, v234\n\t"
"v_mfma_f32_16x16x32_bf16 a[36:39], v[200:203], v[140:143], a[36:39]\n\t"
"v_sub_f32 v42, 0, v232\n\t"
"v_sub_f32 v46, 0, v234\n\t"
"v_sub_f32 v43, 0, v232\n\t"
"v_mfma_f32_16x16x32_bf16 a[48:51], v[204:207], v[132:135], a[48:51]\n\t"
"v_sub_f32 v47, 0, v234\n\t"
"v_exp_f32 v48, v96\n\t"
"ds_read_b128 a[220:223], v1 offset:7168\n\t"
"v_exp_f32 v49, v97\n\t"
"v_mfma_f32_16x16x32_bf16 a[52:55], v[204:207], v[140:143], a[52:55]\n\t"
"v_exp_f32 v50, v98\n\t"
"v_cvt_pk_bf16_f32 v144, v48, v49\n\t"
"v_exp_f32 v51, v99\n\t"
"v_mfma_f32_16x16x32_bf16 a[64:67], v[208:211], v[132:135], a[64:67]\n\t"
"v_exp_f32 v52, v100\n\t"
"v_cvt_pk_bf16_f32 v145, v50, v51\n\t"
"v_exp_f32 v53, v101\n\t"
"v_exp_f32 v54, v102\n\t"
"v_mfma_f32_16x16x32_bf16 a[68:71], v[208:211], v[140:143], a[68:71]\n\t"
"v_cvt_pk_bf16_f32 v146, v52, v53\n\t"
"v_exp_f32 v55, v103\n\t"
"ds_read_b128 a[224:227], v1 offset:8192\n\t"
"v_mfma_f32_16x16x32_bf16 a[80:83], v[212:215], v[132:135], a[80:83]\n\t"
"v_exp_f32 v48, v112\n\t"
"v_cvt_pk_bf16_f32 v147, v54, v55\n\t"
"v_exp_f32 v49, v113\n\t"
"s_nop 0\n\t"
"v_mfma_f32_16x16x32_bf16 v[16:19], v[4:7], v[144:147], 0\n\t"
"v_mfma_f32_16x16x32_bf16 a[84:87], v[212:215], v[140:143], a[84:87]\n\t"
"v_exp_f32 v50, v114\n\t"
"v_cvt_pk_bf16_f32 v152, v48, v49\n\t"
"v_exp_f32 v51, v115\n\t"
"s_waitcnt lgkmcnt(1)\n\t"
"v_mfma_f32_16x16x32_bf16 a[96:99], v[216:219], v[132:135], a[96:99]\n\t"
"v_exp_f32 v52, v116\n\t"
"v_cvt_pk_bf16_f32 v153, v50, v51\n\t"
"v_exp_f32 v53, v117\n\t"
"ds_read_b128 a[228:231], v1 offset:9216\n\t"
"v_mfma_f32_16x16x32_bf16 a[100:103], v[216:219], v[140:143], a[100:103]\n\t"
"v_exp_f32 v54, v118\n\t"
"v_cvt_pk_bf16_f32 v154, v52, v53\n\t"
"v_exp_f32 v55, v119\n\t"
"v_mfma_f32_16x16x32_bf16 a[112:115], v[220:223], v[132:135], a[112:115]\n\t"
"v_exp_f32 v48, v104\n\t"
"v_cvt_pk_bf16_f32 v155, v54, v55\n\t"
"v_exp_f32 v49, v105\n\t"
"s_nop 0\n\t"
"v_mfma_f32_16x16x32_bf16 v[20:23], v[4:7], v[152:155], 0\n\t"
"v_mfma_f32_16x16x32_bf16 a[116:119], v[220:223], v[140:143], a[116:119]\n\t"
"v_exp_f32 v50, v106\n\t"
"v_cvt_pk_bf16_f32 v148, v48, v49\n\t"
"v_exp_f32 v51, v107\n\t"
"s_sub_u32 s66, s66, 1\n\t"
"s_cmp_eq_u32 s66, 0\n\t"
"s_cbranch_scc1 LDRAIN14_%=\n\t"
"s_cmp_eq_u32 s66, 1\n\t"
"s_cbranch_scc1 LLAST18_%=\n\t"
"LMID13_%=:\n\t"
"v_mfma_f32_16x16x32_bf16 v[64:67], a[192:195], a[128:131], v[32:35]\n\t"
"v_exp_f32 v52, v108\n\t"
"v_mfma_f32_16x16x32_bf16 v[80:83], a[192:195], a[144:147], v[36:39]\n\t"
"v_cvt_pk_bf16_f32 v149, v50, v51\n\t"
"v_mfma_f32_16x16x32_bf16 v[64:67], a[196:199], a[132:135], v[64:67]\n\t"
"v_exp_f32 v53, v109\n\t"
"v_mfma_f32_16x16x32_bf16 v[80:83], a[196:199], a[148:151], v[80:83]\n\t"
"ds_read_b128 a[232:235], v1 offset:10240\n\t"
"v_mfma_f32_16x16x32_bf16 v[64:67], a[200:203], a[136:139], v[64:67]\n\t"
"v_exp_f32 v54, v110\n\t"
"v_mfma_f32_16x16x32_bf16 v[80:83], a[200:203], a[152:155], v[80:83]\n\t"
"v_cvt_pk_bf16_f32 v150, v52, v53\n\t"
"v_mfma_f32_16x16x32_bf16 v[64:67], a[204:207], a[140:143], v[64:67]\n\t"
"v_exp_f32 v55, v111\n\t"
"v_mfma_f32_16x16x32_bf16 v[80:83], a[204:207], a[156:159], v[80:83]\n\t"
"ds_read_b128 a[236:239], v1 offset:11264\n\t"
"v_mfma_f32_16x16x32_bf16 v[68:71], a[208:211], a[128:131], v[32:35]\n\t"
"v_exp_f32 v48, v120\n\t"
"v_mfma_f32_16x16x32_bf16 v[84:87], a[208:211], a[144:147], v[36:39]\n\t"
"v_cvt_pk_bf16_f32 v151, v54, v55\n\t"
"v_mfma_f32_16x16x32_bf16 v[68:71], a[212:215], a[132:135], v[68:71]\n\t"
"v_exp_f32 v49, v121\n\t"
"v_mfma_f32_16x16x32_bf16 v[84:87], a[212:215], a[148:151], v[84:87]\n\t"
"v_mfma_f32_16x16x32_bf16 v[16:19], v[4:7], v[148:151], v[16:19]\n\t"
"ds_read_b128 a[240:243], v1 offset:12288\n\t"
"v_mfma_f32_16x16x32_bf16 v[68:71], a[216:219], a[136:139], v[68:71]\n\t"
"v_exp_f32 v50, v122\n\t"
"v_mfma_f32_16x16x32_bf16 v[84:87], a[216:219], a[152:155], v[84:87]\n\t"
"v_cvt_pk_bf16_f32 v156, v48, v49\n\t"
"s_waitcnt lgkmcnt(2)\n\t"
"v_mfma_f32_16x16x32_bf16 v[68:71], a[220:223], a[140:143], v[68:71]\n\t"
"v_exp_f32 v51, v123\n\t"
"v_mfma_f32_16x16x32_bf16 v[84:87], a[220:223], a[156:159], v[84:87]\n\t"
"ds_read_b128 a[244:247], v1 offset:13312\n\t"
"v_mfma_f32_16x16x32_bf16 v[72:75], a[224:227], a[128:131], v[32:35]\n\t"
"v_exp_f32 v52, v124\n\t"
"v_mfma_f32_16x16x32_bf16 v[88:91], a[224:227], a[144:147], v[36:39]\n\t"
"v_cvt_pk_bf16_f32 v157, v50, v51\n\t"
"v_mfma_f32_16x16x32_bf16 v[72:75], a[228:231], a[132:135], v[72:75]\n\t"
"v_exp_f32 v53, v125\n\t"
"v_mfma_f32_16x16x32_bf16 v[88:91], a[228:231], a[148:151], v[88:91]\n\t"
"ds_read_b128 a[248:251], v1 offset:14336\n\t"
"v_mfma_f32_16x16x32_bf16 v[72:75], a[232:235], a[136:139], v[72:75]\n\t"
"v_exp_f32 v54, v126\n\t"
"v_mfma_f32_16x16x32_bf16 v[88:91], a[232:235], a[152:155], v[88:91]\n\t"
"v_cvt_pk_bf16_f32 v158, v52, v53\n\t"
"s_waitcnt lgkmcnt(2)\n\t"
"v_mfma_f32_16x16x32_bf16 v[72:75], a[236:239], a[140:143], v[72:75]\n\t"
"v_exp_f32 v55, v127\n\t"
"v_mfma_f32_16x16x32_bf16 v[88:91], a[236:239], a[156:159], v[88:91]\n\t"
"ds_read_b128 a[252:255], v1 offset:15360\n\t"
"v_mfma_f32_16x16x32_bf16 v[76:79], a[240:243], a[128:131], v[32:35]\n\t"
"v_exp_f32 v48, v64\n\t"
"v_mfma_f32_16x16x32_bf16 v[92:95], a[240:243], a[144:147], v[36:39]\n\t"
"v_cvt_pk_bf16_f32 v159, v54, v55\n\t"
"s_waitcnt lgkmcnt(2)\n\t"
"v_mfma_f32_16x16x32_bf16 v[76:79], a[244:247], a[132:135], v[76:79]\n\t"
"v_exp_f32 v49, v65\n\t"
"v_mfma_f32_16x16x32_bf16 v[92:95], a[244:247], a[148:151], v[92:95]\n\t"
"v_mfma_f32_16x16x32_bf16 v[20:23], v[4:7], v[156:159], v[20:23]\n\t"
"v_xor_b32 v1, 65536, v1\n\t"
"s_waitcnt lgkmcnt(1)\n\t"
"v_mfma_f32_16x16x32_bf16 v[76:79], a[248:251], a[136:139], v[76:79]\n\t"
"v_exp_f32 v50, v66\n\t"
"v_mfma_f32_16x16x32_bf16 v[92:95], a[248:251], a[152:155], v[92:95]\n\t"
"v_cvt_pk_bf16_f32 v128, v48, v49\n\t"
"s_waitcnt lgkmcnt(0)\n\t"
"v_mfma_f32_16x16x32_bf16 v[76:79], a[252:255], a[140:143], v[76:79]\n\t"
"v_exp_f32 v51, v67\n\t"
"v_mfma_f32_16x16x32_bf16 v[92:95], a[252:255], a[156:159], v[92:95]\n\t"
"v_add_f32 v56, v16, v20\n\t"
"v_cmp_nge_f32 vcc, s68, v56\n\t"
"s_cbranch_vccnz LSP26_%=\n\t"
"LSR27_%=:\n\t"
"v_add_f32 v30, v30, v16\n\t"
"v_add_f32 v31, v31, v20\n\t"
"s_waitcnt vmcnt(0)\n\t"
"s_barrier\n\t"
"v_mfma_f32_16x16x32_bf16 a[8:11], v[160:163], v[144:147], a[8:11]\n\t"
"v_exp_f32 v52, v68\n\t"
"v_mfma_f32_16x16x32_bf16 a[12:15], v[160:163], v[152:155], a[12:15]\n\t"
"v_cvt_pk_bf16_f32 v129, v50, v51\n\t"
"s_mov_b32 m0, s65\n\t"
"s_add_u32 s71, s61, 4096\n\t"
"buffer_load_dwordx4 v0, s[52:55], s61 offen offset:0 sc1 lds\n\t"
"v_mfma_f32_16x16x32_bf16 a[24:27], v[164:167], v[144:147], a[24:27]\n\t"
"v_exp_f32 v53, v69\n\t"
"v_mfma_f32_16x16x32_bf16 a[28:31], v[164:167], v[152:155], a[28:31]\n\t"
"ds_read_b128 v[160:163], v2 offset:0\n\t"
"v_mfma_f32_16x16x32_bf16 a[40:43], v[168:171], v[144:147], a[40:43]\n\t"
"v_exp_f32 v54, v70\n\t"
"v_mfma_f32_16x16x32_bf16 a[44:47], v[168:171], v[152:155], a[44:47]\n\t"
"v_cvt_pk_bf16_f32 v130, v52, v53\n\t"
"buffer_load_dwordx4 v0, s[52:55], s61 offen offset:1024 sc1 lds\n\t"
"v_mfma_f32_16x16x32_bf16 a[56:59], v[172:175], v[144:147], a[56:59]\n\t"
"v_exp_f32 v55, v71\n\t"
"v_mfma_f32_16x16x32_bf16 a[60:63], v[172:175], v[152:155], a[60:63]\n\t"
"ds_read_b128 v[164:167], v2 offset:1024\n\t"
"v_mfma_f32_16x16x32_bf16 a[72:75], v[176:179], v[144:147], a[72:75]\n\t"
"v_exp_f32 v48, v80\n\t"
"v_mfma_f32_16x16x32_bf16 a[76:79], v[176:179], v[152:155], a[76:79]\n\t"
"v_cvt_pk_bf16_f32 v131, v54, v55\n\t"
"buffer_load_dwordx4 v0, s[52:55], s61 offen offset:2048 sc1 lds\n\t"
"v_mfma_f32_16x16x32_bf16 a[88:91], v[180:183], v[144:147], a[88:91]\n\t"
"v_exp_f32 v49, v81\n\t"
"v_mfma_f32_16x16x32_bf16 a[92:95], v[180:183], v[152:155], a[92:95]\n\t"
"v_mfma_f32_16x16x32_bf16 v[8:11], v[4:7], v[128:131], 0\n\t"
"ds_read_b128 v[168:171], v2 offset:2048\n\t"
"v_mfma_f32_16x16x32_bf16 a[104:107], v[184:187], v[144:147], a[104:107]\n\t"
"v_exp_f32 v50, v82\n\t"
"v_mfma_f32_16x16x32_bf16 a[108:111], v[184:187], v[152:155], a[108:111]\n\t"
"v_cvt_pk_bf16_f32 v136, v48, v49\n\t"
"buffer_load_dwordx4 v0, s[52:55], s61 offen offset:3072 sc1 lds\n\t"
"v_mfma_f32_16x16x32_bf16 a[120:123], v[188:191], v[144:147], a[120:123]\n\t"
"v_exp_f32 v51, v83\n\t"
"v_mfma_f32_16x16x32_bf16 a[124:127], v[188:191], v[152:155], a[124:127]\n\t"
"ds_read_b128 v[172:175], v2 offset:3072\n\t"
"v_mfma_f32_16x16x32_bf16 a[8:11], v[192:195], v[148:151], a[8:11]\n\t"
"v_exp_f32 v52, v84\n\t"
"v_mfma_f32_16x16x32_bf16 a[12:15], v[192:195], v[156:159], a[12:15]\n\t"
"v_cvt_pk_bf16_f32 v137, v50, v51\n\t"
"s_add_u32 m0, s65, 4096\n\t"
"s_add_u32 s61, s61, 32768\n\t"
"buffer_load_dwordx4 v0, s[52:55], s71 offen offset:0 sc1 lds\n\t"
"v_mfma_f32_16x16x32_bf16 a[24:27], v[196:199], v[148:151], a[24:27]\n\t"
"v_exp_f32 v53, v85\n\t"
"v_mfma_f32_16x16x32_bf16 a[28:31], v[196:199], v[156:159], a[28:31]\n\t"
"ds_read_b128 v[176:179], v2 offset:4096\n\t"
"v_mfma_f32_16x16x32_bf16 a[40:43], v[200:203], v[148:151], a[40:43]\n\t"
"v_exp_f32 v54, v86\n\t"
"v_mfma_f32_16x16x32_bf16 a[44:47], v[200:203], v[156:159], a[44:47]\n\t"
"v_cvt_pk_bf16_f32 v138, v52, v53\n\t"
"s_sub_u32 s63, s63, 1\n\t"
"s_cmp_eq_u32 s63, 0\n\t"
"s_cselect_b32 s61, s70, s61\n\t"
"s_cselect_b32 s63, s69, s63\n\t"
"buffer_load_dwordx4 v0, s[52:55], s71 offen offset:1024 sc1 lds\n\t"
"v_mfma_f32_16x16x32_bf16 a[56:59], v[204:207], v[148:151], a[56:59]\n\t"
"v_exp_f32 v55, v87\n\t"
"v_mfma_f32_16x16x32_bf16 a[60:63], v[204:207], v[156:159], a[60:63]\n\t"
"ds_read_b128 v[180:183], v2 offset:5120\n\t"
"v_mfma_f32_16x16x32_bf16 a[72:75], v[208:211], v[148:151], a[72:75]\n\t"
"v_exp_f32 v48, v72\n\t"
"v_mfma_f32_16x16x32_bf16 a[76:79], v[208:211], v[156:159], a[76:79]\n\t"
"v_cvt_pk_bf16_f32 v139, v54, v55\n\t"
"s_xor_b32 s65, s65, 16384\n\t"
"buffer_load_dwordx4 v0, s[52:55], s71 offen offset:2048 sc1 lds\n\t"
"v_mfma_f32_16x16x32_bf16 a[88:91], v[212:215], v[148:151], a[88:91]\n\t"
"v_exp_f32 v49, v73\n\t"
"v_mfma_f32_16x16x32_bf16 a[92:95], v[212:215], v[156:159], a[92:95]\n\t"
"v_mfma_f32_16x16x32_bf16 v[12:15], v[4:7], v[136:139], 0\n\t"
"ds_read_b128 v[184:187], v2 offset:6144\n\t"
"v_mfma_f32_16x16x32_bf16 a[104:107], v[216:219], v[148:151], a[104:107]\n\t"
"v_exp_f32 v50, v74\n\t"
"v_mfma_f32_16x16x32_bf16 a[108:111], v[216:219], v[156:159], a[108:111]\n\t"
"v_cvt_pk_bf16_f32 v132, v48, v49\n\t"
"buffer_load_dwordx4 v0, s[52:55], s71 offen offset:3072 sc1 lds\n\t"
"v_mfma_f32_16x16x32_bf16 a[120:123], v[220:223], v[148:151], a[120:123]\n\t"
"v_exp_f32 v51, v75\n\t"
"v_mfma_f32_16x16x32_bf16 a[124:127], v[220:223], v[156:159], a[124:127]\n\t"
"ds_read_b128 v[188:191], v2 offset:7168\n\t"
"v_mfma_f32_16x16x32_bf16 v[96:99], a[192:195], a[160:163], v[40:43]\n\t"
"v_exp_f32 v52, v76\n\t"
"v_mfma_f32_16x16x32_bf16 v[112:115], a[192:195], a[176:179], v[44:47]\n\t"
"v_cvt_pk_bf16_f32 v133, v50, v51\n\t"
"s_mov_b32 m0, s64\n\t"
"s_add_u32 s71, s60, 4096\n\t"
"buffer_load_dwordx4 v0, s[48:51], s60 offen offset:0 sc1 lds\n\t"
"v_mfma_f32_16x16x32_bf16 v[96:99], a[196:199], a[164:167], v[96:99]\n\t"
"v_exp_f32 v53, v77\n\t"
"v_mfma_f32_16x16x32_bf16 v[112:115], a[196:199], a[180:183], v[112:115]\n\t"
"ds_read_b128 v[192:195], v2 offset:8192\n\t"
"v_mfma_f32_16x16x32_bf16 v[96:99], a[200:203], a[168:171], v[96:99]\n\t"
"v_exp_f32 v54, v78\n\t"
"v_mfma_f32_16x16x32_bf16 v[112:115], a[200:203], a[184:187], v[112:115]\n\t"
"v_cvt_pk_bf16_f32 v134, v52, v53\n\t"
"buffer_load_dwordx4 v0, s[48:51], s60 offen offset:1024 sc1 lds\n\t"
"v_mfma_f32_16x16x32_bf16 v[96:99], a[204:207], a[172:175], v[96:99]\n\t"
"v_exp_f32 v55, v79\n\t"
"v_mfma_f32_16x16x32_bf16 v[112:115], a[204:207], a[188:191], v[112:115]\n\t"
"ds_read_b128 v[196:199], v2 offset:9216\n\t"
"v_mfma_f32_16x16x32_bf16 v[100:103], a[208:211], a[160:163], v[40:43]\n\t"
"v_exp_f32 v48, v88\n\t"
"v_mfma_f32_16x16x32_bf16 v[116:119], a[208:211], a[176:179], v[44:47]\n\t"
"v_cvt_pk_bf16_f32 v135, v54, v55\n\t"
"buffer_load_dwordx4 v0, s[48:51], s60 offen offset:2048 sc1 lds\n\t"
"v_mfma_f32_16x16x32_bf16 v[100:103], a[212:215], a[164:167], v[100:103]\n\t"
"v_exp_f32 v49, v89\n\t"
"v_mfma_f32_16x16x32_bf16 v[116:119], a[212:215], a[180:183], v[116:119]\n\t"
"v_mfma_f32_16x16x32_bf16 v[8:11], v[4:7], v[132:135], v[8:11]\n\t"
"ds_read_b128 v[200:203], v2 offset:10240\n\t"
"v_mfma_f32_16x16x32_bf16 v[100:103], a[216:219], a[168:171], v[100:103]\n\t"
"v_exp_f32 v50, v90\n\t"
"v_mfma_f32_16x16x32_bf16 v[116:119], a[216:219], a[184:187], v[116:119]\n\t"
"v_cvt_pk_bf16_f32 v140, v48, v49\n\t"
"buffer_load_dwordx4 v0, s[48:51], s60 offen offset:3072 sc1 lds\n\t"
"v_mfma_f32_16x16x32_bf16 v[100:103], a[220:223], a[172:175], v[100:103]\n\t"
"v_exp_f32 v51, v91\n\t"
"v_mfma_f32_16x16x32_bf16 v[116:119], a[220:223], a[188:191], v[116:119]\n\t"
"ds_read_b128 v[204:207], v2 offset:11264\n\t"
"v_mfma_f32_16x16x32_bf16 v[104:107], a[224:227], a[160:163], v[40:43]\n\t"
"v_exp_f32 v52, v92\n\t"
"v_mfma_f32_16x16x32_bf16 v[120:123], a[224:227], a[176:179], v[44:47]\n\t"
"v_cvt_pk_bf16_f32 v141, v50, v51\n\t"
"s_add_u32 m0, s64, 4096\n\t"
"s_add_u32 s60, s60, 32768\n\t"
"buffer_load_dwordx4 v0, s[48:51], s71 offen offset:0 sc1 lds\n\t"
"v_mfma_f32_16x16x32_bf16 v[104:107], a[228:231], a[164:167], v[104:107]\n\t"
"v_exp_f32 v53, v93\n\t"
"v_mfma_f32_16x16x32_bf16 v[120:123], a[228:231], a[180:183], v[120:123]\n\t"
"ds_read_b128 v[208:211], v2 offset:12288\n\t"
"v_mfma_f32_16x16x32_bf16 v[104:107], a[232:235], a[168:171], v[104:107]\n\t"
"v_exp_f32 v54, v94\n\t"
"v_mfma_f32_16x16x32_bf16 v[120:123], a[232:235], a[184:187], v[120:123]\n\t"
"v_cvt_pk_bf16_f32 v142, v52, v53\n\t"
"s_sub_u32 s62, s62, 1\n\t"
"s_cmp_eq_u32 s62, 0\n\t"
"s_cselect_b32 s60, s70, s60\n\t"
"s_cselect_b32 s62, s69, s62\n\t"
"buffer_load_dwordx4 v0, s[48:51], s71 offen offset:1024 sc1 lds\n\t"
"v_mfma_f32_16x16x32_bf16 v[104:107], a[236:239], a[172:175], v[104:107]\n\t"
"v_exp_f32 v55, v95\n\t"
"v_mfma_f32_16x16x32_bf16 v[120:123], a[236:239], a[188:191], v[120:123]\n\t"
"ds_read_b128 v[212:215], v2 offset:13312\n\t"
"v_mfma_f32_16x16x32_bf16 v[108:111], a[240:243], a[160:163], v[40:43]\n\t"
"v_exp_f32 v48, v96\n\t"
"v_mfma_f32_16x16x32_bf16 v[124:127], a[240:243], a[176:179], v[44:47]\n\t"
"v_cvt_pk_bf16_f32 v143, v54, v55\n\t"
"s_xor_b32 s64, s64, 65536\n\t"
"buffer_load_dwordx4 v0, s[48:51], s71 offen offset:2048 sc1 lds\n\t"
"v_mfma_f32_16x16x32_bf16 v[108:111], a[244:247], a[164:167], v[108:111]\n\t"
"v_exp_f32 v49, v97\n\t"
"v_mfma_f32_16x16x32_bf16 v[124:127], a[244:247], a[180:183], v[124:127]\n\t"
"v_mfma_f32_16x16x32_bf16 v[12:15], v[4:7], v[140:143], v[12:15]\n\t"
"ds_read_b128 v[216:219], v2 offset:14336\n\t"
"v_mfma_f32_16x16x32_bf16 v[108:111], a[248:251], a[168:171], v[108:111]\n\t"
"v_exp_f32 v50, v98\n\t"
"v_mfma_f32_16x16x32_bf16 v[124:127], a[248:251], a[184:187], v[124:127]\n\t"
"v_cvt_pk_bf16_f32 v144, v48, v49\n\t"
"buffer_load_dwordx4 v0, s[48:51], s71 offen offset:3072 sc1 lds\n\t"
"v_mfma_f32_16x16x32_bf16 v[108:111], a[252:255], a[172:175], v[108:111]\n\t"
"v_exp_f32 v51, v99\n\t"
"v_mfma_f32_16x16x32_bf16 v[124:127], a[252:255], a[188:191], v[124:127]\n\t"
"ds_read_b128 v[220:223], v2 offset:15360\n\t"
"s_waitcnt lgkmcnt(2)\n\t"
"v_xor_b32 v2, 16384, v2\n\t"
"v_add_f32 v56, v8, v12\n\t"
"v_cmp_nge_f32 vcc, s68, v56\n\t"
"s_cbranch_vccnz LSP28_%=\n\t"
"LSR29_%=:\n\t"
"v_add_f32 v28, v28, v8\n\t"
"v_add_f32 v29, v29, v12\n\t"
"v_mfma_f32_16x16x32_bf16 a[0:3], v[160:163], v[128:131], a[0:3]\n\t"
"v_exp_f32 v52, v100\n\t"
"v_mfma_f32_16x16x32_bf16 a[4:7], v[160:163], v[136:139], a[4:7]\n\t"
"v_cvt_pk_bf16_f32 v145, v50, v51\n\t"
"ds_read_b128 a[192:195], v1 offset:0\n\t"
"v_mfma_f32_16x16x32_bf16 a[16:19], v[164:167], v[128:131], a[16:19]\n\t"
"v_exp_f32 v53, v101\n\t"
"v_mfma_f32_16x16x32_bf16 a[20:23], v[164:167], v[136:139], a[20:23]\n\t"
"v_mfma_f32_16x16x32_bf16 a[32:35], v[168:171], v[128:131], a[32:35]\n\t"
"v_exp_f32 v54, v102\n\t"
"v_mfma_f32_16x16x32_bf16 a[36:39], v[168:171], v[136:139], a[36:39]\n\t"
"v_cvt_pk_bf16_f32 v146, v52, v53\n\t"
"ds_read_b128 a[196:199], v1 offset:1024\n\t"
"v_mfma_f32_16x16x32_bf16 a[48:51], v[172:175], v[128:131], a[48:51]\n\t"
"v_exp_f32 v55, v103\n\t"
"v_mfma_f32_16x16x32_bf16 a[52:55], v[172:175], v[136:139], a[52:55]\n\t"
"ds_read_b128 a[200:203], v1 offset:2048\n\t"
"v_mfma_f32_16x16x32_bf16 a[64:67], v[176:179], v[128:131], a[64:67]\n\t"
"v_exp_f32 v48, v112\n\t"
"v_mfma_f32_16x16x32_bf16 a[68:71], v[176:179], v[136:139], a[68:71]\n\t"
"v_cvt_pk_bf16_f32 v147, v54, v55\n\t"
"v_mfma_f32_16x16x32_bf16 a[80:83], v[180:183], v[128:131], a[80:83]\n\t"
"v_exp_f32 v49, v113\n\t"
"v_mfma_f32_16x16x32_bf16 a[84:87], v[180:183], v[136:139], a[84:87]\n\t"
"v_mfma_f32_16x16x32_bf16 v[16:19], v[4:7], v[144:147], 0\n\t"
"ds_read_b128 a[204:207], v1 offset:3072\n\t"
"v_mfma_f32_16x16x32_bf16 a[96:99], v[184:187], v[128:131], a[96:99]\n\t"
"v_exp_f32 v50, v114\n\t"
"v_mfma_f32_16x16x32_bf16 a[100:103], v[184:187], v[136:139], a[100:103]\n\t"
"v_cvt_pk_bf16_f32 v152, v48, v49\n\t"
"ds_read_b128 a[208:211], v1 offset:4096\n\t"
"v_mfma_f32_16x16x32_bf16 a[112:115], v[188:191], v[128:131], a[112:115]\n\t"
"v_exp_f32 v51, v115\n\t"
"v_mfma_f32_16x16x32_bf16 a[116:119], v[188:191], v[136:139], a[116:119]\n\t"
"v_mfma_f32_16x16x32_bf16 a[0:3], v[192:195], v[132:135], a[0:3]\n\t"
"v_exp_f32 v52, v116\n\t"
"v_mfma_f32_16x16x32_bf16 a[4:7], v[192:195], v[140:143], a[4:7]\n\t"
"v_cvt_pk_bf16_f32 v153, v50, v51\n\t"
"ds_read_b128 a[212:215], v1 offset:5120\n\t"
"v_mfma_f32_16x16x32_bf16 a[16:19], v[196:199], v[132:135], a[16:19]\n\t"
"v_exp_f32 v53, v117\n\t"
"v_mfma_f32_16x16x32_bf16 a[20:23], v[196:199], v[140:143], a[20:23]\n\t"
"ds_read_b128 a[216:219], v1 offset:6144\n\t"
"v_mfma_f32_16x16x32_bf16 a[32:35], v[200:203], v[132:135], a[32:35]\n\t"
"v_exp_f32 v54, v118\n\t"
"v_mfma_f32_16x16x32_bf16 a[36:39], v[200:203], v[140:143], a[36:39]\n\t"
"v_cvt_pk_bf16_f32 v154, v52, v53\n\t"
"v_mfma_f32_16x16x32_bf16 a[48:51], v[204:207], v[132:135], a[48:51]\n\t"
"v_exp_f32 v55, v119\n\t"
"v_mfma_f32_16x16x32_bf16 a[52:55], v[204:207], v[140:143], a[52:55]\n\t"
"ds_read_b128 a[220:223], v1 offset:7168\n\t"
"v_mfma_f32_16x16x32_bf16 a[64:67], v[208:211], v[132:135], a[64:67]\n\t"
"v_exp_f32 v48, v104\n\t"
"v_mfma_f32_16x16x32_bf16 a[68:71], v[208:211], v[140:143], a[68:71]\n\t"
"v_cvt_pk_bf16_f32 v155, v54, v55\n\t"
"v_mfma_f32_16x16x32_bf16 a[80:83], v[212:215], v[132:135], a[80:83]\n\t"
"v_exp_f32 v49, v105\n\t"
"v_mfma_f32_16x16x32_bf16 a[84:87], v[212:215], v[140:143], a[84:87]\n\t"
"v_mfma_f32_16x16x32_bf16 v[20:23], v[4:7], v[152:155], 0\n\t"
"ds_read_b128 a[224:227], v1 offset:8192\n\t"
"s_waitcnt lgkmcnt(2)\n\t"
"v_mfma_f32_16x16x32_bf16 a[96:99], v[216:219], v[132:135], a[96:99]\n\t"
"v_exp_f32 v50, v106\n\t"
"v_mfma_f32_16x16x32_bf16 a[100:103], v[216:219], v[140:143], a[100:103]\n\t"
"v_cvt_pk_bf16_f32 v148, v48, v49\n\t"
"ds_read_b128 a[228:231], v1 offset:9216\n\t"
"v_mfma_f32_16x16x32_bf16 a[112:115], v[220:223], v[132:135], a[112:115]\n\t"
"v_exp_f32 v51, v107\n\t"
"v_mfma_f32_16x16x32_bf16 a[116:119], v[220:223], v[140:143], a[116:119]\n\t"
"s_sub_u32 s66, s66, 1\n\t"
"s_cmp_lg_u32 s66, 1\n\t"
"s_cbranch_scc1 LMID13_%=\n\t"
"LLAST18_%=:\n\t"
"v_min_f32 v224, v32, v60\n\t"
"v_min_f32 v225, v33, v61\n\t"
"v_min_f32 v226, v34, v62\n\t"
"v_min_f32 v227, v35, v63\n\t"
"v_min_f32 v228, v36, v60\n\t"
"v_min_f32 v229, v37, v61\n\t"
"v_min_f32 v230, v38, v62\n\t"
"v_min_f32 v231, v39, v63\n\t"
"v_min_f32 v232, s77, v32\n\t"
"v_min_f32 v233, s77, v33\n\t"
"v_min_f32 v234, s77, v34\n\t"
"v_min_f32 v235, s77, v35\n\t"
"v_min_f32 v236, s77, v36\n\t"
"v_min_f32 v237, s77, v37\n\t"
"v_min_f32 v238, s77, v38\n\t"
"v_min_f32 v239, s77, v39\n\t"
"v_min_f32 v36, s80, v36\n\t"
"v_min_f32 v37, s80, v37\n\t"
"v_min_f32 v38, s80, v38\n\t"
"v_min_f32 v39, s80, v39\n\t"
"v_mfma_f32_16x16x32_bf16 v[64:67], a[192:195], a[128:131], v[224:227]\n\t"
"v_exp_f32 v52, v108\n\t"
"v_mfma_f32_16x16x32_bf16 v[80:83], a[192:195], a[144:147], v[36:39]\n\t"
"v_cvt_pk_bf16_f32 v149, v50, v51\n\t"
"v_mfma_f32_16x16x32_bf16 v[64:67], a[196:199], a[132:135], v[64:67]\n\t"
"v_exp_f32 v53, v109\n\t"
"v_mfma_f32_16x16x32_bf16 v[80:83], a[196:199], a[148:151], v[80:83]\n\t"
"ds_read_b128 a[232:235], v1 offset:10240\n\t"
"v_mfma_f32_16x16x32_bf16 v[64:67], a[200:203], a[136:139], v[64:67]\n\t"
"v_exp_f32 v54, v110\n\t"
"v_mfma_f32_16x16x32_bf16 v[80:83], a[200:203], a[152:155], v[80:83]\n\t"
"v_cvt_pk_bf16_f32 v150, v52, v53\n\t"
"v_mfma_f32_16x16x32_bf16 v[64:67], a[204:207], a[140:143], v[64:67]\n\t"
"v_exp_f32 v55, v111\n\t"
"v_mfma_f32_16x16x32_bf16 v[80:83], a[204:207], a[156:159], v[80:83]\n\t"
"ds_read_b128 a[236:239], v1 offset:11264\n\t"
"v_mfma_f32_16x16x32_bf16 v[68:71], a[208:211], a[128:131], v[232:235]\n\t"
"v_exp_f32 v48, v120\n\t"
"v_mfma_f32_16x16x32_bf16 v[84:87], a[208:211], a[144:147], v[228:231]\n\t"
"v_cvt_pk_bf16_f32 v151, v54, v55\n\t"
"v_mfma_f32_16x16x32_bf16 v[68:71], a[212:215], a[132:135], v[68:71]\n\t"
"v_exp_f32 v49, v121\n\t"
"v_mfma_f32_16x16x32_bf16 v[84:87], a[212:215], a[148:151], v[84:87]\n\t"
"v_mfma_f32_16x16x32_bf16 v[16:19], v[4:7], v[148:151], v[16:19]\n\t"
"ds_read_b128 a[240:243], v1 offset:12288\n\t"
"v_mfma_f32_16x16x32_bf16 v[68:71], a[216:219], a[136:139], v[68:71]\n\t"
"v_exp_f32 v50, v122\n\t"
"v_mfma_f32_16x16x32_bf16 v[84:87], a[216:219], a[152:155], v[84:87]\n\t"
"v_cvt_pk_bf16_f32 v156, v48, v49\n\t"
"s_waitcnt lgkmcnt(2)\n\t"
"v_mfma_f32_16x16x32_bf16 v[68:71], a[220:223], a[140:143], v[68:71]\n\t"
"v_exp_f32 v51, v123\n\t"
"v_mfma_f32_16x16x32_bf16 v[84:87], a[220:223], a[156:159], v[84:87]\n\t"
"ds_read_b128 a[244:247], v1 offset:13312\n\t"
"v_mfma_f32_16x16x32_bf16 v[72:75], a[224:227], a[128:131], v[232:235]\n\t"
"v_exp_f32 v52, v124\n\t"
"v_mfma_f32_16x16x32_bf16 v[88:91], a[224:227], a[144:147], v[236:239]\n\t"
"v_cvt_pk_bf16_f32 v157, v50, v51\n\t"
"v_mfma_f32_16x16x32_bf16 v[72:75], a[228:231], a[132:135], v[72:75]\n\t"
"v_exp_f32 v53, v125\n\t"
"v_mfma_f32_16x16x32_bf16 v[88:91], a[228:231], a[148:151], v[88:91]\n\t"
"ds_read_b128 a[248:251], v1 offset:14336\n\t"
"v_mfma_f32_16x16x32_bf16 v[72:75], a[232:235], a[136:139], v[72:75]\n\t"
"v_exp_f32 v54, v126\n\t"
"v_mfma_f32_16x16x32_bf16 v[88:91], a[232:235], a[152:155], v[88:91]\n\t"
"v_cvt_pk_bf16_f32 v158, v52, v53\n\t"
"s_waitcnt lgkmcnt(2)\n\t"
"v_mfma_f32_16x16x32_bf16 v[72:75], a[236:239], a[140:143], v[72:75]\n\t"
"v_exp_f32 v55, v127\n\t"
"v_mfma_f32_16x16x32_bf16 v[88:91], a[236:239], a[156:159], v[88:91]\n\t"
"ds_read_b128 a[252:255], v1 offset:15360\n\t"
"v_mfma_f32_16x16x32_bf16 v[76:79], a[240:243], a[128:131], v[232:235]\n\t"
"v_exp_f32 v48, v64\n\t"
"v_mfma_f32_16x16x32_bf16 v[92:95], a[240:243], a[144:147], v[236:239]\n\t"
"v_cvt_pk_bf16_f32 v159, v54, v55\n\t"
"s_waitcnt lgkmcnt(2)\n\t"
"v_mfma_f32_16x16x32_bf16 v[76:79], a[244:247], a[132:135], v[76:79]\n\t"
"v_exp_f32 v49, v65\n\t"
"v_mfma_f32_16x16x32_bf16 v[92:95], a[244:247], a[148:151], v[92:95]\n\t"
"v_mfma_f32_16x16x32_bf16 v[20:23], v[4:7], v[156:159], v[20:23]\n\t"
"v_xor_b32 v1, 65536, v1\n\t"
"s_waitcnt lgkmcnt(1)\n\t"
"v_mfma_f32_16x16x32_bf16 v[76:79], a[248:251], a[136:139], v[76:79]\n\t"
"v_exp_f32 v50, v66\n\t"
"v_mfma_f32_16x16x32_bf16 v[92:95], a[248:251], a[152:155], v[92:95]\n\t"
"v_cvt_pk_bf16_f32 v128, v48, v49\n\t"
"s_waitcnt lgkmcnt(0)\n\t"
"v_mfma_f32_16x16x32_bf16 v[76:79], a[252:255], a[140:143], v[76:79]\n\t"
"v_exp_f32 v51, v67\n\t"
"v_mfma_f32_16x16x32_bf16 v[92:95], a[252:255], a[156:159], v[92:95]\n\t"
"v_add_f32 v56, v16, v20\n\t"
"v_cmp_nge_f32 vcc, s68, v56\n\t"
"s_cbranch_vccnz LSP30_%=\n\t"
"LSR31_%=:\n\t"
"v_add_f32 v30, v30, v16\n\t"
"v_add_f32 v31, v31, v20\n\t"
"s_waitcnt vmcnt(0)\n\t"
"s_barrier\n\t"
"v_mfma_f32_16x16x32_bf16 a[8:11], v[160:163], v[144:147], a[8:11]\n\t"
"v_exp_f32 v52, v68\n\t"
"v_mfma_f32_16x16x32_bf16 a[12:15], v[160:163], v[152:155], a[12:15]\n\t"
"v_cvt_pk_bf16_f32 v129, v50, v51\n\t"
"s_mov_b32 m0, s65\n\t"
"s_add_u32 s71, s61, 4096\n\t"
"buffer_load_dwordx4 v0, s[52:55], s61 offen offset:0 sc1 lds\n\t"
"v_mfma_f32_16x16x32_bf16 a[24:27], v[164:167], v[144:147], a[24:27]\n\t"
"v_exp_f32 v53, v69\n\t"
"v_mfma_f32_16x16x32_bf16 a[28:31], v[164:167], v[152:155], a[28:31]\n\t"
"v_min_f32 v224, v40, v60\n\t"
"v_min_f32 v232, s77, v40\n\t"
"v_min_f32 v44, s80, v44\n\t"
"ds_read_b128 v[160:163], v2 offset:0\n\t"
"v_mfma_f32_16x16x32_bf16 a[40:43], v[168:171], v[144:147], a[40:43]\n\t"
"v_exp_f32 v54, v70\n\t"
"v_mfma_f32_16x16x32_bf16 a[44:47], v[168:171], v[152:155], a[44:47]\n\t"
"v_cvt_pk_bf16_f32 v130, v52, v53\n\t"
"buffer_load_dwordx4 v0, s[52:55], s61 offen offset:1024 sc1 lds\n\t"
"v_mfma_f32_16x16x32_bf16 a[56:59], v[172:175], v[144:147], a[56:59]\n\t"
"v_exp_f32 v55, v71\n\t"
"v_mfma_f32_16x16x32_bf16 a[60:63], v[172:175], v[152:155], a[60:63]\n\t"
"v_min_f32 v225, v41, v61\n\t"
"v_min_f32 v233, s77, v41\n\t"
"v_min_f32 v45, s80, v45\n\t"
"ds_read_b128 v[164:167], v2 offset:1024\n\t"
"v_mfma_f32_16x16x32_bf16 a[72:75], v[176:179], v[144:147], a[72:75]\n\t"
"v_exp_f32 v48, v80\n\t"
"v_mfma_f32_16x16x32_bf16 a[76:79], v[176:179], v[152:155], a[76:79]\n\t"
"v_cvt_pk_bf16_f32 v131, v54, v55\n\t"
"buffer_load_dwordx4 v0, s[52:55], s61 offen offset:2048 sc1 lds\n\t"
"v_mfma_f32_16x16x32_bf16 a[88:91], v[180:183], v[144:147], a[88:91]\n\t"
"v_exp_f32 v49, v81\n\t"
"v_mfma_f32_16x16x32_bf16 a[92:95], v[180:183], v[152:155], a[92:95]\n\t"
"v_mfma_f32_16x16x32_bf16 v[8:11], v[4:7], v[128:131], 0\n\t"
"v_min_f32 v226, v42, v62\n\t"
"v_min_f32 v234, s77, v42\n\t"
"v_min_f32 v46, s80, v46\n\t"
"ds_read_b128 v[168:171], v2 offset:2048\n\t"
"v_mfma_f32_16x16x32_bf16 a[104:107], v[184:187], v[144:147], a[104:107]\n\t"
"v_exp_f32 v50, v82\n\t"
"v_mfma_f32_16x16x32_bf16 a[108:111], v[184:187], v[152:155], a[108:111]\n\t"
"v_cvt_pk_bf16_f32 v136, v48, v49\n\t"
"buffer_load_dwordx4 v0, s[52:55], s61 offen offset:3072 sc1 lds\n\t"
"v_mfma_f32_16x16x32_bf16 a[120:123], v[188:191], v[144:147], a[120:123]\n\t"
"v_exp_f32 v51, v83\n\t"
"v_mfma_f32_16x16x32_bf16 a[124:127], v[188:191], v[152:155], a[124:127]\n\t"
"v_min_f32 v227, v43, v63\n\t"
"v_min_f32 v235, s77, v43\n\t"
"v_min_f32 v47, s80, v47\n\t"
"ds_read_b128 v[172:175], v2 offset:3072\n\t"
"v_mfma_f32_16x16x32_bf16 a[8:11], v[192:195], v[148:151], a[8:11]\n\t"
"v_exp_f32 v52, v84\n\t"
"v_mfma_f32_16x16x32_bf16 a[12:15], v[192:195], v[156:159], a[12:15]\n\t"
"v_cvt_pk_bf16_f32 v137, v50, v51\n\t"
"s_add_u32 m0, s65, 4096\n\t"
"s_add_u32 s61, s61, 32768\n\t"
"buffer_load_dwordx4 v0, s[52:55], s71 offen offset:0 sc1 lds\n\t"
"v_mfma_f32_16x16x32_bf16 a[24:27], v[196:199], v[148:151], a[24:27]\n\t"
"v_exp_f32 v53, v85\n\t"
"v_mfma_f32_16x16x32_bf16 a[28:31], v[196:199], v[156:159], a[28:31]\n\t"
"v_min_f32 v228, v44, v60\n\t"
"v_min_f32 v40, s80, v40\n\t"
"ds_read_b128 v[176:179], v2 offset:4096\n\t"
"v_mfma_f32_16x16x32_bf16 a[40:43], v[200:203], v[148:151], a[40:43]\n\t"
"v_exp_f32 v54, v86\n\t"
"v_mfma_f32_16x16x32_bf16 a[44:47], v[200:203], v[156:159], a[44:47]\n\t"
"v_cvt_pk_bf16_f32 v138, v52, v53\n\t"
"s_sub_u32 s63, s63, 1\n\t"
"s_cmp_eq_u32 s63, 0\n\t"
"s_cselect_b32 s61, s70, s61\n\t"
"s_cselect_b32 s63, s69, s63\n\t"
"buffer_load_dwordx4 v0, s[52:55], s71 offen offset:1024 sc1 lds\n\t"
"v_mfma_f32_16x16x32_bf16 a[56:59], v[204:207], v[148:151], a[56:59]\n\t"
"v_exp_f32 v55, v87\n\t"
"v_mfma_f32_16x16x32_bf16 a[60:63], v[204:207], v[156:159], a[60:63]\n\t"
"v_min_f32 v229, v45, v61\n\t"
"v_min_f32 v41, s80, v41\n\t"
"ds_read_b128 v[180:183], v2 offset:5120\n\t"
"v_mfma_f32_16x16x32_bf16 a[72:75], v[208:211], v[148:151], a[72:75]\n\t"
"v_exp_f32 v48, v72\n\t"
"v_mfma_f32_16x16x32_bf16 a[76:79], v[208:211], v[156:159], a[76:79]\n\t"
"v_cvt_pk_bf16_f32 v139, v54, v55\n\t"
"s_xor_b32 s65, s65, 16384\n\t"
"buffer_load_dwordx4 v0, s[52:55], s71 offen offset:2048 sc1 lds\n\t"
"v_mfma_f32_16x16x32_bf16 a[88:91], v[212:215], v[148:151], a[88:91]\n\t"
"v_exp_f32 v49, v73\n\t"
"v_mfma_f32_16x16x32_bf16 a[92:95], v[212:215], v[156:159], a[92:95]\n\t"
"v_mfma_f32_16x16x32_bf16 v[12:15], v[4:7], v[136:139], 0\n\t"
"v_min_f32 v230, v46, v62\n\t"
"v_min_f32 v42, s80, v42\n\t"
"ds_read_b128 v[184:187], v2 offset:6144\n\t"
"v_mfma_f32_16x16x32_bf16 a[104:107], v[216:219], v[148:151], a[104:107]\n\t"
"v_exp_f32 v50, v74\n\t"
"v_mfma_f32_16x16x32_bf16 a[108:111], v[216:219], v[156:159], a[108:111]\n\t"
"v_cvt_pk_bf16_f32 v132, v48, v49\n\t"
"buffer_load_dwordx4 v0, s[52:55], s71 offen offset:3072 sc1 lds\n\t"
"v_mfma_f32_16x16x32_bf16 a[120:123], v[220:223], v[148:151], a[120:123]\n\t"
"v_exp_f32 v51, v75\n\t"
"v_mfma_f32_16x16x32_bf16 a[124:127], v[220:223], v[156:159], a[124:127]\n\t"
"v_min_f32 v231, v47, v63\n\t"
"v_min_f32 v43, s80, v43\n\t"
"ds_read_b128 v[188:191], v2 offset:7168\n\t"
"s_nop 0\n\t"
"v_mfma_f32_16x16x32_bf16 v[96:99], a[192:195], a[160:163], v[40:43]\n\t"
"v_exp_f32 v52, v76\n\t"
"v_mfma_f32_16x16x32_bf16 v[112:115], a[192:195], a[176:179], v[44:47]\n\t"
"v_cvt_pk_bf16_f32 v133, v50, v51\n\t"
"s_mov_b32 m0, s64\n\t"
"s_add_u32 s71, s60, 4096\n\t"
"buffer_load_dwordx4 v0, s[48:51], s60 offen offset:0 sc1 lds\n\t"
"v_mfma_f32_16x16x32_bf16 v[96:99], a[196:199], a[164:167], v[96:99]\n\t"
"v_exp_f32 v53, v77\n\t"
"v_mfma_f32_16x16x32_bf16 v[112:115], a[196:199], a[180:183], v[112:115]\n\t"
"ds_read_b128 v[192:195], v2 offset:8192\n\t"
"v_mfma_f32_16x16x32_bf16 v[96:99], a[200:203], a[168:171], v[96:99]\n\t"
"v_exp_f32 v54, v78\n\t"
"v_mfma_f32_16x16x32_bf16 v[112:115], a[200:203], a[184:187], v[112:115]\n\t"
"v_cvt_pk_bf16_f32 v134, v52, v53\n\t"
"buffer_load_dwordx4 v0, s[48:51], s60 offen offset:1024 sc1 lds\n\t"
"v_mfma_f32_16x16x32_bf16 v[96:99], a[204:207], a[172:175], v[96:99]\n\t"
"v_exp_f32 v55, v79\n\t"
"v_mfma_f32_16x16x32_bf16 v[112:115], a[204:207], a[188:191], v[112:115]\n\t"
"ds_read_b128 v[196:199], v2 offset:9216\n\t"
"v_mfma_f32_16x16x32_bf16 v[100:103], a[208:211], a[160:163], v[40:43]\n\t"
"v_exp_f32 v48, v88\n\t"
"v_mfma_f32_16x16x32_bf16 v[116:119], a[208:211], a[176:179], v[44:47]\n\t"
"v_cvt_pk_bf16_f32 v135, v54, v55\n\t"
"buffer_load_dwordx4 v0, s[48:51], s60 offen offset:2048 sc1 lds\n\t"
"v_mfma_f32_16x16x32_bf16 v[100:103], a[212:215], a[164:167], v[100:103]\n\t"
"v_exp_f32 v49, v89\n\t"
"v_mfma_f32_16x16x32_bf16 v[116:119], a[212:215], a[180:183], v[116:119]\n\t"
"v_mfma_f32_16x16x32_bf16 v[8:11], v[4:7], v[132:135], v[8:11]\n\t"
"ds_read_b128 v[200:203], v2 offset:10240\n\t"
"v_mfma_f32_16x16x32_bf16 v[100:103], a[216:219], a[168:171], v[100:103]\n\t"
"v_exp_f32 v50, v90\n\t"
"v_mfma_f32_16x16x32_bf16 v[116:119], a[216:219], a[184:187], v[116:119]\n\t"
"v_cvt_pk_bf16_f32 v140, v48, v49\n\t"
"buffer_load_dwordx4 v0, s[48:51], s60 offen offset:3072 sc1 lds\n\t"
"v_mfma_f32_16x16x32_bf16 v[100:103], a[220:223], a[172:175], v[100:103]\n\t"
"v_exp_f32 v51, v91\n\t"
"v_mfma_f32_16x16x32_bf16 v[116:119], a[220:223], a[188:191], v[116:119]\n\t"
"ds_read_b128 v[204:207], v2 offset:11264\n\t"
"v_mfma_f32_16x16x32_bf16 v[104:107], a[224:227], a[160:163], v[224:227]\n\t"
"v_exp_f32 v52, v92\n\t"
"v_mfma_f32_16x16x32_bf16 v[120:123], a[224:227], a[176:179], v[44:47]\n\t"
"v_cvt_pk_bf16_f32 v141, v50, v51\n\t"
"s_add_u32 m0, s64, 4096\n\t"
"s_add_u32 s60, s60, 32768\n\t"
"buffer_load_dwordx4 v0, s[48:51], s71 offen offset:0 sc1 lds\n\t"
"v_mfma_f32_16x16x32_bf16 v[104:107], a[228:231], a[164:167], v[104:107]\n\t"
"v_exp_f32 v53, v93\n\t"
"v_mfma_f32_16x16x32_bf16 v[120:123], a[228:231], a[180:183], v[120:123]\n\t"
"ds_read_b128 v[208:211], v2 offset:12288\n\t"
"v_mfma_f32_16x16x32_bf16 v[104:107], a[232:235], a[168:171], v[104:107]\n\t"
"v_exp_f32 v54, v94\n\t"
"v_mfma_f32_16x16x32_bf16 v[120:123], a[232:235], a[184:187], v[120:123]\n\t"
"v_cvt_pk_bf16_f32 v142, v52, v53\n\t"
"s_sub_u32 s62, s62, 1\n\t"
"s_cmp_eq_u32 s62, 0\n\t"
"s_cselect_b32 s60, s70, s60\n\t"
"s_cselect_b32 s62, s69, s62\n\t"
"buffer_load_dwordx4 v0, s[48:51], s71 offen offset:1024 sc1 lds\n\t"
"v_mfma_f32_16x16x32_bf16 v[104:107], a[236:239], a[172:175], v[104:107]\n\t"
"v_exp_f32 v55, v95\n\t"
"v_mfma_f32_16x16x32_bf16 v[120:123], a[236:239], a[188:191], v[120:123]\n\t"
"ds_read_b128 v[212:215], v2 offset:13312\n\t"
"v_mfma_f32_16x16x32_bf16 v[108:111], a[240:243], a[160:163], v[232:235]\n\t"
"v_exp_f32 v48, v96\n\t"
"v_mfma_f32_16x16x32_bf16 v[124:127], a[240:243], a[176:179], v[228:231]\n\t"
"v_cvt_pk_bf16_f32 v143, v54, v55\n\t"
"s_xor_b32 s64, s64, 65536\n\t"
"buffer_load_dwordx4 v0, s[48:51], s71 offen offset:2048 sc1 lds\n\t"
"v_mfma_f32_16x16x32_bf16 v[108:111], a[244:247], a[164:167], v[108:111]\n\t"
"v_exp_f32 v49, v97\n\t"
"v_mfma_f32_16x16x32_bf16 v[124:127], a[244:247], a[180:183], v[124:127]\n\t"
"v_mfma_f32_16x16x32_bf16 v[12:15], v[4:7], v[140:143], v[12:15]\n\t"
"ds_read_b128 v[216:219], v2 offset:14336\n\t"
"v_mfma_f32_16x16x32_bf16 v[108:111], a[248:251], a[168:171], v[108:111]\n\t"
"v_exp_f32 v50, v98\n\t"
"v_mfma_f32_16x16x32_bf16 v[124:127], a[248:251], a[184:187], v[124:127]\n\t"
"v_cvt_pk_bf16_f32 v144, v48, v49\n\t"
"buffer_load_dwordx4 v0, s[48:51], s71 offen offset:3072 sc1 lds\n\t"
"v_mfma_f32_16x16x32_bf16 v[108:111], a[252:255], a[172:175], v[108:111]\n\t"
"v_exp_f32 v51, v99\n\t"
"v_mfma_f32_16x16x32_bf16 v[124:127], a[252:255], a[188:191], v[124:127]\n\t"
"ds_read_b128 v[220:223], v2 offset:15360\n\t"
"s_waitcnt lgkmcnt(2)\n\t"
"v_xor_b32 v2, 16384, v2\n\t"
"v_add_f32 v56, v8, v12\n\t"
"v_cmp_nge_f32 vcc, s68, v56\n\t"
"s_cbranch_vccnz LSP32_%=\n\t"
"LSR33_%=:\n\t"
"v_add_f32 v28, v28, v8\n\t"
"v_add_f32 v29, v29, v12\n\t"
"v_mfma_f32_16x16x32_bf16 a[0:3], v[160:163], v[128:131], a[0:3]\n\t"
"v_exp_f32 v52, v100\n\t"
"v_mfma_f32_16x16x32_bf16 a[4:7], v[160:163], v[136:139], a[4:7]\n\t"
"v_cvt_pk_bf16_f32 v145, v50, v51\n\t"
"ds_read_b128 a[192:195], v1 offset:0\n\t"
"v_mfma_f32_16x16x32_bf16 a[16:19], v[164:167], v[128:131], a[16:19]\n\t"
"v_exp_f32 v53, v101\n\t"
"v_mfma_f32_16x16x32_bf16 a[20:23], v[164:167], v[136:139], a[20:23]\n\t"
"v_mfma_f32_16x16x32_bf16 a[32:35], v[168:171], v[128:131], a[32:35]\n\t"
"v_exp_f32 v54, v102\n\t"
"v_mfma_f32_16x16x32_bf16 a[36:39], v[168:171], v[136:139], a[36:39]\n\t"
"v_cvt_pk_bf16_f32 v146, v52, v53\n\t"
"ds_read_b128 a[196:199], v1 offset:1024\n\t"
"v_mfma_f32_16x16x32_bf16 a[48:51], v[172:175], v[128:131], a[48:51]\n\t"
"v_exp_f32 v55, v103\n\t"
"v_mfma_f32_16x16x32_bf16 a[52:55], v[172:175], v[136:139], a[52:55]\n\t"
"ds_read_b128 a[200:203], v1 offset:2048\n\t"
"v_mfma_f32_16x16x32_bf16 a[64:67], v[176:179], v[128:131], a[64:67]\n\t"
"v_exp_f32 v48, v112\n\t"
"v_mfma_f32_16x16x32_bf16 a[68:71], v[176:179], v[136:139], a[68:71]\n\t"
"v_cvt_pk_bf16_f32 v147, v54, v55\n\t"
"v_mfma_f32_16x16x32_bf16 a[80:83], v[180:183], v[128:131], a[80:83]\n\t"
"v_exp_f32 v49, v113\n\t"
"v_mfma_f32_16x16x32_bf16 a[84:87], v[180:183], v[136:139], a[84:87]\n\t"
"v_mfma_f32_16x16x32_bf16 v[16:19], v[4:7], v[144:147], 0\n\t"
"ds_read_b128 a[204:207], v1 offset:3072\n\t"
"v_mfma_f32_16x16x32_bf16 a[96:99], v[184:187], v[128:131], a[96:99]\n\t"
"v_exp_f32 v50, v114\n\t"
"v_mfma_f32_16x16x32_bf16 a[100:103], v[184:187], v[136:139], a[100:103]\n\t"
"v_cvt_pk_bf16_f32 v152, v48, v49\n\t"
"ds_read_b128 a[208:211], v1 offset:4096\n\t"
"v_mfma_f32_16x16x32_bf16 a[112:115], v[188:191], v[128:131], a[112:115]\n\t"
"v_exp_f32 v51, v115\n\t"
"v_mfma_f32_16x16x32_bf16 a[116:119], v[188:191], v[136:139], a[116:119]\n\t"
"v_mfma_f32_16x16x32_bf16 a[0:3], v[192:195], v[132:135], a[0:3]\n\t"
"v_exp_f32 v52, v116\n\t"
"v_mfma_f32_16x16x32_bf16 a[4:7], v[192:195], v[140:143], a[4:7]\n\t"
"v_cvt_pk_bf16_f32 v153, v50, v51\n\t"
"ds_read_b128 a[212:215], v1 offset:5120\n\t"
"v_mfma_f32_16x16x32_bf16 a[16:19], v[196:199], v[132:135], a[16:19]\n\t"
"v_exp_f32 v53, v117\n\t"
"v_mfma_f32_16x16x32_bf16 a[20:23], v[196:199], v[140:143], a[20:23]\n\t"
"ds_read_b128 a[216:219], v1 offset:6144\n\t"
"v_mfma_f32_16x16x32_bf16 a[32:35], v[200:203], v[132:135], a[32:35]\n\t"
"v_exp_f32 v54, v118\n\t"
"v_mfma_f32_16x16x32_bf16 a[36:39], v[200:203], v[140:143], a[36:39]\n\t"
"v_cvt_pk_bf16_f32 v154, v52, v53\n\t"
"v_mfma_f32_16x16x32_bf16 a[48:51], v[204:207], v[132:135], a[48:51]\n\t"
"v_exp_f32 v55, v119\n\t"
"v_mfma_f32_16x16x32_bf16 a[52:55], v[204:207], v[140:143], a[52:55]\n\t"
"ds_read_b128 a[220:223], v1 offset:7168\n\t"
"v_mfma_f32_16x16x32_bf16 a[64:67], v[208:211], v[132:135], a[64:67]\n\t"
"v_exp_f32 v48, v104\n\t"
"v_mfma_f32_16x16x32_bf16 a[68:71], v[208:211], v[140:143], a[68:71]\n\t"
"v_cvt_pk_bf16_f32 v155, v54, v55\n\t"
"v_mfma_f32_16x16x32_bf16 a[80:83], v[212:215], v[132:135], a[80:83]\n\t"
"v_exp_f32 v49, v105\n\t"
"v_mfma_f32_16x16x32_bf16 a[84:87], v[212:215], v[140:143], a[84:87]\n\t"
"v_mfma_f32_16x16x32_bf16 v[20:23], v[4:7], v[152:155], 0\n\t"
"ds_read_b128 a[224:227], v1 offset:8192\n\t"
"s_waitcnt lgkmcnt(2)\n\t"
"v_mfma_f32_16x16x32_bf16 a[96:99], v[216:219], v[132:135], a[96:99]\n\t"
"v_exp_f32 v50, v106\n\t"
"v_mfma_f32_16x16x32_bf16 a[100:103], v[216:219], v[140:143], a[100:103]\n\t"
"v_cvt_pk_bf16_f32 v148, v48, v49\n\t"
"ds_read_b128 a[228:231], v1 offset:9216\n\t"
"v_mfma_f32_16x16x32_bf16 a[112:115], v[220:223], v[132:135], a[112:115]\n\t"
"v_exp_f32 v51, v107\n\t"
"v_mfma_f32_16x16x32_bf16 a[116:119], v[220:223], v[140:143], a[116:119]\n\t"
"LDRAIN14_%=:\n\t"
"ds_read_b128 a[232:235], v1 offset:10240\n\t"
"ds_read_b128 a[236:239], v1 offset:11264\n\t"
"ds_read_b128 a[240:243], v1 offset:12288\n\t"
"ds_read_b128 a[244:247], v1 offset:13312\n\t"
"ds_read_b128 a[248:251], v1 offset:14336\n\t"
"ds_read_b128 a[252:255], v1 offset:15360\n\t"
"v_xor_b32 v1, 65536, v1\n\t"
"s_sub_u32 s72, 15, s41\n\t"
"s_and_b32 s75, s38, 7\n\t"
"s_lshl_b32 s75, s75, 4\n\t"
"s_add_u32 s72, s75, s72\n\t"
"s_lshl_b32 s72, s72, 7\n\t"
"s_add_u32 s72, s72, 8192\n\t"
"v_mbcnt_lo_u32_b32 v57, -1, 0\n\t"
"v_mbcnt_hi_u32_b32 v57, -1, v57\n\t"
"v_min_u32 v57, 15, v57\n\t"
"v_lshl_add_u32 v57, v57, 3, s72\n\t"
"global_load_dwordx2 v[58:59], v57, s[42:43] sc1\n\t"
"s_lshl_b32 s72, s44, 14\n\t"
"s_add_u32 s72, s72, 131072\n\t"
"v_add_u32 v254, s72, v0\n\t"
"v_exp_f32 v52, v108\n\t"
"v_cvt_pk_bf16_f32 v149, v50, v51\n\t"
"v_exp_f32 v53, v109\n\t"
"v_exp_f32 v54, v110\n\t"
"v_cvt_pk_bf16_f32 v150, v52, v53\n\t"
"v_exp_f32 v55, v111\n\t"
"v_exp_f32 v48, v120\n\t"
"v_cvt_pk_bf16_f32 v151, v54, v55\n\t"
"v_exp_f32 v49, v121\n\t"
"s_nop 0\n\t"
"v_mfma_f32_16x16x32_bf16 v[16:19], v[4:7], v[148:151], v[16:19]\n\t"
"v_exp_f32 v50, v122\n\t"
"v_cvt_pk_bf16_f32 v156, v48, v49\n\t"
"v_exp_f32 v51, v123\n\t"
"v_exp_f32 v52, v124\n\t"
"v_cvt_pk_bf16_f32 v157, v50, v51\n\t"
"v_exp_f32 v53, v125\n\t"
"v_exp_f32 v54, v126\n\t"
"v_cvt_pk_bf16_f32 v158, v52, v53\n\t"
"v_exp_f32 v55, v127\n\t"
"s_nop 0\n\t"
"v_cvt_pk_bf16_f32 v159, v54, v55\n\t"
"s_nop 1\n\t"
"v_mfma_f32_16x16x32_bf16 v[20:23], v[4:7], v[156:159], v[20:23]\n\t"
"s_nop 7\n\t"
"v_add_f32 v56, v16, v20\n\t"
"v_cmp_nge_f32 vcc, s68, v56\n\t"
"s_cbranch_vccnz LSP34_%=\n\t"
"LSR35_%=:\n\t"
"v_add_f32 v30, v30, v16\n\t"
"v_add_f32 v31, v31, v20\n\t"
"v_mfma_f32_16x16x32_bf16 a[8:11], v[160:163], v[144:147], a[8:11]\n\t"
"ds_read_b128 a[128:131], v254 offset:0\n\t"
"v_mfma_f32_16x16x32_bf16 a[12:15], v[160:163], v[152:155], a[12:15]\n\t"
"v_mfma_f32_16x16x32_bf16 a[24:27], v[164:167], v[144:147], a[24:27]\n\t"
"ds_read_b128 a[132:135], v254 offset:1024\n\t"
"v_mfma_f32_16x16x32_bf16 a[28:31], v[164:167], v[152:155], a[28:31]\n\t"
"v_mfma_f32_16x16x32_bf16 a[40:43], v[168:171], v[144:147], a[40:43]\n\t"
"ds_read_b128 a[136:139], v254 offset:2048\n\t"
"v_mfma_f32_16x16x32_bf16 a[44:47], v[168:171], v[152:155], a[44:47]\n\t"
"v_mfma_f32_16x16x32_bf16 a[56:59], v[172:175], v[144:147], a[56:59]\n\t"
"ds_read_b128 a[140:143], v254 offset:3072\n\t"
"v_mfma_f32_16x16x32_bf16 a[60:63], v[172:175], v[152:155], a[60:63]\n\t"
"v_mfma_f32_16x16x32_bf16 a[72:75], v[176:179], v[144:147], a[72:75]\n\t"
"ds_read_b128 a[144:147], v254 offset:4096\n\t"
"v_mfma_f32_16x16x32_bf16 a[76:79], v[176:179], v[152:155], a[76:79]\n\t"
"v_mfma_f32_16x16x32_bf16 a[88:91], v[180:183], v[144:147], a[88:91]\n\t"
"ds_read_b128 a[148:151], v254 offset:5120\n\t"
"v_mfma_f32_16x16x32_bf16 a[92:95], v[180:183], v[152:155], a[92:95]\n\t"
"v_mfma_f32_16x16x32_bf16 a[104:107], v[184:187], v[144:147], a[104:107]\n\t"
"ds_read_b128 a[152:155], v254 offset:6144\n\t"
"s_waitcnt lgkmcnt(5)\n\t"
"v_mfma_f32_16x16x32_bf16 a[108:111], v[184:187], v[152:155], a[108:111]\n\t"
"v_mfma_f32_16x16x32_bf16 a[120:123], v[188:191], v[144:147], a[120:123]\n\t"
"ds_read_b128 a[156:159], v254 offset:7168\n\t"
"v_mfma_f32_16x16x32_bf16 a[124:127], v[188:191], v[152:155], a[124:127]\n\t"
"v_mfma_f32_16x16x32_bf16 a[8:11], v[192:195], v[148:151], a[8:11]\n\t"
"ds_read_b128 a[160:163], v254 offset:8192\n\t"
"v_mfma_f32_16x16x32_bf16 a[12:15], v[192:195], v[156:159], a[12:15]\n\t"
"v_mfma_f32_16x16x32_bf16 a[24:27], v[196:199], v[148:151], a[24:27]\n\t"
"ds_read_b128 a[164:167], v254 offset:9216\n\t"
"v_mfma_f32_16x16x32_bf16 a[28:31], v[196:199], v[156:159], a[28:31]\n\t"
"v_mfma_f32_16x16x32_bf16 a[40:43], v[200:203], v[148:151], a[40:43]\n\t"
"ds_read_b128 a[168:171], v254 offset:10240\n\t"
"v_mfma_f32_16x16x32_bf16 a[44:47], v[200:203], v[156:159], a[44:47]\n\t"
"v_mfma_f32_16x16x32_bf16 a[56:59], v[204:207], v[148:151], a[56:59]\n\t"
"ds_read_b128 a[172:175], v254 offset:11264\n\t"
"v_mfma_f32_16x16x32_bf16 a[60:63], v[204:207], v[156:159], a[60:63]\n\t"
"v_mfma_f32_16x16x32_bf16 a[72:75], v[208:211], v[148:151], a[72:75]\n\t"
"ds_read_b128 a[176:179], v254 offset:12288\n\t"
"v_mfma_f32_16x16x32_bf16 a[76:79], v[208:211], v[156:159], a[76:79]\n\t"
"v_mfma_f32_16x16x32_bf16 a[88:91], v[212:215], v[148:151], a[88:91]\n\t"
"ds_read_b128 a[180:183], v254 offset:13312\n\t"
"v_mfma_f32_16x16x32_bf16 a[92:95], v[212:215], v[156:159], a[92:95]\n\t"
"v_mfma_f32_16x16x32_bf16 a[104:107], v[216:219], v[148:151], a[104:107]\n\t"
"ds_read_b128 a[184:187], v254 offset:14336\n\t"
"v_mfma_f32_16x16x32_bf16 a[108:111], v[216:219], v[156:159], a[108:111]\n\t"
"v_mfma_f32_16x16x32_bf16 a[120:123], v[220:223], v[148:151], a[120:123]\n\t"
"ds_read_b128 a[188:191], v254 offset:15360\n\t"
"v_mfma_f32_16x16x32_bf16 a[124:127], v[220:223], v[156:159], a[124:127]\n\t"
"s_cmp_eq_u32 s44, 0\n\t"
"s_cselect_b64 vcc, -1, 0\n\t"
"s_mul_i32 s72, s44, 49152\n\t"
"s_add_u32 s72, s72, 16384\n\t"
"v_cndmask_b32 v224, v1, v2, vcc\n\t"
"v_xor_b32 v224, s72, v224\n\t"
"s_xor_b32 s72, s72, 81920\n\t"
"v_xor_b32 v225, s72, v224\n\t"
"s_lshl_b32 s72, s39, 11\n\t"
"s_add_u32 s72, s72, 131072\n\t"
"v_add_u32 v226, s72, v0\n\t"
"v_xor_b32 v227, 4096, v226\n\t"
"s_waitcnt lgkmcnt(0)\n\t"
"s_barrier\n\t"
"s_cmp_lg_u32 s45, 0\n\t"
"s_cbranch_scc1 LMRG115_%=\n\t"
"s_nop 7\n\t"
"ds_write_b128 v226, v[24:27] offset:0\n\t"
"v_accvgpr_read_b32 v64, a0\n\t"
"v_accvgpr_read_b32 v65, a1\n\t"
"v_accvgpr_read_b32 v66, a2\n\t"
"v_accvgpr_read_b32 v67, a3\n\t"
"ds_write_b128 v226, v[28:31] offset:1024\n\t"
"v_accvgpr_read_b32 v68, a4\n\t"
"v_accvgpr_read_b32 v69, a5\n\t"
"v_accvgpr_read_b32 v70, a6\n\t"
"v_accvgpr_read_b32 v71, a7\n\t"
"ds_write_b128 v224, a[64:67] offset:0\n\t"
"v_accvgpr_read_b32 v72, a8\n\t"
"v_accvgpr_read_b32 v73, a9\n\t"
"v_accvgpr_read_b32 v74, a10\n\t"
"ds_write_b128 v224, a[68:71] offset:1024\n\t"
"v_accvgpr_read_b32 v75, a11\n\t"
"v_accvgpr_read_b32 v76, a12\n\t"
"v_accvgpr_read_b32 v77, a13\n\t"
"v_accvgpr_read_b32 v78, a14\n\t"
"ds_write_b128 v224, a[72:75] offset:2048\n\t"
"v_accvgpr_read_b32 v79, a15\n\t"
"v_accvgpr_read_b32 v80, a16\n\t"
"v_accvgpr_read_b32 v81, a17\n\t"
"ds_write_b128 v224, a[76:79] offset:3072\n\t"
"v_accvgpr_read_b32 v82, a18\n\t"
"v_accvgpr_read_b32 v83, a19\n\t"
"v_accvgpr_read_b32 v84, a20\n\t"
"v_accvgpr_read_b32 v85, a21\n\t"
"ds_write_b128 v224, a[80:83] offset:4096\n\t"
"v_accvgpr_read_b32 v86, a22\n\t"
"v_accvgpr_read_b32 v87, a23\n\t"
"v_accvgpr_read_b32 v88, a24\n\t"
"ds_write_b128 v224, a[84:87] offset:5120\n\t"
"v_accvgpr_read_b32 v89, a25\n\t"
"v_accvgpr_read_b32 v90, a26\n\t"
"v_accvgpr_read_b32 v91, a27\n\t"
"v_accvgpr_read_b32 v92, a28\n\t"
"ds_write_b128 v224, a[88:91] offset:6144\n\t"
"v_accvgpr_read_b32 v93, a29\n\t"
"v_accvgpr_read_b32 v94, a30\n\t"
"v_accvgpr_read_b32 v95, a31\n\t"
"ds_write_b128 v224, a[92:95] offset:7168\n\t"
"v_accvgpr_read_b32 v96, a32\n\t"
"v_accvgpr_read_b32 v97, a33\n\t"
"v_accvgpr_read_b32 v98, a34\n\t"
"v_accvgpr_read_b32 v99, a35\n\t"
"ds_write_b128 v224, a[96:99] offset:8192\n\t"
"v_accvgpr_read_b32 v100, a36\n\t"
"v_accvgpr_read_b32 v101, a37\n\t"
"v_accvgpr_read_b32 v102, a38\n\t"
"v_accvgpr_read_b32 v103, a39\n\t"
"ds_write_b128 v224, a[100:103] offset:9216\n\t"
"v_accvgpr_read_b32 v104, a40\n\t"
"v_accvgpr_read_b32 v105, a41\n\t"
"v_accvgpr_read_b32 v106, a42\n\t"
"ds_write_b128 v224, a[104:107] offset:10240\n\t"
"v_accvgpr_read_b32 v107, a43\n\t"
"v_accvgpr_read_b32 v108, a44\n\t"
"v_accvgpr_read_b32 v109, a45\n\t"
"v_accvgpr_read_b32 v110, a46\n\t"
"ds_write_b128 v224, a[108:111] offset:11264\n\t"
"v_accvgpr_read_b32 v111, a47\n\t"
"v_accvgpr_read_b32 v112, a48\n\t"
"v_accvgpr_read_b32 v113, a49\n\t"
"ds_write_b128 v224, a[112:115] offset:12288\n\t"
"v_accvgpr_read_b32 v114, a50\n\t"
"v_accvgpr_read_b32 v115, a51\n\t"
"v_accvgpr_read_b32 v116, a52\n\t"
"v_accvgpr_read_b32 v117, a53\n\t"
"ds_write_b128 v224, a[116:119] offset:13312\n\t"
"s_waitcnt lgkmcnt(9)\n\t"
"v_accvgpr_read_b32 v118, a54\n\t"
"v_accvgpr_read_b32 v119, a55\n\t"
"v_accvgpr_read_b32 v120, a56\n\t"
"ds_write_b128 v224, a[120:123] offset:14336\n\t"
"v_accvgpr_read_b32 v121, a57\n\t"
"v_accvgpr_read_b32 v122, a58\n\t"
"v_accvgpr_read_b32 v123, a59\n\t"
"v_accvgpr_read_b32 v124, a60\n\t"
"ds_write_b128 v224, a[124:127] offset:15360\n\t"
"v_accvgpr_read_b32 v125, a61\n\t"
"v_accvgpr_read_b32 v126, a62\n\t"
"v_accvgpr_read_b32 v127, a63\n\t"
"s_waitcnt lgkmcnt(0)\n\t"
"s_barrier\n\t"
"ds_read_b128 v[228:231], v227 offset:0\n\t"
"ds_read_b128 v[232:235], v227 offset:1024\n\t"
"ds_read_b128 v[160:163], v225 offset:0\n\t"
"ds_read_b128 v[164:167], v225 offset:1024\n\t"
"ds_read_b128 v[168:171], v225 offset:2048\n\t"
"ds_read_b128 v[172:175], v225 offset:3072\n\t"
"ds_read_b128 v[176:179], v225 offset:4096\n\t"
"ds_read_b128 v[180:183], v225 offset:5120\n\t"
"ds_read_b128 v[184:187], v225 offset:6144\n\t"
"ds_read_b128 v[188:191], v225 offset:7168\n\t"
"ds_read_b128 v[192:195], v225 offset:8192\n\t"
"ds_read_b128 v[196:199], v225 offset:9216\n\t"
"ds_read_b128 v[200:203], v225 offset:10240\n\t"
"ds_read_b128 v[204:207], v225 offset:11264\n\t"
"ds_read_b128 v[208:211], v225 offset:12288\n\t"
"ds_read_b128 v[212:215], v225 offset:13312\n\t"
"s_waitcnt lgkmcnt(14)\n\t"
"ds_read_b128 v[216:219], v225 offset:14336\n\t"
"ds_read_b128 v[220:223], v225 offset:15360\n\t"
"s_waitcnt lgkmcnt(14)\n\t"
"v_max_f32 v236, v24, v228\n\t"
"v_sub_f32 v237, v24, v236\n\t"
"v_sub_f32 v238, v228, v236\n\t"
"v_max_f32 v240, v25, v229\n\t"
"v_sub_f32 v241, v25, v240\n\t"
"v_sub_f32 v242, v229, v240\n\t"
"v_max_f32 v244, v26, v230\n\t"
"v_sub_f32 v245, v26, v244\n\t"
"v_sub_f32 v246, v230, v244\n\t"
"v_max_f32 v248, v27, v231\n\t"
"v_sub_f32 v249, v27, v248\n\t"
"v_sub_f32 v250, v231, v248\n\t"
"v_exp_f32 v237, v237\n\t"
"v_exp_f32 v238, v238\n\t"
"v_exp_f32 v241, v241\n\t"
"v_exp_f32 v242, v242\n\t"
"v_exp_f32 v245, v245\n\t"
"v_exp_f32 v246, v246\n\t"
"v_exp_f32 v249, v249\n\t"
"v_exp_f32 v250, v250\n\t"
"v_mul_f32 v239, v28, v237\n\t"
"v_fmac_f32 v239, v232, v238\n\t"
"v_mul_f32 v243, v29, v241\n\t"
"v_fmac_f32 v243, v233, v242\n\t"
"v_mul_f32 v247, v30, v245\n\t"
"v_fmac_f32 v247, v234, v246\n\t"
"v_mul_f32 v251, v31, v249\n\t"
"v_fmac_f32 v251, v235, v250\n\t"
"v_rcp_f32 v239, v239\n\t"
"v_rcp_f32 v243, v243\n\t"
"v_rcp_f32 v247, v247\n\t"
"v_rcp_f32 v251, v251\n\t"
"v_mul_f32 v8, v237, v239\n\t"
"v_mul_f32 v16, v238, v239\n\t"
"v_mul_f32 v10, v241, v243\n\t"
"v_mul_f32 v18, v242, v243\n\t"
"v_mul_f32 v12, v245, v247\n\t"
"v_mul_f32 v20, v246, v247\n\t"
"v_mul_f32 v14, v249, v251\n\t"
"v_mul_f32 v22, v250, v251\n\t"
"s_add_u32 s72, s74, 131072\n\t"
"s_add_u32 s75, s74, 262144\n\t"
"s_add_u32 s76, s74, 393216\n\t"
"v_pk_mul_f32 v[64:65], v[64:65], v[8:9] op_sel_hi:[1,0]\n\t"
"v_pk_fma_f32 v[64:65], v[160:161], v[16:17], v[64:65] op_sel_hi:[1,0,1]\n\t"
"v_pk_mul_f32 v[66:67], v[66:67], v[8:9] op_sel_hi:[1,0]\n\t"
"v_pk_fma_f32 v[66:67], v[162:163], v[16:17], v[66:67] op_sel_hi:[1,0,1]\n\t"
"v_cvt_pk_bf16_f32 v128, v64, v65\n\t"
"v_cvt_pk_bf16_f32 v129, v66, v67\n\t"
"v_pk_mul_f32 v[80:81], v[80:81], v[8:9] op_sel_hi:[1,0]\n\t"
"s_waitcnt lgkmcnt(0)\n\t"
"v_pk_fma_f32 v[80:81], v[176:177], v[16:17], v[80:81] op_sel_hi:[1,0,1]\n\t"
"v_pk_mul_f32 v[82:83], v[82:83], v[8:9] op_sel_hi:[1,0]\n\t"
"v_pk_fma_f32 v[82:83], v[178:179], v[16:17], v[82:83] op_sel_hi:[1,0,1]\n\t"
"v_cvt_pk_bf16_f32 v130, v80, v81\n\t"
"v_cvt_pk_bf16_f32 v131, v82, v83\n\t"
"buffer_store_dwordx4 v[128:131], v0, s[56:59], s74 offen offset:0 sc1\n\t"
"v_pk_mul_f32 v[68:69], v[68:69], v[10:11] op_sel_hi:[1,0]\n\t"
"v_pk_fma_f32 v[68:69], v[164:165], v[18:19], v[68:69] op_sel_hi:[1,0,1]\n\t"
"v_pk_mul_f32 v[70:71], v[70:71], v[10:11] op_sel_hi:[1,0]\n\t"
"v_pk_fma_f32 v[70:71], v[166:167], v[18:19], v[70:71] op_sel_hi:[1,0,1]\n\t"
"v_cvt_pk_bf16_f32 v132, v68, v69\n\t"
"v_cvt_pk_bf16_f32 v133, v70, v71\n\t"
"v_pk_mul_f32 v[84:85], v[84:85], v[10:11] op_sel_hi:[1,0]\n\t"
"v_pk_fma_f32 v[84:85], v[180:181], v[18:19], v[84:85] op_sel_hi:[1,0,1]\n\t"
"v_pk_mul_f32 v[86:87], v[86:87], v[10:11] op_sel_hi:[1,0]\n\t"
"v_pk_fma_f32 v[86:87], v[182:183], v[18:19], v[86:87] op_sel_hi:[1,0,1]\n\t"
"v_cvt_pk_bf16_f32 v134, v84, v85\n\t"
"v_cvt_pk_bf16_f32 v135, v86, v87\n\t"
"buffer_store_dwordx4 v[132:135], v0, s[56:59], s72 offen offset:0 sc1\n\t"
"v_pk_mul_f32 v[72:73], v[72:73], v[12:13] op_sel_hi:[1,0]\n\t"
"v_pk_fma_f32 v[72:73], v[168:169], v[20:21], v[72:73] op_sel_hi:[1,0,1]\n\t"
"v_pk_mul_f32 v[74:75], v[74:75], v[12:13] op_sel_hi:[1,0]\n\t"
"v_pk_fma_f32 v[74:75], v[170:171], v[20:21], v[74:75] op_sel_hi:[1,0,1]\n\t"
"v_cvt_pk_bf16_f32 v136, v72, v73\n\t"
"v_cvt_pk_bf16_f32 v137, v74, v75\n\t"
"v_pk_mul_f32 v[88:89], v[88:89], v[12:13] op_sel_hi:[1,0]\n\t"
"v_pk_fma_f32 v[88:89], v[184:185], v[20:21], v[88:89] op_sel_hi:[1,0,1]\n\t"
"v_pk_mul_f32 v[90:91], v[90:91], v[12:13] op_sel_hi:[1,0]\n\t"
"v_pk_fma_f32 v[90:91], v[186:187], v[20:21], v[90:91] op_sel_hi:[1,0,1]\n\t"
"v_cvt_pk_bf16_f32 v138, v88, v89\n\t"
"v_cvt_pk_bf16_f32 v139, v90, v91\n\t"
"buffer_store_dwordx4 v[136:139], v0, s[56:59], s75 offen offset:0 sc1\n\t"
"v_pk_mul_f32 v[76:77], v[76:77], v[14:15] op_sel_hi:[1,0]\n\t"
"v_pk_fma_f32 v[76:77], v[172:173], v[22:23], v[76:77] op_sel_hi:[1,0,1]\n\t"
"v_pk_mul_f32 v[78:79], v[78:79], v[14:15] op_sel_hi:[1,0]\n\t"
"v_pk_fma_f32 v[78:79], v[174:175], v[22:23], v[78:79] op_sel_hi:[1,0,1]\n\t"
"v_cvt_pk_bf16_f32 v140, v76, v77\n\t"
"v_cvt_pk_bf16_f32 v141, v78, v79\n\t"
"v_pk_mul_f32 v[92:93], v[92:93], v[14:15] op_sel_hi:[1,0]\n\t"
"v_pk_fma_f32 v[92:93], v[188:189], v[22:23], v[92:93] op_sel_hi:[1,0,1]\n\t"
"v_pk_mul_f32 v[94:95], v[94:95], v[14:15] op_sel_hi:[1,0]\n\t"
"v_pk_fma_f32 v[94:95], v[190:191], v[22:23], v[94:95] op_sel_hi:[1,0,1]\n\t"
"v_cvt_pk_bf16_f32 v142, v92, v93\n\t"
"v_cvt_pk_bf16_f32 v143, v94, v95\n\t"
"buffer_store_dwordx4 v[140:143], v0, s[56:59], s76 offen offset:0 sc1\n\t"
"v_pk_mul_f32 v[96:97], v[96:97], v[8:9] op_sel_hi:[1,0]\n\t"
"v_pk_fma_f32 v[96:97], v[192:193], v[16:17], v[96:97] op_sel_hi:[1,0,1]\n\t"
"v_pk_mul_f32 v[98:99], v[98:99], v[8:9] op_sel_hi:[1,0]\n\t"
"v_pk_fma_f32 v[98:99], v[194:195], v[16:17], v[98:99] op_sel_hi:[1,0,1]\n\t"
"v_cvt_pk_bf16_f32 v144, v96, v97\n\t"
"v_cvt_pk_bf16_f32 v145, v98, v99\n\t"
"v_pk_mul_f32 v[112:113], v[112:113], v[8:9] op_sel_hi:[1,0]\n\t"
"v_pk_fma_f32 v[112:113], v[208:209], v[16:17], v[112:113] op_sel_hi:[1,0,1]\n\t"
"v_pk_mul_f32 v[114:115], v[114:115], v[8:9] op_sel_hi:[1,0]\n\t"
"v_pk_fma_f32 v[114:115], v[210:211], v[16:17], v[114:115] op_sel_hi:[1,0,1]\n\t"
"v_cvt_pk_bf16_f32 v146, v112, v113\n\t"
"v_cvt_pk_bf16_f32 v147, v114, v115\n\t"
"buffer_store_dwordx4 v[144:147], v0, s[56:59], s74 offen offset:1024 sc1\n\t"
"v_pk_mul_f32 v[100:101], v[100:101], v[10:11] op_sel_hi:[1,0]\n\t"
"v_pk_fma_f32 v[100:101], v[196:197], v[18:19], v[100:101] op_sel_hi:[1,0,1]\n\t"
"v_pk_mul_f32 v[102:103], v[102:103], v[10:11] op_sel_hi:[1,0]\n\t"
"v_pk_fma_f32 v[102:103], v[198:199], v[18:19], v[102:103] op_sel_hi:[1,0,1]\n\t"
"v_cvt_pk_bf16_f32 v148, v100, v101\n\t"
"v_cvt_pk_bf16_f32 v149, v102, v103\n\t"
"v_pk_mul_f32 v[116:117], v[116:117], v[10:11] op_sel_hi:[1,0]\n\t"
"v_pk_fma_f32 v[116:117], v[212:213], v[18:19], v[116:117] op_sel_hi:[1,0,1]\n\t"
"v_pk_mul_f32 v[118:119], v[118:119], v[10:11] op_sel_hi:[1,0]\n\t"
"v_pk_fma_f32 v[118:119], v[214:215], v[18:19], v[118:119] op_sel_hi:[1,0,1]\n\t"
"v_cvt_pk_bf16_f32 v150, v116, v117\n\t"
"v_cvt_pk_bf16_f32 v151, v118, v119\n\t"
"buffer_store_dwordx4 v[148:151], v0, s[56:59], s72 offen offset:1024 sc1\n\t"
"v_pk_mul_f32 v[104:105], v[104:105], v[12:13] op_sel_hi:[1,0]\n\t"
"v_pk_fma_f32 v[104:105], v[200:201], v[20:21], v[104:105] op_sel_hi:[1,0,1]\n\t"
"v_pk_mul_f32 v[106:107], v[106:107], v[12:13] op_sel_hi:[1,0]\n\t"
"v_pk_fma_f32 v[106:107], v[202:203], v[20:21], v[106:107] op_sel_hi:[1,0,1]\n\t"
"v_cvt_pk_bf16_f32 v152, v104, v105\n\t"
"v_cvt_pk_bf16_f32 v153, v106, v107\n\t"
"v_pk_mul_f32 v[120:121], v[120:121], v[12:13] op_sel_hi:[1,0]\n\t"
"v_pk_fma_f32 v[120:121], v[216:217], v[20:21], v[120:121] op_sel_hi:[1,0,1]\n\t"
"v_pk_mul_f32 v[122:123], v[122:123], v[12:13] op_sel_hi:[1,0]\n\t"
"v_pk_fma_f32 v[122:123], v[218:219], v[20:21], v[122:123] op_sel_hi:[1,0,1]\n\t"
"v_cvt_pk_bf16_f32 v154, v120, v121\n\t"
"v_cvt_pk_bf16_f32 v155, v122, v123\n\t"
"buffer_store_dwordx4 v[152:155], v0, s[56:59], s75 offen offset:1024 sc1\n\t"
"v_pk_mul_f32 v[108:109], v[108:109], v[14:15] op_sel_hi:[1,0]\n\t"
"v_pk_fma_f32 v[108:109], v[204:205], v[22:23], v[108:109] op_sel_hi:[1,0,1]\n\t"
"v_pk_mul_f32 v[110:111], v[110:111], v[14:15] op_sel_hi:[1,0]\n\t"
"v_pk_fma_f32 v[110:111], v[206:207], v[22:23], v[110:111] op_sel_hi:[1,0,1]\n\t"
"v_cvt_pk_bf16_f32 v156, v108, v109\n\t"
"v_cvt_pk_bf16_f32 v157, v110, v111\n\t"
"v_pk_mul_f32 v[124:125], v[124:125], v[14:15] op_sel_hi:[1,0]\n\t"
"v_pk_fma_f32 v[124:125], v[220:221], v[22:23], v[124:125] op_sel_hi:[1,0,1]\n\t"
"v_pk_mul_f32 v[126:127], v[126:127], v[14:15] op_sel_hi:[1,0]\n\t"
"v_pk_fma_f32 v[126:127], v[222:223], v[22:23], v[126:127] op_sel_hi:[1,0,1]\n\t"
"v_cvt_pk_bf16_f32 v158, v124, v125\n\t"
"v_cvt_pk_bf16_f32 v159, v126, v127\n\t"
"buffer_store_dwordx4 v[156:159], v0, s[56:59], s76 offen offset:1024 sc1\n\t"
"s_branch LNEXT16_%=\n\t"
"LMRG115_%=:\n\t"
"s_nop 7\n\t"
"ds_write_b128 v226, v[24:27] offset:0\n\t"
"v_accvgpr_read_b32 v64, a64\n\t"
"v_accvgpr_read_b32 v65, a65\n\t"
"v_accvgpr_read_b32 v66, a66\n\t"
"v_accvgpr_read_b32 v67, a67\n\t"
"ds_write_b128 v226, v[28:31] offset:1024\n\t"
"v_accvgpr_read_b32 v68, a68\n\t"
"v_accvgpr_read_b32 v69, a69\n\t"
"v_accvgpr_read_b32 v70, a70\n\t"
"v_accvgpr_read_b32 v71, a71\n\t"
"ds_write_b128 v224, a[0:3] offset:0\n\t"
"v_accvgpr_read_b32 v72, a72\n\t"
"v_accvgpr_read_b32 v73, a73\n\t"
"v_accvgpr_read_b32 v74, a74\n\t"
"ds_write_b128 v224, a[4:7] offset:1024\n\t"
"v_accvgpr_read_b32 v75, a75\n\t"
"v_accvgpr_read_b32 v76, a76\n\t"
"v_accvgpr_read_b32 v77, a77\n\t"
"v_accvgpr_read_b32 v78, a78\n\t"
"ds_write_b128 v224, a[8:11] offset:2048\n\t"
"v_accvgpr_read_b32 v79, a79\n\t"
"v_accvgpr_read_b32 v80, a80\n\t"
"v_accvgpr_read_b32 v81, a81\n\t"
"ds_write_b128 v224, a[12:15] offset:3072\n\t"
"v_accvgpr_read_b32 v82, a82\n\t"
"v_accvgpr_read_b32 v83, a83\n\t"
"v_accvgpr_read_b32 v84, a84\n\t"
"v_accvgpr_read_b32 v85, a85\n\t"
"ds_write_b128 v224, a[16:19] offset:4096\n\t"
"v_accvgpr_read_b32 v86, a86\n\t"
"v_accvgpr_read_b32 v87, a87\n\t"
"v_accvgpr_read_b32 v88, a88\n\t"
"ds_write_b128 v224, a[20:23] offset:5120\n\t"
"v_accvgpr_read_b32 v89, a89\n\t"
"v_accvgpr_read_b32 v90, a90\n\t"
"v_accvgpr_read_b32 v91, a91\n\t"
"v_accvgpr_read_b32 v92, a92\n\t"
"ds_write_b128 v224, a[24:27] offset:6144\n\t"
"v_accvgpr_read_b32 v93, a93\n\t"
"v_accvgpr_read_b32 v94, a94\n\t"
"v_accvgpr_read_b32 v95, a95\n\t"
"ds_write_b128 v224, a[28:31] offset:7168\n\t"
"v_accvgpr_read_b32 v96, a96\n\t"
"v_accvgpr_read_b32 v97, a97\n\t"
"v_accvgpr_read_b32 v98, a98\n\t"
"v_accvgpr_read_b32 v99, a99\n\t"
"ds_write_b128 v224, a[32:35] offset:8192\n\t"
"v_accvgpr_read_b32 v100, a100\n\t"
"v_accvgpr_read_b32 v101, a101\n\t"
"v_accvgpr_read_b32 v102, a102\n\t"
"v_accvgpr_read_b32 v103, a103\n\t"
"ds_write_b128 v224, a[36:39] offset:9216\n\t"
"v_accvgpr_read_b32 v104, a104\n\t"
"v_accvgpr_read_b32 v105, a105\n\t"
"v_accvgpr_read_b32 v106, a106\n\t"
"ds_write_b128 v224, a[40:43] offset:10240\n\t"
"v_accvgpr_read_b32 v107, a107\n\t"
"v_accvgpr_read_b32 v108, a108\n\t"
"v_accvgpr_read_b32 v109, a109\n\t"
"v_accvgpr_read_b32 v110, a110\n\t"
"ds_write_b128 v224, a[44:47] offset:11264\n\t"
"v_accvgpr_read_b32 v111, a111\n\t"
"v_accvgpr_read_b32 v112, a112\n\t"
"v_accvgpr_read_b32 v113, a113\n\t"
"ds_write_b128 v224, a[48:51] offset:12288\n\t"
"v_accvgpr_read_b32 v114, a114\n\t"
"v_accvgpr_read_b32 v115, a115\n\t"
"v_accvgpr_read_b32 v116, a116\n\t"
"v_accvgpr_read_b32 v117, a117\n\t"
"ds_write_b128 v224, a[52:55] offset:13312\n\t"
"s_waitcnt lgkmcnt(9)\n\t"
"v_accvgpr_read_b32 v118, a118\n\t"
"v_accvgpr_read_b32 v119, a119\n\t"
"v_accvgpr_read_b32 v120, a120\n\t"
"ds_write_b128 v224, a[56:59] offset:14336\n\t"
"v_accvgpr_read_b32 v121, a121\n\t"
"v_accvgpr_read_b32 v122, a122\n\t"
"v_accvgpr_read_b32 v123, a123\n\t"
"v_accvgpr_read_b32 v124, a124\n\t"
"ds_write_b128 v224, a[60:63] offset:15360\n\t"
"v_accvgpr_read_b32 v125, a125\n\t"
"v_accvgpr_read_b32 v126, a126\n\t"
"v_accvgpr_read_b32 v127, a127\n\t"
"s_waitcnt lgkmcnt(0)\n\t"
"s_barrier\n\t"
"ds_read_b128 v[228:231], v227 offset:0\n\t"
"ds_read_b128 v[232:235], v227 offset:1024\n\t"
"ds_read_b128 v[160:163], v225 offset:0\n\t"
"ds_read_b128 v[164:167], v225 offset:1024\n\t"
"ds_read_b128 v[168:171], v225 offset:2048\n\t"
"ds_read_b128 v[172:175], v225 offset:3072\n\t"
"ds_read_b128 v[176:179], v225 offset:4096\n\t"
"ds_read_b128 v[180:183], v225 offset:5120\n\t"
"ds_read_b128 v[184:187], v225 offset:6144\n\t"
"ds_read_b128 v[188:191], v225 offset:7168\n\t"
"ds_read_b128 v[192:195], v225 offset:8192\n\t"
"ds_read_b128 v[196:199], v225 offset:9216\n\t"
"ds_read_b128 v[200:203], v225 offset:10240\n\t"
"ds_read_b128 v[204:207], v225 offset:11264\n\t"
"ds_read_b128 v[208:211], v225 offset:12288\n\t"
"ds_read_b128 v[212:215], v225 offset:13312\n\t"
"s_waitcnt lgkmcnt(14)\n\t"
"ds_read_b128 v[216:219], v225 offset:14336\n\t"
"ds_read_b128 v[220:223], v225 offset:15360\n\t"
"s_waitcnt lgkmcnt(14)\n\t"
"v_max_f32 v236, v24, v228\n\t"
"v_sub_f32 v237, v24, v236\n\t"
"v_sub_f32 v238, v228, v236\n\t"
"v_max_f32 v240, v25, v229\n\t"
"v_sub_f32 v241, v25, v240\n\t"
"v_sub_f32 v242, v229, v240\n\t"
"v_max_f32 v244, v26, v230\n\t"
"v_sub_f32 v245, v26, v244\n\t"
"v_sub_f32 v246, v230, v244\n\t"
"v_max_f32 v248, v27, v231\n\t"
"v_sub_f32 v249, v27, v248\n\t"
"v_sub_f32 v250, v231, v248\n\t"
"v_exp_f32 v237, v237\n\t"
"v_exp_f32 v238, v238\n\t"
"v_exp_f32 v241, v241\n\t"
"v_exp_f32 v242, v242\n\t"
"v_exp_f32 v245, v245\n\t"
"v_exp_f32 v246, v246\n\t"
"v_exp_f32 v249, v249\n\t"
"v_exp_f32 v250, v250\n\t"
"v_mul_f32 v239, v28, v237\n\t"
"v_fmac_f32 v239, v232, v238\n\t"
"v_mul_f32 v243, v29, v241\n\t"
"v_fmac_f32 v243, v233, v242\n\t"
"v_mul_f32 v247, v30, v245\n\t"
"v_fmac_f32 v247, v234, v246\n\t"
"v_mul_f32 v251, v31, v249\n\t"
"v_fmac_f32 v251, v235, v250\n\t"
"v_rcp_f32 v239, v239\n\t"
"v_rcp_f32 v243, v243\n\t"
"v_rcp_f32 v247, v247\n\t"
"v_rcp_f32 v251, v251\n\t"
"v_mul_f32 v8, v237, v239\n\t"
"v_mul_f32 v16, v238, v239\n\t"
"v_mul_f32 v10, v241, v243\n\t"
"v_mul_f32 v18, v242, v243\n\t"
"v_mul_f32 v12, v245, v247\n\t"
"v_mul_f32 v20, v246, v247\n\t"
"v_mul_f32 v14, v249, v251\n\t"
"v_mul_f32 v22, v250, v251\n\t"
"s_add_u32 s72, s74, 131072\n\t"
"s_add_u32 s75, s74, 262144\n\t"
"s_add_u32 s76, s74, 393216\n\t"
"v_pk_mul_f32 v[64:65], v[64:65], v[8:9] op_sel_hi:[1,0]\n\t"
"v_pk_fma_f32 v[64:65], v[160:161], v[16:17], v[64:65] op_sel_hi:[1,0,1]\n\t"
"v_pk_mul_f32 v[66:67], v[66:67], v[8:9] op_sel_hi:[1,0]\n\t"
"v_pk_fma_f32 v[66:67], v[162:163], v[16:17], v[66:67] op_sel_hi:[1,0,1]\n\t"
"v_cvt_pk_bf16_f32 v128, v64, v65\n\t"
"v_cvt_pk_bf16_f32 v129, v66, v67\n\t"
"v_pk_mul_f32 v[80:81], v[80:81], v[8:9] op_sel_hi:[1,0]\n\t"
"s_waitcnt lgkmcnt(0)\n\t"
"v_pk_fma_f32 v[80:81], v[176:177], v[16:17], v[80:81] op_sel_hi:[1,0,1]\n\t"
"v_pk_mul_f32 v[82:83], v[82:83], v[8:9] op_sel_hi:[1,0]\n\t"
"v_pk_fma_f32 v[82:83], v[178:179], v[16:17], v[82:83] op_sel_hi:[1,0,1]\n\t"
"v_cvt_pk_bf16_f32 v130, v80, v81\n\t"
"v_cvt_pk_bf16_f32 v131, v82, v83\n\t"
"buffer_store_dwordx4 v[128:131], v0, s[56:59], s74 offen offset:0 sc1\n\t"
"v_pk_mul_f32 v[68:69], v[68:69], v[10:11] op_sel_hi:[1,0]\n\t"
"v_pk_fma_f32 v[68:69], v[164:165], v[18:19], v[68:69] op_sel_hi:[1,0,1]\n\t"
"v_pk_mul_f32 v[70:71], v[70:71], v[10:11] op_sel_hi:[1,0]\n\t"
"v_pk_fma_f32 v[70:71], v[166:167], v[18:19], v[70:71] op_sel_hi:[1,0,1]\n\t"
"v_cvt_pk_bf16_f32 v132, v68, v69\n\t"
"v_cvt_pk_bf16_f32 v133, v70, v71\n\t"
"v_pk_mul_f32 v[84:85], v[84:85], v[10:11] op_sel_hi:[1,0]\n\t"
"v_pk_fma_f32 v[84:85], v[180:181], v[18:19], v[84:85] op_sel_hi:[1,0,1]\n\t"
"v_pk_mul_f32 v[86:87], v[86:87], v[10:11] op_sel_hi:[1,0]\n\t"
"v_pk_fma_f32 v[86:87], v[182:183], v[18:19], v[86:87] op_sel_hi:[1,0,1]\n\t"
"v_cvt_pk_bf16_f32 v134, v84, v85\n\t"
"v_cvt_pk_bf16_f32 v135, v86, v87\n\t"
"buffer_store_dwordx4 v[132:135], v0, s[56:59], s72 offen offset:0 sc1\n\t"
"v_pk_mul_f32 v[72:73], v[72:73], v[12:13] op_sel_hi:[1,0]\n\t"
"v_pk_fma_f32 v[72:73], v[168:169], v[20:21], v[72:73] op_sel_hi:[1,0,1]\n\t"
"v_pk_mul_f32 v[74:75], v[74:75], v[12:13] op_sel_hi:[1,0]\n\t"
"v_pk_fma_f32 v[74:75], v[170:171], v[20:21], v[74:75] op_sel_hi:[1,0,1]\n\t"
"v_cvt_pk_bf16_f32 v136, v72, v73\n\t"
"v_cvt_pk_bf16_f32 v137, v74, v75\n\t"
"v_pk_mul_f32 v[88:89], v[88:89], v[12:13] op_sel_hi:[1,0]\n\t"
"v_pk_fma_f32 v[88:89], v[184:185], v[20:21], v[88:89] op_sel_hi:[1,0,1]\n\t"
"v_pk_mul_f32 v[90:91], v[90:91], v[12:13] op_sel_hi:[1,0]\n\t"
"v_pk_fma_f32 v[90:91], v[186:187], v[20:21], v[90:91] op_sel_hi:[1,0,1]\n\t"
"v_cvt_pk_bf16_f32 v138, v88, v89\n\t"
"v_cvt_pk_bf16_f32 v139, v90, v91\n\t"
"buffer_store_dwordx4 v[136:139], v0, s[56:59], s75 offen offset:0 sc1\n\t"
"v_pk_mul_f32 v[76:77], v[76:77], v[14:15] op_sel_hi:[1,0]\n\t"
"v_pk_fma_f32 v[76:77], v[172:173], v[22:23], v[76:77] op_sel_hi:[1,0,1]\n\t"
"v_pk_mul_f32 v[78:79], v[78:79], v[14:15] op_sel_hi:[1,0]\n\t"
"v_pk_fma_f32 v[78:79], v[174:175], v[22:23], v[78:79] op_sel_hi:[1,0,1]\n\t"
"v_cvt_pk_bf16_f32 v140, v76, v77\n\t"
"v_cvt_pk_bf16_f32 v141, v78, v79\n\t"
"v_pk_mul_f32 v[92:93], v[92:93], v[14:15] op_sel_hi:[1,0]\n\t"
"v_pk_fma_f32 v[92:93], v[188:189], v[22:23], v[92:93] op_sel_hi:[1,0,1]\n\t"
"v_pk_mul_f32 v[94:95], v[94:95], v[14:15] op_sel_hi:[1,0]\n\t"
"v_pk_fma_f32 v[94:95], v[190:191], v[22:23], v[94:95] op_sel_hi:[1,0,1]\n\t"
"v_cvt_pk_bf16_f32 v142, v92, v93\n\t"
"v_cvt_pk_bf16_f32 v143, v94, v95\n\t"
"buffer_store_dwordx4 v[140:143], v0, s[56:59], s76 offen offset:0 sc1\n\t"
"v_pk_mul_f32 v[96:97], v[96:97], v[8:9] op_sel_hi:[1,0]\n\t"
"v_pk_fma_f32 v[96:97], v[192:193], v[16:17], v[96:97] op_sel_hi:[1,0,1]\n\t"
"v_pk_mul_f32 v[98:99], v[98:99], v[8:9] op_sel_hi:[1,0]\n\t"
"v_pk_fma_f32 v[98:99], v[194:195], v[16:17], v[98:99] op_sel_hi:[1,0,1]\n\t"
"v_cvt_pk_bf16_f32 v144, v96, v97\n\t"
"v_cvt_pk_bf16_f32 v145, v98, v99\n\t"
"v_pk_mul_f32 v[112:113], v[112:113], v[8:9] op_sel_hi:[1,0]\n\t"
"v_pk_fma_f32 v[112:113], v[208:209], v[16:17], v[112:113] op_sel_hi:[1,0,1]\n\t"
"v_pk_mul_f32 v[114:115], v[114:115], v[8:9] op_sel_hi:[1,0]\n\t"
"v_pk_fma_f32 v[114:115], v[210:211], v[16:17], v[114:115] op_sel_hi:[1,0,1]\n\t"
"v_cvt_pk_bf16_f32 v146, v112, v113\n\t"
"v_cvt_pk_bf16_f32 v147, v114, v115\n\t"
"buffer_store_dwordx4 v[144:147], v0, s[56:59], s74 offen offset:1024 sc1\n\t"
"v_pk_mul_f32 v[100:101], v[100:101], v[10:11] op_sel_hi:[1,0]\n\t"
"v_pk_fma_f32 v[100:101], v[196:197], v[18:19], v[100:101] op_sel_hi:[1,0,1]\n\t"
"v_pk_mul_f32 v[102:103], v[102:103], v[10:11] op_sel_hi:[1,0]\n\t"
"v_pk_fma_f32 v[102:103], v[198:199], v[18:19], v[102:103] op_sel_hi:[1,0,1]\n\t"
"v_cvt_pk_bf16_f32 v148, v100, v101\n\t"
"v_cvt_pk_bf16_f32 v149, v102, v103\n\t"
"v_pk_mul_f32 v[116:117], v[116:117], v[10:11] op_sel_hi:[1,0]\n\t"
"v_pk_fma_f32 v[116:117], v[212:213], v[18:19], v[116:117] op_sel_hi:[1,0,1]\n\t"
"v_pk_mul_f32 v[118:119], v[118:119], v[10:11] op_sel_hi:[1,0]\n\t"
"v_pk_fma_f32 v[118:119], v[214:215], v[18:19], v[118:119] op_sel_hi:[1,0,1]\n\t"
"v_cvt_pk_bf16_f32 v150, v116, v117\n\t"
"v_cvt_pk_bf16_f32 v151, v118, v119\n\t"
"buffer_store_dwordx4 v[148:151], v0, s[56:59], s72 offen offset:1024 sc1\n\t"
"v_pk_mul_f32 v[104:105], v[104:105], v[12:13] op_sel_hi:[1,0]\n\t"
"v_pk_fma_f32 v[104:105], v[200:201], v[20:21], v[104:105] op_sel_hi:[1,0,1]\n\t"
"v_pk_mul_f32 v[106:107], v[106:107], v[12:13] op_sel_hi:[1,0]\n\t"
"v_pk_fma_f32 v[106:107], v[202:203], v[20:21], v[106:107] op_sel_hi:[1,0,1]\n\t"
"v_cvt_pk_bf16_f32 v152, v104, v105\n\t"
"v_cvt_pk_bf16_f32 v153, v106, v107\n\t"
"v_pk_mul_f32 v[120:121], v[120:121], v[12:13] op_sel_hi:[1,0]\n\t"
"v_pk_fma_f32 v[120:121], v[216:217], v[20:21], v[120:121] op_sel_hi:[1,0,1]\n\t"
"v_pk_mul_f32 v[122:123], v[122:123], v[12:13] op_sel_hi:[1,0]\n\t"
"v_pk_fma_f32 v[122:123], v[218:219], v[20:21], v[122:123] op_sel_hi:[1,0,1]\n\t"
"v_cvt_pk_bf16_f32 v154, v120, v121\n\t"
"v_cvt_pk_bf16_f32 v155, v122, v123\n\t"
"buffer_store_dwordx4 v[152:155], v0, s[56:59], s75 offen offset:1024 sc1\n\t"
"v_pk_mul_f32 v[108:109], v[108:109], v[14:15] op_sel_hi:[1,0]\n\t"
"v_pk_fma_f32 v[108:109], v[204:205], v[22:23], v[108:109] op_sel_hi:[1,0,1]\n\t"
"v_pk_mul_f32 v[110:111], v[110:111], v[14:15] op_sel_hi:[1,0]\n\t"
"v_pk_fma_f32 v[110:111], v[206:207], v[22:23], v[110:111] op_sel_hi:[1,0,1]\n\t"
"v_cvt_pk_bf16_f32 v156, v108, v109\n\t"
"v_cvt_pk_bf16_f32 v157, v110, v111\n\t"
"v_pk_mul_f32 v[124:125], v[124:125], v[14:15] op_sel_hi:[1,0]\n\t"
"v_pk_fma_f32 v[124:125], v[220:221], v[22:23], v[124:125] op_sel_hi:[1,0,1]\n\t"
"v_pk_mul_f32 v[126:127], v[126:127], v[14:15] op_sel_hi:[1,0]\n\t"
"v_pk_fma_f32 v[126:127], v[222:223], v[22:23], v[126:127] op_sel_hi:[1,0,1]\n\t"
"v_cvt_pk_bf16_f32 v158, v124, v125\n\t"
"v_cvt_pk_bf16_f32 v159, v126, v127\n\t"
"buffer_store_dwordx4 v[156:159], v0, s[56:59], s76 offen offset:1024 sc1\n\t"
"LNEXT16_%=:\n\t"
"s_cmp_lg_u32 s73, 0\n\t"
"s_cbranch_scc1 LAEND17_%=\n\t"
"s_mov_b32 s73, 1\n\t"
"s_mov_b32 s66, s69\n\t"
"s_lshl_b32 s74, s41, 3\n\t"
"s_lshl_b32 s72, s44, 2\n\t"
"s_add_u32 s74, s74, s72\n\t"
"s_lshl_b32 s74, s74, 7\n\t"
"s_lshl_b32 s72, s40, 2\n\t"
"s_add_u32 s74, s74, s72\n\t"
"s_lshl_b32 s72, s45, 1\n\t"
"s_add_u32 s74, s74, s72\n\t"
"s_lshl_b32 s74, s74, 10\n\t"
"s_waitcnt lgkmcnt(0)\n\t"
"s_branch LFIRST12_%=\n\t"
"LMH20_%=:\n\t"
"s_nop 7\n\t"
"v_subrev_f32 v224, 0x00000000, v3\n\t"
"v_min_f32 v64, v64, v224\n\t"
"v_subrev_f32 v225, 0x7149f2ca, v3\n\t"
"v_min_f32 v65, v65, v225\n\t"
"v_subrev_f32 v226, 0x71c9f2ca, v3\n\t"
"v_min_f32 v66, v66, v226\n\t"
"v_subrev_f32 v227, 0x72177617, v3\n\t"
"v_min_f32 v67, v67, v227\n\t"
"v_subrev_f32 v228, 0x7349f2ca, v3\n\t"
"v_min_f32 v68, v68, v228\n\t"
"v_subrev_f32 v229, 0x735691f6, v3\n\t"
"v_min_f32 v69, v69, v229\n\t"
"v_subrev_f32 v230, 0x73633123, v3\n\t"
"v_min_f32 v70, v70, v230\n\t"
"v_subrev_f32 v231, 0x736fd050, v3\n\t"
"v_min_f32 v71, v71, v231\n\t"
"v_subrev_f32 v224, 0x73c9f2ca, v3\n\t"
"v_min_f32 v72, v72, v224\n\t"
"v_subrev_f32 v225, 0x73d04260, v3\n\t"
"v_min_f32 v73, v73, v225\n\t"
"v_subrev_f32 v226, 0x73d691f6, v3\n\t"
"v_min_f32 v74, v74, v226\n\t"
"v_subrev_f32 v227, 0x73dce18d, v3\n\t"
"v_min_f32 v75, v75, v227\n\t"
"v_subrev_f32 v228, 0x74177617, v3\n\t"
"v_min_f32 v76, v76, v228\n\t"
"v_subrev_f32 v229, 0x741a9de3, v3\n\t"
"v_min_f32 v77, v77, v229\n\t"
"v_subrev_f32 v230, 0x741dc5ae, v3\n\t"
"v_min_f32 v78, v78, v230\n\t"
"v_subrev_f32 v231, 0x7420ed79, v3\n\t"
"v_min_f32 v79, v79, v231\n\t"
"v_subrev_f32 v224, 0xf349f2ca, v3\n\t"
"v_min_f32 v80, v80, v224\n\t"
"v_subrev_f32 v225, 0xf33d539d, v3\n\t"
"v_min_f32 v81, v81, v225\n\t"
"v_subrev_f32 v226, 0xf330b471, v3\n\t"
"v_min_f32 v82, v82, v226\n\t"
"v_subrev_f32 v227, 0xf3241544, v3\n\t"
"v_min_f32 v83, v83, v227\n\t"
"v_subrev_f32 v228, 0x00000000, v3\n\t"
"v_min_f32 v84, v84, v228\n\t"
"v_subrev_f32 v229, 0x7149f2ca, v3\n\t"
"v_min_f32 v85, v85, v229\n\t"
"v_subrev_f32 v230, 0x71c9f2ca, v3\n\t"
"v_min_f32 v86, v86, v230\n\t"
"v_subrev_f32 v231, 0x72177617, v3\n\t"
"v_min_f32 v87, v87, v231\n\t"
"v_subrev_f32 v224, 0x7349f2ca, v3\n\t"
"v_min_f32 v88, v88, v224\n\t"
"v_subrev_f32 v225, 0x735691f6, v3\n\t"
"v_min_f32 v89, v89, v225\n\t"
"v_subrev_f32 v226, 0x73633123, v3\n\t"
"v_min_f32 v90, v90, v226\n\t"
"v_subrev_f32 v227, 0x736fd050, v3\n\t"
"v_min_f32 v91, v91, v227\n\t"
"v_subrev_f32 v228, 0x73c9f2ca, v3\n\t"
"v_min_f32 v92, v92, v228\n\t"
"v_subrev_f32 v229, 0x73d04260, v3\n\t"
"v_min_f32 v93, v93, v229\n\t"
"v_subrev_f32 v230, 0x73d691f6, v3\n\t"
"v_min_f32 v94, v94, v230\n\t"
"v_subrev_f32 v231, 0x73dce18d, v3\n\t"
"v_min_f32 v95, v95, v231\n\t"
"s_branch LMR21_%=\n\t"
"LSP22_%=:\n\t"
"s_nop 15\n\t"
"v_max3_f32 v232, v64, v65, v66\n\t"
"v_max3_f32 v233, v67, v68, v69\n\t"
"v_max3_f32 v232, v232, v70, v71\n\t"
"v_max3_f32 v233, v233, v72, v73\n\t"
"v_max3_f32 v232, v232, v74, v75\n\t"
"v_max3_f32 v233, v233, v76, v77\n\t"
"v_max3_f32 v232, v232, v78, v79\n\t"
"v_max_f32 v232, v232, v233\n\t"
"v_mov_b32 v233, v232\n\t"
"s_nop 1\n\t"
"v_permlane32_swap_b32 v232, v233\n\t"
"v_max_f32 v232, v232, v233\n\t"
"v_mov_b32 v233, v232\n\t"
"s_nop 1\n\t"
"v_permlane16_swap_b32 v232, v233\n\t"
"v_max_f32 v232, v232, v233\n\t"
"v_max_f32 v232, 0, v232\n\t"
"v_add_f32 v24, v24, v232\n\t"
"v_exp_f32_e64 v234, -v232\n\t"
"v_sub_f32 v64, v64, v232\n\t"
"v_sub_f32 v65, v65, v232\n\t"
"v_sub_f32 v66, v66, v232\n\t"
"v_sub_f32 v67, v67, v232\n\t"
"v_sub_f32 v68, v68, v232\n\t"
"v_sub_f32 v69, v69, v232\n\t"
"v_sub_f32 v70, v70, v232\n\t"
"v_sub_f32 v71, v71, v232\n\t"
"v_sub_f32 v72, v72, v232\n\t"
"v_sub_f32 v73, v73, v232\n\t"
"v_sub_f32 v74, v74, v232\n\t"
"v_sub_f32 v75, v75, v232\n\t"
"v_sub_f32 v76, v76, v232\n\t"
"v_sub_f32 v77, v77, v232\n\t"
"v_sub_f32 v78, v78, v232\n\t"
"v_sub_f32 v79, v79, v232\n\t"
"v_mul_f32 v28, v28, v234\n\t"
"v_sub_f32 v32, 0, v24\n\t"
"v_sub_f32 v33, 0, v24\n\t"
"v_sub_f32 v34, 0, v24\n\t"
"v_sub_f32 v35, 0, v24\n\t"
"v_exp_f32 v236, v64\n\t"
"v_exp_f32 v237, v65\n\t"
"s_nop 0\n\t"
"v_cvt_pk_bf16_f32 v128, v236, v237\n\t"
"v_exp_f32 v238, v66\n\t"
"v_exp_f32 v239, v67\n\t"
"s_nop 0\n\t"
"v_cvt_pk_bf16_f32 v129, v238, v239\n\t"
"v_exp_f32 v240, v68\n\t"
"v_exp_f32 v241, v69\n\t"
"s_nop 0\n\t"
"v_cvt_pk_bf16_f32 v130, v240, v241\n\t"
"v_exp_f32 v242, v70\n\t"
"v_exp_f32 v243, v71\n\t"
"s_nop 0\n\t"
"v_cvt_pk_bf16_f32 v131, v242, v243\n\t"
"v_exp_f32 v236, v72\n\t"
"v_exp_f32 v237, v73\n\t"
"s_nop 0\n\t"
"v_cvt_pk_bf16_f32 v132, v236, v237\n\t"
"v_exp_f32 v238, v74\n\t"
"v_exp_f32 v239, v75\n\t"
"s_nop 0\n\t"
"v_cvt_pk_bf16_f32 v133, v238, v239\n\t"
"v_exp_f32 v240, v76\n\t"
"v_exp_f32 v241, v77\n\t"
"s_nop 0\n\t"
"v_cvt_pk_bf16_f32 v134, v240, v241\n\t"
"v_exp_f32 v242, v78\n\t"
"v_exp_f32 v243, v79\n\t"
"s_nop 0\n\t"
"v_cvt_pk_bf16_f32 v135, v242, v243\n\t"
"v_mfma_f32_16x16x32_bf16 v[8:11], v[4:7], v[128:131], 0\n\t"
"s_nop 0\n\t"
"v_mfma_f32_16x16x32_bf16 v[8:11], v[4:7], v[132:135], v[8:11]\n\t"
"v_accvgpr_read_b32 v244, a0\n\t"
"v_mul_f32 v244, v244, v234\n\t"
"v_accvgpr_write_b32 a0, v244\n\t"
"v_accvgpr_read_b32 v245, a1\n\t"
"v_mul_f32 v245, v245, v234\n\t"
"v_accvgpr_write_b32 a1, v245\n\t"
"v_accvgpr_read_b32 v246, a2\n\t"
"v_mul_f32 v246, v246, v234\n\t"
"v_accvgpr_write_b32 a2, v246\n\t"
"v_accvgpr_read_b32 v247, a3\n\t"
"v_mul_f32 v247, v247, v234\n\t"
"v_accvgpr_write_b32 a3, v247\n\t"
"v_accvgpr_read_b32 v248, a16\n\t"
"v_mul_f32 v248, v248, v234\n\t"
"v_accvgpr_write_b32 a16, v248\n\t"
"v_accvgpr_read_b32 v249, a17\n\t"
"v_mul_f32 v249, v249, v234\n\t"
"v_accvgpr_write_b32 a17, v249\n\t"
"v_accvgpr_read_b32 v250, a18\n\t"
"v_mul_f32 v250, v250, v234\n\t"
"v_accvgpr_write_b32 a18, v250\n\t"
"v_accvgpr_read_b32 v251, a19\n\t"
"v_mul_f32 v251, v251, v234\n\t"
"v_accvgpr_write_b32 a19, v251\n\t"
"v_accvgpr_read_b32 v244, a32\n\t"
"v_mul_f32 v244, v244, v234\n\t"
"v_accvgpr_write_b32 a32, v244\n\t"
"v_accvgpr_read_b32 v245, a33\n\t"
"v_mul_f32 v245, v245, v234\n\t"
"v_accvgpr_write_b32 a33, v245\n\t"
"v_accvgpr_read_b32 v246, a34\n\t"
"v_mul_f32 v246, v246, v234\n\t"
"v_accvgpr_write_b32 a34, v246\n\t"
"v_accvgpr_read_b32 v247, a35\n\t"
"v_mul_f32 v247, v247, v234\n\t"
"v_accvgpr_write_b32 a35, v247\n\t"
"v_accvgpr_read_b32 v248, a48\n\t"
"v_mul_f32 v248, v248, v234\n\t"
"v_accvgpr_write_b32 a48, v248\n\t"
"v_accvgpr_read_b32 v249, a49\n\t"
"v_mul_f32 v249, v249, v234\n\t"
"v_accvgpr_write_b32 a49, v249\n\t"
"v_accvgpr_read_b32 v250, a50\n\t"
"v_mul_f32 v250, v250, v234\n\t"
"v_accvgpr_write_b32 a50, v250\n\t"
"v_accvgpr_read_b32 v251, a51\n\t"
"v_mul_f32 v251, v251, v234\n\t"
"v_accvgpr_write_b32 a51, v251\n\t"
"v_accvgpr_read_b32 v244, a64\n\t"
"v_mul_f32 v244, v244, v234\n\t"
"v_accvgpr_write_b32 a64, v244\n\t"
"v_accvgpr_read_b32 v245, a65\n\t"
"v_mul_f32 v245, v245, v234\n\t"
"v_accvgpr_write_b32 a65, v245\n\t"
"v_accvgpr_read_b32 v246, a66\n\t"
"v_mul_f32 v246, v246, v234\n\t"
"v_accvgpr_write_b32 a66, v246\n\t"
"v_accvgpr_read_b32 v247, a67\n\t"
"v_mul_f32 v247, v247, v234\n\t"
"v_accvgpr_write_b32 a67, v247\n\t"
"v_accvgpr_read_b32 v248, a80\n\t"
"v_mul_f32 v248, v248, v234\n\t"
"v_accvgpr_write_b32 a80, v248\n\t"
"v_accvgpr_read_b32 v249, a81\n\t"
"v_mul_f32 v249, v249, v234\n\t"
"v_accvgpr_write_b32 a81, v249\n\t"
"v_accvgpr_read_b32 v250, a82\n\t"
"v_mul_f32 v250, v250, v234\n\t"
"v_accvgpr_write_b32 a82, v250\n\t"
"v_accvgpr_read_b32 v251, a83\n\t"
"v_mul_f32 v251, v251, v234\n\t"
"v_accvgpr_write_b32 a83, v251\n\t"
"v_accvgpr_read_b32 v244, a96\n\t"
"v_mul_f32 v244, v244, v234\n\t"
"v_accvgpr_write_b32 a96, v244\n\t"
"v_accvgpr_read_b32 v245, a97\n\t"
"v_mul_f32 v245, v245, v234\n\t"
"v_accvgpr_write_b32 a97, v245\n\t"
"v_accvgpr_read_b32 v246, a98\n\t"
"v_mul_f32 v246, v246, v234\n\t"
"v_accvgpr_write_b32 a98, v246\n\t"
"v_accvgpr_read_b32 v247, a99\n\t"
"v_mul_f32 v247, v247, v234\n\t"
"v_accvgpr_write_b32 a99, v247\n\t"
"v_accvgpr_read_b32 v248, a112\n\t"
"v_mul_f32 v248, v248, v234\n\t"
"v_accvgpr_write_b32 a112, v248\n\t"
"v_accvgpr_read_b32 v249, a113\n\t"
"v_mul_f32 v249, v249, v234\n\t"
"v_accvgpr_write_b32 a113, v249\n\t"
"v_accvgpr_read_b32 v250, a114\n\t"
"v_mul_f32 v250, v250, v234\n\t"
"v_accvgpr_write_b32 a114, v250\n\t"
"v_accvgpr_read_b32 v251, a115\n\t"
"v_mul_f32 v251, v251, v234\n\t"
"v_accvgpr_write_b32 a115, v251\n\t"
"v_max3_f32 v232, v80, v81, v82\n\t"
"v_max3_f32 v233, v83, v84, v85\n\t"
"v_max3_f32 v232, v232, v86, v87\n\t"
"v_max3_f32 v233, v233, v88, v89\n\t"
"v_max3_f32 v232, v232, v90, v91\n\t"
"v_max3_f32 v233, v233, v92, v93\n\t"
"v_max3_f32 v232, v232, v94, v95\n\t"
"v_max_f32 v232, v232, v233\n\t"
"v_mov_b32 v233, v232\n\t"
"s_nop 1\n\t"
"v_permlane32_swap_b32 v232, v233\n\t"
"v_max_f32 v232, v232, v233\n\t"
"v_mov_b32 v233, v232\n\t"
"s_nop 1\n\t"
"v_permlane16_swap_b32 v232, v233\n\t"
"v_max_f32 v232, v232, v233\n\t"
"v_max_f32 v232, 0, v232\n\t"
"v_add_f32 v25, v25, v232\n\t"
"v_exp_f32_e64 v234, -v232\n\t"
"v_sub_f32 v80, v80, v232\n\t"
"v_sub_f32 v81, v81, v232\n\t"
"v_sub_f32 v82, v82, v232\n\t"
"v_sub_f32 v83, v83, v232\n\t"
"v_sub_f32 v84, v84, v232\n\t"
"v_sub_f32 v85, v85, v232\n\t"
"v_sub_f32 v86, v86, v232\n\t"
"v_sub_f32 v87, v87, v232\n\t"
"v_sub_f32 v88, v88, v232\n\t"
"v_sub_f32 v89, v89, v232\n\t"
"v_sub_f32 v90, v90, v232\n\t"
"v_sub_f32 v91, v91, v232\n\t"
"v_sub_f32 v92, v92, v232\n\t"
"v_sub_f32 v93, v93, v232\n\t"
"v_sub_f32 v94, v94, v232\n\t"
"v_sub_f32 v95, v95, v232\n\t"
"v_mul_f32 v29, v29, v234\n\t"
"v_sub_f32 v36, 0, v25\n\t"
"v_sub_f32 v37, 0, v25\n\t"
"v_sub_f32 v38, 0, v25\n\t"
"v_sub_f32 v39, 0, v25\n\t"
"v_exp_f32 v236, v80\n\t"
"v_exp_f32 v237, v81\n\t"
"s_nop 0\n\t"
"v_cvt_pk_bf16_f32 v136, v236, v237\n\t"
"v_exp_f32 v238, v82\n\t"
"v_exp_f32 v239, v83\n\t"
"s_nop 0\n\t"
"v_cvt_pk_bf16_f32 v137, v238, v239\n\t"
"v_exp_f32 v240, v84\n\t"
"v_exp_f32 v241, v85\n\t"
"s_nop 0\n\t"
"v_cvt_pk_bf16_f32 v138, v240, v241\n\t"
"v_exp_f32 v242, v86\n\t"
"v_exp_f32 v243, v87\n\t"
"s_nop 0\n\t"
"v_cvt_pk_bf16_f32 v139, v242, v243\n\t"
"v_exp_f32 v236, v88\n\t"
"v_exp_f32 v237, v89\n\t"
"s_nop 0\n\t"
"v_cvt_pk_bf16_f32 v140, v236, v237\n\t"
"v_exp_f32 v238, v90\n\t"
"v_exp_f32 v239, v91\n\t"
"s_nop 0\n\t"
"v_cvt_pk_bf16_f32 v141, v238, v239\n\t"
"v_exp_f32 v240, v92\n\t"
"v_exp_f32 v241, v93\n\t"
"s_nop 0\n\t"
"v_cvt_pk_bf16_f32 v142, v240, v241\n\t"
"v_exp_f32 v242, v94\n\t"
"v_exp_f32 v243, v95\n\t"
"s_nop 0\n\t"
"v_cvt_pk_bf16_f32 v143, v242, v243\n\t"
"v_mfma_f32_16x16x32_bf16 v[12:15], v[4:7], v[136:139], 0\n\t"
"s_nop 0\n\t"
"v_mfma_f32_16x16x32_bf16 v[12:15], v[4:7], v[140:143], v[12:15]\n\t"
"v_accvgpr_read_b32 v244, a4\n\t"
"v_mul_f32 v244, v244, v234\n\t"
"v_accvgpr_write_b32 a4, v244\n\t"
"v_accvgpr_read_b32 v245, a5\n\t"
"v_mul_f32 v245, v245, v234\n\t"
"v_accvgpr_write_b32 a5, v245\n\t"
"v_accvgpr_read_b32 v246, a6\n\t"
"v_mul_f32 v246, v246, v234\n\t"
"v_accvgpr_write_b32 a6, v246\n\t"
"v_accvgpr_read_b32 v247, a7\n\t"
"v_mul_f32 v247, v247, v234\n\t"
"v_accvgpr_write_b32 a7, v247\n\t"
"v_accvgpr_read_b32 v248, a20\n\t"
"v_mul_f32 v248, v248, v234\n\t"
"v_accvgpr_write_b32 a20, v248\n\t"
"v_accvgpr_read_b32 v249, a21\n\t"
"v_mul_f32 v249, v249, v234\n\t"
"v_accvgpr_write_b32 a21, v249\n\t"
"v_accvgpr_read_b32 v250, a22\n\t"
"v_mul_f32 v250, v250, v234\n\t"
"v_accvgpr_write_b32 a22, v250\n\t"
"v_accvgpr_read_b32 v251, a23\n\t"
"v_mul_f32 v251, v251, v234\n\t"
"v_accvgpr_write_b32 a23, v251\n\t"
"v_accvgpr_read_b32 v244, a36\n\t"
"v_mul_f32 v244, v244, v234\n\t"
"v_accvgpr_write_b32 a36, v244\n\t"
"v_accvgpr_read_b32 v245, a37\n\t"
"v_mul_f32 v245, v245, v234\n\t"
"v_accvgpr_write_b32 a37, v245\n\t"
"v_accvgpr_read_b32 v246, a38\n\t"
"v_mul_f32 v246, v246, v234\n\t"
"v_accvgpr_write_b32 a38, v246\n\t"
"v_accvgpr_read_b32 v247, a39\n\t"
"v_mul_f32 v247, v247, v234\n\t"
"v_accvgpr_write_b32 a39, v247\n\t"
"v_accvgpr_read_b32 v248, a52\n\t"
"v_mul_f32 v248, v248, v234\n\t"
"v_accvgpr_write_b32 a52, v248\n\t"
"v_accvgpr_read_b32 v249, a53\n\t"
"v_mul_f32 v249, v249, v234\n\t"
"v_accvgpr_write_b32 a53, v249\n\t"
"v_accvgpr_read_b32 v250, a54\n\t"
"v_mul_f32 v250, v250, v234\n\t"
"v_accvgpr_write_b32 a54, v250\n\t"
"v_accvgpr_read_b32 v251, a55\n\t"
"v_mul_f32 v251, v251, v234\n\t"
"v_accvgpr_write_b32 a55, v251\n\t"
"v_accvgpr_read_b32 v244, a68\n\t"
"v_mul_f32 v244, v244, v234\n\t"
"v_accvgpr_write_b32 a68, v244\n\t"
"v_accvgpr_read_b32 v245, a69\n\t"
"v_mul_f32 v245, v245, v234\n\t"
"v_accvgpr_write_b32 a69, v245\n\t"
"v_accvgpr_read_b32 v246, a70\n\t"
"v_mul_f32 v246, v246, v234\n\t"
"v_accvgpr_write_b32 a70, v246\n\t"
"v_accvgpr_read_b32 v247, a71\n\t"
"v_mul_f32 v247, v247, v234\n\t"
"v_accvgpr_write_b32 a71, v247\n\t"
"v_accvgpr_read_b32 v248, a84\n\t"
"v_mul_f32 v248, v248, v234\n\t"
"v_accvgpr_write_b32 a84, v248\n\t"
"v_accvgpr_read_b32 v249, a85\n\t"
"v_mul_f32 v249, v249, v234\n\t"
"v_accvgpr_write_b32 a85, v249\n\t"
"v_accvgpr_read_b32 v250, a86\n\t"
"v_mul_f32 v250, v250, v234\n\t"
"v_accvgpr_write_b32 a86, v250\n\t"
"v_accvgpr_read_b32 v251, a87\n\t"
"v_mul_f32 v251, v251, v234\n\t"
"v_accvgpr_write_b32 a87, v251\n\t"
"v_accvgpr_read_b32 v244, a100\n\t"
"v_mul_f32 v244, v244, v234\n\t"
"v_accvgpr_write_b32 a100, v244\n\t"
"v_accvgpr_read_b32 v245, a101\n\t"
"v_mul_f32 v245, v245, v234\n\t"
"v_accvgpr_write_b32 a101, v245\n\t"
"v_accvgpr_read_b32 v246, a102\n\t"
"v_mul_f32 v246, v246, v234\n\t"
"v_accvgpr_write_b32 a102, v246\n\t"
"v_accvgpr_read_b32 v247, a103\n\t"
"v_mul_f32 v247, v247, v234\n\t"
"v_accvgpr_write_b32 a103, v247\n\t"
"v_accvgpr_read_b32 v248, a116\n\t"
"v_mul_f32 v248, v248, v234\n\t"
"v_accvgpr_write_b32 a116, v248\n\t"
"v_accvgpr_read_b32 v249, a117\n\t"
"v_mul_f32 v249, v249, v234\n\t"
"v_accvgpr_write_b32 a117, v249\n\t"
"v_accvgpr_read_b32 v250, a118\n\t"
"v_mul_f32 v250, v250, v234\n\t"
"v_accvgpr_write_b32 a118, v250\n\t"
"v_accvgpr_read_b32 v251, a119\n\t"
"v_mul_f32 v251, v251, v234\n\t"
"v_accvgpr_write_b32 a119, v251\n\t"
"s_nop 7\n\t"
"s_branch LSR23_%=\n\t"
"LMH24_%=:\n\t"
"s_nop 7\n\t"
"v_subrev_f32 v224, 0xf3c9f2ca, v3\n\t"
"v_min_f32 v96, v96, v224\n\t"
"v_subrev_f32 v225, 0xf3c3a333, v3\n\t"
"v_min_f32 v97, v97, v225\n\t"
"v_subrev_f32 v226, 0xf3bd539d, v3\n\t"
"v_min_f32 v98, v98, v226\n\t"
"v_subrev_f32 v227, 0xf3b70407, v3\n\t"
"v_min_f32 v99, v99, v227\n\t"
"v_subrev_f32 v228, 0xf349f2ca, v3\n\t"
"v_min_f32 v100, v100, v228\n\t"
"v_subrev_f32 v229, 0xf33d539d, v3\n\t"
"v_min_f32 v101, v101, v229\n\t"
"v_subrev_f32 v230, 0xf330b471, v3\n\t"
"v_min_f32 v102, v102, v230\n\t"
"v_subrev_f32 v231, 0xf3241544, v3\n\t"
"v_min_f32 v103, v103, v231\n\t"
"v_subrev_f32 v224, 0x00000000, v3\n\t"
"v_min_f32 v104, v104, v224\n\t"
"v_subrev_f32 v225, 0x7149f2ca, v3\n\t"
"v_min_f32 v105, v105, v225\n\t"
"v_subrev_f32 v226, 0x71c9f2ca, v3\n\t"
"v_min_f32 v106, v106, v226\n\t"
"v_subrev_f32 v227, 0x72177617, v3\n\t"
"v_min_f32 v107, v107, v227\n\t"
"v_subrev_f32 v228, 0x7349f2ca, v3\n\t"
"v_min_f32 v108, v108, v228\n\t"
"v_subrev_f32 v229, 0x735691f6, v3\n\t"
"v_min_f32 v109, v109, v229\n\t"
"v_subrev_f32 v230, 0x73633123, v3\n\t"
"v_min_f32 v110, v110, v230\n\t"
"v_subrev_f32 v231, 0x736fd050, v3\n\t"
"v_min_f32 v111, v111, v231\n\t"
"v_subrev_f32 v224, 0xf4177617, v3\n\t"
"v_min_f32 v112, v112, v224\n\t"
"v_subrev_f32 v225, 0xf4144e4c, v3\n\t"
"v_min_f32 v113, v113, v225\n\t"
"v_subrev_f32 v226, 0xf4112681, v3\n\t"
"v_min_f32 v114, v114, v226\n\t"
"v_subrev_f32 v227, 0xf40dfeb6, v3\n\t"
"v_min_f32 v115, v115, v227\n\t"
"v_subrev_f32 v228, 0xf3c9f2ca, v3\n\t"
"v_min_f32 v116, v116, v228\n\t"
"v_subrev_f32 v229, 0xf3c3a333, v3\n\t"
"v_min_f32 v117, v117, v229\n\t"
"v_subrev_f32 v230, 0xf3bd539d, v3\n\t"
"v_min_f32 v118, v118, v230\n\t"
"v_subrev_f32 v231, 0xf3b70407, v3\n\t"
"v_min_f32 v119, v119, v231\n\t"
"v_subrev_f32 v224, 0xf349f2ca, v3\n\t"
"v_min_f32 v120, v120, v224\n\t"
"v_subrev_f32 v225, 0xf33d539d, v3\n\t"
"v_min_f32 v121, v121, v225\n\t"
"v_subrev_f32 v226, 0xf330b471, v3\n\t"
"v_min_f32 v122, v122, v226\n\t"
"v_subrev_f32 v227, 0xf3241544, v3\n\t"
"v_min_f32 v123, v123, v227\n\t"
"v_subrev_f32 v228, 0x00000000, v3\n\t"
"v_min_f32 v124, v124, v228\n\t"
"v_subrev_f32 v229, 0x7149f2ca, v3\n\t"
"v_min_f32 v125, v125, v229\n\t"
"v_subrev_f32 v230, 0x71c9f2ca, v3\n\t"
"v_min_f32 v126, v126, v230\n\t"
"v_subrev_f32 v231, 0x72177617, v3\n\t"
"v_min_f32 v127, v127, v231\n\t"
"s_branch LMR25_%=\n\t"
"LSP26_%=:\n\t"
"s_nop 15\n\t"
"v_max3_f32 v232, v96, v97, v98\n\t"
"v_max3_f32 v233, v99, v100, v101\n\t"
"v_max3_f32 v232, v232, v102, v103\n\t"
"v_max3_f32 v233, v233, v104, v105\n\t"
"v_max3_f32 v232, v232, v106, v107\n\t"
"v_max3_f32 v233, v233, v108, v109\n\t"
"v_max3_f32 v232, v232, v110, v111\n\t"
"v_max_f32 v232, v232, v233\n\t"
"v_mov_b32 v233, v232\n\t"
"s_nop 1\n\t"
"v_permlane32_swap_b32 v232, v233\n\t"
"v_max_f32 v232, v232, v233\n\t"
"v_mov_b32 v233, v232\n\t"
"s_nop 1\n\t"
"v_permlane16_swap_b32 v232, v233\n\t"
"v_max_f32 v232, v232, v233\n\t"
"v_max_f32 v232, 0, v232\n\t"
"v_add_f32 v26, v26, v232\n\t"
"v_exp_f32_e64 v234, -v232\n\t"
"v_sub_f32 v96, v96, v232\n\t"
"v_sub_f32 v97, v97, v232\n\t"
"v_sub_f32 v98, v98, v232\n\t"
"v_sub_f32 v99, v99, v232\n\t"
"v_sub_f32 v100, v100, v232\n\t"
"v_sub_f32 v101, v101, v232\n\t"
"v_sub_f32 v102, v102, v232\n\t"
"v_sub_f32 v103, v103, v232\n\t"
"v_sub_f32 v104, v104, v232\n\t"
"v_sub_f32 v105, v105, v232\n\t"
"v_sub_f32 v106, v106, v232\n\t"
"v_sub_f32 v107, v107, v232\n\t"
"v_sub_f32 v108, v108, v232\n\t"
"v_sub_f32 v109, v109, v232\n\t"
"v_sub_f32 v110, v110, v232\n\t"
"v_sub_f32 v111, v111, v232\n\t"
"v_mul_f32 v30, v30, v234\n\t"
"v_sub_f32 v40, 0, v26\n\t"
"v_sub_f32 v41, 0, v26\n\t"
"v_sub_f32 v42, 0, v26\n\t"
"v_sub_f32 v43, 0, v26\n\t"
"v_exp_f32 v236, v96\n\t"
"v_exp_f32 v237, v97\n\t"
"s_nop 0\n\t"
"v_cvt_pk_bf16_f32 v144, v236, v237\n\t"
"v_exp_f32 v238, v98\n\t"
"v_exp_f32 v239, v99\n\t"
"s_nop 0\n\t"
"v_cvt_pk_bf16_f32 v145, v238, v239\n\t"
"v_exp_f32 v240, v100\n\t"
"v_exp_f32 v241, v101\n\t"
"s_nop 0\n\t"
"v_cvt_pk_bf16_f32 v146, v240, v241\n\t"
"v_exp_f32 v242, v102\n\t"
"v_exp_f32 v243, v103\n\t"
"s_nop 0\n\t"
"v_cvt_pk_bf16_f32 v147, v242, v243\n\t"
"v_exp_f32 v236, v104\n\t"
"v_exp_f32 v237, v105\n\t"
"s_nop 0\n\t"
"v_cvt_pk_bf16_f32 v148, v236, v237\n\t"
"v_exp_f32 v238, v106\n\t"
"v_exp_f32 v239, v107\n\t"
"s_nop 0\n\t"
"v_cvt_pk_bf16_f32 v149, v238, v239\n\t"
"v_exp_f32 v240, v108\n\t"
"v_exp_f32 v241, v109\n\t"
"s_nop 0\n\t"
"v_cvt_pk_bf16_f32 v150, v240, v241\n\t"
"v_exp_f32 v242, v110\n\t"
"v_exp_f32 v243, v111\n\t"
"s_nop 0\n\t"
"v_cvt_pk_bf16_f32 v151, v242, v243\n\t"
"v_mfma_f32_16x16x32_bf16 v[16:19], v[4:7], v[144:147], 0\n\t"
"s_nop 0\n\t"
"v_mfma_f32_16x16x32_bf16 v[16:19], v[4:7], v[148:151], v[16:19]\n\t"
"v_accvgpr_read_b32 v244, a8\n\t"
"v_mul_f32 v244, v244, v234\n\t"
"v_accvgpr_write_b32 a8, v244\n\t"
"v_accvgpr_read_b32 v245, a9\n\t"
"v_mul_f32 v245, v245, v234\n\t"
"v_accvgpr_write_b32 a9, v245\n\t"
"v_accvgpr_read_b32 v246, a10\n\t"
"v_mul_f32 v246, v246, v234\n\t"
"v_accvgpr_write_b32 a10, v246\n\t"
"v_accvgpr_read_b32 v247, a11\n\t"
"v_mul_f32 v247, v247, v234\n\t"
"v_accvgpr_write_b32 a11, v247\n\t"
"v_accvgpr_read_b32 v248, a24\n\t"
"v_mul_f32 v248, v248, v234\n\t"
"v_accvgpr_write_b32 a24, v248\n\t"
"v_accvgpr_read_b32 v249, a25\n\t"
"v_mul_f32 v249, v249, v234\n\t"
"v_accvgpr_write_b32 a25, v249\n\t"
"v_accvgpr_read_b32 v250, a26\n\t"
"v_mul_f32 v250, v250, v234\n\t"
"v_accvgpr_write_b32 a26, v250\n\t"
"v_accvgpr_read_b32 v251, a27\n\t"
"v_mul_f32 v251, v251, v234\n\t"
"v_accvgpr_write_b32 a27, v251\n\t"
"v_accvgpr_read_b32 v244, a40\n\t"
"v_mul_f32 v244, v244, v234\n\t"
"v_accvgpr_write_b32 a40, v244\n\t"
"v_accvgpr_read_b32 v245, a41\n\t"
"v_mul_f32 v245, v245, v234\n\t"
"v_accvgpr_write_b32 a41, v245\n\t"
"v_accvgpr_read_b32 v246, a42\n\t"
"v_mul_f32 v246, v246, v234\n\t"
"v_accvgpr_write_b32 a42, v246\n\t"
"v_accvgpr_read_b32 v247, a43\n\t"
"v_mul_f32 v247, v247, v234\n\t"
"v_accvgpr_write_b32 a43, v247\n\t"
"v_accvgpr_read_b32 v248, a56\n\t"
"v_mul_f32 v248, v248, v234\n\t"
"v_accvgpr_write_b32 a56, v248\n\t"
"v_accvgpr_read_b32 v249, a57\n\t"
"v_mul_f32 v249, v249, v234\n\t"
"v_accvgpr_write_b32 a57, v249\n\t"
"v_accvgpr_read_b32 v250, a58\n\t"
"v_mul_f32 v250, v250, v234\n\t"
"v_accvgpr_write_b32 a58, v250\n\t"
"v_accvgpr_read_b32 v251, a59\n\t"
"v_mul_f32 v251, v251, v234\n\t"
"v_accvgpr_write_b32 a59, v251\n\t"
"v_accvgpr_read_b32 v244, a72\n\t"
"v_mul_f32 v244, v244, v234\n\t"
"v_accvgpr_write_b32 a72, v244\n\t"
"v_accvgpr_read_b32 v245, a73\n\t"
"v_mul_f32 v245, v245, v234\n\t"
"v_accvgpr_write_b32 a73, v245\n\t"
"v_accvgpr_read_b32 v246, a74\n\t"
"v_mul_f32 v246, v246, v234\n\t"
"v_accvgpr_write_b32 a74, v246\n\t"
"v_accvgpr_read_b32 v247, a75\n\t"
"v_mul_f32 v247, v247, v234\n\t"
"v_accvgpr_write_b32 a75, v247\n\t"
"v_accvgpr_read_b32 v248, a88\n\t"
"v_mul_f32 v248, v248, v234\n\t"
"v_accvgpr_write_b32 a88, v248\n\t"
"v_accvgpr_read_b32 v249, a89\n\t"
"v_mul_f32 v249, v249, v234\n\t"
"v_accvgpr_write_b32 a89, v249\n\t"
"v_accvgpr_read_b32 v250, a90\n\t"
"v_mul_f32 v250, v250, v234\n\t"
"v_accvgpr_write_b32 a90, v250\n\t"
"v_accvgpr_read_b32 v251, a91\n\t"
"v_mul_f32 v251, v251, v234\n\t"
"v_accvgpr_write_b32 a91, v251\n\t"
"v_accvgpr_read_b32 v244, a104\n\t"
"v_mul_f32 v244, v244, v234\n\t"
"v_accvgpr_write_b32 a104, v244\n\t"
"v_accvgpr_read_b32 v245, a105\n\t"
"v_mul_f32 v245, v245, v234\n\t"
"v_accvgpr_write_b32 a105, v245\n\t"
"v_accvgpr_read_b32 v246, a106\n\t"
"v_mul_f32 v246, v246, v234\n\t"
"v_accvgpr_write_b32 a106, v246\n\t"
"v_accvgpr_read_b32 v247, a107\n\t"
"v_mul_f32 v247, v247, v234\n\t"
"v_accvgpr_write_b32 a107, v247\n\t"
"v_accvgpr_read_b32 v248, a120\n\t"
"v_mul_f32 v248, v248, v234\n\t"
"v_accvgpr_write_b32 a120, v248\n\t"
"v_accvgpr_read_b32 v249, a121\n\t"
"v_mul_f32 v249, v249, v234\n\t"
"v_accvgpr_write_b32 a121, v249\n\t"
"v_accvgpr_read_b32 v250, a122\n\t"
"v_mul_f32 v250, v250, v234\n\t"
"v_accvgpr_write_b32 a122, v250\n\t"
"v_accvgpr_read_b32 v251, a123\n\t"
"v_mul_f32 v251, v251, v234\n\t"
"v_accvgpr_write_b32 a123, v251\n\t"
"v_max3_f32 v232, v112, v113, v114\n\t"
"v_max3_f32 v233, v115, v116, v117\n\t"
"v_max3_f32 v232, v232, v118, v119\n\t"
"v_max3_f32 v233, v233, v120, v121\n\t"
"v_max3_f32 v232, v232, v122, v123\n\t"
"v_max3_f32 v233, v233, v124, v125\n\t"
"v_max3_f32 v232, v232, v126, v127\n\t"
"v_max_f32 v232, v232, v233\n\t"
"v_mov_b32 v233, v232\n\t"
"s_nop 1\n\t"
"v_permlane32_swap_b32 v232, v233\n\t"
"v_max_f32 v232, v232, v233\n\t"
"v_mov_b32 v233, v232\n\t"
"s_nop 1\n\t"
"v_permlane16_swap_b32 v232, v233\n\t"
"v_max_f32 v232, v232, v233\n\t"
"v_max_f32 v232, 0, v232\n\t"
"v_add_f32 v27, v27, v232\n\t"
"v_exp_f32_e64 v234, -v232\n\t"
"v_sub_f32 v112, v112, v232\n\t"
"v_sub_f32 v113, v113, v232\n\t"
"v_sub_f32 v114, v114, v232\n\t"
"v_sub_f32 v115, v115, v232\n\t"
"v_sub_f32 v116, v116, v232\n\t"
"v_sub_f32 v117, v117, v232\n\t"
"v_sub_f32 v118, v118, v232\n\t"
"v_sub_f32 v119, v119, v232\n\t"
"v_sub_f32 v120, v120, v232\n\t"
"v_sub_f32 v121, v121, v232\n\t"
"v_sub_f32 v122, v122, v232\n\t"
"v_sub_f32 v123, v123, v232\n\t"
"v_sub_f32 v124, v124, v232\n\t"
"v_sub_f32 v125, v125, v232\n\t"
"v_sub_f32 v126, v126, v232\n\t"
"v_sub_f32 v127, v127, v232\n\t"
"v_mul_f32 v31, v31, v234\n\t"
"v_sub_f32 v44, 0, v27\n\t"
"v_sub_f32 v45, 0, v27\n\t"
"v_sub_f32 v46, 0, v27\n\t"
"v_sub_f32 v47, 0, v27\n\t"
"v_exp_f32 v236, v112\n\t"
"v_exp_f32 v237, v113\n\t"
"s_nop 0\n\t"
"v_cvt_pk_bf16_f32 v152, v236, v237\n\t"
"v_exp_f32 v238, v114\n\t"
"v_exp_f32 v239, v115\n\t"
"s_nop 0\n\t"
"v_cvt_pk_bf16_f32 v153, v238, v239\n\t"
"v_exp_f32 v240, v116\n\t"
"v_exp_f32 v241, v117\n\t"
"s_nop 0\n\t"
"v_cvt_pk_bf16_f32 v154, v240, v241\n\t"
"v_exp_f32 v242, v118\n\t"
"v_exp_f32 v243, v119\n\t"
"s_nop 0\n\t"
"v_cvt_pk_bf16_f32 v155, v242, v243\n\t"
"v_exp_f32 v236, v120\n\t"
"v_exp_f32 v237, v121\n\t"
"s_nop 0\n\t"
"v_cvt_pk_bf16_f32 v156, v236, v237\n\t"
"v_exp_f32 v238, v122\n\t"
"v_exp_f32 v239, v123\n\t"
"s_nop 0\n\t"
"v_cvt_pk_bf16_f32 v157, v238, v239\n\t"
"v_exp_f32 v240, v124\n\t"
"v_exp_f32 v241, v125\n\t"
"s_nop 0\n\t"
"v_cvt_pk_bf16_f32 v158, v240, v241\n\t"
"v_exp_f32 v242, v126\n\t"
"v_exp_f32 v243, v127\n\t"
"s_nop 0\n\t"
"v_cvt_pk_bf16_f32 v159, v242, v243\n\t"
"v_mfma_f32_16x16x32_bf16 v[20:23], v[4:7], v[152:155], 0\n\t"
"s_nop 0\n\t"
"v_mfma_f32_16x16x32_bf16 v[20:23], v[4:7], v[156:159], v[20:23]\n\t"
"v_accvgpr_read_b32 v244, a12\n\t"
"v_mul_f32 v244, v244, v234\n\t"
"v_accvgpr_write_b32 a12, v244\n\t"
"v_accvgpr_read_b32 v245, a13\n\t"
"v_mul_f32 v245, v245, v234\n\t"
"v_accvgpr_write_b32 a13, v245\n\t"
"v_accvgpr_read_b32 v246, a14\n\t"
"v_mul_f32 v246, v246, v234\n\t"
"v_accvgpr_write_b32 a14, v246\n\t"
"v_accvgpr_read_b32 v247, a15\n\t"
"v_mul_f32 v247, v247, v234\n\t"
"v_accvgpr_write_b32 a15, v247\n\t"
"v_accvgpr_read_b32 v248, a28\n\t"
"v_mul_f32 v248, v248, v234\n\t"
"v_accvgpr_write_b32 a28, v248\n\t"
"v_accvgpr_read_b32 v249, a29\n\t"
"v_mul_f32 v249, v249, v234\n\t"
"v_accvgpr_write_b32 a29, v249\n\t"
"v_accvgpr_read_b32 v250, a30\n\t"
"v_mul_f32 v250, v250, v234\n\t"
"v_accvgpr_write_b32 a30, v250\n\t"
"v_accvgpr_read_b32 v251, a31\n\t"
"v_mul_f32 v251, v251, v234\n\t"
"v_accvgpr_write_b32 a31, v251\n\t"
"v_accvgpr_read_b32 v244, a44\n\t"
"v_mul_f32 v244, v244, v234\n\t"
"v_accvgpr_write_b32 a44, v244\n\t"
"v_accvgpr_read_b32 v245, a45\n\t"
"v_mul_f32 v245, v245, v234\n\t"
"v_accvgpr_write_b32 a45, v245\n\t"
"v_accvgpr_read_b32 v246, a46\n\t"
"v_mul_f32 v246, v246, v234\n\t"
"v_accvgpr_write_b32 a46, v246\n\t"
"v_accvgpr_read_b32 v247, a47\n\t"
"v_mul_f32 v247, v247, v234\n\t"
"v_accvgpr_write_b32 a47, v247\n\t"
"v_accvgpr_read_b32 v248, a60\n\t"
"v_mul_f32 v248, v248, v234\n\t"
"v_accvgpr_write_b32 a60, v248\n\t"
"v_accvgpr_read_b32 v249, a61\n\t"
"v_mul_f32 v249, v249, v234\n\t"
"v_accvgpr_write_b32 a61, v249\n\t"
"v_accvgpr_read_b32 v250, a62\n\t"
"v_mul_f32 v250, v250, v234\n\t"
"v_accvgpr_write_b32 a62, v250\n\t"
"v_accvgpr_read_b32 v251, a63\n\t"
"v_mul_f32 v251, v251, v234\n\t"
"v_accvgpr_write_b32 a63, v251\n\t"
"v_accvgpr_read_b32 v244, a76\n\t"
"v_mul_f32 v244, v244, v234\n\t"
"v_accvgpr_write_b32 a76, v244\n\t"
"v_accvgpr_read_b32 v245, a77\n\t"
"v_mul_f32 v245, v245, v234\n\t"
"v_accvgpr_write_b32 a77, v245\n\t"
"v_accvgpr_read_b32 v246, a78\n\t"
"v_mul_f32 v246, v246, v234\n\t"
"v_accvgpr_write_b32 a78, v246\n\t"
"v_accvgpr_read_b32 v247, a79\n\t"
"v_mul_f32 v247, v247, v234\n\t"
"v_accvgpr_write_b32 a79, v247\n\t"
"v_accvgpr_read_b32 v248, a92\n\t"
"v_mul_f32 v248, v248, v234\n\t"
"v_accvgpr_write_b32 a92, v248\n\t"
"v_accvgpr_read_b32 v249, a93\n\t"
"v_mul_f32 v249, v249, v234\n\t"
"v_accvgpr_write_b32 a93, v249\n\t"
"v_accvgpr_read_b32 v250, a94\n\t"
"v_mul_f32 v250, v250, v234\n\t"
"v_accvgpr_write_b32 a94, v250\n\t"
"v_accvgpr_read_b32 v251, a95\n\t"
"v_mul_f32 v251, v251, v234\n\t"
"v_accvgpr_write_b32 a95, v251\n\t"
"v_accvgpr_read_b32 v244, a108\n\t"
"v_mul_f32 v244, v244, v234\n\t"
"v_accvgpr_write_b32 a108, v244\n\t"
"v_accvgpr_read_b32 v245, a109\n\t"
"v_mul_f32 v245, v245, v234\n\t"
"v_accvgpr_write_b32 a109, v245\n\t"
"v_accvgpr_read_b32 v246, a110\n\t"
"v_mul_f32 v246, v246, v234\n\t"
"v_accvgpr_write_b32 a110, v246\n\t"
"v_accvgpr_read_b32 v247, a111\n\t"
"v_mul_f32 v247, v247, v234\n\t"
"v_accvgpr_write_b32 a111, v247\n\t"
"v_accvgpr_read_b32 v248, a124\n\t"
"v_mul_f32 v248, v248, v234\n\t"
"v_accvgpr_write_b32 a124, v248\n\t"
"v_accvgpr_read_b32 v249, a125\n\t"
"v_mul_f32 v249, v249, v234\n\t"
"v_accvgpr_write_b32 a125, v249\n\t"
"v_accvgpr_read_b32 v250, a126\n\t"
"v_mul_f32 v250, v250, v234\n\t"
"v_accvgpr_write_b32 a126, v250\n\t"
"v_accvgpr_read_b32 v251, a127\n\t"
"v_mul_f32 v251, v251, v234\n\t"
"v_accvgpr_write_b32 a127, v251\n\t"
"s_nop 7\n\t"
"s_branch LSR27_%=\n\t"
"LSP28_%=:\n\t"
"s_nop 15\n\t"
"v_max3_f32 v232, v64, v65, v66\n\t"
"v_max3_f32 v233, v67, v68, v69\n\t"
"v_max3_f32 v232, v232, v70, v71\n\t"
"v_max3_f32 v233, v233, v72, v73\n\t"
"v_max3_f32 v232, v232, v74, v75\n\t"
"v_max3_f32 v233, v233, v76, v77\n\t"
"v_max3_f32 v232, v232, v78, v79\n\t"
"v_max_f32 v232, v232, v233\n\t"
"v_mov_b32 v233, v232\n\t"
"s_nop 1\n\t"
"v_permlane32_swap_b32 v232, v233\n\t"
"v_max_f32 v232, v232, v233\n\t"
"v_mov_b32 v233, v232\n\t"
"s_nop 1\n\t"
"v_permlane16_swap_b32 v232, v233\n\t"
"v_max_f32 v232, v232, v233\n\t"
"v_max_f32 v232, 0, v232\n\t"
"v_add_f32 v24, v24, v232\n\t"
"v_exp_f32_e64 v234, -v232\n\t"
"v_sub_f32 v64, v64, v232\n\t"
"v_sub_f32 v65, v65, v232\n\t"
"v_sub_f32 v66, v66, v232\n\t"
"v_sub_f32 v67, v67, v232\n\t"
"v_sub_f32 v68, v68, v232\n\t"
"v_sub_f32 v69, v69, v232\n\t"
"v_sub_f32 v70, v70, v232\n\t"
"v_sub_f32 v71, v71, v232\n\t"
"v_sub_f32 v72, v72, v232\n\t"
"v_sub_f32 v73, v73, v232\n\t"
"v_sub_f32 v74, v74, v232\n\t"
"v_sub_f32 v75, v75, v232\n\t"
"v_sub_f32 v76, v76, v232\n\t"
"v_sub_f32 v77, v77, v232\n\t"
"v_sub_f32 v78, v78, v232\n\t"
"v_sub_f32 v79, v79, v232\n\t"
"v_mul_f32 v28, v28, v234\n\t"
"v_sub_f32 v32, 0, v24\n\t"
"v_sub_f32 v33, 0, v24\n\t"
"v_sub_f32 v34, 0, v24\n\t"
"v_sub_f32 v35, 0, v24\n\t"
"v_exp_f32 v236, v64\n\t"
"v_exp_f32 v237, v65\n\t"
"s_nop 0\n\t"
"v_cvt_pk_bf16_f32 v128, v236, v237\n\t"
"v_exp_f32 v238, v66\n\t"
"v_exp_f32 v239, v67\n\t"
"s_nop 0\n\t"
"v_cvt_pk_bf16_f32 v129, v238, v239\n\t"
"v_exp_f32 v240, v68\n\t"
"v_exp_f32 v241, v69\n\t"
"s_nop 0\n\t"
"v_cvt_pk_bf16_f32 v130, v240, v241\n\t"
"v_exp_f32 v242, v70\n\t"
"v_exp_f32 v243, v71\n\t"
"s_nop 0\n\t"
"v_cvt_pk_bf16_f32 v131, v242, v243\n\t"
"v_exp_f32 v236, v72\n\t"
"v_exp_f32 v237, v73\n\t"
"s_nop 0\n\t"
"v_cvt_pk_bf16_f32 v132, v236, v237\n\t"
"v_exp_f32 v238, v74\n\t"
"v_exp_f32 v239, v75\n\t"
"s_nop 0\n\t"
"v_cvt_pk_bf16_f32 v133, v238, v239\n\t"
"v_exp_f32 v240, v76\n\t"
"v_exp_f32 v241, v77\n\t"
"s_nop 0\n\t"
"v_cvt_pk_bf16_f32 v134, v240, v241\n\t"
"v_exp_f32 v242, v78\n\t"
"v_exp_f32 v243, v79\n\t"
"s_nop 0\n\t"
"v_cvt_pk_bf16_f32 v135, v242, v243\n\t"
"v_mfma_f32_16x16x32_bf16 v[8:11], v[4:7], v[128:131], 0\n\t"
"s_nop 0\n\t"
"v_mfma_f32_16x16x32_bf16 v[8:11], v[4:7], v[132:135], v[8:11]\n\t"
"v_accvgpr_read_b32 v244, a0\n\t"
"v_mul_f32 v244, v244, v234\n\t"
"v_accvgpr_write_b32 a0, v244\n\t"
"v_accvgpr_read_b32 v245, a1\n\t"
"v_mul_f32 v245, v245, v234\n\t"
"v_accvgpr_write_b32 a1, v245\n\t"
"v_accvgpr_read_b32 v246, a2\n\t"
"v_mul_f32 v246, v246, v234\n\t"
"v_accvgpr_write_b32 a2, v246\n\t"
"v_accvgpr_read_b32 v247, a3\n\t"
"v_mul_f32 v247, v247, v234\n\t"
"v_accvgpr_write_b32 a3, v247\n\t"
"v_accvgpr_read_b32 v248, a16\n\t"
"v_mul_f32 v248, v248, v234\n\t"
"v_accvgpr_write_b32 a16, v248\n\t"
"v_accvgpr_read_b32 v249, a17\n\t"
"v_mul_f32 v249, v249, v234\n\t"
"v_accvgpr_write_b32 a17, v249\n\t"
"v_accvgpr_read_b32 v250, a18\n\t"
"v_mul_f32 v250, v250, v234\n\t"
"v_accvgpr_write_b32 a18, v250\n\t"
"v_accvgpr_read_b32 v251, a19\n\t"
"v_mul_f32 v251, v251, v234\n\t"
"v_accvgpr_write_b32 a19, v251\n\t"
"v_accvgpr_read_b32 v244, a32\n\t"
"v_mul_f32 v244, v244, v234\n\t"
"v_accvgpr_write_b32 a32, v244\n\t"
"v_accvgpr_read_b32 v245, a33\n\t"
"v_mul_f32 v245, v245, v234\n\t"
"v_accvgpr_write_b32 a33, v245\n\t"
"v_accvgpr_read_b32 v246, a34\n\t"
"v_mul_f32 v246, v246, v234\n\t"
"v_accvgpr_write_b32 a34, v246\n\t"
"v_accvgpr_read_b32 v247, a35\n\t"
"v_mul_f32 v247, v247, v234\n\t"
"v_accvgpr_write_b32 a35, v247\n\t"
"v_accvgpr_read_b32 v248, a48\n\t"
"v_mul_f32 v248, v248, v234\n\t"
"v_accvgpr_write_b32 a48, v248\n\t"
"v_accvgpr_read_b32 v249, a49\n\t"
"v_mul_f32 v249, v249, v234\n\t"
"v_accvgpr_write_b32 a49, v249\n\t"
"v_accvgpr_read_b32 v250, a50\n\t"
"v_mul_f32 v250, v250, v234\n\t"
"v_accvgpr_write_b32 a50, v250\n\t"
"v_accvgpr_read_b32 v251, a51\n\t"
"v_mul_f32 v251, v251, v234\n\t"
"v_accvgpr_write_b32 a51, v251\n\t"
"v_accvgpr_read_b32 v244, a64\n\t"
"v_mul_f32 v244, v244, v234\n\t"
"v_accvgpr_write_b32 a64, v244\n\t"
"v_accvgpr_read_b32 v245, a65\n\t"
"v_mul_f32 v245, v245, v234\n\t"
"v_accvgpr_write_b32 a65, v245\n\t"
"v_accvgpr_read_b32 v246, a66\n\t"
"v_mul_f32 v246, v246, v234\n\t"
"v_accvgpr_write_b32 a66, v246\n\t"
"v_accvgpr_read_b32 v247, a67\n\t"
"v_mul_f32 v247, v247, v234\n\t"
"v_accvgpr_write_b32 a67, v247\n\t"
"v_accvgpr_read_b32 v248, a80\n\t"
"v_mul_f32 v248, v248, v234\n\t"
"v_accvgpr_write_b32 a80, v248\n\t"
"v_accvgpr_read_b32 v249, a81\n\t"
"v_mul_f32 v249, v249, v234\n\t"
"v_accvgpr_write_b32 a81, v249\n\t"
"v_accvgpr_read_b32 v250, a82\n\t"
"v_mul_f32 v250, v250, v234\n\t"
"v_accvgpr_write_b32 a82, v250\n\t"
"v_accvgpr_read_b32 v251, a83\n\t"
"v_mul_f32 v251, v251, v234\n\t"
"v_accvgpr_write_b32 a83, v251\n\t"
"v_accvgpr_read_b32 v244, a96\n\t"
"v_mul_f32 v244, v244, v234\n\t"
"v_accvgpr_write_b32 a96, v244\n\t"
"v_accvgpr_read_b32 v245, a97\n\t"
"v_mul_f32 v245, v245, v234\n\t"
"v_accvgpr_write_b32 a97, v245\n\t"
"v_accvgpr_read_b32 v246, a98\n\t"
"v_mul_f32 v246, v246, v234\n\t"
"v_accvgpr_write_b32 a98, v246\n\t"
"v_accvgpr_read_b32 v247, a99\n\t"
"v_mul_f32 v247, v247, v234\n\t"
"v_accvgpr_write_b32 a99, v247\n\t"
"v_accvgpr_read_b32 v248, a112\n\t"
"v_mul_f32 v248, v248, v234\n\t"
"v_accvgpr_write_b32 a112, v248\n\t"
"v_accvgpr_read_b32 v249, a113\n\t"
"v_mul_f32 v249, v249, v234\n\t"
"v_accvgpr_write_b32 a113, v249\n\t"
"v_accvgpr_read_b32 v250, a114\n\t"
"v_mul_f32 v250, v250, v234\n\t"
"v_accvgpr_write_b32 a114, v250\n\t"
"v_accvgpr_read_b32 v251, a115\n\t"
"v_mul_f32 v251, v251, v234\n\t"
"v_accvgpr_write_b32 a115, v251\n\t"
"v_max3_f32 v232, v80, v81, v82\n\t"
"v_max3_f32 v233, v83, v84, v85\n\t"
"v_max3_f32 v232, v232, v86, v87\n\t"
"v_max3_f32 v233, v233, v88, v89\n\t"
"v_max3_f32 v232, v232, v90, v91\n\t"
"v_max3_f32 v233, v233, v92, v93\n\t"
"v_max3_f32 v232, v232, v94, v95\n\t"
"v_max_f32 v232, v232, v233\n\t"
"v_mov_b32 v233, v232\n\t"
"s_nop 1\n\t"
"v_permlane32_swap_b32 v232, v233\n\t"
"v_max_f32 v232, v232, v233\n\t"
"v_mov_b32 v233, v232\n\t"
"s_nop 1\n\t"
"v_permlane16_swap_b32 v232, v233\n\t"
"v_max_f32 v232, v232, v233\n\t"
"v_max_f32 v232, 0, v232\n\t"
"v_add_f32 v25, v25, v232\n\t"
"v_exp_f32_e64 v234, -v232\n\t"
"v_sub_f32 v80, v80, v232\n\t"
"v_sub_f32 v81, v81, v232\n\t"
"v_sub_f32 v82, v82, v232\n\t"
"v_sub_f32 v83, v83, v232\n\t"
"v_sub_f32 v84, v84, v232\n\t"
"v_sub_f32 v85, v85, v232\n\t"
"v_sub_f32 v86, v86, v232\n\t"
"v_sub_f32 v87, v87, v232\n\t"
"v_sub_f32 v88, v88, v232\n\t"
"v_sub_f32 v89, v89, v232\n\t"
"v_sub_f32 v90, v90, v232\n\t"
"v_sub_f32 v91, v91, v232\n\t"
"v_sub_f32 v92, v92, v232\n\t"
"v_sub_f32 v93, v93, v232\n\t"
"v_sub_f32 v94, v94, v232\n\t"
"v_sub_f32 v95, v95, v232\n\t"
"v_mul_f32 v29, v29, v234\n\t"
"v_sub_f32 v36, 0, v25\n\t"
"v_sub_f32 v37, 0, v25\n\t"
"v_sub_f32 v38, 0, v25\n\t"
"v_sub_f32 v39, 0, v25\n\t"
"v_exp_f32 v236, v80\n\t"
"v_exp_f32 v237, v81\n\t"
"s_nop 0\n\t"
"v_cvt_pk_bf16_f32 v136, v236, v237\n\t"
"v_exp_f32 v238, v82\n\t"
"v_exp_f32 v239, v83\n\t"
"s_nop 0\n\t"
"v_cvt_pk_bf16_f32 v137, v238, v239\n\t"
"v_exp_f32 v240, v84\n\t"
"v_exp_f32 v241, v85\n\t"
"s_nop 0\n\t"
"v_cvt_pk_bf16_f32 v138, v240, v241\n\t"
"v_exp_f32 v242, v86\n\t"
"v_exp_f32 v243, v87\n\t"
"s_nop 0\n\t"
"v_cvt_pk_bf16_f32 v139, v242, v243\n\t"
"v_exp_f32 v236, v88\n\t"
"v_exp_f32 v237, v89\n\t"
"s_nop 0\n\t"
"v_cvt_pk_bf16_f32 v140, v236, v237\n\t"
"v_exp_f32 v238, v90\n\t"
"v_exp_f32 v239, v91\n\t"
"s_nop 0\n\t"
"v_cvt_pk_bf16_f32 v141, v238, v239\n\t"
"v_exp_f32 v240, v92\n\t"
"v_exp_f32 v241, v93\n\t"
"s_nop 0\n\t"
"v_cvt_pk_bf16_f32 v142, v240, v241\n\t"
"v_exp_f32 v242, v94\n\t"
"v_exp_f32 v243, v95\n\t"
"s_nop 0\n\t"
"v_cvt_pk_bf16_f32 v143, v242, v243\n\t"
"v_mfma_f32_16x16x32_bf16 v[12:15], v[4:7], v[136:139], 0\n\t"
"s_nop 0\n\t"
"v_mfma_f32_16x16x32_bf16 v[12:15], v[4:7], v[140:143], v[12:15]\n\t"
"v_accvgpr_read_b32 v244, a4\n\t"
"v_mul_f32 v244, v244, v234\n\t"
"v_accvgpr_write_b32 a4, v244\n\t"
"v_accvgpr_read_b32 v245, a5\n\t"
"v_mul_f32 v245, v245, v234\n\t"
"v_accvgpr_write_b32 a5, v245\n\t"
"v_accvgpr_read_b32 v246, a6\n\t"
"v_mul_f32 v246, v246, v234\n\t"
"v_accvgpr_write_b32 a6, v246\n\t"
"v_accvgpr_read_b32 v247, a7\n\t"
"v_mul_f32 v247, v247, v234\n\t"
"v_accvgpr_write_b32 a7, v247\n\t"
"v_accvgpr_read_b32 v248, a20\n\t"
"v_mul_f32 v248, v248, v234\n\t"
"v_accvgpr_write_b32 a20, v248\n\t"
"v_accvgpr_read_b32 v249, a21\n\t"
"v_mul_f32 v249, v249, v234\n\t"
"v_accvgpr_write_b32 a21, v249\n\t"
"v_accvgpr_read_b32 v250, a22\n\t"
"v_mul_f32 v250, v250, v234\n\t"
"v_accvgpr_write_b32 a22, v250\n\t"
"v_accvgpr_read_b32 v251, a23\n\t"
"v_mul_f32 v251, v251, v234\n\t"
"v_accvgpr_write_b32 a23, v251\n\t"
"v_accvgpr_read_b32 v244, a36\n\t"
"v_mul_f32 v244, v244, v234\n\t"
"v_accvgpr_write_b32 a36, v244\n\t"
"v_accvgpr_read_b32 v245, a37\n\t"
"v_mul_f32 v245, v245, v234\n\t"
"v_accvgpr_write_b32 a37, v245\n\t"
"v_accvgpr_read_b32 v246, a38\n\t"
"v_mul_f32 v246, v246, v234\n\t"
"v_accvgpr_write_b32 a38, v246\n\t"
"v_accvgpr_read_b32 v247, a39\n\t"
"v_mul_f32 v247, v247, v234\n\t"
"v_accvgpr_write_b32 a39, v247\n\t"
"v_accvgpr_read_b32 v248, a52\n\t"
"v_mul_f32 v248, v248, v234\n\t"
"v_accvgpr_write_b32 a52, v248\n\t"
"v_accvgpr_read_b32 v249, a53\n\t"
"v_mul_f32 v249, v249, v234\n\t"
"v_accvgpr_write_b32 a53, v249\n\t"
"v_accvgpr_read_b32 v250, a54\n\t"
"v_mul_f32 v250, v250, v234\n\t"
"v_accvgpr_write_b32 a54, v250\n\t"
"v_accvgpr_read_b32 v251, a55\n\t"
"v_mul_f32 v251, v251, v234\n\t"
"v_accvgpr_write_b32 a55, v251\n\t"
"v_accvgpr_read_b32 v244, a68\n\t"
"v_mul_f32 v244, v244, v234\n\t"
"v_accvgpr_write_b32 a68, v244\n\t"
"v_accvgpr_read_b32 v245, a69\n\t"
"v_mul_f32 v245, v245, v234\n\t"
"v_accvgpr_write_b32 a69, v245\n\t"
"v_accvgpr_read_b32 v246, a70\n\t"
"v_mul_f32 v246, v246, v234\n\t"
"v_accvgpr_write_b32 a70, v246\n\t"
"v_accvgpr_read_b32 v247, a71\n\t"
"v_mul_f32 v247, v247, v234\n\t"
"v_accvgpr_write_b32 a71, v247\n\t"
"v_accvgpr_read_b32 v248, a84\n\t"
"v_mul_f32 v248, v248, v234\n\t"
"v_accvgpr_write_b32 a84, v248\n\t"
"v_accvgpr_read_b32 v249, a85\n\t"
"v_mul_f32 v249, v249, v234\n\t"
"v_accvgpr_write_b32 a85, v249\n\t"
"v_accvgpr_read_b32 v250, a86\n\t"
"v_mul_f32 v250, v250, v234\n\t"
"v_accvgpr_write_b32 a86, v250\n\t"
"v_accvgpr_read_b32 v251, a87\n\t"
"v_mul_f32 v251, v251, v234\n\t"
"v_accvgpr_write_b32 a87, v251\n\t"
"v_accvgpr_read_b32 v244, a100\n\t"
"v_mul_f32 v244, v244, v234\n\t"
"v_accvgpr_write_b32 a100, v244\n\t"
"v_accvgpr_read_b32 v245, a101\n\t"
"v_mul_f32 v245, v245, v234\n\t"
"v_accvgpr_write_b32 a101, v245\n\t"
"v_accvgpr_read_b32 v246, a102\n\t"
"v_mul_f32 v246, v246, v234\n\t"
"v_accvgpr_write_b32 a102, v246\n\t"
"v_accvgpr_read_b32 v247, a103\n\t"
"v_mul_f32 v247, v247, v234\n\t"
"v_accvgpr_write_b32 a103, v247\n\t"
"v_accvgpr_read_b32 v248, a116\n\t"
"v_mul_f32 v248, v248, v234\n\t"
"v_accvgpr_write_b32 a116, v248\n\t"
"v_accvgpr_read_b32 v249, a117\n\t"
"v_mul_f32 v249, v249, v234\n\t"
"v_accvgpr_write_b32 a117, v249\n\t"
"v_accvgpr_read_b32 v250, a118\n\t"
"v_mul_f32 v250, v250, v234\n\t"
"v_accvgpr_write_b32 a118, v250\n\t"
"v_accvgpr_read_b32 v251, a119\n\t"
"v_mul_f32 v251, v251, v234\n\t"
"v_accvgpr_write_b32 a119, v251\n\t"
"s_nop 7\n\t"
"s_branch LSR29_%=\n\t"
"LSP30_%=:\n\t"
"s_nop 15\n\t"
"v_max3_f32 v232, v96, v97, v98\n\t"
"v_max3_f32 v233, v99, v100, v101\n\t"
"v_max3_f32 v232, v232, v102, v103\n\t"
"v_max3_f32 v233, v233, v104, v105\n\t"
"v_max3_f32 v232, v232, v106, v107\n\t"
"v_max3_f32 v233, v233, v108, v109\n\t"
"v_max3_f32 v232, v232, v110, v111\n\t"
"v_max_f32 v232, v232, v233\n\t"
"v_mov_b32 v233, v232\n\t"
"s_nop 1\n\t"
"v_permlane32_swap_b32 v232, v233\n\t"
"v_max_f32 v232, v232, v233\n\t"
"v_mov_b32 v233, v232\n\t"
"s_nop 1\n\t"
"v_permlane16_swap_b32 v232, v233\n\t"
"v_max_f32 v232, v232, v233\n\t"
"v_max_f32 v232, 0, v232\n\t"
"v_add_f32 v26, v26, v232\n\t"
"v_exp_f32_e64 v234, -v232\n\t"
"v_sub_f32 v96, v96, v232\n\t"
"v_sub_f32 v97, v97, v232\n\t"
"v_sub_f32 v98, v98, v232\n\t"
"v_sub_f32 v99, v99, v232\n\t"
"v_sub_f32 v100, v100, v232\n\t"
"v_sub_f32 v101, v101, v232\n\t"
"v_sub_f32 v102, v102, v232\n\t"
"v_sub_f32 v103, v103, v232\n\t"
"v_sub_f32 v104, v104, v232\n\t"
"v_sub_f32 v105, v105, v232\n\t"
"v_sub_f32 v106, v106, v232\n\t"
"v_sub_f32 v107, v107, v232\n\t"
"v_sub_f32 v108, v108, v232\n\t"
"v_sub_f32 v109, v109, v232\n\t"
"v_sub_f32 v110, v110, v232\n\t"
"v_sub_f32 v111, v111, v232\n\t"
"v_mul_f32 v30, v30, v234\n\t"
"v_sub_f32 v40, 0, v26\n\t"
"v_sub_f32 v41, 0, v26\n\t"
"v_sub_f32 v42, 0, v26\n\t"
"v_sub_f32 v43, 0, v26\n\t"
"v_exp_f32 v236, v96\n\t"
"v_exp_f32 v237, v97\n\t"
"s_nop 0\n\t"
"v_cvt_pk_bf16_f32 v144, v236, v237\n\t"
"v_exp_f32 v238, v98\n\t"
"v_exp_f32 v239, v99\n\t"
"s_nop 0\n\t"
"v_cvt_pk_bf16_f32 v145, v238, v239\n\t"
"v_exp_f32 v240, v100\n\t"
"v_exp_f32 v241, v101\n\t"
"s_nop 0\n\t"
"v_cvt_pk_bf16_f32 v146, v240, v241\n\t"
"v_exp_f32 v242, v102\n\t"
"v_exp_f32 v243, v103\n\t"
"s_nop 0\n\t"
"v_cvt_pk_bf16_f32 v147, v242, v243\n\t"
"v_exp_f32 v236, v104\n\t"
"v_exp_f32 v237, v105\n\t"
"s_nop 0\n\t"
"v_cvt_pk_bf16_f32 v148, v236, v237\n\t"
"v_exp_f32 v238, v106\n\t"
"v_exp_f32 v239, v107\n\t"
"s_nop 0\n\t"
"v_cvt_pk_bf16_f32 v149, v238, v239\n\t"
"v_exp_f32 v240, v108\n\t"
"v_exp_f32 v241, v109\n\t"
"s_nop 0\n\t"
"v_cvt_pk_bf16_f32 v150, v240, v241\n\t"
"v_exp_f32 v242, v110\n\t"
"v_exp_f32 v243, v111\n\t"
"s_nop 0\n\t"
"v_cvt_pk_bf16_f32 v151, v242, v243\n\t"
"v_mfma_f32_16x16x32_bf16 v[16:19], v[4:7], v[144:147], 0\n\t"
"s_nop 0\n\t"
"v_mfma_f32_16x16x32_bf16 v[16:19], v[4:7], v[148:151], v[16:19]\n\t"
"v_accvgpr_read_b32 v244, a8\n\t"
"v_mul_f32 v244, v244, v234\n\t"
"v_accvgpr_write_b32 a8, v244\n\t"
"v_accvgpr_read_b32 v245, a9\n\t"
"v_mul_f32 v245, v245, v234\n\t"
"v_accvgpr_write_b32 a9, v245\n\t"
"v_accvgpr_read_b32 v246, a10\n\t"
"v_mul_f32 v246, v246, v234\n\t"
"v_accvgpr_write_b32 a10, v246\n\t"
"v_accvgpr_read_b32 v247, a11\n\t"
"v_mul_f32 v247, v247, v234\n\t"
"v_accvgpr_write_b32 a11, v247\n\t"
"v_accvgpr_read_b32 v248, a24\n\t"
"v_mul_f32 v248, v248, v234\n\t"
"v_accvgpr_write_b32 a24, v248\n\t"
"v_accvgpr_read_b32 v249, a25\n\t"
"v_mul_f32 v249, v249, v234\n\t"
"v_accvgpr_write_b32 a25, v249\n\t"
"v_accvgpr_read_b32 v250, a26\n\t"
"v_mul_f32 v250, v250, v234\n\t"
"v_accvgpr_write_b32 a26, v250\n\t"
"v_accvgpr_read_b32 v251, a27\n\t"
"v_mul_f32 v251, v251, v234\n\t"
"v_accvgpr_write_b32 a27, v251\n\t"
"v_accvgpr_read_b32 v244, a40\n\t"
"v_mul_f32 v244, v244, v234\n\t"
"v_accvgpr_write_b32 a40, v244\n\t"
"v_accvgpr_read_b32 v245, a41\n\t"
"v_mul_f32 v245, v245, v234\n\t"
"v_accvgpr_write_b32 a41, v245\n\t"
"v_accvgpr_read_b32 v246, a42\n\t"
"v_mul_f32 v246, v246, v234\n\t"
"v_accvgpr_write_b32 a42, v246\n\t"
"v_accvgpr_read_b32 v247, a43\n\t"
"v_mul_f32 v247, v247, v234\n\t"
"v_accvgpr_write_b32 a43, v247\n\t"
"v_accvgpr_read_b32 v248, a56\n\t"
"v_mul_f32 v248, v248, v234\n\t"
"v_accvgpr_write_b32 a56, v248\n\t"
"v_accvgpr_read_b32 v249, a57\n\t"
"v_mul_f32 v249, v249, v234\n\t"
"v_accvgpr_write_b32 a57, v249\n\t"
"v_accvgpr_read_b32 v250, a58\n\t"
"v_mul_f32 v250, v250, v234\n\t"
"v_accvgpr_write_b32 a58, v250\n\t"
"v_accvgpr_read_b32 v251, a59\n\t"
"v_mul_f32 v251, v251, v234\n\t"
"v_accvgpr_write_b32 a59, v251\n\t"
"v_accvgpr_read_b32 v244, a72\n\t"
"v_mul_f32 v244, v244, v234\n\t"
"v_accvgpr_write_b32 a72, v244\n\t"
"v_accvgpr_read_b32 v245, a73\n\t"
"v_mul_f32 v245, v245, v234\n\t"
"v_accvgpr_write_b32 a73, v245\n\t"
"v_accvgpr_read_b32 v246, a74\n\t"
"v_mul_f32 v246, v246, v234\n\t"
"v_accvgpr_write_b32 a74, v246\n\t"
"v_accvgpr_read_b32 v247, a75\n\t"
"v_mul_f32 v247, v247, v234\n\t"
"v_accvgpr_write_b32 a75, v247\n\t"
"v_accvgpr_read_b32 v248, a88\n\t"
"v_mul_f32 v248, v248, v234\n\t"
"v_accvgpr_write_b32 a88, v248\n\t"
"v_accvgpr_read_b32 v249, a89\n\t"
"v_mul_f32 v249, v249, v234\n\t"
"v_accvgpr_write_b32 a89, v249\n\t"
"v_accvgpr_read_b32 v250, a90\n\t"
"v_mul_f32 v250, v250, v234\n\t"
"v_accvgpr_write_b32 a90, v250\n\t"
"v_accvgpr_read_b32 v251, a91\n\t"
"v_mul_f32 v251, v251, v234\n\t"
"v_accvgpr_write_b32 a91, v251\n\t"
"v_accvgpr_read_b32 v244, a104\n\t"
"v_mul_f32 v244, v244, v234\n\t"
"v_accvgpr_write_b32 a104, v244\n\t"
"v_accvgpr_read_b32 v245, a105\n\t"
"v_mul_f32 v245, v245, v234\n\t"
"v_accvgpr_write_b32 a105, v245\n\t"
"v_accvgpr_read_b32 v246, a106\n\t"
"v_mul_f32 v246, v246, v234\n\t"
"v_accvgpr_write_b32 a106, v246\n\t"
"v_accvgpr_read_b32 v247, a107\n\t"
"v_mul_f32 v247, v247, v234\n\t"
"v_accvgpr_write_b32 a107, v247\n\t"
"v_accvgpr_read_b32 v248, a120\n\t"
"v_mul_f32 v248, v248, v234\n\t"
"v_accvgpr_write_b32 a120, v248\n\t"
"v_accvgpr_read_b32 v249, a121\n\t"
"v_mul_f32 v249, v249, v234\n\t"
"v_accvgpr_write_b32 a121, v249\n\t"
"v_accvgpr_read_b32 v250, a122\n\t"
"v_mul_f32 v250, v250, v234\n\t"
"v_accvgpr_write_b32 a122, v250\n\t"
"v_accvgpr_read_b32 v251, a123\n\t"
"v_mul_f32 v251, v251, v234\n\t"
"v_accvgpr_write_b32 a123, v251\n\t"
"v_max3_f32 v232, v112, v113, v114\n\t"
"v_max3_f32 v233, v115, v116, v117\n\t"
"v_max3_f32 v232, v232, v118, v119\n\t"
"v_max3_f32 v233, v233, v120, v121\n\t"
"v_max3_f32 v232, v232, v122, v123\n\t"
"v_max3_f32 v233, v233, v124, v125\n\t"
"v_max3_f32 v232, v232, v126, v127\n\t"
"v_max_f32 v232, v232, v233\n\t"
"v_mov_b32 v233, v232\n\t"
"s_nop 1\n\t"
"v_permlane32_swap_b32 v232, v233\n\t"
"v_max_f32 v232, v232, v233\n\t"
"v_mov_b32 v233, v232\n\t"
"s_nop 1\n\t"
"v_permlane16_swap_b32 v232, v233\n\t"
"v_max_f32 v232, v232, v233\n\t"
"v_max_f32 v232, 0, v232\n\t"
"v_add_f32 v27, v27, v232\n\t"
"v_exp_f32_e64 v234, -v232\n\t"
"v_sub_f32 v112, v112, v232\n\t"
"v_sub_f32 v113, v113, v232\n\t"
"v_sub_f32 v114, v114, v232\n\t"
"v_sub_f32 v115, v115, v232\n\t"
"v_sub_f32 v116, v116, v232\n\t"
"v_sub_f32 v117, v117, v232\n\t"
"v_sub_f32 v118, v118, v232\n\t"
"v_sub_f32 v119, v119, v232\n\t"
"v_sub_f32 v120, v120, v232\n\t"
"v_sub_f32 v121, v121, v232\n\t"
"v_sub_f32 v122, v122, v232\n\t"
"v_sub_f32 v123, v123, v232\n\t"
"v_sub_f32 v124, v124, v232\n\t"
"v_sub_f32 v125, v125, v232\n\t"
"v_sub_f32 v126, v126, v232\n\t"
"v_sub_f32 v127, v127, v232\n\t"
"v_mul_f32 v31, v31, v234\n\t"
"v_sub_f32 v44, 0, v27\n\t"
"v_sub_f32 v45, 0, v27\n\t"
"v_sub_f32 v46, 0, v27\n\t"
"v_sub_f32 v47, 0, v27\n\t"
"v_exp_f32 v236, v112\n\t"
"v_exp_f32 v237, v113\n\t"
"s_nop 0\n\t"
"v_cvt_pk_bf16_f32 v152, v236, v237\n\t"
"v_exp_f32 v238, v114\n\t"
"v_exp_f32 v239, v115\n\t"
"s_nop 0\n\t"
"v_cvt_pk_bf16_f32 v153, v238, v239\n\t"
"v_exp_f32 v240, v116\n\t"
"v_exp_f32 v241, v117\n\t"
"s_nop 0\n\t"
"v_cvt_pk_bf16_f32 v154, v240, v241\n\t"
"v_exp_f32 v242, v118\n\t"
"v_exp_f32 v243, v119\n\t"
"s_nop 0\n\t"
"v_cvt_pk_bf16_f32 v155, v242, v243\n\t"
"v_exp_f32 v236, v120\n\t"
"v_exp_f32 v237, v121\n\t"
"s_nop 0\n\t"
"v_cvt_pk_bf16_f32 v156, v236, v237\n\t"
"v_exp_f32 v238, v122\n\t"
"v_exp_f32 v239, v123\n\t"
"s_nop 0\n\t"
"v_cvt_pk_bf16_f32 v157, v238, v239\n\t"
"v_exp_f32 v240, v124\n\t"
"v_exp_f32 v241, v125\n\t"
"s_nop 0\n\t"
"v_cvt_pk_bf16_f32 v158, v240, v241\n\t"
"v_exp_f32 v242, v126\n\t"
"v_exp_f32 v243, v127\n\t"
"s_nop 0\n\t"
"v_cvt_pk_bf16_f32 v159, v242, v243\n\t"
"v_mfma_f32_16x16x32_bf16 v[20:23], v[4:7], v[152:155], 0\n\t"
"s_nop 0\n\t"
"v_mfma_f32_16x16x32_bf16 v[20:23], v[4:7], v[156:159], v[20:23]\n\t"
"v_accvgpr_read_b32 v244, a12\n\t"
"v_mul_f32 v244, v244, v234\n\t"
"v_accvgpr_write_b32 a12, v244\n\t"
"v_accvgpr_read_b32 v245, a13\n\t"
"v_mul_f32 v245, v245, v234\n\t"
"v_accvgpr_write_b32 a13, v245\n\t"
"v_accvgpr_read_b32 v246, a14\n\t"
"v_mul_f32 v246, v246, v234\n\t"
"v_accvgpr_write_b32 a14, v246\n\t"
"v_accvgpr_read_b32 v247, a15\n\t"
"v_mul_f32 v247, v247, v234\n\t"
"v_accvgpr_write_b32 a15, v247\n\t"
"v_accvgpr_read_b32 v248, a28\n\t"
"v_mul_f32 v248, v248, v234\n\t"
"v_accvgpr_write_b32 a28, v248\n\t"
"v_accvgpr_read_b32 v249, a29\n\t"
"v_mul_f32 v249, v249, v234\n\t"
"v_accvgpr_write_b32 a29, v249\n\t"
"v_accvgpr_read_b32 v250, a30\n\t"
"v_mul_f32 v250, v250, v234\n\t"
"v_accvgpr_write_b32 a30, v250\n\t"
"v_accvgpr_read_b32 v251, a31\n\t"
"v_mul_f32 v251, v251, v234\n\t"
"v_accvgpr_write_b32 a31, v251\n\t"
"v_accvgpr_read_b32 v244, a44\n\t"
"v_mul_f32 v244, v244, v234\n\t"
"v_accvgpr_write_b32 a44, v244\n\t"
"v_accvgpr_read_b32 v245, a45\n\t"
"v_mul_f32 v245, v245, v234\n\t"
"v_accvgpr_write_b32 a45, v245\n\t"
"v_accvgpr_read_b32 v246, a46\n\t"
"v_mul_f32 v246, v246, v234\n\t"
"v_accvgpr_write_b32 a46, v246\n\t"
"v_accvgpr_read_b32 v247, a47\n\t"
"v_mul_f32 v247, v247, v234\n\t"
"v_accvgpr_write_b32 a47, v247\n\t"
"v_accvgpr_read_b32 v248, a60\n\t"
"v_mul_f32 v248, v248, v234\n\t"
"v_accvgpr_write_b32 a60, v248\n\t"
"v_accvgpr_read_b32 v249, a61\n\t"
"v_mul_f32 v249, v249, v234\n\t"
"v_accvgpr_write_b32 a61, v249\n\t"
"v_accvgpr_read_b32 v250, a62\n\t"
"v_mul_f32 v250, v250, v234\n\t"
"v_accvgpr_write_b32 a62, v250\n\t"
"v_accvgpr_read_b32 v251, a63\n\t"
"v_mul_f32 v251, v251, v234\n\t"
"v_accvgpr_write_b32 a63, v251\n\t"
"v_accvgpr_read_b32 v244, a76\n\t"
"v_mul_f32 v244, v244, v234\n\t"
"v_accvgpr_write_b32 a76, v244\n\t"
"v_accvgpr_read_b32 v245, a77\n\t"
"v_mul_f32 v245, v245, v234\n\t"
"v_accvgpr_write_b32 a77, v245\n\t"
"v_accvgpr_read_b32 v246, a78\n\t"
"v_mul_f32 v246, v246, v234\n\t"
"v_accvgpr_write_b32 a78, v246\n\t"
"v_accvgpr_read_b32 v247, a79\n\t"
"v_mul_f32 v247, v247, v234\n\t"
"v_accvgpr_write_b32 a79, v247\n\t"
"v_accvgpr_read_b32 v248, a92\n\t"
"v_mul_f32 v248, v248, v234\n\t"
"v_accvgpr_write_b32 a92, v248\n\t"
"v_accvgpr_read_b32 v249, a93\n\t"
"v_mul_f32 v249, v249, v234\n\t"
"v_accvgpr_write_b32 a93, v249\n\t"
"v_accvgpr_read_b32 v250, a94\n\t"
"v_mul_f32 v250, v250, v234\n\t"
"v_accvgpr_write_b32 a94, v250\n\t"
"v_accvgpr_read_b32 v251, a95\n\t"
"v_mul_f32 v251, v251, v234\n\t"
"v_accvgpr_write_b32 a95, v251\n\t"
"v_accvgpr_read_b32 v244, a108\n\t"
"v_mul_f32 v244, v244, v234\n\t"
"v_accvgpr_write_b32 a108, v244\n\t"
"v_accvgpr_read_b32 v245, a109\n\t"
"v_mul_f32 v245, v245, v234\n\t"
"v_accvgpr_write_b32 a109, v245\n\t"
"v_accvgpr_read_b32 v246, a110\n\t"
"v_mul_f32 v246, v246, v234\n\t"
"v_accvgpr_write_b32 a110, v246\n\t"
"v_accvgpr_read_b32 v247, a111\n\t"
"v_mul_f32 v247, v247, v234\n\t"
"v_accvgpr_write_b32 a111, v247\n\t"
"v_accvgpr_read_b32 v248, a124\n\t"
"v_mul_f32 v248, v248, v234\n\t"
"v_accvgpr_write_b32 a124, v248\n\t"
"v_accvgpr_read_b32 v249, a125\n\t"
"v_mul_f32 v249, v249, v234\n\t"
"v_accvgpr_write_b32 a125, v249\n\t"
"v_accvgpr_read_b32 v250, a126\n\t"
"v_mul_f32 v250, v250, v234\n\t"
"v_accvgpr_write_b32 a126, v250\n\t"
"v_accvgpr_read_b32 v251, a127\n\t"
"v_mul_f32 v251, v251, v234\n\t"
"v_accvgpr_write_b32 a127, v251\n\t"
"s_nop 7\n\t"
"s_branch LSR31_%=\n\t"
"LSP32_%=:\n\t"
"s_nop 15\n\t"
"v_max3_f32 v232, v64, v65, v66\n\t"
"v_max3_f32 v233, v67, v68, v69\n\t"
"v_max3_f32 v232, v232, v70, v71\n\t"
"v_max3_f32 v233, v233, v72, v73\n\t"
"v_max3_f32 v232, v232, v74, v75\n\t"
"v_max3_f32 v233, v233, v76, v77\n\t"
"v_max3_f32 v232, v232, v78, v79\n\t"
"v_max_f32 v232, v232, v233\n\t"
"v_mov_b32 v233, v232\n\t"
"s_nop 1\n\t"
"v_permlane32_swap_b32 v232, v233\n\t"
"v_max_f32 v232, v232, v233\n\t"
"v_mov_b32 v233, v232\n\t"
"s_nop 1\n\t"
"v_permlane16_swap_b32 v232, v233\n\t"
"v_max_f32 v232, v232, v233\n\t"
"v_max_f32 v232, 0, v232\n\t"
"v_add_f32 v24, v24, v232\n\t"
"v_exp_f32_e64 v234, -v232\n\t"
"v_sub_f32 v64, v64, v232\n\t"
"v_sub_f32 v65, v65, v232\n\t"
"v_sub_f32 v66, v66, v232\n\t"
"v_sub_f32 v67, v67, v232\n\t"
"v_sub_f32 v68, v68, v232\n\t"
"v_sub_f32 v69, v69, v232\n\t"
"v_sub_f32 v70, v70, v232\n\t"
"v_sub_f32 v71, v71, v232\n\t"
"v_sub_f32 v72, v72, v232\n\t"
"v_sub_f32 v73, v73, v232\n\t"
"v_sub_f32 v74, v74, v232\n\t"
"v_sub_f32 v75, v75, v232\n\t"
"v_sub_f32 v76, v76, v232\n\t"
"v_sub_f32 v77, v77, v232\n\t"
"v_sub_f32 v78, v78, v232\n\t"
"v_sub_f32 v79, v79, v232\n\t"
"v_mul_f32 v28, v28, v234\n\t"
"v_sub_f32 v32, 0, v24\n\t"
"v_sub_f32 v33, 0, v24\n\t"
"v_sub_f32 v34, 0, v24\n\t"
"v_sub_f32 v35, 0, v24\n\t"
"v_exp_f32 v236, v64\n\t"
"v_exp_f32 v237, v65\n\t"
"s_nop 0\n\t"
"v_cvt_pk_bf16_f32 v128, v236, v237\n\t"
"v_exp_f32 v238, v66\n\t"
"v_exp_f32 v239, v67\n\t"
"s_nop 0\n\t"
"v_cvt_pk_bf16_f32 v129, v238, v239\n\t"
"v_exp_f32 v240, v68\n\t"
"v_exp_f32 v241, v69\n\t"
"s_nop 0\n\t"
"v_cvt_pk_bf16_f32 v130, v240, v241\n\t"
"v_exp_f32 v242, v70\n\t"
"v_exp_f32 v243, v71\n\t"
"s_nop 0\n\t"
"v_cvt_pk_bf16_f32 v131, v242, v243\n\t"
"v_exp_f32 v236, v72\n\t"
"v_exp_f32 v237, v73\n\t"
"s_nop 0\n\t"
"v_cvt_pk_bf16_f32 v132, v236, v237\n\t"
"v_exp_f32 v238, v74\n\t"
"v_exp_f32 v239, v75\n\t"
"s_nop 0\n\t"
"v_cvt_pk_bf16_f32 v133, v238, v239\n\t"
"v_exp_f32 v240, v76\n\t"
"v_exp_f32 v241, v77\n\t"
"s_nop 0\n\t"
"v_cvt_pk_bf16_f32 v134, v240, v241\n\t"
"v_exp_f32 v242, v78\n\t"
"v_exp_f32 v243, v79\n\t"
"s_nop 0\n\t"
"v_cvt_pk_bf16_f32 v135, v242, v243\n\t"
"v_mfma_f32_16x16x32_bf16 v[8:11], v[4:7], v[128:131], 0\n\t"
"s_nop 0\n\t"
"v_mfma_f32_16x16x32_bf16 v[8:11], v[4:7], v[132:135], v[8:11]\n\t"
"v_accvgpr_read_b32 v244, a0\n\t"
"v_mul_f32 v244, v244, v234\n\t"
"v_accvgpr_write_b32 a0, v244\n\t"
"v_accvgpr_read_b32 v245, a1\n\t"
"v_mul_f32 v245, v245, v234\n\t"
"v_accvgpr_write_b32 a1, v245\n\t"
"v_accvgpr_read_b32 v246, a2\n\t"
"v_mul_f32 v246, v246, v234\n\t"
"v_accvgpr_write_b32 a2, v246\n\t"
"v_accvgpr_read_b32 v247, a3\n\t"
"v_mul_f32 v247, v247, v234\n\t"
"v_accvgpr_write_b32 a3, v247\n\t"
"v_accvgpr_read_b32 v248, a16\n\t"
"v_mul_f32 v248, v248, v234\n\t"
"v_accvgpr_write_b32 a16, v248\n\t"
"v_accvgpr_read_b32 v249, a17\n\t"
"v_mul_f32 v249, v249, v234\n\t"
"v_accvgpr_write_b32 a17, v249\n\t"
"v_accvgpr_read_b32 v250, a18\n\t"
"v_mul_f32 v250, v250, v234\n\t"
"v_accvgpr_write_b32 a18, v250\n\t"
"v_accvgpr_read_b32 v251, a19\n\t"
"v_mul_f32 v251, v251, v234\n\t"
"v_accvgpr_write_b32 a19, v251\n\t"
"v_accvgpr_read_b32 v244, a32\n\t"
"v_mul_f32 v244, v244, v234\n\t"
"v_accvgpr_write_b32 a32, v244\n\t"
"v_accvgpr_read_b32 v245, a33\n\t"
"v_mul_f32 v245, v245, v234\n\t"
"v_accvgpr_write_b32 a33, v245\n\t"
"v_accvgpr_read_b32 v246, a34\n\t"
"v_mul_f32 v246, v246, v234\n\t"
"v_accvgpr_write_b32 a34, v246\n\t"
"v_accvgpr_read_b32 v247, a35\n\t"
"v_mul_f32 v247, v247, v234\n\t"
"v_accvgpr_write_b32 a35, v247\n\t"
"v_accvgpr_read_b32 v248, a48\n\t"
"v_mul_f32 v248, v248, v234\n\t"
"v_accvgpr_write_b32 a48, v248\n\t"
"v_accvgpr_read_b32 v249, a49\n\t"
"v_mul_f32 v249, v249, v234\n\t"
"v_accvgpr_write_b32 a49, v249\n\t"
"v_accvgpr_read_b32 v250, a50\n\t"
"v_mul_f32 v250, v250, v234\n\t"
"v_accvgpr_write_b32 a50, v250\n\t"
"v_accvgpr_read_b32 v251, a51\n\t"
"v_mul_f32 v251, v251, v234\n\t"
"v_accvgpr_write_b32 a51, v251\n\t"
"v_accvgpr_read_b32 v244, a64\n\t"
"v_mul_f32 v244, v244, v234\n\t"
"v_accvgpr_write_b32 a64, v244\n\t"
"v_accvgpr_read_b32 v245, a65\n\t"
"v_mul_f32 v245, v245, v234\n\t"
"v_accvgpr_write_b32 a65, v245\n\t"
"v_accvgpr_read_b32 v246, a66\n\t"
"v_mul_f32 v246, v246, v234\n\t"
"v_accvgpr_write_b32 a66, v246\n\t"
"v_accvgpr_read_b32 v247, a67\n\t"
"v_mul_f32 v247, v247, v234\n\t"
"v_accvgpr_write_b32 a67, v247\n\t"
"v_accvgpr_read_b32 v248, a80\n\t"
"v_mul_f32 v248, v248, v234\n\t"
"v_accvgpr_write_b32 a80, v248\n\t"
"v_accvgpr_read_b32 v249, a81\n\t"
"v_mul_f32 v249, v249, v234\n\t"
"v_accvgpr_write_b32 a81, v249\n\t"
"v_accvgpr_read_b32 v250, a82\n\t"
"v_mul_f32 v250, v250, v234\n\t"
"v_accvgpr_write_b32 a82, v250\n\t"
"v_accvgpr_read_b32 v251, a83\n\t"
"v_mul_f32 v251, v251, v234\n\t"
"v_accvgpr_write_b32 a83, v251\n\t"
"v_accvgpr_read_b32 v244, a96\n\t"
"v_mul_f32 v244, v244, v234\n\t"
"v_accvgpr_write_b32 a96, v244\n\t"
"v_accvgpr_read_b32 v245, a97\n\t"
"v_mul_f32 v245, v245, v234\n\t"
"v_accvgpr_write_b32 a97, v245\n\t"
"v_accvgpr_read_b32 v246, a98\n\t"
"v_mul_f32 v246, v246, v234\n\t"
"v_accvgpr_write_b32 a98, v246\n\t"
"v_accvgpr_read_b32 v247, a99\n\t"
"v_mul_f32 v247, v247, v234\n\t"
"v_accvgpr_write_b32 a99, v247\n\t"
"v_accvgpr_read_b32 v248, a112\n\t"
"v_mul_f32 v248, v248, v234\n\t"
"v_accvgpr_write_b32 a112, v248\n\t"
"v_accvgpr_read_b32 v249, a113\n\t"
"v_mul_f32 v249, v249, v234\n\t"
"v_accvgpr_write_b32 a113, v249\n\t"
"v_accvgpr_read_b32 v250, a114\n\t"
"v_mul_f32 v250, v250, v234\n\t"
"v_accvgpr_write_b32 a114, v250\n\t"
"v_accvgpr_read_b32 v251, a115\n\t"
"v_mul_f32 v251, v251, v234\n\t"
"v_accvgpr_write_b32 a115, v251\n\t"
"v_max3_f32 v232, v80, v81, v82\n\t"
"v_max3_f32 v233, v83, v84, v85\n\t"
"v_max3_f32 v232, v232, v86, v87\n\t"
"v_max3_f32 v233, v233, v88, v89\n\t"
"v_max3_f32 v232, v232, v90, v91\n\t"
"v_max3_f32 v233, v233, v92, v93\n\t"
"v_max3_f32 v232, v232, v94, v95\n\t"
"v_max_f32 v232, v232, v233\n\t"
"v_mov_b32 v233, v232\n\t"
"s_nop 1\n\t"
"v_permlane32_swap_b32 v232, v233\n\t"
"v_max_f32 v232, v232, v233\n\t"
"v_mov_b32 v233, v232\n\t"
"s_nop 1\n\t"
"v_permlane16_swap_b32 v232, v233\n\t"
"v_max_f32 v232, v232, v233\n\t"
"v_max_f32 v232, 0, v232\n\t"
"v_add_f32 v25, v25, v232\n\t"
"v_exp_f32_e64 v234, -v232\n\t"
"v_sub_f32 v80, v80, v232\n\t"
"v_sub_f32 v81, v81, v232\n\t"
"v_sub_f32 v82, v82, v232\n\t"
"v_sub_f32 v83, v83, v232\n\t"
"v_sub_f32 v84, v84, v232\n\t"
"v_sub_f32 v85, v85, v232\n\t"
"v_sub_f32 v86, v86, v232\n\t"
"v_sub_f32 v87, v87, v232\n\t"
"v_sub_f32 v88, v88, v232\n\t"
"v_sub_f32 v89, v89, v232\n\t"
"v_sub_f32 v90, v90, v232\n\t"
"v_sub_f32 v91, v91, v232\n\t"
"v_sub_f32 v92, v92, v232\n\t"
"v_sub_f32 v93, v93, v232\n\t"
"v_sub_f32 v94, v94, v232\n\t"
"v_sub_f32 v95, v95, v232\n\t"
"v_mul_f32 v29, v29, v234\n\t"
"v_sub_f32 v36, 0, v25\n\t"
"v_sub_f32 v37, 0, v25\n\t"
"v_sub_f32 v38, 0, v25\n\t"
"v_sub_f32 v39, 0, v25\n\t"
"v_exp_f32 v236, v80\n\t"
"v_exp_f32 v237, v81\n\t"
"s_nop 0\n\t"
"v_cvt_pk_bf16_f32 v136, v236, v237\n\t"
"v_exp_f32 v238, v82\n\t"
"v_exp_f32 v239, v83\n\t"
"s_nop 0\n\t"
"v_cvt_pk_bf16_f32 v137, v238, v239\n\t"
"v_exp_f32 v240, v84\n\t"
"v_exp_f32 v241, v85\n\t"
"s_nop 0\n\t"
"v_cvt_pk_bf16_f32 v138, v240, v241\n\t"
"v_exp_f32 v242, v86\n\t"
"v_exp_f32 v243, v87\n\t"
"s_nop 0\n\t"
"v_cvt_pk_bf16_f32 v139, v242, v243\n\t"
"v_exp_f32 v236, v88\n\t"
"v_exp_f32 v237, v89\n\t"
"s_nop 0\n\t"
"v_cvt_pk_bf16_f32 v140, v236, v237\n\t"
"v_exp_f32 v238, v90\n\t"
"v_exp_f32 v239, v91\n\t"
"s_nop 0\n\t"
"v_cvt_pk_bf16_f32 v141, v238, v239\n\t"
"v_exp_f32 v240, v92\n\t"
"v_exp_f32 v241, v93\n\t"
"s_nop 0\n\t"
"v_cvt_pk_bf16_f32 v142, v240, v241\n\t"
"v_exp_f32 v242, v94\n\t"
"v_exp_f32 v243, v95\n\t"
"s_nop 0\n\t"
"v_cvt_pk_bf16_f32 v143, v242, v243\n\t"
"v_mfma_f32_16x16x32_bf16 v[12:15], v[4:7], v[136:139], 0\n\t"
"s_nop 0\n\t"
"v_mfma_f32_16x16x32_bf16 v[12:15], v[4:7], v[140:143], v[12:15]\n\t"
"v_accvgpr_read_b32 v244, a4\n\t"
"v_mul_f32 v244, v244, v234\n\t"
"v_accvgpr_write_b32 a4, v244\n\t"
"v_accvgpr_read_b32 v245, a5\n\t"
"v_mul_f32 v245, v245, v234\n\t"
"v_accvgpr_write_b32 a5, v245\n\t"
"v_accvgpr_read_b32 v246, a6\n\t"
"v_mul_f32 v246, v246, v234\n\t"
"v_accvgpr_write_b32 a6, v246\n\t"
"v_accvgpr_read_b32 v247, a7\n\t"
"v_mul_f32 v247, v247, v234\n\t"
"v_accvgpr_write_b32 a7, v247\n\t"
"v_accvgpr_read_b32 v248, a20\n\t"
"v_mul_f32 v248, v248, v234\n\t"
"v_accvgpr_write_b32 a20, v248\n\t"
"v_accvgpr_read_b32 v249, a21\n\t"
"v_mul_f32 v249, v249, v234\n\t"
"v_accvgpr_write_b32 a21, v249\n\t"
"v_accvgpr_read_b32 v250, a22\n\t"
"v_mul_f32 v250, v250, v234\n\t"
"v_accvgpr_write_b32 a22, v250\n\t"
"v_accvgpr_read_b32 v251, a23\n\t"
"v_mul_f32 v251, v251, v234\n\t"
"v_accvgpr_write_b32 a23, v251\n\t"
"v_accvgpr_read_b32 v244, a36\n\t"
"v_mul_f32 v244, v244, v234\n\t"
"v_accvgpr_write_b32 a36, v244\n\t"
"v_accvgpr_read_b32 v245, a37\n\t"
"v_mul_f32 v245, v245, v234\n\t"
"v_accvgpr_write_b32 a37, v245\n\t"
"v_accvgpr_read_b32 v246, a38\n\t"
"v_mul_f32 v246, v246, v234\n\t"
"v_accvgpr_write_b32 a38, v246\n\t"
"v_accvgpr_read_b32 v247, a39\n\t"
"v_mul_f32 v247, v247, v234\n\t"
"v_accvgpr_write_b32 a39, v247\n\t"
"v_accvgpr_read_b32 v248, a52\n\t"
"v_mul_f32 v248, v248, v234\n\t"
"v_accvgpr_write_b32 a52, v248\n\t"
"v_accvgpr_read_b32 v249, a53\n\t"
"v_mul_f32 v249, v249, v234\n\t"
"v_accvgpr_write_b32 a53, v249\n\t"
"v_accvgpr_read_b32 v250, a54\n\t"
"v_mul_f32 v250, v250, v234\n\t"
"v_accvgpr_write_b32 a54, v250\n\t"
"v_accvgpr_read_b32 v251, a55\n\t"
"v_mul_f32 v251, v251, v234\n\t"
"v_accvgpr_write_b32 a55, v251\n\t"
"v_accvgpr_read_b32 v244, a68\n\t"
"v_mul_f32 v244, v244, v234\n\t"
"v_accvgpr_write_b32 a68, v244\n\t"
"v_accvgpr_read_b32 v245, a69\n\t"
"v_mul_f32 v245, v245, v234\n\t"
"v_accvgpr_write_b32 a69, v245\n\t"
"v_accvgpr_read_b32 v246, a70\n\t"
"v_mul_f32 v246, v246, v234\n\t"
"v_accvgpr_write_b32 a70, v246\n\t"
"v_accvgpr_read_b32 v247, a71\n\t"
"v_mul_f32 v247, v247, v234\n\t"
"v_accvgpr_write_b32 a71, v247\n\t"
"v_accvgpr_read_b32 v248, a84\n\t"
"v_mul_f32 v248, v248, v234\n\t"
"v_accvgpr_write_b32 a84, v248\n\t"
"v_accvgpr_read_b32 v249, a85\n\t"
"v_mul_f32 v249, v249, v234\n\t"
"v_accvgpr_write_b32 a85, v249\n\t"
"v_accvgpr_read_b32 v250, a86\n\t"
"v_mul_f32 v250, v250, v234\n\t"
"v_accvgpr_write_b32 a86, v250\n\t"
"v_accvgpr_read_b32 v251, a87\n\t"
"v_mul_f32 v251, v251, v234\n\t"
"v_accvgpr_write_b32 a87, v251\n\t"
"v_accvgpr_read_b32 v244, a100\n\t"
"v_mul_f32 v244, v244, v234\n\t"
"v_accvgpr_write_b32 a100, v244\n\t"
"v_accvgpr_read_b32 v245, a101\n\t"
"v_mul_f32 v245, v245, v234\n\t"
"v_accvgpr_write_b32 a101, v245\n\t"
"v_accvgpr_read_b32 v246, a102\n\t"
"v_mul_f32 v246, v246, v234\n\t"
"v_accvgpr_write_b32 a102, v246\n\t"
"v_accvgpr_read_b32 v247, a103\n\t"
"v_mul_f32 v247, v247, v234\n\t"
"v_accvgpr_write_b32 a103, v247\n\t"
"v_accvgpr_read_b32 v248, a116\n\t"
"v_mul_f32 v248, v248, v234\n\t"
"v_accvgpr_write_b32 a116, v248\n\t"
"v_accvgpr_read_b32 v249, a117\n\t"
"v_mul_f32 v249, v249, v234\n\t"
"v_accvgpr_write_b32 a117, v249\n\t"
"v_accvgpr_read_b32 v250, a118\n\t"
"v_mul_f32 v250, v250, v234\n\t"
"v_accvgpr_write_b32 a118, v250\n\t"
"v_accvgpr_read_b32 v251, a119\n\t"
"v_mul_f32 v251, v251, v234\n\t"
"v_accvgpr_write_b32 a119, v251\n\t"
"s_nop 7\n\t"
"s_branch LSR33_%=\n\t"
"LSP34_%=:\n\t"
"s_nop 15\n\t"
"v_max3_f32 v232, v96, v97, v98\n\t"
"v_max3_f32 v233, v99, v100, v101\n\t"
"v_max3_f32 v232, v232, v102, v103\n\t"
"v_max3_f32 v233, v233, v104, v105\n\t"
"v_max3_f32 v232, v232, v106, v107\n\t"
"v_max3_f32 v233, v233, v108, v109\n\t"
"v_max3_f32 v232, v232, v110, v111\n\t"
"v_max_f32 v232, v232, v233\n\t"
"v_mov_b32 v233, v232\n\t"
"s_nop 1\n\t"
"v_permlane32_swap_b32 v232, v233\n\t"
"v_max_f32 v232, v232, v233\n\t"
"v_mov_b32 v233, v232\n\t"
"s_nop 1\n\t"
"v_permlane16_swap_b32 v232, v233\n\t"
"v_max_f32 v232, v232, v233\n\t"
"v_max_f32 v232, 0, v232\n\t"
"v_add_f32 v26, v26, v232\n\t"
"v_exp_f32_e64 v234, -v232\n\t"
"v_sub_f32 v96, v96, v232\n\t"
"v_sub_f32 v97, v97, v232\n\t"
"v_sub_f32 v98, v98, v232\n\t"
"v_sub_f32 v99, v99, v232\n\t"
"v_sub_f32 v100, v100, v232\n\t"
"v_sub_f32 v101, v101, v232\n\t"
"v_sub_f32 v102, v102, v232\n\t"
"v_sub_f32 v103, v103, v232\n\t"
"v_sub_f32 v104, v104, v232\n\t"
"v_sub_f32 v105, v105, v232\n\t"
"v_sub_f32 v106, v106, v232\n\t"
"v_sub_f32 v107, v107, v232\n\t"
"v_sub_f32 v108, v108, v232\n\t"
"v_sub_f32 v109, v109, v232\n\t"
"v_sub_f32 v110, v110, v232\n\t"
"v_sub_f32 v111, v111, v232\n\t"
"v_mul_f32 v30, v30, v234\n\t"
"v_sub_f32 v40, 0, v26\n\t"
"v_sub_f32 v41, 0, v26\n\t"
"v_sub_f32 v42, 0, v26\n\t"
"v_sub_f32 v43, 0, v26\n\t"
"v_exp_f32 v236, v96\n\t"
"v_exp_f32 v237, v97\n\t"
"s_nop 0\n\t"
"v_cvt_pk_bf16_f32 v144, v236, v237\n\t"
"v_exp_f32 v238, v98\n\t"
"v_exp_f32 v239, v99\n\t"
"s_nop 0\n\t"
"v_cvt_pk_bf16_f32 v145, v238, v239\n\t"
"v_exp_f32 v240, v100\n\t"
"v_exp_f32 v241, v101\n\t"
"s_nop 0\n\t"
"v_cvt_pk_bf16_f32 v146, v240, v241\n\t"
"v_exp_f32 v242, v102\n\t"
"v_exp_f32 v243, v103\n\t"
"s_nop 0\n\t"
"v_cvt_pk_bf16_f32 v147, v242, v243\n\t"
"v_exp_f32 v236, v104\n\t"
"v_exp_f32 v237, v105\n\t"
"s_nop 0\n\t"
"v_cvt_pk_bf16_f32 v148, v236, v237\n\t"
"v_exp_f32 v238, v106\n\t"
"v_exp_f32 v239, v107\n\t"
"s_nop 0\n\t"
"v_cvt_pk_bf16_f32 v149, v238, v239\n\t"
"v_exp_f32 v240, v108\n\t"
"v_exp_f32 v241, v109\n\t"
"s_nop 0\n\t"
"v_cvt_pk_bf16_f32 v150, v240, v241\n\t"
"v_exp_f32 v242, v110\n\t"
"v_exp_f32 v243, v111\n\t"
"s_nop 0\n\t"
"v_cvt_pk_bf16_f32 v151, v242, v243\n\t"
"v_mfma_f32_16x16x32_bf16 v[16:19], v[4:7], v[144:147], 0\n\t"
"s_nop 0\n\t"
"v_mfma_f32_16x16x32_bf16 v[16:19], v[4:7], v[148:151], v[16:19]\n\t"
"v_accvgpr_read_b32 v244, a8\n\t"
"v_mul_f32 v244, v244, v234\n\t"
"v_accvgpr_write_b32 a8, v244\n\t"
"v_accvgpr_read_b32 v245, a9\n\t"
"v_mul_f32 v245, v245, v234\n\t"
"v_accvgpr_write_b32 a9, v245\n\t"
"v_accvgpr_read_b32 v246, a10\n\t"
"v_mul_f32 v246, v246, v234\n\t"
"v_accvgpr_write_b32 a10, v246\n\t"
"v_accvgpr_read_b32 v247, a11\n\t"
"v_mul_f32 v247, v247, v234\n\t"
"v_accvgpr_write_b32 a11, v247\n\t"
"v_accvgpr_read_b32 v248, a24\n\t"
"v_mul_f32 v248, v248, v234\n\t"
"v_accvgpr_write_b32 a24, v248\n\t"
"v_accvgpr_read_b32 v249, a25\n\t"
"v_mul_f32 v249, v249, v234\n\t"
"v_accvgpr_write_b32 a25, v249\n\t"
"v_accvgpr_read_b32 v250, a26\n\t"
"v_mul_f32 v250, v250, v234\n\t"
"v_accvgpr_write_b32 a26, v250\n\t"
"v_accvgpr_read_b32 v251, a27\n\t"
"v_mul_f32 v251, v251, v234\n\t"
"v_accvgpr_write_b32 a27, v251\n\t"
"v_accvgpr_read_b32 v244, a40\n\t"
"v_mul_f32 v244, v244, v234\n\t"
"v_accvgpr_write_b32 a40, v244\n\t"
"v_accvgpr_read_b32 v245, a41\n\t"
"v_mul_f32 v245, v245, v234\n\t"
"v_accvgpr_write_b32 a41, v245\n\t"
"v_accvgpr_read_b32 v246, a42\n\t"
"v_mul_f32 v246, v246, v234\n\t"
"v_accvgpr_write_b32 a42, v246\n\t"
"v_accvgpr_read_b32 v247, a43\n\t"
"v_mul_f32 v247, v247, v234\n\t"
"v_accvgpr_write_b32 a43, v247\n\t"
"v_accvgpr_read_b32 v248, a56\n\t"
"v_mul_f32 v248, v248, v234\n\t"
"v_accvgpr_write_b32 a56, v248\n\t"
"v_accvgpr_read_b32 v249, a57\n\t"
"v_mul_f32 v249, v249, v234\n\t"
"v_accvgpr_write_b32 a57, v249\n\t"
"v_accvgpr_read_b32 v250, a58\n\t"
"v_mul_f32 v250, v250, v234\n\t"
"v_accvgpr_write_b32 a58, v250\n\t"
"v_accvgpr_read_b32 v251, a59\n\t"
"v_mul_f32 v251, v251, v234\n\t"
"v_accvgpr_write_b32 a59, v251\n\t"
"v_accvgpr_read_b32 v244, a72\n\t"
"v_mul_f32 v244, v244, v234\n\t"
"v_accvgpr_write_b32 a72, v244\n\t"
"v_accvgpr_read_b32 v245, a73\n\t"
"v_mul_f32 v245, v245, v234\n\t"
"v_accvgpr_write_b32 a73, v245\n\t"
"v_accvgpr_read_b32 v246, a74\n\t"
"v_mul_f32 v246, v246, v234\n\t"
"v_accvgpr_write_b32 a74, v246\n\t"
"v_accvgpr_read_b32 v247, a75\n\t"
"v_mul_f32 v247, v247, v234\n\t"
"v_accvgpr_write_b32 a75, v247\n\t"
"v_accvgpr_read_b32 v248, a88\n\t"
"v_mul_f32 v248, v248, v234\n\t"
"v_accvgpr_write_b32 a88, v248\n\t"
"v_accvgpr_read_b32 v249, a89\n\t"
"v_mul_f32 v249, v249, v234\n\t"
"v_accvgpr_write_b32 a89, v249\n\t"
"v_accvgpr_read_b32 v250, a90\n\t"
"v_mul_f32 v250, v250, v234\n\t"
"v_accvgpr_write_b32 a90, v250\n\t"
"v_accvgpr_read_b32 v251, a91\n\t"
"v_mul_f32 v251, v251, v234\n\t"
"v_accvgpr_write_b32 a91, v251\n\t"
"v_accvgpr_read_b32 v244, a104\n\t"
"v_mul_f32 v244, v244, v234\n\t"
"v_accvgpr_write_b32 a104, v244\n\t"
"v_accvgpr_read_b32 v245, a105\n\t"
"v_mul_f32 v245, v245, v234\n\t"
"v_accvgpr_write_b32 a105, v245\n\t"
"v_accvgpr_read_b32 v246, a106\n\t"
"v_mul_f32 v246, v246, v234\n\t"
"v_accvgpr_write_b32 a106, v246\n\t"
"v_accvgpr_read_b32 v247, a107\n\t"
"v_mul_f32 v247, v247, v234\n\t"
"v_accvgpr_write_b32 a107, v247\n\t"
"v_accvgpr_read_b32 v248, a120\n\t"
"v_mul_f32 v248, v248, v234\n\t"
"v_accvgpr_write_b32 a120, v248\n\t"
"v_accvgpr_read_b32 v249, a121\n\t"
"v_mul_f32 v249, v249, v234\n\t"
"v_accvgpr_write_b32 a121, v249\n\t"
"v_accvgpr_read_b32 v250, a122\n\t"
"v_mul_f32 v250, v250, v234\n\t"
"v_accvgpr_write_b32 a122, v250\n\t"
"v_accvgpr_read_b32 v251, a123\n\t"
"v_mul_f32 v251, v251, v234\n\t"
"v_accvgpr_write_b32 a123, v251\n\t"
"v_max3_f32 v232, v112, v113, v114\n\t"
"v_max3_f32 v233, v115, v116, v117\n\t"
"v_max3_f32 v232, v232, v118, v119\n\t"
"v_max3_f32 v233, v233, v120, v121\n\t"
"v_max3_f32 v232, v232, v122, v123\n\t"
"v_max3_f32 v233, v233, v124, v125\n\t"
"v_max3_f32 v232, v232, v126, v127\n\t"
"v_max_f32 v232, v232, v233\n\t"
"v_mov_b32 v233, v232\n\t"
"s_nop 1\n\t"
"v_permlane32_swap_b32 v232, v233\n\t"
"v_max_f32 v232, v232, v233\n\t"
"v_mov_b32 v233, v232\n\t"
"s_nop 1\n\t"
"v_permlane16_swap_b32 v232, v233\n\t"
"v_max_f32 v232, v232, v233\n\t"
"v_max_f32 v232, 0, v232\n\t"
"v_add_f32 v27, v27, v232\n\t"
"v_exp_f32_e64 v234, -v232\n\t"
"v_sub_f32 v112, v112, v232\n\t"
"v_sub_f32 v113, v113, v232\n\t"
"v_sub_f32 v114, v114, v232\n\t"
"v_sub_f32 v115, v115, v232\n\t"
"v_sub_f32 v116, v116, v232\n\t"
"v_sub_f32 v117, v117, v232\n\t"
"v_sub_f32 v118, v118, v232\n\t"
"v_sub_f32 v119, v119, v232\n\t"
"v_sub_f32 v120, v120, v232\n\t"
"v_sub_f32 v121, v121, v232\n\t"
"v_sub_f32 v122, v122, v232\n\t"
"v_sub_f32 v123, v123, v232\n\t"
"v_sub_f32 v124, v124, v232\n\t"
"v_sub_f32 v125, v125, v232\n\t"
"v_sub_f32 v126, v126, v232\n\t"
"v_sub_f32 v127, v127, v232\n\t"
"v_mul_f32 v31, v31, v234\n\t"
"v_sub_f32 v44, 0, v27\n\t"
"v_sub_f32 v45, 0, v27\n\t"
"v_sub_f32 v46, 0, v27\n\t"
"v_sub_f32 v47, 0, v27\n\t"
"v_exp_f32 v236, v112\n\t"
"v_exp_f32 v237, v113\n\t"
"s_nop 0\n\t"
"v_cvt_pk_bf16_f32 v152, v236, v237\n\t"
"v_exp_f32 v238, v114\n\t"
"v_exp_f32 v239, v115\n\t"
"s_nop 0\n\t"
"v_cvt_pk_bf16_f32 v153, v238, v239\n\t"
"v_exp_f32 v240, v116\n\t"
"v_exp_f32 v241, v117\n\t"
"s_nop 0\n\t"
"v_cvt_pk_bf16_f32 v154, v240, v241\n\t"
"v_exp_f32 v242, v118\n\t"
"v_exp_f32 v243, v119\n\t"
"s_nop 0\n\t"
"v_cvt_pk_bf16_f32 v155, v242, v243\n\t"
"v_exp_f32 v236, v120\n\t"
"v_exp_f32 v237, v121\n\t"
"s_nop 0\n\t"
"v_cvt_pk_bf16_f32 v156, v236, v237\n\t"
"v_exp_f32 v238, v122\n\t"
"v_exp_f32 v239, v123\n\t"
"s_nop 0\n\t"
"v_cvt_pk_bf16_f32 v157, v238, v239\n\t"
"v_exp_f32 v240, v124\n\t"
"v_exp_f32 v241, v125\n\t"
"s_nop 0\n\t"
"v_cvt_pk_bf16_f32 v158, v240, v241\n\t"
"v_exp_f32 v242, v126\n\t"
"v_exp_f32 v243, v127\n\t"
"s_nop 0\n\t"
"v_cvt_pk_bf16_f32 v159, v242, v243\n\t"
"v_mfma_f32_16x16x32_bf16 v[20:23], v[4:7], v[152:155], 0\n\t"
"s_nop 0\n\t"
"v_mfma_f32_16x16x32_bf16 v[20:23], v[4:7], v[156:159], v[20:23]\n\t"
"v_accvgpr_read_b32 v244, a12\n\t"
"v_mul_f32 v244, v244, v234\n\t"
"v_accvgpr_write_b32 a12, v244\n\t"
"v_accvgpr_read_b32 v245, a13\n\t"
"v_mul_f32 v245, v245, v234\n\t"
"v_accvgpr_write_b32 a13, v245\n\t"
"v_accvgpr_read_b32 v246, a14\n\t"
"v_mul_f32 v246, v246, v234\n\t"
"v_accvgpr_write_b32 a14, v246\n\t"
"v_accvgpr_read_b32 v247, a15\n\t"
"v_mul_f32 v247, v247, v234\n\t"
"v_accvgpr_write_b32 a15, v247\n\t"
"v_accvgpr_read_b32 v248, a28\n\t"
"v_mul_f32 v248, v248, v234\n\t"
"v_accvgpr_write_b32 a28, v248\n\t"
"v_accvgpr_read_b32 v249, a29\n\t"
"v_mul_f32 v249, v249, v234\n\t"
"v_accvgpr_write_b32 a29, v249\n\t"
"v_accvgpr_read_b32 v250, a30\n\t"
"v_mul_f32 v250, v250, v234\n\t"
"v_accvgpr_write_b32 a30, v250\n\t"
"v_accvgpr_read_b32 v251, a31\n\t"
"v_mul_f32 v251, v251, v234\n\t"
"v_accvgpr_write_b32 a31, v251\n\t"
"v_accvgpr_read_b32 v244, a44\n\t"
"v_mul_f32 v244, v244, v234\n\t"
"v_accvgpr_write_b32 a44, v244\n\t"
"v_accvgpr_read_b32 v245, a45\n\t"
"v_mul_f32 v245, v245, v234\n\t"
"v_accvgpr_write_b32 a45, v245\n\t"
"v_accvgpr_read_b32 v246, a46\n\t"
"v_mul_f32 v246, v246, v234\n\t"
"v_accvgpr_write_b32 a46, v246\n\t"
"v_accvgpr_read_b32 v247, a47\n\t"
"v_mul_f32 v247, v247, v234\n\t"
"v_accvgpr_write_b32 a47, v247\n\t"
"v_accvgpr_read_b32 v248, a60\n\t"
"v_mul_f32 v248, v248, v234\n\t"
"v_accvgpr_write_b32 a60, v248\n\t"
"v_accvgpr_read_b32 v249, a61\n\t"
"v_mul_f32 v249, v249, v234\n\t"
"v_accvgpr_write_b32 a61, v249\n\t"
"v_accvgpr_read_b32 v250, a62\n\t"
"v_mul_f32 v250, v250, v234\n\t"
"v_accvgpr_write_b32 a62, v250\n\t"
"v_accvgpr_read_b32 v251, a63\n\t"
"v_mul_f32 v251, v251, v234\n\t"
"v_accvgpr_write_b32 a63, v251\n\t"
"v_accvgpr_read_b32 v244, a76\n\t"
"v_mul_f32 v244, v244, v234\n\t"
"v_accvgpr_write_b32 a76, v244\n\t"
"v_accvgpr_read_b32 v245, a77\n\t"
"v_mul_f32 v245, v245, v234\n\t"
"v_accvgpr_write_b32 a77, v245\n\t"
"v_accvgpr_read_b32 v246, a78\n\t"
"v_mul_f32 v246, v246, v234\n\t"
"v_accvgpr_write_b32 a78, v246\n\t"
"v_accvgpr_read_b32 v247, a79\n\t"
"v_mul_f32 v247, v247, v234\n\t"
"v_accvgpr_write_b32 a79, v247\n\t"
"v_accvgpr_read_b32 v248, a92\n\t"
"v_mul_f32 v248, v248, v234\n\t"
"v_accvgpr_write_b32 a92, v248\n\t"
"v_accvgpr_read_b32 v249, a93\n\t"
"v_mul_f32 v249, v249, v234\n\t"
"v_accvgpr_write_b32 a93, v249\n\t"
"v_accvgpr_read_b32 v250, a94\n\t"
"v_mul_f32 v250, v250, v234\n\t"
"v_accvgpr_write_b32 a94, v250\n\t"
"v_accvgpr_read_b32 v251, a95\n\t"
"v_mul_f32 v251, v251, v234\n\t"
"v_accvgpr_write_b32 a95, v251\n\t"
"v_accvgpr_read_b32 v244, a108\n\t"
"v_mul_f32 v244, v244, v234\n\t"
"v_accvgpr_write_b32 a108, v244\n\t"
"v_accvgpr_read_b32 v245, a109\n\t"
"v_mul_f32 v245, v245, v234\n\t"
"v_accvgpr_write_b32 a109, v245\n\t"
"v_accvgpr_read_b32 v246, a110\n\t"
"v_mul_f32 v246, v246, v234\n\t"
"v_accvgpr_write_b32 a110, v246\n\t"
"v_accvgpr_read_b32 v247, a111\n\t"
"v_mul_f32 v247, v247, v234\n\t"
"v_accvgpr_write_b32 a111, v247\n\t"
"v_accvgpr_read_b32 v248, a124\n\t"
"v_mul_f32 v248, v248, v234\n\t"
"v_accvgpr_write_b32 a124, v248\n\t"
"v_accvgpr_read_b32 v249, a125\n\t"
"v_mul_f32 v249, v249, v234\n\t"
"v_accvgpr_write_b32 a125, v249\n\t"
"v_accvgpr_read_b32 v250, a126\n\t"
"v_mul_f32 v250, v250, v234\n\t"
"v_accvgpr_write_b32 a126, v250\n\t"
"v_accvgpr_read_b32 v251, a127\n\t"
"v_mul_f32 v251, v251, v234\n\t"
"v_accvgpr_write_b32 a127, v251\n\t"
"s_nop 7\n\t"
"s_branch LSR35_%=\n\t"
"LAEND17_%=:\n\t"
"s_load_dwordx2 s[56:57], s[36:37], 120\n\t"
"s_load_dwordx2 s[52:53], s[36:37], 40\n\t"
"s_and_b32 s71, s38, 7\n\t"
"s_lshr_b32 s72, s38, 6\n\t"
"s_sub_u32 s73, 15, s41\n\t"
"s_mov_b32 s74, s41\n\t"
"v_mbcnt_lo_u32_b32 v14, -1, 0\n\t"
"v_mbcnt_hi_u32_b32 v14, -1, v14\n\t"
"v_lshlrev_b32 v0, 4, v14\n\t"
"v_lshrrev_b32 v15, 4, v14\n\t"
"v_bfe_u32 v16, v14, 1, 3\n\t"
"v_xor_b32 v16, v16, v15\n\t"
"v_and_b32 v17, 1, v14\n\t"
"v_lshlrev_b32 v17, 4, v17\n\t"
"v_lshl_add_u32 v17, v15, 10, v17\n\t"
"v_xor_b32 v18, 0, v16\n\t"
"v_lshl_add_u32 v1, v18, 5, v17\n\t"
"v_xor_b32 v18, 4, v16\n\t"
"v_lshl_add_u32 v2, v18, 5, v17\n\t"
"v_add_u32 v2, 3072, v2\n\t"
"v_xor_b32 v18, 0, v16\n\t"
"v_lshl_add_u32 v3, v18, 5, v17\n\t"
"v_add_u32 v3, 6144, v3\n\t"
"v_xor_b32 v18, 4, v16\n\t"
"v_lshl_add_u32 v4, v18, 5, v17\n\t"
"v_add_u32 v4, 9216, v4\n\t"
"v_lshrrev_b32 v15, 2, v14\n\t"
"v_lshlrev_b32 v16, 8, v15\n\t"
"v_and_b32 v17, 3, v14\n\t"
"v_lshl_add_u32 v16, v17, 3, v16\n\t"
"v_and_b32 v15, 7, v15\n\t"
"v_xor_b32 v17, 0, v15\n\t"
"v_lshl_add_u32 v6, v17, 5, v16\n\t"
"v_xor_b32 v17, 1, v15\n\t"
"v_lshl_add_u32 v7, v17, 5, v16\n\t"
"v_xor_b32 v17, 2, v15\n\t"
"v_lshl_add_u32 v8, v17, 5, v16\n\t"
"v_xor_b32 v17, 3, v15\n\t"
"v_lshl_add_u32 v9, v17, 5, v16\n\t"
"v_xor_b32 v17, 4, v15\n\t"
"v_lshl_add_u32 v10, v17, 5, v16\n\t"
"v_xor_b32 v17, 5, v15\n\t"
"v_lshl_add_u32 v11, v17, 5, v16\n\t"
"v_xor_b32 v17, 6, v15\n\t"
"v_lshl_add_u32 v12, v17, 5, v16\n\t"
"v_xor_b32 v17, 7, v15\n\t"
"v_lshl_add_u32 v13, v17, 5, v16\n\t"
"s_waitcnt lgkmcnt(0)\n\t"
"s_and_b32 s57, s57, 0xffff\n\t"
"s_mov_b32 s58, 8388608\n\t"
"s_mov_b32 s59, 0x00020000\n\t"
"s_and_b32 s53, s53, 0xffff\n\t"
"s_mov_b32 s54, 4194304\n\t"
"s_mov_b32 s55, 0x00020000\n\t"
"s_add_u32 s48, s42, 41943040\n\t"
"s_addc_u32 s49, s43, 0\n\t"
"s_and_b32 s49, s49, 0xffff\n\t"
"s_mov_b32 s50, 16777216\n\t"
"s_mov_b32 s51, 0x00020000\n\t"
"s_add_u32 s60, s42, 58720256\n\t"
"s_addc_u32 s61, s43, 0\n\t"
"s_and_b32 s61, s61, 0xffff\n\t"
"s_mov_b32 s62, 33554432\n\t"
"s_mov_b32 s63, 0x00020000\n\t"
"s_lshl_b32 s64, s73, 20\n\t"
"s_lshl_b32 s68, s39, 18\n\t"
"s_add_u32 s64, s64, s68\n\t"
"s_lshl_b32 s68, s71, 14\n\t"
"s_add_u32 s64, s64, s68\n\t"
"s_lshl_b32 s65, s74, 20\n\t"
"s_lshl_b32 s68, s39, 18\n\t"
"s_add_u32 s65, s65, s68\n\t"
"s_lshl_b32 s68, s71, 14\n\t"
"s_add_u32 s65, s65, s68\n\t"
"s_lshl_b32 s66, s71, 19\n\t"
"s_lshl_b32 s68, s39, 14\n\t"
"s_add_u32 s66, s66, s68\n\t"
"s_lshl_b32 s68, s72, 8\n\t"
"s_add_u32 s66, s66, s68\n\t"
"s_lshl_b32 s67, s39, 12\n\t"
"s_barrier\n\t"
"s_add_u32 m0, s67, 0\n\t"
"s_add_u32 s68, s66, 0\n\t"
"buffer_load_dwordx4 v1, s[52:55], s68 offen offset:0 lds\n\t"
"buffer_load_dwordx4 v2, s[52:55], s68 offen offset:1024 lds\n\t"
"buffer_load_dwordx4 v3, s[52:55], s68 offen offset:2048 lds\n\t"
"buffer_load_dwordx4 v4, s[52:55], s68 offen offset:3072 lds\n\t"
"s_add_u32 m0, s67, 16384\n\t"
"s_add_u32 s68, s66, 65536\n\t"
"buffer_load_dwordx4 v1, s[52:55], s68 offen offset:0 lds\n\t"
"buffer_load_dwordx4 v2, s[52:55], s68 offen offset:1024 lds\n\t"
"buffer_load_dwordx4 v3, s[52:55], s68 offen offset:2048 lds\n\t"
"buffer_load_dwordx4 v4, s[52:55], s68 offen offset:3072 lds\n\t"
"s_mov_b32 s70, 0\n\t"
"s_waitcnt vmcnt(16)\n\t"
"LFP36_%=:\n\t"
"v_cmp_eq_u64 vcc, s[26:27], v[58:59]\n\t"
"s_nop 1\n\t"
"s_cmp_eq_u64 vcc, exec\n\t"
"s_cbranch_scc1 LFO37_%=\n\t"
"s_sleep 1\n\t"
"global_load_dwordx2 v[58:59], v57, s[42:43] sc1\n\t"
"s_waitcnt vmcnt(0)\n\t"
"s_add_u32 s70, s70, 1\n\t"
"s_cmp_lt_u32 s70, 0x40000\n\t"
"s_cbranch_scc1 LFP36_%=\n\t"
"LFO37_%=:\n\t"
"s_add_u32 s69, s64, 0\n\t"
"buffer_load_dwordx4 v[96:99], v0, s[48:51], s69 offen offset:0 sc1\n\t"
"buffer_load_dwordx4 v[104:107], v0, s[48:51], s69 offen offset:1024 sc1\n\t"
"buffer_load_dwordx4 v[112:115], v0, s[48:51], s69 offen offset:2048 sc1\n\t"
"buffer_load_dwordx4 v[120:123], v0, s[48:51], s69 offen offset:3072 sc1\n\t"
"s_add_u32 s69, s64, 131072\n\t"
"buffer_load_dwordx4 v[100:103], v0, s[48:51], s69 offen offset:0 sc1\n\t"
"buffer_load_dwordx4 v[108:111], v0, s[48:51], s69 offen offset:1024 sc1\n\t"
"buffer_load_dwordx4 v[116:119], v0, s[48:51], s69 offen offset:2048 sc1\n\t"
"buffer_load_dwordx4 v[124:127], v0, s[48:51], s69 offen offset:3072 sc1\n\t"
"s_add_u32 m0, s67, 32768\n\t"
"s_add_u32 s68, s66, 131072\n\t"
"buffer_load_dwordx4 v1, s[52:55], s68 offen offset:0 lds\n\t"
"buffer_load_dwordx4 v2, s[52:55], s68 offen offset:1024 lds\n\t"
"buffer_load_dwordx4 v3, s[52:55], s68 offen offset:2048 lds\n\t"
"buffer_load_dwordx4 v4, s[52:55], s68 offen offset:3072 lds\n\t"
"s_add_u32 m0, s67, 49152\n\t"
"s_add_u32 s68, s66, 196608\n\t"
"buffer_load_dwordx4 v1, s[52:55], s68 offen offset:0 lds\n\t"
"buffer_load_dwordx4 v2, s[52:55], s68 offen offset:1024 lds\n\t"
"buffer_load_dwordx4 v3, s[52:55], s68 offen offset:2048 lds\n\t"
"buffer_load_dwordx4 v4, s[52:55], s68 offen offset:3072 lds\n\t"
"s_add_u32 s69, s64, 4096\n\t"
"buffer_load_dwordx4 v[128:131], v0, s[48:51], s69 offen offset:0 sc1\n\t"
"buffer_load_dwordx4 v[136:139], v0, s[48:51], s69 offen offset:1024 sc1\n\t"
"buffer_load_dwordx4 v[144:147], v0, s[48:51], s69 offen offset:2048 sc1\n\t"
"buffer_load_dwordx4 v[152:155], v0, s[48:51], s69 offen offset:3072 sc1\n\t"
"s_add_u32 s69, s64, 135168\n\t"
"buffer_load_dwordx4 v[132:135], v0, s[48:51], s69 offen offset:0 sc1\n\t"
"buffer_load_dwordx4 v[140:143], v0, s[48:51], s69 offen offset:1024 sc1\n\t"
"buffer_load_dwordx4 v[148:151], v0, s[48:51], s69 offen offset:2048 sc1\n\t"
"buffer_load_dwordx4 v[156:159], v0, s[48:51], s69 offen offset:3072 sc1\n\t"
"s_waitcnt vmcnt(16)\n\t"
"s_barrier\n\t"
"ds_read_b64_tr_b16 v[224:225], v6 offset:0\n\t"
"ds_read_b64_tr_b16 v[226:227], v6 offset:4096\n\t"
"ds_read_b64_tr_b16 v[228:229], v7 offset:0\n\t"
"ds_read_b64_tr_b16 v[230:231], v7 offset:4096\n\t"
"ds_read_b64_tr_b16 v[232:233], v8 offset:0\n\t"
"ds_read_b64_tr_b16 v[234:235], v8 offset:4096\n\t"
"ds_read_b64_tr_b16 v[236:237], v9 offset:0\n\t"
"ds_read_b64_tr_b16 v[238:239], v9 offset:4096\n\t"
"ds_read_b64_tr_b16 v[240:241], v10 offset:0\n\t"
"ds_read_b64_tr_b16 v[242:243], v10 offset:4096\n\t"
"ds_read_b64_tr_b16 v[244:245], v11 offset:0\n\t"
"ds_read_b64_tr_b16 v[246:247], v11 offset:4096\n\t"
"ds_read_b64_tr_b16 v[248:249], v12 offset:0\n\t"
"ds_read_b64_tr_b16 v[250:251], v12 offset:4096\n\t"
"ds_read_b64_tr_b16 v[252:253], v13 offset:0\n\t"
"ds_read_b64_tr_b16 v[254:255], v13 offset:4096\n\t"
"s_waitcnt lgkmcnt(14)\n\t"
"s_and_b32 s70, s38, 7\n\t"
"s_lshl_b32 s70, s70, 4\n\t"
"s_add_u32 s69, s70, s41\n\t"
"s_lshl_b32 s69, s69, 7\n\t"
"s_add_u32 s69, s69, 8192\n\t"
"s_lshl_b32 s70, s72, 5\n\t"
"s_add_u32 s69, s69, s70\n\t"
"s_lshl_b32 s70, s39, 3\n\t"
"s_add_u32 s69, s69, s70\n\t"
"v_mov_b32 v60, s69\n\t"
"v_mov_b32 v62, s26\n\t"
"v_mov_b32 v63, s27\n\t"
"s_mov_b64 s[76:77], exec\n\t"
"s_mov_b64 exec, 1\n\t"
"s_nop 1\n\t"
"global_store_dwordx2 v60, v[62:63], s[42:43] sc1\n\t"
"s_mov_b64 exec, s[76:77]\n\t"
"s_nop 1\n\t"
"v_and_b32 v16, 15, v14\n\t"
"v_lshrrev_b32 v17, 4, v14\n\t"
"v_lshlrev_b32 v17, 4, v17\n\t"
"v_lshl_add_u32 v16, v16, 12, v17\n\t"
"v_mfma_f32_16x16x32_bf16 a[128:131], v[224:227], v[96:99], 0\n\t"
"ds_read_b64_tr_b16 v[64:65], v6 offset:8192\n\t"
"s_add_u32 m0, s67, 65536\n\t"
"v_mfma_f32_16x16x32_bf16 a[160:163], v[224:227], v[100:103], 0\n\t"
"ds_read_b64_tr_b16 v[66:67], v6 offset:12288\n\t"
"s_waitcnt lgkmcnt(9)\n\t"
"s_add_u32 s68, s66, 262144\n\t"
"v_mfma_f32_16x16x32_bf16 a[132:135], v[228:231], v[96:99], 0\n\t"
"ds_read_b64_tr_b16 v[68:69], v7 offset:8192\n\t"
"buffer_load_dwordx4 v1, s[52:55], s68 offen offset:0 lds\n\t"
"v_mfma_f32_16x16x32_bf16 a[164:167], v[228:231], v[100:103], 0\n\t"
"ds_read_b64_tr_b16 v[70:71], v7 offset:12288\n\t"
"ds_read_b64_tr_b16 v[72:73], v8 offset:8192\n\t"
"v_mfma_f32_16x16x32_bf16 a[136:139], v[232:235], v[96:99], 0\n\t"
"buffer_load_dwordx4 v2, s[52:55], s68 offen offset:1024 lds\n\t"
"ds_read_b64_tr_b16 v[74:75], v8 offset:12288\n\t"
"v_mfma_f32_16x16x32_bf16 a[168:171], v[232:235], v[100:103], 0\n\t"
"buffer_load_dwordx4 v3, s[52:55], s68 offen offset:2048 lds\n\t"
"ds_read_b64_tr_b16 v[76:77], v9 offset:8192\n\t"
"v_mfma_f32_16x16x32_bf16 a[140:143], v[236:239], v[96:99], 0\n\t"
"buffer_load_dwordx4 v4, s[52:55], s68 offen offset:3072 lds\n\t"
"ds_read_b64_tr_b16 v[78:79], v9 offset:12288\n\t"
"v_mfma_f32_16x16x32_bf16 a[172:175], v[236:239], v[100:103], 0\n\t"
"ds_read_b64_tr_b16 v[80:81], v10 offset:8192\n\t"
"s_waitcnt lgkmcnt(8)\n\t"
"s_add_u32 m0, s67, 81920\n\t"
"v_mfma_f32_16x16x32_bf16 a[144:147], v[240:243], v[96:99], 0\n\t"
"ds_read_b64_tr_b16 v[82:83], v10 offset:12288\n\t"
"s_add_u32 s68, s66, 327680\n\t"
"v_mfma_f32_16x16x32_bf16 a[176:179], v[240:243], v[100:103], 0\n\t"
"ds_read_b64_tr_b16 v[84:85], v11 offset:8192\n\t"
"buffer_load_dwordx4 v1, s[52:55], s68 offen offset:0 lds\n\t"
"v_mfma_f32_16x16x32_bf16 a[148:151], v[244:247], v[96:99], 0\n\t"
"ds_read_b64_tr_b16 v[86:87], v11 offset:12288\n\t"
"ds_read_b64_tr_b16 v[88:89], v12 offset:8192\n\t"
"v_mfma_f32_16x16x32_bf16 a[180:183], v[244:247], v[100:103], 0\n\t"
"buffer_load_dwordx4 v2, s[52:55], s68 offen offset:1024 lds\n\t"
"ds_read_b64_tr_b16 v[90:91], v12 offset:12288\n\t"
"v_mfma_f32_16x16x32_bf16 a[152:155], v[248:251], v[96:99], 0\n\t"
"buffer_load_dwordx4 v3, s[52:55], s68 offen offset:2048 lds\n\t"
"ds_read_b64_tr_b16 v[92:93], v13 offset:8192\n\t"
"v_mfma_f32_16x16x32_bf16 a[184:187], v[248:251], v[100:103], 0\n\t"
"buffer_load_dwordx4 v4, s[52:55], s68 offen offset:3072 lds\n\t"
"ds_read_b64_tr_b16 v[94:95], v13 offset:12288\n\t"
"v_mfma_f32_16x16x32_bf16 a[156:159], v[252:255], v[96:99], 0\n\t"
"v_mfma_f32_16x16x32_bf16 a[188:191], v[252:255], v[100:103], 0\n\t"
"s_waitcnt lgkmcnt(6)\n\t"
"v_mfma_f32_16x16x32_bf16 a[128:131], v[64:67], v[104:107], a[128:131]\n\t"
"ds_read_b64_tr_b16 v[224:225], v6 offset:16384\n\t"
"s_add_u32 s69, s64, 8192\n\t"
"v_mfma_f32_16x16x32_bf16 a[160:163], v[64:67], v[108:111], a[160:163]\n\t"
"ds_read_b64_tr_b16 v[226:227], v6 offset:20480\n\t"
"buffer_load_dwordx4 v[160:163], v0, s[48:51], s69 offen offset:0 sc1\n\t"
"v_mfma_f32_16x16x32_bf16 a[132:135], v[68:71], v[104:107], a[132:135]\n\t"
"ds_read_b64_tr_b16 v[228:229], v7 offset:16384\n\t"
"ds_read_b64_tr_b16 v[230:231], v7 offset:20480\n\t"
"v_mfma_f32_16x16x32_bf16 a[164:167], v[68:71], v[108:111], a[164:167]\n\t"
"buffer_load_dwordx4 v[168:171], v0, s[48:51], s69 offen offset:1024 sc1\n\t"
"ds_read_b64_tr_b16 v[232:233], v8 offset:16384\n\t"
"v_mfma_f32_16x16x32_bf16 a[136:139], v[72:75], v[104:107], a[136:139]\n\t"
"buffer_load_dwordx4 v[176:179], v0, s[48:51], s69 offen offset:2048 sc1\n\t"
"ds_read_b64_tr_b16 v[234:235], v8 offset:20480\n\t"
"v_mfma_f32_16x16x32_bf16 a[168:171], v[72:75], v[108:111], a[168:171]\n\t"
"ds_read_b64_tr_b16 v[236:237], v9 offset:16384\n\t"
"buffer_load_dwordx4 v[184:187], v0, s[48:51], s69 offen offset:3072 sc1\n\t"
"v_mfma_f32_16x16x32_bf16 a[140:143], v[76:79], v[104:107], a[140:143]\n\t"
"ds_read_b64_tr_b16 v[238:239], v9 offset:20480\n\t"
"v_mfma_f32_16x16x32_bf16 a[172:175], v[76:79], v[108:111], a[172:175]\n\t"
"ds_read_b64_tr_b16 v[240:241], v10 offset:16384\n\t"
"s_add_u32 s69, s64, 139264\n\t"
"v_mfma_f32_16x16x32_bf16 a[144:147], v[80:83], v[104:107], a[144:147]\n\t"
"ds_read_b64_tr_b16 v[242:243], v10 offset:20480\n\t"
"s_waitcnt lgkmcnt(8)\n\t"
"buffer_load_dwordx4 v[164:167], v0, s[48:51], s69 offen offset:0 sc1\n\t"
"v_mfma_f32_16x16x32_bf16 a[176:179], v[80:83], v[108:111], a[176:179]\n\t"
"ds_read_b64_tr_b16 v[244:245], v11 offset:16384\n\t"
"ds_read_b64_tr_b16 v[246:247], v11 offset:20480\n\t"
"v_mfma_f32_16x16x32_bf16 a[148:151], v[84:87], v[104:107], a[148:151]\n\t"
"buffer_load_dwordx4 v[172:175], v0, s[48:51], s69 offen offset:1024 sc1\n\t"
"ds_read_b64_tr_b16 v[248:249], v12 offset:16384\n\t"
"v_mfma_f32_16x16x32_bf16 a[180:183], v[84:87], v[108:111], a[180:183]\n\t"
"buffer_load_dwordx4 v[180:183], v0, s[48:51], s69 offen offset:2048 sc1\n\t"
"ds_read_b64_tr_b16 v[250:251], v12 offset:20480\n\t"
"v_mfma_f32_16x16x32_bf16 a[152:155], v[88:91], v[104:107], a[152:155]\n\t"
"ds_read_b64_tr_b16 v[252:253], v13 offset:16384\n\t"
"buffer_load_dwordx4 v[188:191], v0, s[48:51], s69 offen offset:3072 sc1\n\t"
"v_mfma_f32_16x16x32_bf16 a[184:187], v[88:91], v[108:111], a[184:187]\n\t"
"ds_read_b64_tr_b16 v[254:255], v13 offset:20480\n\t"
"v_mfma_f32_16x16x32_bf16 a[156:159], v[92:95], v[104:107], a[156:159]\n\t"
"v_mfma_f32_16x16x32_bf16 a[188:191], v[92:95], v[108:111], a[188:191]\n\t"
"v_mfma_f32_16x16x32_bf16 a[128:131], v[224:227], v[112:115], a[128:131]\n\t"
"ds_read_b64_tr_b16 v[64:65], v6 offset:24576\n\t"
"s_add_u32 m0, s67, 98304\n\t"
"v_mfma_f32_16x16x32_bf16 a[160:163], v[224:227], v[116:119], a[160:163]\n\t"
"ds_read_b64_tr_b16 v[66:67], v6 offset:28672\n\t"
"s_waitcnt lgkmcnt(6)\n\t"
"s_add_u32 s68, s66, 393216\n\t"
"v_mfma_f32_16x16x32_bf16 a[132:135], v[228:231], v[112:115], a[132:135]\n\t"
"ds_read_b64_tr_b16 v[68:69], v7 offset:24576\n\t"
"buffer_load_dwordx4 v1, s[52:55], s68 offen offset:0 lds\n\t"
"v_mfma_f32_16x16x32_bf16 a[164:167], v[228:231], v[116:119], a[164:167]\n\t"
"ds_read_b64_tr_b16 v[70:71], v7 offset:28672\n\t"
"ds_read_b64_tr_b16 v[72:73], v8 offset:24576\n\t"
"v_mfma_f32_16x16x32_bf16 a[136:139], v[232:235], v[112:115], a[136:139]\n\t"
"buffer_load_dwordx4 v2, s[52:55], s68 offen offset:1024 lds\n\t"
"ds_read_b64_tr_b16 v[74:75], v8 offset:28672\n\t"
"v_mfma_f32_16x16x32_bf16 a[168:171], v[232:235], v[116:119], a[168:171]\n\t"
"buffer_load_dwordx4 v3, s[52:55], s68 offen offset:2048 lds\n\t"
"ds_read_b64_tr_b16 v[76:77], v9 offset:24576\n\t"
"v_mfma_f32_16x16x32_bf16 a[140:143], v[236:239], v[112:115], a[140:143]\n\t"
"buffer_load_dwordx4 v4, s[52:55], s68 offen offset:3072 lds\n\t"
"ds_read_b64_tr_b16 v[78:79], v9 offset:28672\n\t"
"v_mfma_f32_16x16x32_bf16 a[172:175], v[236:239], v[116:119], a[172:175]\n\t"
"ds_read_b64_tr_b16 v[80:81], v10 offset:24576\n\t"
"s_add_u32 m0, s67, 114688\n\t"
"v_mfma_f32_16x16x32_bf16 a[144:147], v[240:243], v[112:115], a[144:147]\n\t"
"ds_read_b64_tr_b16 v[82:83], v10 offset:28672\n\t"
"s_add_u32 s68, s66, 458752\n\t"
"v_mfma_f32_16x16x32_bf16 a[176:179], v[240:243], v[116:119], a[176:179]\n\t"
"ds_read_b64_tr_b16 v[84:85], v11 offset:24576\n\t"
"buffer_load_dwordx4 v1, s[52:55], s68 offen offset:0 lds\n\t"
"v_mfma_f32_16x16x32_bf16 a[148:151], v[244:247], v[112:115], a[148:151]\n\t"
"ds_read_b64_tr_b16 v[86:87], v11 offset:28672\n\t"
"s_waitcnt lgkmcnt(7)\n\t"
"ds_read_b64_tr_b16 v[88:89], v12 offset:24576\n\t"
"v_mfma_f32_16x16x32_bf16 a[180:183], v[244:247], v[116:119], a[180:183]\n\t"
"buffer_load_dwordx4 v2, s[52:55], s68 offen offset:1024 lds\n\t"
"ds_read_b64_tr_b16 v[90:91], v12 offset:28672\n\t"
"v_mfma_f32_16x16x32_bf16 a[152:155], v[248:251], v[112:115], a[152:155]\n\t"
"buffer_load_dwordx4 v3, s[52:55], s68 offen offset:2048 lds\n\t"
"ds_read_b64_tr_b16 v[92:93], v13 offset:24576\n\t"
"v_mfma_f32_16x16x32_bf16 a[184:187], v[248:251], v[116:119], a[184:187]\n\t"
"buffer_load_dwordx4 v4, s[52:55], s68 offen offset:3072 lds\n\t"
"ds_read_b64_tr_b16 v[94:95], v13 offset:28672\n\t"
"v_mfma_f32_16x16x32_bf16 a[156:159], v[252:255], v[112:115], a[156:159]\n\t"
"v_mfma_f32_16x16x32_bf16 a[188:191], v[252:255], v[116:119], a[188:191]\n\t"
"s_waitcnt vmcnt(25)\n\t"
"s_barrier\n\t"
"v_mfma_f32_16x16x32_bf16 a[128:131], v[64:67], v[120:123], a[128:131]\n\t"
"ds_read_b64_tr_b16 v[224:225], v6 offset:32768\n\t"
"s_add_u32 s69, s64, 12288\n\t"
"v_mfma_f32_16x16x32_bf16 a[160:163], v[64:67], v[124:127], a[160:163]\n\t"
"ds_read_b64_tr_b16 v[226:227], v6 offset:36864\n\t"
"buffer_load_dwordx4 v[192:195], v0, s[48:51], s69 offen offset:0 sc1\n\t"
"v_mfma_f32_16x16x32_bf16 a[132:135], v[68:71], v[120:123], a[132:135]\n\t"
"ds_read_b64_tr_b16 v[228:229], v7 offset:32768\n\t"
"ds_read_b64_tr_b16 v[230:231], v7 offset:36864\n\t"
"v_mfma_f32_16x16x32_bf16 a[164:167], v[68:71], v[124:127], a[164:167]\n\t"
"buffer_load_dwordx4 v[200:203], v0, s[48:51], s69 offen offset:1024 sc1\n\t"
"ds_read_b64_tr_b16 v[232:233], v8 offset:32768\n\t"
"s_waitcnt lgkmcnt(6)\n\t"
"v_mfma_f32_16x16x32_bf16 a[136:139], v[72:75], v[120:123], a[136:139]\n\t"
"buffer_load_dwordx4 v[208:211], v0, s[48:51], s69 offen offset:2048 sc1\n\t"
"ds_read_b64_tr_b16 v[234:235], v8 offset:36864\n\t"
"v_mfma_f32_16x16x32_bf16 a[168:171], v[72:75], v[124:127], a[168:171]\n\t"
"ds_read_b64_tr_b16 v[236:237], v9 offset:32768\n\t"
"buffer_load_dwordx4 v[216:219], v0, s[48:51], s69 offen offset:3072 sc1\n\t"
"v_mfma_f32_16x16x32_bf16 a[140:143], v[76:79], v[120:123], a[140:143]\n\t"
"ds_read_b64_tr_b16 v[238:239], v9 offset:36864\n\t"
"v_mfma_f32_16x16x32_bf16 a[172:175], v[76:79], v[124:127], a[172:175]\n\t"
"ds_read_b64_tr_b16 v[240:241], v10 offset:32768\n\t"
"s_add_u32 s69, s64, 143360\n\t"
"v_mfma_f32_16x16x32_bf16 a[144:147], v[80:83], v[120:123], a[144:147]\n\t"
"ds_read_b64_tr_b16 v[242:243], v10 offset:36864\n\t"
"buffer_load_dwordx4 v[196:199], v0, s[48:51], s69 offen offset:0 sc1\n\t"
"v_mfma_f32_16x16x32_bf16 a[176:179], v[80:83], v[124:127], a[176:179]\n\t"
"ds_read_b64_tr_b16 v[244:245], v11 offset:32768\n\t"
"ds_read_b64_tr_b16 v[246:247], v11 offset:36864\n\t"
"v_mfma_f32_16x16x32_bf16 a[148:151], v[84:87], v[120:123], a[148:151]\n\t"
"buffer_load_dwordx4 v[204:207], v0, s[48:51], s69 offen offset:1024 sc1\n\t"
"ds_read_b64_tr_b16 v[248:249], v12 offset:32768\n\t"
"v_mfma_f32_16x16x32_bf16 a[180:183], v[84:87], v[124:127], a[180:183]\n\t"
"buffer_load_dwordx4 v[212:215], v0, s[48:51], s69 offen offset:2048 sc1\n\t"
"ds_read_b64_tr_b16 v[250:251], v12 offset:36864\n\t"
"v_mfma_f32_16x16x32_bf16 a[152:155], v[88:91], v[120:123], a[152:155]\n\t"
"ds_read_b64_tr_b16 v[252:253], v13 offset:32768\n\t"
"s_waitcnt lgkmcnt(8)\n\t"
"buffer_load_dwordx4 v[220:223], v0, s[48:51], s69 offen offset:3072 sc1\n\t"
"v_mfma_f32_16x16x32_bf16 a[184:187], v[88:91], v[124:127], a[184:187]\n\t"
"ds_read_b64_tr_b16 v[254:255], v13 offset:36864\n\t"
"v_mfma_f32_16x16x32_bf16 a[156:159], v[92:95], v[120:123], a[156:159]\n\t"
"v_mfma_f32_16x16x32_bf16 a[188:191], v[92:95], v[124:127], a[188:191]\n\t"
"v_mfma_f32_16x16x32_bf16 a[128:131], v[224:227], v[128:131], a[128:131]\n\t"
"ds_read_b64_tr_b16 v[64:65], v6 offset:40960\n\t"
"ds_read_b64_tr_b16 v[66:67], v6 offset:45056\n\t"
"v_mfma_f32_16x16x32_bf16 a[160:163], v[224:227], v[132:135], a[160:163]\n\t"
"ds_read_b64_tr_b16 v[68:69], v7 offset:40960\n\t"
"v_mfma_f32_16x16x32_bf16 a[132:135], v[228:231], v[128:131], a[132:135]\n\t"
"ds_read_b64_tr_b16 v[70:71], v7 offset:45056\n\t"
"v_mfma_f32_16x16x32_bf16 a[164:167], v[228:231], v[132:135], a[164:167]\n\t"
"ds_read_b64_tr_b16 v[72:73], v8 offset:40960\n\t"
"v_mfma_f32_16x16x32_bf16 a[136:139], v[232:235], v[128:131], a[136:139]\n\t"
"ds_read_b64_tr_b16 v[74:75], v8 offset:45056\n\t"
"v_mfma_f32_16x16x32_bf16 a[168:171], v[232:235], v[132:135], a[168:171]\n\t"
"ds_read_b64_tr_b16 v[76:77], v9 offset:40960\n\t"
"s_waitcnt lgkmcnt(8)\n\t"
"v_mfma_f32_16x16x32_bf16 a[140:143], v[236:239], v[128:131], a[140:143]\n\t"
"ds_read_b64_tr_b16 v[78:79], v9 offset:45056\n\t"
"v_mfma_f32_16x16x32_bf16 a[172:175], v[236:239], v[132:135], a[172:175]\n\t"
"ds_read_b64_tr_b16 v[80:81], v10 offset:40960\n\t"
"ds_read_b64_tr_b16 v[82:83], v10 offset:45056\n\t"
"v_mfma_f32_16x16x32_bf16 a[144:147], v[240:243], v[128:131], a[144:147]\n\t"
"ds_read_b64_tr_b16 v[84:85], v11 offset:40960\n\t"
"v_mfma_f32_16x16x32_bf16 a[176:179], v[240:243], v[132:135], a[176:179]\n\t"
"ds_read_b64_tr_b16 v[86:87], v11 offset:45056\n\t"
"v_mfma_f32_16x16x32_bf16 a[148:151], v[244:247], v[128:131], a[148:151]\n\t"
"ds_read_b64_tr_b16 v[88:89], v12 offset:40960\n\t"
"v_mfma_f32_16x16x32_bf16 a[180:183], v[244:247], v[132:135], a[180:183]\n\t"
"ds_read_b64_tr_b16 v[90:91], v12 offset:45056\n\t"
"v_mfma_f32_16x16x32_bf16 a[152:155], v[248:251], v[128:131], a[152:155]\n\t"
"ds_read_b64_tr_b16 v[92:93], v13 offset:40960\n\t"
"s_waitcnt lgkmcnt(9)\n\t"
"v_mfma_f32_16x16x32_bf16 a[184:187], v[248:251], v[132:135], a[184:187]\n\t"
"ds_read_b64_tr_b16 v[94:95], v13 offset:45056\n\t"
"v_mfma_f32_16x16x32_bf16 a[156:159], v[252:255], v[128:131], a[156:159]\n\t"
"v_mfma_f32_16x16x32_bf16 a[188:191], v[252:255], v[132:135], a[188:191]\n\t"
"v_mfma_f32_16x16x32_bf16 a[128:131], v[64:67], v[136:139], a[128:131]\n\t"
"ds_read_b64_tr_b16 v[224:225], v6 offset:49152\n\t"
"ds_read_b64_tr_b16 v[226:227], v6 offset:53248\n\t"
"v_mfma_f32_16x16x32_bf16 a[160:163], v[64:67], v[140:143], a[160:163]\n\t"
"ds_read_b64_tr_b16 v[228:229], v7 offset:49152\n\t"
"v_mfma_f32_16x16x32_bf16 a[132:135], v[68:71], v[136:139], a[132:135]\n\t"
"ds_read_b64_tr_b16 v[230:231], v7 offset:53248\n\t"
"v_mfma_f32_16x16x32_bf16 a[164:167], v[68:71], v[140:143], a[164:167]\n\t"
"ds_read_b64_tr_b16 v[232:233], v8 offset:49152\n\t"
"v_mfma_f32_16x16x32_bf16 a[136:139], v[72:75], v[136:139], a[136:139]\n\t"
"ds_read_b64_tr_b16 v[234:235], v8 offset:53248\n\t"
"s_waitcnt lgkmcnt(8)\n\t"
"v_mfma_f32_16x16x32_bf16 a[168:171], v[72:75], v[140:143], a[168:171]\n\t"
"ds_read_b64_tr_b16 v[236:237], v9 offset:49152\n\t"
"v_mfma_f32_16x16x32_bf16 a[140:143], v[76:79], v[136:139], a[140:143]\n\t"
"ds_read_b64_tr_b16 v[238:239], v9 offset:53248\n\t"
"v_mfma_f32_16x16x32_bf16 a[172:175], v[76:79], v[140:143], a[172:175]\n\t"
"ds_read_b64_tr_b16 v[240:241], v10 offset:49152\n\t"
"ds_read_b64_tr_b16 v[242:243], v10 offset:53248\n\t"
"v_mfma_f32_16x16x32_bf16 a[144:147], v[80:83], v[136:139], a[144:147]\n\t"
"ds_read_b64_tr_b16 v[244:245], v11 offset:49152\n\t"
"v_mfma_f32_16x16x32_bf16 a[176:179], v[80:83], v[140:143], a[176:179]\n\t"
"ds_read_b64_tr_b16 v[246:247], v11 offset:53248\n\t"
"v_mfma_f32_16x16x32_bf16 a[148:151], v[84:87], v[136:139], a[148:151]\n\t"
"ds_read_b64_tr_b16 v[248:249], v12 offset:49152\n\t"
"v_mfma_f32_16x16x32_bf16 a[180:183], v[84:87], v[140:143], a[180:183]\n\t"
"ds_read_b64_tr_b16 v[250:251], v12 offset:53248\n\t"
"s_waitcnt lgkmcnt(9)\n\t"
"v_mfma_f32_16x16x32_bf16 a[152:155], v[88:91], v[136:139], a[152:155]\n\t"
"ds_read_b64_tr_b16 v[252:253], v13 offset:49152\n\t"
"v_mfma_f32_16x16x32_bf16 a[184:187], v[88:91], v[140:143], a[184:187]\n\t"
"ds_read_b64_tr_b16 v[254:255], v13 offset:53248\n\t"
"v_mfma_f32_16x16x32_bf16 a[156:159], v[92:95], v[136:139], a[156:159]\n\t"
"v_mfma_f32_16x16x32_bf16 a[188:191], v[92:95], v[140:143], a[188:191]\n\t"
"s_and_b32 s70, s38, 7\n\t"
"s_lshl_b32 s70, s70, 4\n\t"
"s_add_u32 s69, s70, s41\n\t"
"s_lshl_b32 s69, s69, 7\n\t"
"s_add_u32 s69, s69, 8192\n\t"
"v_mbcnt_lo_u32_b32 v57, -1, 0\n\t"
"v_mbcnt_hi_u32_b32 v57, -1, v57\n\t"
"v_min_u32 v57, 15, v57\n\t"
"v_lshl_add_u32 v57, v57, 3, s69\n\t"
"global_load_dwordx2 v[58:59], v57, s[42:43] sc1\n\t"
"v_mfma_f32_16x16x32_bf16 a[128:131], v[224:227], v[144:147], a[128:131]\n\t"
"ds_read_b64_tr_b16 v[64:65], v6 offset:57344\n\t"
"ds_read_b64_tr_b16 v[66:67], v6 offset:61440\n\t"
"v_mfma_f32_16x16x32_bf16 a[160:163], v[224:227], v[148:151], a[160:163]\n\t"
"ds_read_b64_tr_b16 v[68:69], v7 offset:57344\n\t"
"v_mfma_f32_16x16x32_bf16 a[132:135], v[228:231], v[144:147], a[132:135]\n\t"
"ds_read_b64_tr_b16 v[70:71], v7 offset:61440\n\t"
"v_mfma_f32_16x16x32_bf16 a[164:167], v[228:231], v[148:151], a[164:167]\n\t"
"ds_read_b64_tr_b16 v[72:73], v8 offset:57344\n\t"
"s_waitcnt lgkmcnt(6)\n\t"
"v_mfma_f32_16x16x32_bf16 a[136:139], v[232:235], v[144:147], a[136:139]\n\t"
"ds_read_b64_tr_b16 v[74:75], v8 offset:61440\n\t"
"v_mfma_f32_16x16x32_bf16 a[168:171], v[232:235], v[148:151], a[168:171]\n\t"
"ds_read_b64_tr_b16 v[76:77], v9 offset:57344\n\t"
"v_mfma_f32_16x16x32_bf16 a[140:143], v[236:239], v[144:147], a[140:143]\n\t"
"ds_read_b64_tr_b16 v[78:79], v9 offset:61440\n\t"
"v_mfma_f32_16x16x32_bf16 a[172:175], v[236:239], v[148:151], a[172:175]\n\t"
"ds_read_b64_tr_b16 v[80:81], v10 offset:57344\n\t"
"ds_read_b64_tr_b16 v[82:83], v10 offset:61440\n\t"
"v_mfma_f32_16x16x32_bf16 a[144:147], v[240:243], v[144:147], a[144:147]\n\t"
"ds_read_b64_tr_b16 v[84:85], v11 offset:57344\n\t"
"v_mfma_f32_16x16x32_bf16 a[176:179], v[240:243], v[148:151], a[176:179]\n\t"
"ds_read_b64_tr_b16 v[86:87], v11 offset:61440\n\t"
"v_mfma_f32_16x16x32_bf16 a[148:151], v[244:247], v[144:147], a[148:151]\n\t"
"ds_read_b64_tr_b16 v[88:89], v12 offset:57344\n\t"
"v_mfma_f32_16x16x32_bf16 a[180:183], v[244:247], v[148:151], a[180:183]\n\t"
"ds_read_b64_tr_b16 v[90:91], v12 offset:61440\n\t"
"v_mfma_f32_16x16x32_bf16 a[152:155], v[248:251], v[144:147], a[152:155]\n\t"
"ds_read_b64_tr_b16 v[92:93], v13 offset:57344\n\t"
"s_waitcnt lgkmcnt(9)\n\t"
"v_mfma_f32_16x16x32_bf16 a[184:187], v[248:251], v[148:151], a[184:187]\n\t"
"ds_read_b64_tr_b16 v[94:95], v13 offset:61440\n\t"
"v_mfma_f32_16x16x32_bf16 a[156:159], v[252:255], v[144:147], a[156:159]\n\t"
"v_mfma_f32_16x16x32_bf16 a[188:191], v[252:255], v[148:151], a[188:191]\n\t"
"s_waitcnt vmcnt(17)\n\t"
"s_barrier\n\t"
"v_mfma_f32_16x16x32_bf16 a[128:131], v[64:67], v[152:155], a[128:131]\n\t"
"v_add_u32 v6, 65536, v6\n\t"
"v_add_u32 v7, 65536, v7\n\t"
"v_mfma_f32_16x16x32_bf16 a[160:163], v[64:67], v[156:159], a[160:163]\n\t"
"v_add_u32 v8, 65536, v8\n\t"
"v_add_u32 v9, 65536, v9\n\t"
"v_mfma_f32_16x16x32_bf16 a[132:135], v[68:71], v[152:155], a[132:135]\n\t"
"v_add_u32 v10, 65536, v10\n\t"
"v_add_u32 v11, 65536, v11\n\t"
"v_mfma_f32_16x16x32_bf16 a[164:167], v[68:71], v[156:159], a[164:167]\n\t"
"v_add_u32 v12, 65536, v12\n\t"
"v_mfma_f32_16x16x32_bf16 a[136:139], v[72:75], v[152:155], a[136:139]\n\t"
"v_add_u32 v13, 65536, v13\n\t"
"ds_read_b64_tr_b16 v[224:225], v6 offset:0\n\t"
"v_mfma_f32_16x16x32_bf16 a[168:171], v[72:75], v[156:159], a[168:171]\n\t"
"ds_read_b64_tr_b16 v[226:227], v6 offset:4096\n\t"
"ds_read_b64_tr_b16 v[228:229], v7 offset:0\n\t"
"s_waitcnt lgkmcnt(3)\n\t"
"v_mfma_f32_16x16x32_bf16 a[140:143], v[76:79], v[152:155], a[140:143]\n\t"
"ds_read_b64_tr_b16 v[230:231], v7 offset:4096\n\t"
"v_mfma_f32_16x16x32_bf16 a[172:175], v[76:79], v[156:159], a[172:175]\n\t"
"ds_read_b64_tr_b16 v[232:233], v8 offset:0\n\t"
"ds_read_b64_tr_b16 v[234:235], v8 offset:4096\n\t"
"v_mfma_f32_16x16x32_bf16 a[144:147], v[80:83], v[152:155], a[144:147]\n\t"
"ds_read_b64_tr_b16 v[236:237], v9 offset:0\n\t"
"ds_read_b64_tr_b16 v[238:239], v9 offset:4096\n\t"
"v_mfma_f32_16x16x32_bf16 a[176:179], v[80:83], v[156:159], a[176:179]\n\t"
"ds_read_b64_tr_b16 v[240:241], v10 offset:0\n\t"
"ds_read_b64_tr_b16 v[242:243], v10 offset:4096\n\t"
"v_mfma_f32_16x16x32_bf16 a[148:151], v[84:87], v[152:155], a[148:151]\n\t"
"ds_read_b64_tr_b16 v[244:245], v11 offset:0\n\t"
"v_mfma_f32_16x16x32_bf16 a[180:183], v[84:87], v[156:159], a[180:183]\n\t"
"ds_read_b64_tr_b16 v[246:247], v11 offset:4096\n\t"
"ds_read_b64_tr_b16 v[248:249], v12 offset:0\n\t"
"v_mfma_f32_16x16x32_bf16 a[152:155], v[88:91], v[152:155], a[152:155]\n\t"
"ds_read_b64_tr_b16 v[250:251], v12 offset:4096\n\t"
"ds_read_b64_tr_b16 v[252:253], v13 offset:0\n\t"
"v_mfma_f32_16x16x32_bf16 a[184:187], v[88:91], v[156:159], a[184:187]\n\t"
"ds_read_b64_tr_b16 v[254:255], v13 offset:4096\n\t"
"s_waitcnt lgkmcnt(12)\n\t"
"v_mfma_f32_16x16x32_bf16 a[156:159], v[92:95], v[152:155], a[156:159]\n\t"
"v_mfma_f32_16x16x32_bf16 a[188:191], v[92:95], v[156:159], a[188:191]\n\t"
"v_mfma_f32_16x16x32_bf16 a[128:131], v[224:227], v[160:163], a[128:131]\n\t"
"ds_read_b64_tr_b16 v[64:65], v6 offset:8192\n\t"
"ds_read_b64_tr_b16 v[66:67], v6 offset:12288\n\t"
"v_mfma_f32_16x16x32_bf16 a[160:163], v[224:227], v[164:167], a[160:163]\n\t"
"ds_read_b64_tr_b16 v[68:69], v7 offset:8192\n\t"
"v_mfma_f32_16x16x32_bf16 a[132:135], v[228:231], v[160:163], a[132:135]\n\t"
"ds_read_b64_tr_b16 v[70:71], v7 offset:12288\n\t"
"s_waitcnt lgkmcnt(9)\n\t"
"v_mfma_f32_16x16x32_bf16 a[164:167], v[228:231], v[164:167], a[164:167]\n\t"
"ds_read_b64_tr_b16 v[72:73], v8 offset:8192\n\t"
"v_mfma_f32_16x16x32_bf16 a[136:139], v[232:235], v[160:163], a[136:139]\n\t"
"ds_read_b64_tr_b16 v[74:75], v8 offset:12288\n\t"
"v_mfma_f32_16x16x32_bf16 a[168:171], v[232:235], v[164:167], a[168:171]\n\t"
"ds_read_b64_tr_b16 v[76:77], v9 offset:8192\n\t"
"v_mfma_f32_16x16x32_bf16 a[140:143], v[236:239], v[160:163], a[140:143]\n\t"
"ds_read_b64_tr_b16 v[78:79], v9 offset:12288\n\t"
"v_mfma_f32_16x16x32_bf16 a[172:175], v[236:239], v[164:167], a[172:175]\n\t"
"ds_read_b64_tr_b16 v[80:81], v10 offset:8192\n\t"
"ds_read_b64_tr_b16 v[82:83], v10 offset:12288\n\t"
"v_mfma_f32_16x16x32_bf16 a[144:147], v[240:243], v[160:163], a[144:147]\n\t"
"ds_read_b64_tr_b16 v[84:85], v11 offset:8192\n\t"
"s_waitcnt lgkmcnt(9)\n\t"
"v_mfma_f32_16x16x32_bf16 a[176:179], v[240:243], v[164:167], a[176:179]\n\t"
"ds_read_b64_tr_b16 v[86:87], v11 offset:12288\n\t"
"v_mfma_f32_16x16x32_bf16 a[148:151], v[244:247], v[160:163], a[148:151]\n\t"
"ds_read_b64_tr_b16 v[88:89], v12 offset:8192\n\t"
"v_mfma_f32_16x16x32_bf16 a[180:183], v[244:247], v[164:167], a[180:183]\n\t"
"ds_read_b64_tr_b16 v[90:91], v12 offset:12288\n\t"
"v_mfma_f32_16x16x32_bf16 a[152:155], v[248:251], v[160:163], a[152:155]\n\t"
"ds_read_b64_tr_b16 v[92:93], v13 offset:8192\n\t"
"v_mfma_f32_16x16x32_bf16 a[184:187], v[248:251], v[164:167], a[184:187]\n\t"
"ds_read_b64_tr_b16 v[94:95], v13 offset:12288\n\t"
"v_mfma_f32_16x16x32_bf16 a[156:159], v[252:255], v[160:163], a[156:159]\n\t"
"v_mfma_f32_16x16x32_bf16 a[188:191], v[252:255], v[164:167], a[188:191]\n\t"
"s_mov_b32 s70, 0\n\t"
"s_waitcnt vmcnt(0)\n\t"
"LFP38_%=:\n\t"
"v_cmp_eq_u64 vcc, s[26:27], v[58:59]\n\t"
"s_nop 1\n\t"
"s_cmp_eq_u64 vcc, exec\n\t"
"s_cbranch_scc1 LFO39_%=\n\t"
"s_sleep 1\n\t"
"global_load_dwordx2 v[58:59], v57, s[42:43] sc1\n\t"
"s_waitcnt vmcnt(0)\n\t"
"s_add_u32 s70, s70, 1\n\t"
"s_cmp_lt_u32 s70, 0x40000\n\t"
"s_cbranch_scc1 LFP38_%=\n\t"
"LFO39_%=:\n\t"
"v_mfma_f32_16x16x32_bf16 a[128:131], v[64:67], v[168:171], a[128:131]\n\t"
"ds_read_b64_tr_b16 v[224:225], v6 offset:16384\n\t"
"s_add_u32 s69, s65, 0\n\t"
"v_mfma_f32_16x16x32_bf16 a[160:163], v[64:67], v[172:175], a[160:163]\n\t"
"ds_read_b64_tr_b16 v[226:227], v6 offset:20480\n\t"
"s_waitcnt lgkmcnt(4)\n\t"
"buffer_load_dwordx4 a[0:3], v0, s[48:51], s69 offen offset:0 sc1\n\t"
"v_mfma_f32_16x16x32_bf16 a[132:135], v[68:71], v[168:171], a[132:135]\n\t"
"ds_read_b64_tr_b16 v[228:229], v7 offset:16384\n\t"
"ds_read_b64_tr_b16 v[230:231], v7 offset:20480\n\t"
"v_mfma_f32_16x16x32_bf16 a[164:167], v[68:71], v[172:175], a[164:167]\n\t"
"buffer_load_dwordx4 a[8:11], v0, s[48:51], s69 offen offset:1024 sc1\n\t"
"ds_read_b64_tr_b16 v[232:233], v8 offset:16384\n\t"
"v_mfma_f32_16x16x32_bf16 a[136:139], v[72:75], v[168:171], a[136:139]\n\t"
"buffer_load_dwordx4 a[16:19], v0, s[48:51], s69 offen offset:2048 sc1\n\t"
"ds_read_b64_tr_b16 v[234:235], v8 offset:20480\n\t"
"v_mfma_f32_16x16x32_bf16 a[168:171], v[72:75], v[172:175], a[168:171]\n\t"
"ds_read_b64_tr_b16 v[236:237], v9 offset:16384\n\t"
"buffer_load_dwordx4 a[24:27], v0, s[48:51], s69 offen offset:3072 sc1\n\t"
"v_mfma_f32_16x16x32_bf16 a[140:143], v[76:79], v[168:171], a[140:143]\n\t"
"ds_read_b64_tr_b16 v[238:239], v9 offset:20480\n\t"
"v_mfma_f32_16x16x32_bf16 a[172:175], v[76:79], v[172:175], a[172:175]\n\t"
"ds_read_b64_tr_b16 v[240:241], v10 offset:16384\n\t"
"s_add_u32 s69, s65, 131072\n\t"
"v_mfma_f32_16x16x32_bf16 a[144:147], v[80:83], v[168:171], a[144:147]\n\t"
"ds_read_b64_tr_b16 v[242:243], v10 offset:20480\n\t"
"buffer_load_dwordx4 a[4:7], v0, s[48:51], s69 offen offset:0 sc1\n\t"
"v_mfma_f32_16x16x32_bf16 a[176:179], v[80:83], v[172:175], a[176:179]\n\t"
"ds_read_b64_tr_b16 v[244:245], v11 offset:16384\n\t"
"ds_read_b64_tr_b16 v[246:247], v11 offset:20480\n\t"
"v_mfma_f32_16x16x32_bf16 a[148:151], v[84:87], v[168:171], a[148:151]\n\t"
"buffer_load_dwordx4 a[12:15], v0, s[48:51], s69 offen offset:1024 sc1\n\t"
"ds_read_b64_tr_b16 v[248:249], v12 offset:16384\n\t"
"v_mfma_f32_16x16x32_bf16 a[180:183], v[84:87], v[172:175], a[180:183]\n\t"
"buffer_load_dwordx4 a[20:23], v0, s[48:51], s69 offen offset:2048 sc1\n\t"
"ds_read_b64_tr_b16 v[250:251], v12 offset:20480\n\t"
"s_waitcnt lgkmcnt(8)\n\t"
"v_mfma_f32_16x16x32_bf16 a[152:155], v[88:91], v[168:171], a[152:155]\n\t"
"ds_read_b64_tr_b16 v[252:253], v13 offset:16384\n\t"
"buffer_load_dwordx4 a[28:31], v0, s[48:51], s69 offen offset:3072 sc1\n\t"
"v_mfma_f32_16x16x32_bf16 a[184:187], v[88:91], v[172:175], a[184:187]\n\t"
"ds_read_b64_tr_b16 v[254:255], v13 offset:20480\n\t"
"v_mfma_f32_16x16x32_bf16 a[156:159], v[92:95], v[168:171], a[156:159]\n\t"
"v_mfma_f32_16x16x32_bf16 a[188:191], v[92:95], v[172:175], a[188:191]\n\t"
"v_mfma_f32_16x16x32_bf16 a[128:131], v[224:227], v[176:179], a[128:131]\n\t"
"ds_read_b64_tr_b16 v[64:65], v6 offset:24576\n\t"
"s_add_u32 s69, s65, 4096\n\t"
"v_mfma_f32_16x16x32_bf16 a[160:163], v[224:227], v[180:183], a[160:163]\n\t"
"ds_read_b64_tr_b16 v[66:67], v6 offset:28672\n\t"
"buffer_load_dwordx4 a[32:35], v0, s[48:51], s69 offen offset:0 sc1\n\t"
"v_mfma_f32_16x16x32_bf16 a[132:135], v[228:231], v[176:179], a[132:135]\n\t"
"ds_read_b64_tr_b16 v[68:69], v7 offset:24576\n\t"
"ds_read_b64_tr_b16 v[70:71], v7 offset:28672\n\t"
"v_mfma_f32_16x16x32_bf16 a[164:167], v[228:231], v[180:183], a[164:167]\n\t"
"buffer_load_dwordx4 a[40:43], v0, s[48:51], s69 offen offset:1024 sc1\n\t"
"ds_read_b64_tr_b16 v[72:73], v8 offset:24576\n\t"
"v_mfma_f32_16x16x32_bf16 a[136:139], v[232:235], v[176:179], a[136:139]\n\t"
"buffer_load_dwordx4 a[48:51], v0, s[48:51], s69 offen offset:2048 sc1\n\t"
"ds_read_b64_tr_b16 v[74:75], v8 offset:28672\n\t"
"s_waitcnt lgkmcnt(7)\n\t"
"v_mfma_f32_16x16x32_bf16 a[168:171], v[232:235], v[180:183], a[168:171]\n\t"
"ds_read_b64_tr_b16 v[76:77], v9 offset:24576\n\t"
"buffer_load_dwordx4 a[56:59], v0, s[48:51], s69 offen offset:3072 sc1\n\t"
"v_mfma_f32_16x16x32_bf16 a[140:143], v[236:239], v[176:179], a[140:143]\n\t"
"ds_read_b64_tr_b16 v[78:79], v9 offset:28672\n\t"
"v_mfma_f32_16x16x32_bf16 a[172:175], v[236:239], v[180:183], a[172:175]\n\t"
"ds_read_b64_tr_b16 v[80:81], v10 offset:24576\n\t"
"s_add_u32 s69, s65, 135168\n\t"
"v_mfma_f32_16x16x32_bf16 a[144:147], v[240:243], v[176:179], a[144:147]\n\t"
"ds_read_b64_tr_b16 v[82:83], v10 offset:28672\n\t"
"buffer_load_dwordx4 a[36:39], v0, s[48:51], s69 offen offset:0 sc1\n\t"
"v_mfma_f32_16x16x32_bf16 a[176:179], v[240:243], v[180:183], a[176:179]\n\t"
"ds_read_b64_tr_b16 v[84:85], v11 offset:24576\n\t"
"ds_read_b64_tr_b16 v[86:87], v11 offset:28672\n\t"
"v_mfma_f32_16x16x32_bf16 a[148:151], v[244:247], v[176:179], a[148:151]\n\t"
"buffer_load_dwordx4 a[44:47], v0, s[48:51], s69 offen offset:1024 sc1\n\t"
"ds_read_b64_tr_b16 v[88:89], v12 offset:24576\n\t"
"v_mfma_f32_16x16x32_bf16 a[180:183], v[244:247], v[180:183], a[180:183]\n\t"
"buffer_load_dwordx4 a[52:55], v0, s[48:51], s69 offen offset:2048 sc1\n\t"
"ds_read_b64_tr_b16 v[90:91], v12 offset:28672\n\t"
"v_mfma_f32_16x16x32_bf16 a[152:155], v[248:251], v[176:179], a[152:155]\n\t"
"ds_read_b64_tr_b16 v[92:93], v13 offset:24576\n\t"
"s_waitcnt lgkmcnt(8)\n\t"
"buffer_load_dwordx4 a[60:63], v0, s[48:51], s69 offen offset:3072 sc1\n\t"
"v_mfma_f32_16x16x32_bf16 a[184:187], v[248:251], v[180:183], a[184:187]\n\t"
"ds_read_b64_tr_b16 v[94:95], v13 offset:28672\n\t"
"v_mfma_f32_16x16x32_bf16 a[156:159], v[252:255], v[176:179], a[156:159]\n\t"
"v_mfma_f32_16x16x32_bf16 a[188:191], v[252:255], v[180:183], a[188:191]\n\t"
"s_waitcnt vmcnt(17)\n\t"
"s_barrier\n\t"
"v_mfma_f32_16x16x32_bf16 a[128:131], v[64:67], v[184:187], a[128:131]\n\t"
"ds_read_b64_tr_b16 v[224:225], v6 offset:32768\n\t"
"s_add_u32 s69, s65, 8192\n\t"
"v_mfma_f32_16x16x32_bf16 a[160:163], v[64:67], v[188:191], a[160:163]\n\t"
"ds_read_b64_tr_b16 v[226:227], v6 offset:36864\n\t"
"buffer_load_dwordx4 a[64:67], v0, s[48:51], s69 offen offset:0 sc1\n\t"
"v_mfma_f32_16x16x32_bf16 a[132:135], v[68:71], v[184:187], a[132:135]\n\t"
"ds_read_b64_tr_b16 v[228:229], v7 offset:32768\n\t"
"ds_read_b64_tr_b16 v[230:231], v7 offset:36864\n\t"
"v_mfma_f32_16x16x32_bf16 a[164:167], v[68:71], v[188:191], a[164:167]\n\t"
"buffer_load_dwordx4 a[72:75], v0, s[48:51], s69 offen offset:1024 sc1\n\t"
"ds_read_b64_tr_b16 v[232:233], v8 offset:32768\n\t"
"v_mfma_f32_16x16x32_bf16 a[136:139], v[72:75], v[184:187], a[136:139]\n\t"
"buffer_load_dwordx4 a[80:83], v0, s[48:51], s69 offen offset:2048 sc1\n\t"
"ds_read_b64_tr_b16 v[234:235], v8 offset:36864\n\t"
"v_mfma_f32_16x16x32_bf16 a[168:171], v[72:75], v[188:191], a[168:171]\n\t"
"ds_read_b64_tr_b16 v[236:237], v9 offset:32768\n\t"
"s_waitcnt lgkmcnt(7)\n\t"
"buffer_load_dwordx4 a[88:91], v0, s[48:51], s69 offen offset:3072 sc1\n\t"
"v_mfma_f32_16x16x32_bf16 a[140:143], v[76:79], v[184:187], a[140:143]\n\t"
"ds_read_b64_tr_b16 v[238:239], v9 offset:36864\n\t"
"v_mfma_f32_16x16x32_bf16 a[172:175], v[76:79], v[188:191], a[172:175]\n\t"
"ds_read_b64_tr_b16 v[240:241], v10 offset:32768\n\t"
"s_add_u32 s69, s65, 139264\n\t"
"v_mfma_f32_16x16x32_bf16 a[144:147], v[80:83], v[184:187], a[144:147]\n\t"
"ds_read_b64_tr_b16 v[242:243], v10 offset:36864\n\t"
"buffer_load_dwordx4 a[68:71], v0, s[48:51], s69 offen offset:0 sc1\n\t"
"v_mfma_f32_16x16x32_bf16 a[176:179], v[80:83], v[188:191], a[176:179]\n\t"
"ds_read_b64_tr_b16 v[244:245], v11 offset:32768\n\t"
"ds_read_b64_tr_b16 v[246:247], v11 offset:36864\n\t"
"v_mfma_f32_16x16x32_bf16 a[148:151], v[84:87], v[184:187], a[148:151]\n\t"
"buffer_load_dwordx4 a[76:79], v0, s[48:51], s69 offen offset:1024 sc1\n\t"
"ds_read_b64_tr_b16 v[248:249], v12 offset:32768\n\t"
"v_mfma_f32_16x16x32_bf16 a[180:183], v[84:87], v[188:191], a[180:183]\n\t"
"buffer_load_dwordx4 a[84:87], v0, s[48:51], s69 offen offset:2048 sc1\n\t"
"ds_read_b64_tr_b16 v[250:251], v12 offset:36864\n\t"
"v_mfma_f32_16x16x32_bf16 a[152:155], v[88:91], v[184:187], a[152:155]\n\t"
"ds_read_b64_tr_b16 v[252:253], v13 offset:32768\n\t"
"buffer_load_dwordx4 a[92:95], v0, s[48:51], s69 offen offset:3072 sc1\n\t"
"v_mfma_f32_16x16x32_bf16 a[184:187], v[88:91], v[188:191], a[184:187]\n\t"
"ds_read_b64_tr_b16 v[254:255], v13 offset:36864\n\t"
"s_waitcnt lgkmcnt(8)\n\t"
"v_mfma_f32_16x16x32_bf16 a[156:159], v[92:95], v[184:187], a[156:159]\n\t"
"v_mfma_f32_16x16x32_bf16 a[188:191], v[92:95], v[188:191], a[188:191]\n\t"
"v_mfma_f32_16x16x32_bf16 a[128:131], v[224:227], v[192:195], a[128:131]\n\t"
"ds_read_b64_tr_b16 v[64:65], v6 offset:40960\n\t"
"s_add_u32 s69, s65, 12288\n\t"
"v_mfma_f32_16x16x32_bf16 a[160:163], v[224:227], v[196:199], a[160:163]\n\t"
"ds_read_b64_tr_b16 v[66:67], v6 offset:45056\n\t"
"buffer_load_dwordx4 a[96:99], v0, s[48:51], s69 offen offset:0 sc1\n\t"
"v_mfma_f32_16x16x32_bf16 a[132:135], v[228:231], v[192:195], a[132:135]\n\t"
"ds_read_b64_tr_b16 v[68:69], v7 offset:40960\n\t"
"ds_read_b64_tr_b16 v[70:71], v7 offset:45056\n\t"
"v_mfma_f32_16x16x32_bf16 a[164:167], v[228:231], v[196:199], a[164:167]\n\t"
"buffer_load_dwordx4 a[104:107], v0, s[48:51], s69 offen offset:1024 sc1\n\t"
"ds_read_b64_tr_b16 v[72:73], v8 offset:40960\n\t"
"v_mfma_f32_16x16x32_bf16 a[136:139], v[232:235], v[192:195], a[136:139]\n\t"
"buffer_load_dwordx4 a[112:115], v0, s[48:51], s69 offen offset:2048 sc1\n\t"
"ds_read_b64_tr_b16 v[74:75], v8 offset:45056\n\t"
"v_mfma_f32_16x16x32_bf16 a[168:171], v[232:235], v[196:199], a[168:171]\n\t"
"ds_read_b64_tr_b16 v[76:77], v9 offset:40960\n\t"
"buffer_load_dwordx4 a[120:123], v0, s[48:51], s69 offen offset:3072 sc1\n\t"
"v_mfma_f32_16x16x32_bf16 a[140:143], v[236:239], v[192:195], a[140:143]\n\t"
"ds_read_b64_tr_b16 v[78:79], v9 offset:45056\n\t"
"s_waitcnt lgkmcnt(8)\n\t"
"v_mfma_f32_16x16x32_bf16 a[172:175], v[236:239], v[196:199], a[172:175]\n\t"
"ds_read_b64_tr_b16 v[80:81], v10 offset:40960\n\t"
"s_add_u32 s69, s65, 143360\n\t"
"v_mfma_f32_16x16x32_bf16 a[144:147], v[240:243], v[192:195], a[144:147]\n\t"
"ds_read_b64_tr_b16 v[82:83], v10 offset:45056\n\t"
"buffer_load_dwordx4 a[100:103], v0, s[48:51], s69 offen offset:0 sc1\n\t"
"v_mfma_f32_16x16x32_bf16 a[176:179], v[240:243], v[196:199], a[176:179]\n\t"
"ds_read_b64_tr_b16 v[84:85], v11 offset:40960\n\t"
"ds_read_b64_tr_b16 v[86:87], v11 offset:45056\n\t"
"v_mfma_f32_16x16x32_bf16 a[148:151], v[244:247], v[192:195], a[148:151]\n\t"
"buffer_load_dwordx4 a[108:111], v0, s[48:51], s69 offen offset:1024 sc1\n\t"
"ds_read_b64_tr_b16 v[88:89], v12 offset:40960\n\t"
"v_mfma_f32_16x16x32_bf16 a[180:183], v[244:247], v[196:199], a[180:183]\n\t"
"buffer_load_dwordx4 a[116:119], v0, s[48:51], s69 offen offset:2048 sc1\n\t"
"ds_read_b64_tr_b16 v[90:91], v12 offset:45056\n\t"
"v_mfma_f32_16x16x32_bf16 a[152:155], v[248:251], v[192:195], a[152:155]\n\t"
"ds_read_b64_tr_b16 v[92:93], v13 offset:40960\n\t"
"buffer_load_dwordx4 a[124:127], v0, s[48:51], s69 offen offset:3072 sc1\n\t"
"v_mfma_f32_16x16x32_bf16 a[184:187], v[248:251], v[196:199], a[184:187]\n\t"
"ds_read_b64_tr_b16 v[94:95], v13 offset:45056\n\t"
"s_waitcnt lgkmcnt(8)\n\t"
"v_mfma_f32_16x16x32_bf16 a[156:159], v[252:255], v[192:195], a[156:159]\n\t"
"v_mfma_f32_16x16x32_bf16 a[188:191], v[252:255], v[196:199], a[188:191]\n\t"
"v_mfma_f32_16x16x32_bf16 a[128:131], v[64:67], v[200:203], a[128:131]\n\t"
"ds_read_b64_tr_b16 v[224:225], v6 offset:49152\n\t"
"ds_read_b64_tr_b16 v[226:227], v6 offset:53248\n\t"
"v_mfma_f32_16x16x32_bf16 a[160:163], v[64:67], v[204:207], a[160:163]\n\t"
"ds_read_b64_tr_b16 v[228:229], v7 offset:49152\n\t"
"v_mfma_f32_16x16x32_bf16 a[132:135], v[68:71], v[200:203], a[132:135]\n\t"
"ds_read_b64_tr_b16 v[230:231], v7 offset:53248\n\t"
"v_mfma_f32_16x16x32_bf16 a[164:167], v[68:71], v[204:207], a[164:167]\n\t"
"ds_read_b64_tr_b16 v[232:233], v8 offset:49152\n\t"
"v_mfma_f32_16x16x32_bf16 a[136:139], v[72:75], v[200:203], a[136:139]\n\t"
"ds_read_b64_tr_b16 v[234:235], v8 offset:53248\n\t"
"v_mfma_f32_16x16x32_bf16 a[168:171], v[72:75], v[204:207], a[168:171]\n\t"
"ds_read_b64_tr_b16 v[236:237], v9 offset:49152\n\t"
"v_mfma_f32_16x16x32_bf16 a[140:143], v[76:79], v[200:203], a[140:143]\n\t"
"ds_read_b64_tr_b16 v[238:239], v9 offset:53248\n\t"
"s_waitcnt lgkmcnt(8)\n\t"
"v_mfma_f32_16x16x32_bf16 a[172:175], v[76:79], v[204:207], a[172:175]\n\t"
"ds_read_b64_tr_b16 v[240:241], v10 offset:49152\n\t"
"ds_read_b64_tr_b16 v[242:243], v10 offset:53248\n\t"
"v_mfma_f32_16x16x32_bf16 a[144:147], v[80:83], v[200:203], a[144:147]\n\t"
"ds_read_b64_tr_b16 v[244:245], v11 offset:49152\n\t"
"v_mfma_f32_16x16x32_bf16 a[176:179], v[80:83], v[204:207], a[176:179]\n\t"
"ds_read_b64_tr_b16 v[246:247], v11 offset:53248\n\t"
"v_mfma_f32_16x16x32_bf16 a[148:151], v[84:87], v[200:203], a[148:151]\n\t"
"ds_read_b64_tr_b16 v[248:249], v12 offset:49152\n\t"
"v_mfma_f32_16x16x32_bf16 a[180:183], v[84:87], v[204:207], a[180:183]\n\t"
"ds_read_b64_tr_b16 v[250:251], v12 offset:53248\n\t"
"v_mfma_f32_16x16x32_bf16 a[152:155], v[88:91], v[200:203], a[152:155]\n\t"
"ds_read_b64_tr_b16 v[252:253], v13 offset:49152\n\t"
"v_mfma_f32_16x16x32_bf16 a[184:187], v[88:91], v[204:207], a[184:187]\n\t"
"ds_read_b64_tr_b16 v[254:255], v13 offset:53248\n\t"
"s_waitcnt lgkmcnt(9)\n\t"
"v_mfma_f32_16x16x32_bf16 a[156:159], v[92:95], v[200:203], a[156:159]\n\t"
"v_mfma_f32_16x16x32_bf16 a[188:191], v[92:95], v[204:207], a[188:191]\n\t"
"v_mfma_f32_16x16x32_bf16 a[128:131], v[224:227], v[208:211], a[128:131]\n\t"
"ds_read_b64_tr_b16 v[64:65], v6 offset:57344\n\t"
"ds_read_b64_tr_b16 v[66:67], v6 offset:61440\n\t"
"v_mfma_f32_16x16x32_bf16 a[160:163], v[224:227], v[212:215], a[160:163]\n\t"
"ds_read_b64_tr_b16 v[68:69], v7 offset:57344\n\t"
"v_mfma_f32_16x16x32_bf16 a[132:135], v[228:231], v[208:211], a[132:135]\n\t"
"ds_read_b64_tr_b16 v[70:71], v7 offset:61440\n\t"
"v_mfma_f32_16x16x32_bf16 a[164:167], v[228:231], v[212:215], a[164:167]\n\t"
"ds_read_b64_tr_b16 v[72:73], v8 offset:57344\n\t"
"v_mfma_f32_16x16x32_bf16 a[136:139], v[232:235], v[208:211], a[136:139]\n\t"
"ds_read_b64_tr_b16 v[74:75], v8 offset:61440\n\t"
"v_mfma_f32_16x16x32_bf16 a[168:171], v[232:235], v[212:215], a[168:171]\n\t"
"ds_read_b64_tr_b16 v[76:77], v9 offset:57344\n\t"
"s_waitcnt lgkmcnt(8)\n\t"
"v_mfma_f32_16x16x32_bf16 a[140:143], v[236:239], v[208:211], a[140:143]\n\t"
"ds_read_b64_tr_b16 v[78:79], v9 offset:61440\n\t"
"v_mfma_f32_16x16x32_bf16 a[172:175], v[236:239], v[212:215], a[172:175]\n\t"
"ds_read_b64_tr_b16 v[80:81], v10 offset:57344\n\t"
"ds_read_b64_tr_b16 v[82:83], v10 offset:61440\n\t"
"v_mfma_f32_16x16x32_bf16 a[144:147], v[240:243], v[208:211], a[144:147]\n\t"
"ds_read_b64_tr_b16 v[84:85], v11 offset:57344\n\t"
"v_mfma_f32_16x16x32_bf16 a[176:179], v[240:243], v[212:215], a[176:179]\n\t"
"ds_read_b64_tr_b16 v[86:87], v11 offset:61440\n\t"
"v_mfma_f32_16x16x32_bf16 a[148:151], v[244:247], v[208:211], a[148:151]\n\t"
"ds_read_b64_tr_b16 v[88:89], v12 offset:57344\n\t"
"v_mfma_f32_16x16x32_bf16 a[180:183], v[244:247], v[212:215], a[180:183]\n\t"
"ds_read_b64_tr_b16 v[90:91], v12 offset:61440\n\t"
"v_mfma_f32_16x16x32_bf16 a[152:155], v[248:251], v[208:211], a[152:155]\n\t"
"ds_read_b64_tr_b16 v[92:93], v13 offset:57344\n\t"
"s_waitcnt lgkmcnt(9)\n\t"
"v_mfma_f32_16x16x32_bf16 a[184:187], v[248:251], v[212:215], a[184:187]\n\t"
"ds_read_b64_tr_b16 v[94:95], v13 offset:61440\n\t"
"v_mfma_f32_16x16x32_bf16 a[156:159], v[252:255], v[208:211], a[156:159]\n\t"
"v_mfma_f32_16x16x32_bf16 a[188:191], v[252:255], v[212:215], a[188:191]\n\t"
"v_mfma_f32_16x16x32_bf16 a[128:131], v[64:67], v[216:219], a[128:131]\n\t"
"v_add_u32 v6, -65536, v6\n\t"
"v_add_u32 v7, -65536, v7\n\t"
"v_mfma_f32_16x16x32_bf16 a[160:163], v[64:67], v[220:223], a[160:163]\n\t"
"v_add_u32 v8, -65536, v8\n\t"
"v_add_u32 v9, -65536, v9\n\t"
"v_mfma_f32_16x16x32_bf16 a[132:135], v[68:71], v[216:219], a[132:135]\n\t"
"v_add_u32 v10, -65536, v10\n\t"
"v_add_u32 v11, -65536, v11\n\t"
"v_mfma_f32_16x16x32_bf16 a[164:167], v[68:71], v[220:223], a[164:167]\n\t"
"v_add_u32 v12, -65536, v12\n\t"
"v_mfma_f32_16x16x32_bf16 a[136:139], v[72:75], v[216:219], a[136:139]\n\t"
"v_add_u32 v13, -65536, v13\n\t"
"ds_read_b64_tr_b16 v[224:225], v6 offset:0\n\t"
"v_mfma_f32_16x16x32_bf16 a[168:171], v[72:75], v[220:223], a[168:171]\n\t"
"ds_read_b64_tr_b16 v[226:227], v6 offset:4096\n\t"
"ds_read_b64_tr_b16 v[228:229], v7 offset:0\n\t"
"s_waitcnt lgkmcnt(3)\n\t"
"v_mfma_f32_16x16x32_bf16 a[140:143], v[76:79], v[216:219], a[140:143]\n\t"
"ds_read_b64_tr_b16 v[230:231], v7 offset:4096\n\t"
"v_mfma_f32_16x16x32_bf16 a[172:175], v[76:79], v[220:223], a[172:175]\n\t"
"ds_read_b64_tr_b16 v[232:233], v8 offset:0\n\t"
"ds_read_b64_tr_b16 v[234:235], v8 offset:4096\n\t"
"v_mfma_f32_16x16x32_bf16 a[144:147], v[80:83], v[216:219], a[144:147]\n\t"
"ds_read_b64_tr_b16 v[236:237], v9 offset:0\n\t"
"ds_read_b64_tr_b16 v[238:239], v9 offset:4096\n\t"
"v_mfma_f32_16x16x32_bf16 a[176:179], v[80:83], v[220:223], a[176:179]\n\t"
"ds_read_b64_tr_b16 v[240:241], v10 offset:0\n\t"
"ds_read_b64_tr_b16 v[242:243], v10 offset:4096\n\t"
"v_mfma_f32_16x16x32_bf16 a[148:151], v[84:87], v[216:219], a[148:151]\n\t"
"ds_read_b64_tr_b16 v[244:245], v11 offset:0\n\t"
"v_mfma_f32_16x16x32_bf16 a[180:183], v[84:87], v[220:223], a[180:183]\n\t"
"ds_read_b64_tr_b16 v[246:247], v11 offset:4096\n\t"
"ds_read_b64_tr_b16 v[248:249], v12 offset:0\n\t"
"v_mfma_f32_16x16x32_bf16 a[152:155], v[88:91], v[216:219], a[152:155]\n\t"
"ds_read_b64_tr_b16 v[250:251], v12 offset:4096\n\t"
"ds_read_b64_tr_b16 v[252:253], v13 offset:0\n\t"
"v_mfma_f32_16x16x32_bf16 a[184:187], v[88:91], v[220:223], a[184:187]\n\t"
"ds_read_b64_tr_b16 v[254:255], v13 offset:4096\n\t"
"s_waitcnt lgkmcnt(12)\n\t"
"v_mfma_f32_16x16x32_bf16 a[156:159], v[92:95], v[216:219], a[156:159]\n\t"
"v_mfma_f32_16x16x32_bf16 a[188:191], v[92:95], v[220:223], a[188:191]\n\t"
"s_waitcnt vmcnt(24)\n\t"
"v_mfma_f32_16x16x32_bf16 a[192:195], v[224:227], a[0:3], 0\n\t"
"ds_read_b64_tr_b16 v[64:65], v6 offset:8192\n\t"
"ds_read_b64_tr_b16 v[66:67], v6 offset:12288\n\t"
"v_mfma_f32_16x16x32_bf16 a[224:227], v[224:227], a[4:7], 0\n\t"
"ds_read_b64_tr_b16 v[68:69], v7 offset:8192\n\t"
"v_mfma_f32_16x16x32_bf16 a[196:199], v[228:231], a[0:3], 0\n\t"
"ds_read_b64_tr_b16 v[70:71], v7 offset:12288\n\t"
"s_waitcnt lgkmcnt(9)\n\t"
"v_mfma_f32_16x16x32_bf16 a[228:231], v[228:231], a[4:7], 0\n\t"
"ds_read_b64_tr_b16 v[72:73], v8 offset:8192\n\t"
"v_mfma_f32_16x16x32_bf16 a[200:203], v[232:235], a[0:3], 0\n\t"
"ds_read_b64_tr_b16 v[74:75], v8 offset:12288\n\t"
"v_mfma_f32_16x16x32_bf16 a[232:235], v[232:235], a[4:7], 0\n\t"
"ds_read_b64_tr_b16 v[76:77], v9 offset:8192\n\t"
"v_mfma_f32_16x16x32_bf16 a[204:207], v[236:239], a[0:3], 0\n\t"
"ds_read_b64_tr_b16 v[78:79], v9 offset:12288\n\t"
"v_mfma_f32_16x16x32_bf16 a[236:239], v[236:239], a[4:7], 0\n\t"
"ds_read_b64_tr_b16 v[80:81], v10 offset:8192\n\t"
"ds_read_b64_tr_b16 v[82:83], v10 offset:12288\n\t"
"v_mfma_f32_16x16x32_bf16 a[208:211], v[240:243], a[0:3], 0\n\t"
"ds_read_b64_tr_b16 v[84:85], v11 offset:8192\n\t"
"s_waitcnt lgkmcnt(9)\n\t"
"v_mfma_f32_16x16x32_bf16 a[240:243], v[240:243], a[4:7], 0\n\t"
"ds_read_b64_tr_b16 v[86:87], v11 offset:12288\n\t"
"v_mfma_f32_16x16x32_bf16 a[212:215], v[244:247], a[0:3], 0\n\t"
"ds_read_b64_tr_b16 v[88:89], v12 offset:8192\n\t"
"v_mfma_f32_16x16x32_bf16 a[244:247], v[244:247], a[4:7], 0\n\t"
"ds_read_b64_tr_b16 v[90:91], v12 offset:12288\n\t"
"v_mfma_f32_16x16x32_bf16 a[216:219], v[248:251], a[0:3], 0\n\t"
"ds_read_b64_tr_b16 v[92:93], v13 offset:8192\n\t"
"v_mfma_f32_16x16x32_bf16 a[248:251], v[248:251], a[4:7], 0\n\t"
"ds_read_b64_tr_b16 v[94:95], v13 offset:12288\n\t"
"v_mfma_f32_16x16x32_bf16 a[220:223], v[252:255], a[0:3], 0\n\t"
"v_mfma_f32_16x16x32_bf16 a[252:255], v[252:255], a[4:7], 0\n\t"
"s_lshl_b32 s69, s73, 2\n\t"
"s_add_u32 s69, s69, s72\n\t"
"s_lshl_b32 s69, s69, 3\n\t"
"s_lshl_b32 s70, s39, 1\n\t"
"s_add_u32 s69, s69, s70\n\t"
"s_lshl_b32 s69, s69, 3\n\t"
"s_add_u32 s69, s69, s71\n\t"
"s_lshl_b32 s75, s69, 12\n\t"
"s_add_u32 s78, s75, 32768\n\t"
"v_mfma_f32_16x16x32_bf16 a[192:195], v[64:67], a[8:11], a[192:195]\n\t"
"ds_read_b64_tr_b16 v[224:225], v6 offset:16384\n\t"
"v_accvgpr_read_b32 v32, a128\n\t"
"ds_read_b64_tr_b16 v[226:227], v6 offset:20480\n\t"
"s_waitcnt lgkmcnt(6)\n\t"
"v_mfma_f32_16x16x32_bf16 a[224:227], v[64:67], a[12:15], a[224:227]\n\t"
"v_accvgpr_read_b32 v33, a129\n\t"
"ds_read_b64_tr_b16 v[228:229], v7 offset:16384\n\t"
"v_mfma_f32_16x16x32_bf16 a[196:199], v[68:71], a[8:11], a[196:199]\n\t"
"v_accvgpr_read_b32 v34, a130\n\t"
"ds_read_b64_tr_b16 v[230:231], v7 offset:20480\n\t"
"v_mfma_f32_16x16x32_bf16 a[228:231], v[68:71], a[12:15], a[228:231]\n\t"
"v_accvgpr_read_b32 v35, a131\n\t"
"ds_read_b64_tr_b16 v[232:233], v8 offset:16384\n\t"
"v_mfma_f32_16x16x32_bf16 a[200:203], v[72:75], a[8:11], a[200:203]\n\t"
"v_accvgpr_read_b32 v36, a132\n\t"
"ds_read_b64_tr_b16 v[234:235], v8 offset:20480\n\t"
"v_mfma_f32_16x16x32_bf16 a[232:235], v[72:75], a[12:15], a[232:235]\n\t"
"ds_read_b64_tr_b16 v[236:237], v9 offset:16384\n\t"
"v_accvgpr_read_b32 v37, a133\n\t"
"v_mfma_f32_16x16x32_bf16 a[204:207], v[76:79], a[8:11], a[204:207]\n\t"
"ds_read_b64_tr_b16 v[238:239], v9 offset:20480\n\t"
"v_accvgpr_read_b32 v38, a134\n\t"
"v_mfma_f32_16x16x32_bf16 a[236:239], v[76:79], a[12:15], a[236:239]\n\t"
"ds_read_b64_tr_b16 v[240:241], v10 offset:16384\n\t"
"v_accvgpr_read_b32 v39, a135\n\t"
"v_mfma_f32_16x16x32_bf16 a[208:211], v[80:83], a[8:11], a[208:211]\n\t"
"ds_read_b64_tr_b16 v[242:243], v10 offset:20480\n\t"
"v_cvt_pk_bf16_f32 v40, v32, v33\n\t"
"v_mfma_f32_16x16x32_bf16 a[240:243], v[80:83], a[12:15], a[240:243]\n\t"
"ds_read_b64_tr_b16 v[244:245], v11 offset:16384\n\t"
"ds_read_b64_tr_b16 v[246:247], v11 offset:20480\n\t"
"s_waitcnt lgkmcnt(8)\n\t"
"v_mfma_f32_16x16x32_bf16 a[212:215], v[84:87], a[8:11], a[212:215]\n\t"
"v_cvt_pk_bf16_f32 v41, v34, v35\n\t"
"ds_read_b64_tr_b16 v[248:249], v12 offset:16384\n\t"
"v_mfma_f32_16x16x32_bf16 a[244:247], v[84:87], a[12:15], a[244:247]\n\t"
"v_cvt_pk_bf16_f32 v42, v36, v37\n\t"
"ds_read_b64_tr_b16 v[250:251], v12 offset:20480\n\t"
"v_mfma_f32_16x16x32_bf16 a[216:219], v[88:91], a[8:11], a[216:219]\n\t"
"v_cvt_pk_bf16_f32 v43, v38, v39\n\t"
"ds_read_b64_tr_b16 v[252:253], v13 offset:16384\n\t"
"v_mfma_f32_16x16x32_bf16 a[248:251], v[88:91], a[12:15], a[248:251]\n\t"
"buffer_store_dwordx4 v[40:43], v0, s[60:63], s75 offen offset:0 sc1\n\t"
"ds_read_b64_tr_b16 v[254:255], v13 offset:20480\n\t"
"v_mfma_f32_16x16x32_bf16 a[220:223], v[92:95], a[8:11], a[220:223]\n\t"
"v_mfma_f32_16x16x32_bf16 a[252:255], v[92:95], a[12:15], a[252:255]\n\t"
"v_mfma_f32_16x16x32_bf16 a[192:195], v[224:227], a[16:19], a[192:195]\n\t"
"ds_read_b64_tr_b16 v[64:65], v6 offset:24576\n\t"
"v_accvgpr_read_b32 v44, a136\n\t"
"ds_read_b64_tr_b16 v[66:67], v6 offset:28672\n\t"
"v_mfma_f32_16x16x32_bf16 a[224:227], v[224:227], a[20:23], a[224:227]\n\t"
"v_accvgpr_read_b32 v45, a137\n\t"
"ds_read_b64_tr_b16 v[68:69], v7 offset:24576\n\t"
"v_mfma_f32_16x16x32_bf16 a[196:199], v[228:231], a[16:19], a[196:199]\n\t"
"v_accvgpr_read_b32 v46, a138\n\t"
"ds_read_b64_tr_b16 v[70:71], v7 offset:28672\n\t"
"s_waitcnt lgkmcnt(7)\n\t"
"v_mfma_f32_16x16x32_bf16 a[228:231], v[228:231], a[20:23], a[228:231]\n\t"
"v_accvgpr_read_b32 v47, a139\n\t"
"ds_read_b64_tr_b16 v[72:73], v8 offset:24576\n\t"
"v_mfma_f32_16x16x32_bf16 a[200:203], v[232:235], a[16:19], a[200:203]\n\t"
"v_accvgpr_read_b32 v48, a140\n\t"
"ds_read_b64_tr_b16 v[74:75], v8 offset:28672\n\t"
"v_mfma_f32_16x16x32_bf16 a[232:235], v[232:235], a[20:23], a[232:235]\n\t"
"ds_read_b64_tr_b16 v[76:77], v9 offset:24576\n\t"
"v_accvgpr_read_b32 v49, a141\n\t"
"v_mfma_f32_16x16x32_bf16 a[204:207], v[236:239], a[16:19], a[204:207]\n\t"
"ds_read_b64_tr_b16 v[78:79], v9 offset:28672\n\t"
"v_accvgpr_read_b32 v50, a142\n\t"
"v_mfma_f32_16x16x32_bf16 a[236:239], v[236:239], a[20:23], a[236:239]\n\t"
"ds_read_b64_tr_b16 v[80:81], v10 offset:24576\n\t"
"v_accvgpr_read_b32 v51, a143\n\t"
"v_mfma_f32_16x16x32_bf16 a[208:211], v[240:243], a[16:19], a[208:211]\n\t"
"ds_read_b64_tr_b16 v[82:83], v10 offset:28672\n\t"
"v_cvt_pk_bf16_f32 v52, v44, v45\n\t"
"v_mfma_f32_16x16x32_bf16 a[240:243], v[240:243], a[20:23], a[240:243]\n\t"
"ds_read_b64_tr_b16 v[84:85], v11 offset:24576\n\t"
"ds_read_b64_tr_b16 v[86:87], v11 offset:28672\n\t"
"v_mfma_f32_16x16x32_bf16 a[212:215], v[244:247], a[16:19], a[212:215]\n\t"
"v_cvt_pk_bf16_f32 v53, v46, v47\n\t"
"ds_read_b64_tr_b16 v[88:89], v12 offset:24576\n\t"
"s_waitcnt lgkmcnt(8)\n\t"
"v_mfma_f32_16x16x32_bf16 a[244:247], v[244:247], a[20:23], a[244:247]\n\t"
"v_cvt_pk_bf16_f32 v54, v48, v49\n\t"
"ds_read_b64_tr_b16 v[90:91], v12 offset:28672\n\t"
"v_mfma_f32_16x16x32_bf16 a[216:219], v[248:251], a[16:19], a[216:219]\n\t"
"v_cvt_pk_bf16_f32 v55, v50, v51\n\t"
"ds_read_b64_tr_b16 v[92:93], v13 offset:24576\n\t"
"v_mfma_f32_16x16x32_bf16 a[248:251], v[248:251], a[20:23], a[248:251]\n\t"
"buffer_store_dwordx4 v[52:55], v0, s[60:63], s75 offen offset:1024 sc1\n\t"
"ds_read_b64_tr_b16 v[94:95], v13 offset:28672\n\t"
"v_mfma_f32_16x16x32_bf16 a[220:223], v[252:255], a[16:19], a[220:223]\n\t"
"v_mfma_f32_16x16x32_bf16 a[252:255], v[252:255], a[20:23], a[252:255]\n\t"
"v_mfma_f32_16x16x32_bf16 a[192:195], v[64:67], a[24:27], a[192:195]\n\t"
"ds_read_b64_tr_b16 v[224:225], v6 offset:32768\n\t"
"v_accvgpr_read_b32 v32, a144\n\t"
"ds_read_b64_tr_b16 v[226:227], v6 offset:36864\n\t"
"v_mfma_f32_16x16x32_bf16 a[224:227], v[64:67], a[28:31], a[224:227]\n\t"
"v_accvgpr_read_b32 v33, a145\n\t"
"ds_read_b64_tr_b16 v[228:229], v7 offset:32768\n\t"
"v_mfma_f32_16x16x32_bf16 a[196:199], v[68:71], a[24:27], a[196:199]\n\t"
"v_accvgpr_read_b32 v34, a146\n\t"
"ds_read_b64_tr_b16 v[230:231], v7 offset:36864\n\t"
"v_mfma_f32_16x16x32_bf16 a[228:231], v[68:71], a[28:31], a[228:231]\n\t"
"v_accvgpr_read_b32 v35, a147\n\t"
"ds_read_b64_tr_b16 v[232:233], v8 offset:32768\n\t"
"s_waitcnt lgkmcnt(7)\n\t"
"v_mfma_f32_16x16x32_bf16 a[200:203], v[72:75], a[24:27], a[200:203]\n\t"
"v_accvgpr_read_b32 v36, a148\n\t"
"ds_read_b64_tr_b16 v[234:235], v8 offset:36864\n\t"
"v_mfma_f32_16x16x32_bf16 a[232:235], v[72:75], a[28:31], a[232:235]\n\t"
"ds_read_b64_tr_b16 v[236:237], v9 offset:32768\n\t"
"v_accvgpr_read_b32 v37, a149\n\t"
"v_mfma_f32_16x16x32_bf16 a[204:207], v[76:79], a[24:27], a[204:207]\n\t"
"ds_read_b64_tr_b16 v[238:239], v9 offset:36864\n\t"
"v_accvgpr_read_b32 v38, a150\n\t"
"v_mfma_f32_16x16x32_bf16 a[236:239], v[76:79], a[28:31], a[236:239]\n\t"
"ds_read_b64_tr_b16 v[240:241], v10 offset:32768\n\t"
"v_accvgpr_read_b32 v39, a151\n\t"
"v_mfma_f32_16x16x32_bf16 a[208:211], v[80:83], a[24:27], a[208:211]\n\t"
"ds_read_b64_tr_b16 v[242:243], v10 offset:36864\n\t"
"v_cvt_pk_bf16_f32 v40, v32, v33\n\t"
"v_mfma_f32_16x16x32_bf16 a[240:243], v[80:83], a[28:31], a[240:243]\n\t"
"ds_read_b64_tr_b16 v[244:245], v11 offset:32768\n\t"
"ds_read_b64_tr_b16 v[246:247], v11 offset:36864\n\t"
"v_mfma_f32_16x16x32_bf16 a[212:215], v[84:87], a[24:27], a[212:215]\n\t"
"v_cvt_pk_bf16_f32 v41, v34, v35\n\t"
"ds_read_b64_tr_b16 v[248:249], v12 offset:32768\n\t"
"v_mfma_f32_16x16x32_bf16 a[244:247], v[84:87], a[28:31], a[244:247]\n\t"
"v_cvt_pk_bf16_f32 v42, v36, v37\n\t"
"ds_read_b64_tr_b16 v[250:251], v12 offset:36864\n\t"
"s_waitcnt lgkmcnt(8)\n\t"
"v_mfma_f32_16x16x32_bf16 a[216:219], v[88:91], a[24:27], a[216:219]\n\t"
"v_cvt_pk_bf16_f32 v43, v38, v39\n\t"
"ds_read_b64_tr_b16 v[252:253], v13 offset:32768\n\t"
"v_mfma_f32_16x16x32_bf16 a[248:251], v[88:91], a[28:31], a[248:251]\n\t"
"buffer_store_dwordx4 v[40:43], v0, s[60:63], s75 offen offset:2048 sc1\n\t"
"ds_read_b64_tr_b16 v[254:255], v13 offset:36864\n\t"
"v_mfma_f32_16x16x32_bf16 a[220:223], v[92:95], a[24:27], a[220:223]\n\t"
"v_mfma_f32_16x16x32_bf16 a[252:255], v[92:95], a[28:31], a[252:255]\n\t"
"s_waitcnt vmcnt(19)\n\t"
"v_mfma_f32_16x16x32_bf16 a[192:195], v[224:227], a[32:35], a[192:195]\n\t"
"ds_read_b64_tr_b16 v[64:65], v6 offset:40960\n\t"
"v_accvgpr_read_b32 v44, a152\n\t"
"ds_read_b64_tr_b16 v[66:67], v6 offset:45056\n\t"
"v_mfma_f32_16x16x32_bf16 a[224:227], v[224:227], a[36:39], a[224:227]\n\t"
"v_accvgpr_read_b32 v45, a153\n\t"
"ds_read_b64_tr_b16 v[68:69], v7 offset:40960\n\t"
"v_mfma_f32_16x16x32_bf16 a[196:199], v[228:231], a[32:35], a[196:199]\n\t"
"v_accvgpr_read_b32 v46, a154\n\t"
"ds_read_b64_tr_b16 v[70:71], v7 offset:45056\n\t"
"v_mfma_f32_16x16x32_bf16 a[228:231], v[228:231], a[36:39], a[228:231]\n\t"
"v_accvgpr_read_b32 v47, a155\n\t"
"ds_read_b64_tr_b16 v[72:73], v8 offset:40960\n\t"
"v_mfma_f32_16x16x32_bf16 a[200:203], v[232:235], a[32:35], a[200:203]\n\t"
"v_accvgpr_read_b32 v48, a156\n\t"
"ds_read_b64_tr_b16 v[74:75], v8 offset:45056\n\t"
"s_waitcnt lgkmcnt(6)\n\t"
"v_mfma_f32_16x16x32_bf16 a[232:235], v[232:235], a[36:39], a[232:235]\n\t"
"ds_read_b64_tr_b16 v[76:77], v9 offset:40960\n\t"
"v_accvgpr_read_b32 v49, a157\n\t"
"v_mfma_f32_16x16x32_bf16 a[204:207], v[236:239], a[32:35], a[204:207]\n\t"
"ds_read_b64_tr_b16 v[78:79], v9 offset:45056\n\t"
"v_accvgpr_read_b32 v50, a158\n\t"
"v_mfma_f32_16x16x32_bf16 a[236:239], v[236:239], a[36:39], a[236:239]\n\t"
"ds_read_b64_tr_b16 v[80:81], v10 offset:40960\n\t"
"v_accvgpr_read_b32 v51, a159\n\t"
"v_mfma_f32_16x16x32_bf16 a[208:211], v[240:243], a[32:35], a[208:211]\n\t"
"ds_read_b64_tr_b16 v[82:83], v10 offset:45056\n\t"
"v_cvt_pk_bf16_f32 v52, v44, v45\n\t"
"v_mfma_f32_16x16x32_bf16 a[240:243], v[240:243], a[36:39], a[240:243]\n\t"
"ds_read_b64_tr_b16 v[84:85], v11 offset:40960\n\t"
"ds_read_b64_tr_b16 v[86:87], v11 offset:45056\n\t"
"v_mfma_f32_16x16x32_bf16 a[212:215], v[244:247], a[32:35], a[212:215]\n\t"
"v_cvt_pk_bf16_f32 v53, v46, v47\n\t"
"ds_read_b64_tr_b16 v[88:89], v12 offset:40960\n\t"
"v_mfma_f32_16x16x32_bf16 a[244:247], v[244:247], a[36:39], a[244:247]\n\t"
"v_cvt_pk_bf16_f32 v54, v48, v49\n\t"
"ds_read_b64_tr_b16 v[90:91], v12 offset:45056\n\t"
"v_mfma_f32_16x16x32_bf16 a[216:219], v[248:251], a[32:35], a[216:219]\n\t"
"v_cvt_pk_bf16_f32 v55, v50, v51\n\t"
"ds_read_b64_tr_b16 v[92:93], v13 offset:40960\n\t"
"v_mfma_f32_16x16x32_bf16 a[248:251], v[248:251], a[36:39], a[248:251]\n\t"
"buffer_store_dwordx4 v[52:55], v0, s[60:63], s75 offen offset:3072 sc1\n\t"
"ds_read_b64_tr_b16 v[94:95], v13 offset:45056\n\t"
"s_waitcnt lgkmcnt(8)\n\t"
"v_mfma_f32_16x16x32_bf16 a[220:223], v[252:255], a[32:35], a[220:223]\n\t"
"v_mfma_f32_16x16x32_bf16 a[252:255], v[252:255], a[36:39], a[252:255]\n\t"
"v_mfma_f32_16x16x32_bf16 a[192:195], v[64:67], a[40:43], a[192:195]\n\t"
"ds_read_b64_tr_b16 v[224:225], v6 offset:49152\n\t"
"v_accvgpr_read_b32 v32, a160\n\t"
"ds_read_b64_tr_b16 v[226:227], v6 offset:53248\n\t"
"v_mfma_f32_16x16x32_bf16 a[224:227], v[64:67], a[44:47], a[224:227]\n\t"
"v_accvgpr_read_b32 v33, a161\n\t"
"ds_read_b64_tr_b16 v[228:229], v7 offset:49152\n\t"
"v_mfma_f32_16x16x32_bf16 a[196:199], v[68:71], a[40:43], a[196:199]\n\t"
"v_accvgpr_read_b32 v34, a162\n\t"
"ds_read_b64_tr_b16 v[230:231], v7 offset:53248\n\t"
"v_mfma_f32_16x16x32_bf16 a[228:231], v[68:71], a[44:47], a[228:231]\n\t"
"v_accvgpr_read_b32 v35, a163\n\t"
"ds_read_b64_tr_b16 v[232:233], v8 offset:49152\n\t"
"v_mfma_f32_16x16x32_bf16 a[200:203], v[72:75], a[40:43], a[200:203]\n\t"
"v_accvgpr_read_b32 v36, a164\n\t"
"ds_read_b64_tr_b16 v[234:235], v8 offset:53248\n\t"
"v_mfma_f32_16x16x32_bf16 a[232:235], v[72:75], a[44:47], a[232:235]\n\t"
"ds_read_b64_tr_b16 v[236:237], v9 offset:49152\n\t"
"v_accvgpr_read_b32 v37, a165\n\t"
"v_mfma_f32_16x16x32_bf16 a[204:207], v[76:79], a[40:43], a[204:207]\n\t"
"ds_read_b64_tr_b16 v[238:239], v9 offset:53248\n\t"
"s_waitcnt lgkmcnt(8)\n\t"
"v_accvgpr_read_b32 v38, a166\n\t"
"v_mfma_f32_16x16x32_bf16 a[236:239], v[76:79], a[44:47], a[236:239]\n\t"
"ds_read_b64_tr_b16 v[240:241], v10 offset:49152\n\t"
"v_accvgpr_read_b32 v39, a167\n\t"
"v_mfma_f32_16x16x32_bf16 a[208:211], v[80:83], a[40:43], a[208:211]\n\t"
"ds_read_b64_tr_b16 v[242:243], v10 offset:53248\n\t"
"v_cvt_pk_bf16_f32 v40, v32, v33\n\t"
"v_mfma_f32_16x16x32_bf16 a[240:243], v[80:83], a[44:47], a[240:243]\n\t"
"ds_read_b64_tr_b16 v[244:245], v11 offset:49152\n\t"
"ds_read_b64_tr_b16 v[246:247], v11 offset:53248\n\t"
"v_mfma_f32_16x16x32_bf16 a[212:215], v[84:87], a[40:43], a[212:215]\n\t"
"v_cvt_pk_bf16_f32 v41, v34, v35\n\t"
"ds_read_b64_tr_b16 v[248:249], v12 offset:49152\n\t"
"v_mfma_f32_16x16x32_bf16 a[244:247], v[84:87], a[44:47], a[244:247]\n\t"
"v_cvt_pk_bf16_f32 v42, v36, v37\n\t"
"ds_read_b64_tr_b16 v[250:251], v12 offset:53248\n\t"
"v_mfma_f32_16x16x32_bf16 a[216:219], v[88:91], a[40:43], a[216:219]\n\t"
"v_cvt_pk_bf16_f32 v43, v38, v39\n\t"
"ds_read_b64_tr_b16 v[252:253], v13 offset:49152\n\t"
"v_mfma_f32_16x16x32_bf16 a[248:251], v[88:91], a[44:47], a[248:251]\n\t"
"buffer_store_dwordx4 v[40:43], v0, s[60:63], s78 offen offset:0 sc1\n\t"
"ds_read_b64_tr_b16 v[254:255], v13 offset:53248\n\t"
"s_waitcnt lgkmcnt(8)\n\t"
"v_mfma_f32_16x16x32_bf16 a[220:223], v[92:95], a[40:43], a[220:223]\n\t"
"v_mfma_f32_16x16x32_bf16 a[252:255], v[92:95], a[44:47], a[252:255]\n\t"
"v_mfma_f32_16x16x32_bf16 a[192:195], v[224:227], a[48:51], a[192:195]\n\t"
"ds_read_b64_tr_b16 v[64:65], v6 offset:57344\n\t"
"v_accvgpr_read_b32 v44, a168\n\t"
"ds_read_b64_tr_b16 v[66:67], v6 offset:61440\n\t"
"v_mfma_f32_16x16x32_bf16 a[224:227], v[224:227], a[52:55], a[224:227]\n\t"
"v_accvgpr_read_b32 v45, a169\n\t"
"ds_read_b64_tr_b16 v[68:69], v7 offset:57344\n\t"
"v_mfma_f32_16x16x32_bf16 a[196:199], v[228:231], a[48:51], a[196:199]\n\t"
"v_accvgpr_read_b32 v46, a170\n\t"
"ds_read_b64_tr_b16 v[70:71], v7 offset:61440\n\t"
"v_mfma_f32_16x16x32_bf16 a[228:231], v[228:231], a[52:55], a[228:231]\n\t"
"v_accvgpr_read_b32 v47, a171\n\t"
"ds_read_b64_tr_b16 v[72:73], v8 offset:57344\n\t"
"v_mfma_f32_16x16x32_bf16 a[200:203], v[232:235], a[48:51], a[200:203]\n\t"
"v_accvgpr_read_b32 v48, a172\n\t"
"ds_read_b64_tr_b16 v[74:75], v8 offset:61440\n\t"
"v_mfma_f32_16x16x32_bf16 a[232:235], v[232:235], a[52:55], a[232:235]\n\t"
"ds_read_b64_tr_b16 v[76:77], v9 offset:57344\n\t"
"v_accvgpr_read_b32 v49, a173\n\t"
"v_mfma_f32_16x16x32_bf16 a[204:207], v[236:239], a[48:51], a[204:207]\n\t"
"ds_read_b64_tr_b16 v[78:79], v9 offset:61440\n\t"
"s_waitcnt lgkmcnt(8)\n\t"
"v_accvgpr_read_b32 v50, a174\n\t"
"v_mfma_f32_16x16x32_bf16 a[236:239], v[236:239], a[52:55], a[236:239]\n\t"
"ds_read_b64_tr_b16 v[80:81], v10 offset:57344\n\t"
"v_accvgpr_read_b32 v51, a175\n\t"
"v_mfma_f32_16x16x32_bf16 a[208:211], v[240:243], a[48:51], a[208:211]\n\t"
"ds_read_b64_tr_b16 v[82:83], v10 offset:61440\n\t"
"v_cvt_pk_bf16_f32 v52, v44, v45\n\t"
"v_mfma_f32_16x16x32_bf16 a[240:243], v[240:243], a[52:55], a[240:243]\n\t"
"ds_read_b64_tr_b16 v[84:85], v11 offset:57344\n\t"
"ds_read_b64_tr_b16 v[86:87], v11 offset:61440\n\t"
"v_mfma_f32_16x16x32_bf16 a[212:215], v[244:247], a[48:51], a[212:215]\n\t"
"v_cvt_pk_bf16_f32 v53, v46, v47\n\t"
"ds_read_b64_tr_b16 v[88:89], v12 offset:57344\n\t"
"v_mfma_f32_16x16x32_bf16 a[244:247], v[244:247], a[52:55], a[244:247]\n\t"
"v_cvt_pk_bf16_f32 v54, v48, v49\n\t"
"ds_read_b64_tr_b16 v[90:91], v12 offset:61440\n\t"
"v_mfma_f32_16x16x32_bf16 a[216:219], v[248:251], a[48:51], a[216:219]\n\t"
"v_cvt_pk_bf16_f32 v55, v50, v51\n\t"
"ds_read_b64_tr_b16 v[92:93], v13 offset:57344\n\t"
"v_mfma_f32_16x16x32_bf16 a[248:251], v[248:251], a[52:55], a[248:251]\n\t"
"buffer_store_dwordx4 v[52:55], v0, s[60:63], s78 offen offset:1024 sc1\n\t"
"ds_read_b64_tr_b16 v[94:95], v13 offset:61440\n\t"
"s_waitcnt lgkmcnt(8)\n\t"
"v_mfma_f32_16x16x32_bf16 a[220:223], v[252:255], a[48:51], a[220:223]\n\t"
"v_mfma_f32_16x16x32_bf16 a[252:255], v[252:255], a[52:55], a[252:255]\n\t"
"v_mfma_f32_16x16x32_bf16 a[192:195], v[64:67], a[56:59], a[192:195]\n\t"
"v_add_u32 v6, 65536, v6\n\t"
"v_accvgpr_read_b32 v32, a176\n\t"
"v_add_u32 v7, 65536, v7\n\t"
"v_mfma_f32_16x16x32_bf16 a[224:227], v[64:67], a[60:63], a[224:227]\n\t"
"v_accvgpr_read_b32 v33, a177\n\t"
"v_add_u32 v8, 65536, v8\n\t"
"v_add_u32 v9, 65536, v9\n\t"
"v_mfma_f32_16x16x32_bf16 a[196:199], v[68:71], a[56:59], a[196:199]\n\t"
"v_accvgpr_read_b32 v34, a178\n\t"
"v_add_u32 v10, 65536, v10\n\t"
"v_mfma_f32_16x16x32_bf16 a[228:231], v[68:71], a[60:63], a[228:231]\n\t"
"v_add_u32 v11, 65536, v11\n\t"
"v_accvgpr_read_b32 v35, a179\n\t"
"v_add_u32 v12, 65536, v12\n\t"
"v_mfma_f32_16x16x32_bf16 a[200:203], v[72:75], a[56:59], a[200:203]\n\t"
"v_add_u32 v13, 65536, v13\n\t"
"v_accvgpr_read_b32 v36, a180\n\t"
"ds_read_b64_tr_b16 v[224:225], v6 offset:0\n\t"
"v_mfma_f32_16x16x32_bf16 a[232:235], v[72:75], a[60:63], a[232:235]\n\t"
"ds_read_b64_tr_b16 v[226:227], v6 offset:4096\n\t"
"v_accvgpr_read_b32 v37, a181\n\t"
"v_mfma_f32_16x16x32_bf16 a[204:207], v[76:79], a[56:59], a[204:207]\n\t"
"ds_read_b64_tr_b16 v[228:229], v7 offset:0\n\t"
"ds_read_b64_tr_b16 v[230:231], v7 offset:4096\n\t"
"v_accvgpr_read_b32 v38, a182\n\t"
"v_mfma_f32_16x16x32_bf16 a[236:239], v[76:79], a[60:63], a[236:239]\n\t"
"ds_read_b64_tr_b16 v[232:233], v8 offset:0\n\t"
"v_accvgpr_read_b32 v39, a183\n\t"
"ds_read_b64_tr_b16 v[234:235], v8 offset:4096\n\t"
"s_waitcnt lgkmcnt(6)\n\t"
"v_mfma_f32_16x16x32_bf16 a[208:211], v[80:83], a[56:59], a[208:211]\n\t"
"ds_read_b64_tr_b16 v[236:237], v9 offset:0\n\t"
"v_cvt_pk_bf16_f32 v40, v32, v33\n\t"
"v_mfma_f32_16x16x32_bf16 a[240:243], v[80:83], a[60:63], a[240:243]\n\t"
"ds_read_b64_tr_b16 v[238:239], v9 offset:4096\n\t"
"ds_read_b64_tr_b16 v[240:241], v10 offset:0\n\t"
"v_cvt_pk_bf16_f32 v41, v34, v35\n\t"
"v_mfma_f32_16x16x32_bf16 a[212:215], v[84:87], a[56:59], a[212:215]\n\t"
"ds_read_b64_tr_b16 v[242:243], v10 offset:4096\n\t"
"ds_read_b64_tr_b16 v[244:245], v11 offset:0\n\t"
"v_cvt_pk_bf16_f32 v42, v36, v37\n\t"
"v_mfma_f32_16x16x32_bf16 a[244:247], v[84:87], a[60:63], a[244:247]\n\t"
"ds_read_b64_tr_b16 v[246:247], v11 offset:4096\n\t"
"ds_read_b64_tr_b16 v[248:249], v12 offset:0\n\t"
"v_mfma_f32_16x16x32_bf16 a[216:219], v[88:91], a[56:59], a[216:219]\n\t"
"v_cvt_pk_bf16_f32 v43, v38, v39\n\t"
"ds_read_b64_tr_b16 v[250:251], v12 offset:4096\n\t"
"ds_read_b64_tr_b16 v[252:253], v13 offset:0\n\t"
"v_mfma_f32_16x16x32_bf16 a[248:251], v[88:91], a[60:63], a[248:251]\n\t"
"buffer_store_dwordx4 v[40:43], v0, s[60:63], s78 offen offset:2048 sc1\n\t"
"ds_read_b64_tr_b16 v[254:255], v13 offset:4096\n\t"
"s_waitcnt lgkmcnt(11)\n\t"
"v_mfma_f32_16x16x32_bf16 a[220:223], v[92:95], a[56:59], a[220:223]\n\t"
"v_mfma_f32_16x16x32_bf16 a[252:255], v[92:95], a[60:63], a[252:255]\n\t"
"s_waitcnt vmcnt(15)\n\t"
"v_mfma_f32_16x16x32_bf16 a[192:195], v[224:227], a[64:67], a[192:195]\n\t"
"ds_read_b64_tr_b16 v[64:65], v6 offset:8192\n\t"
"v_accvgpr_read_b32 v44, a184\n\t"
"ds_read_b64_tr_b16 v[66:67], v6 offset:12288\n\t"
"v_mfma_f32_16x16x32_bf16 a[224:227], v[224:227], a[68:71], a[224:227]\n\t"
"v_accvgpr_read_b32 v45, a185\n\t"
"ds_read_b64_tr_b16 v[68:69], v7 offset:8192\n\t"
"v_mfma_f32_16x16x32_bf16 a[196:199], v[228:231], a[64:67], a[196:199]\n\t"
"v_accvgpr_read_b32 v46, a186\n\t"
"ds_read_b64_tr_b16 v[70:71], v7 offset:12288\n\t"
"v_mfma_f32_16x16x32_bf16 a[228:231], v[228:231], a[68:71], a[228:231]\n\t"
"v_accvgpr_read_b32 v47, a187\n\t"
"ds_read_b64_tr_b16 v[72:73], v8 offset:8192\n\t"
"s_waitcnt lgkmcnt(6)\n\t"
"v_mfma_f32_16x16x32_bf16 a[200:203], v[232:235], a[64:67], a[200:203]\n\t"
"v_accvgpr_read_b32 v48, a188\n\t"
"ds_read_b64_tr_b16 v[74:75], v8 offset:12288\n\t"
"v_mfma_f32_16x16x32_bf16 a[232:235], v[232:235], a[68:71], a[232:235]\n\t"
"ds_read_b64_tr_b16 v[76:77], v9 offset:8192\n\t"
"v_accvgpr_read_b32 v49, a189\n\t"
"v_mfma_f32_16x16x32_bf16 a[204:207], v[236:239], a[64:67], a[204:207]\n\t"
"ds_read_b64_tr_b16 v[78:79], v9 offset:12288\n\t"
"v_accvgpr_read_b32 v50, a190\n\t"
"v_mfma_f32_16x16x32_bf16 a[236:239], v[236:239], a[68:71], a[236:239]\n\t"
"ds_read_b64_tr_b16 v[80:81], v10 offset:8192\n\t"
"v_accvgpr_read_b32 v51, a191\n\t"
"v_mfma_f32_16x16x32_bf16 a[208:211], v[240:243], a[64:67], a[208:211]\n\t"
"ds_read_b64_tr_b16 v[82:83], v10 offset:12288\n\t"
"v_cvt_pk_bf16_f32 v52, v44, v45\n\t"
"v_mfma_f32_16x16x32_bf16 a[240:243], v[240:243], a[68:71], a[240:243]\n\t"
"ds_read_b64_tr_b16 v[84:85], v11 offset:8192\n\t"
"ds_read_b64_tr_b16 v[86:87], v11 offset:12288\n\t"
"v_mfma_f32_16x16x32_bf16 a[212:215], v[244:247], a[64:67], a[212:215]\n\t"
"v_cvt_pk_bf16_f32 v53, v46, v47\n\t"
"ds_read_b64_tr_b16 v[88:89], v12 offset:8192\n\t"
"v_mfma_f32_16x16x32_bf16 a[244:247], v[244:247], a[68:71], a[244:247]\n\t"
"v_cvt_pk_bf16_f32 v54, v48, v49\n\t"
"ds_read_b64_tr_b16 v[90:91], v12 offset:12288\n\t"
"v_mfma_f32_16x16x32_bf16 a[216:219], v[248:251], a[64:67], a[216:219]\n\t"
"v_cvt_pk_bf16_f32 v55, v50, v51\n\t"
"ds_read_b64_tr_b16 v[92:93], v13 offset:8192\n\t"
"s_waitcnt lgkmcnt(8)\n\t"
"v_mfma_f32_16x16x32_bf16 a[248:251], v[248:251], a[68:71], a[248:251]\n\t"
"buffer_store_dwordx4 v[52:55], v0, s[60:63], s78 offen offset:3072 sc1\n\t"
"ds_read_b64_tr_b16 v[94:95], v13 offset:12288\n\t"
"v_mfma_f32_16x16x32_bf16 a[220:223], v[252:255], a[64:67], a[220:223]\n\t"
"v_mfma_f32_16x16x32_bf16 a[252:255], v[252:255], a[68:71], a[252:255]\n\t"
"v_mfma_f32_16x16x32_bf16 a[192:195], v[64:67], a[72:75], a[192:195]\n\t"
"ds_read_b64_tr_b16 v[224:225], v6 offset:16384\n\t"
"ds_read_b64_tr_b16 v[226:227], v6 offset:20480\n\t"
"v_mfma_f32_16x16x32_bf16 a[224:227], v[64:67], a[76:79], a[224:227]\n\t"
"ds_read_b64_tr_b16 v[228:229], v7 offset:16384\n\t"
"v_mfma_f32_16x16x32_bf16 a[196:199], v[68:71], a[72:75], a[196:199]\n\t"
"ds_read_b64_tr_b16 v[230:231], v7 offset:20480\n\t"
"v_mfma_f32_16x16x32_bf16 a[228:231], v[68:71], a[76:79], a[228:231]\n\t"
"ds_read_b64_tr_b16 v[232:233], v8 offset:16384\n\t"
"v_mfma_f32_16x16x32_bf16 a[200:203], v[72:75], a[72:75], a[200:203]\n\t"
"ds_read_b64_tr_b16 v[234:235], v8 offset:20480\n\t"
"v_mfma_f32_16x16x32_bf16 a[232:235], v[72:75], a[76:79], a[232:235]\n\t"
"ds_read_b64_tr_b16 v[236:237], v9 offset:16384\n\t"
"s_waitcnt lgkmcnt(8)\n\t"
"v_mfma_f32_16x16x32_bf16 a[204:207], v[76:79], a[72:75], a[204:207]\n\t"
"ds_read_b64_tr_b16 v[238:239], v9 offset:20480\n\t"
"v_mfma_f32_16x16x32_bf16 a[236:239], v[76:79], a[76:79], a[236:239]\n\t"
"ds_read_b64_tr_b16 v[240:241], v10 offset:16384\n\t"
"ds_read_b64_tr_b16 v[242:243], v10 offset:20480\n\t"
"v_mfma_f32_16x16x32_bf16 a[208:211], v[80:83], a[72:75], a[208:211]\n\t"
"ds_read_b64_tr_b16 v[244:245], v11 offset:16384\n\t"
"v_mfma_f32_16x16x32_bf16 a[240:243], v[80:83], a[76:79], a[240:243]\n\t"
"ds_read_b64_tr_b16 v[246:247], v11 offset:20480\n\t"
"v_mfma_f32_16x16x32_bf16 a[212:215], v[84:87], a[72:75], a[212:215]\n\t"
"ds_read_b64_tr_b16 v[248:249], v12 offset:16384\n\t"
"v_mfma_f32_16x16x32_bf16 a[244:247], v[84:87], a[76:79], a[244:247]\n\t"
"ds_read_b64_tr_b16 v[250:251], v12 offset:20480\n\t"
"v_mfma_f32_16x16x32_bf16 a[216:219], v[88:91], a[72:75], a[216:219]\n\t"
"ds_read_b64_tr_b16 v[252:253], v13 offset:16384\n\t"
"s_waitcnt lgkmcnt(9)\n\t"
"v_mfma_f32_16x16x32_bf16 a[248:251], v[88:91], a[76:79], a[248:251]\n\t"
"ds_read_b64_tr_b16 v[254:255], v13 offset:20480\n\t"
"v_mfma_f32_16x16x32_bf16 a[220:223], v[92:95], a[72:75], a[220:223]\n\t"
"v_mfma_f32_16x16x32_bf16 a[252:255], v[92:95], a[76:79], a[252:255]\n\t"
"v_mfma_f32_16x16x32_bf16 a[192:195], v[224:227], a[80:83], a[192:195]\n\t"
"ds_read_b64_tr_b16 v[64:65], v6 offset:24576\n\t"
"ds_read_b64_tr_b16 v[66:67], v6 offset:28672\n\t"
"v_mfma_f32_16x16x32_bf16 a[224:227], v[224:227], a[84:87], a[224:227]\n\t"
"ds_read_b64_tr_b16 v[68:69], v7 offset:24576\n\t"
"v_mfma_f32_16x16x32_bf16 a[196:199], v[228:231], a[80:83], a[196:199]\n\t"
"ds_read_b64_tr_b16 v[70:71], v7 offset:28672\n\t"
"v_mfma_f32_16x16x32_bf16 a[228:231], v[228:231], a[84:87], a[228:231]\n\t"
"ds_read_b64_tr_b16 v[72:73], v8 offset:24576\n\t"
"v_mfma_f32_16x16x32_bf16 a[200:203], v[232:235], a[80:83], a[200:203]\n\t"
"ds_read_b64_tr_b16 v[74:75], v8 offset:28672\n\t"
"s_waitcnt lgkmcnt(8)\n\t"
"v_mfma_f32_16x16x32_bf16 a[232:235], v[232:235], a[84:87], a[232:235]\n\t"
"ds_read_b64_tr_b16 v[76:77], v9 offset:24576\n\t"
"v_mfma_f32_16x16x32_bf16 a[204:207], v[236:239], a[80:83], a[204:207]\n\t"
"ds_read_b64_tr_b16 v[78:79], v9 offset:28672\n\t"
"v_mfma_f32_16x16x32_bf16 a[236:239], v[236:239], a[84:87], a[236:239]\n\t"
"ds_read_b64_tr_b16 v[80:81], v10 offset:24576\n\t"
"ds_read_b64_tr_b16 v[82:83], v10 offset:28672\n\t"
"v_mfma_f32_16x16x32_bf16 a[208:211], v[240:243], a[80:83], a[208:211]\n\t"
"ds_read_b64_tr_b16 v[84:85], v11 offset:24576\n\t"
"v_mfma_f32_16x16x32_bf16 a[240:243], v[240:243], a[84:87], a[240:243]\n\t"
"ds_read_b64_tr_b16 v[86:87], v11 offset:28672\n\t"
"v_mfma_f32_16x16x32_bf16 a[212:215], v[244:247], a[80:83], a[212:215]\n\t"
"ds_read_b64_tr_b16 v[88:89], v12 offset:24576\n\t"
"v_mfma_f32_16x16x32_bf16 a[244:247], v[244:247], a[84:87], a[244:247]\n\t"
"ds_read_b64_tr_b16 v[90:91], v12 offset:28672\n\t"
"s_waitcnt lgkmcnt(9)\n\t"
"v_mfma_f32_16x16x32_bf16 a[216:219], v[248:251], a[80:83], a[216:219]\n\t"
"ds_read_b64_tr_b16 v[92:93], v13 offset:24576\n\t"
"v_mfma_f32_16x16x32_bf16 a[248:251], v[248:251], a[84:87], a[248:251]\n\t"
"ds_read_b64_tr_b16 v[94:95], v13 offset:28672\n\t"
"v_mfma_f32_16x16x32_bf16 a[220:223], v[252:255], a[80:83], a[220:223]\n\t"
"v_mfma_f32_16x16x32_bf16 a[252:255], v[252:255], a[84:87], a[252:255]\n\t"
"v_mfma_f32_16x16x32_bf16 a[192:195], v[64:67], a[88:91], a[192:195]\n\t"
"ds_read_b64_tr_b16 v[224:225], v6 offset:32768\n\t"
"ds_read_b64_tr_b16 v[226:227], v6 offset:36864\n\t"
"v_mfma_f32_16x16x32_bf16 a[224:227], v[64:67], a[92:95], a[224:227]\n\t"
"ds_read_b64_tr_b16 v[228:229], v7 offset:32768\n\t"
"v_mfma_f32_16x16x32_bf16 a[196:199], v[68:71], a[88:91], a[196:199]\n\t"
"ds_read_b64_tr_b16 v[230:231], v7 offset:36864\n\t"
"v_mfma_f32_16x16x32_bf16 a[228:231], v[68:71], a[92:95], a[228:231]\n\t"
"ds_read_b64_tr_b16 v[232:233], v8 offset:32768\n\t"
"s_waitcnt lgkmcnt(8)\n\t"
"v_mfma_f32_16x16x32_bf16 a[200:203], v[72:75], a[88:91], a[200:203]\n\t"
"ds_read_b64_tr_b16 v[234:235], v8 offset:36864\n\t"
"v_mfma_f32_16x16x32_bf16 a[232:235], v[72:75], a[92:95], a[232:235]\n\t"
"ds_read_b64_tr_b16 v[236:237], v9 offset:32768\n\t"
"v_mfma_f32_16x16x32_bf16 a[204:207], v[76:79], a[88:91], a[204:207]\n\t"
"ds_read_b64_tr_b16 v[238:239], v9 offset:36864\n\t"
"v_mfma_f32_16x16x32_bf16 a[236:239], v[76:79], a[92:95], a[236:239]\n\t"
"ds_read_b64_tr_b16 v[240:241], v10 offset:32768\n\t"
"ds_read_b64_tr_b16 v[242:243], v10 offset:36864\n\t"
"v_mfma_f32_16x16x32_bf16 a[208:211], v[80:83], a[88:91], a[208:211]\n\t"
"ds_read_b64_tr_b16 v[244:245], v11 offset:32768\n\t"
"v_mfma_f32_16x16x32_bf16 a[240:243], v[80:83], a[92:95], a[240:243]\n\t"
"ds_read_b64_tr_b16 v[246:247], v11 offset:36864\n\t"
"v_mfma_f32_16x16x32_bf16 a[212:215], v[84:87], a[88:91], a[212:215]\n\t"
"ds_read_b64_tr_b16 v[248:249], v12 offset:32768\n\t"
"s_waitcnt lgkmcnt(9)\n\t"
"v_mfma_f32_16x16x32_bf16 a[244:247], v[84:87], a[92:95], a[244:247]\n\t"
"ds_read_b64_tr_b16 v[250:251], v12 offset:36864\n\t"
"v_mfma_f32_16x16x32_bf16 a[216:219], v[88:91], a[88:91], a[216:219]\n\t"
"ds_read_b64_tr_b16 v[252:253], v13 offset:32768\n\t"
"v_mfma_f32_16x16x32_bf16 a[248:251], v[88:91], a[92:95], a[248:251]\n\t"
"ds_read_b64_tr_b16 v[254:255], v13 offset:36864\n\t"
"v_mfma_f32_16x16x32_bf16 a[220:223], v[92:95], a[88:91], a[220:223]\n\t"
"v_mfma_f32_16x16x32_bf16 a[252:255], v[92:95], a[92:95], a[252:255]\n\t"
"s_waitcnt vmcnt(8)\n\t"
"s_waitcnt vmcnt(0)\n\t"
"s_lshl_b32 s69, s73, 2\n\t"
"s_add_u32 s69, s69, s72\n\t"
"s_lshl_b32 s69, s69, 3\n\t"
"s_lshl_b32 s70, s39, 1\n\t"
"s_add_u32 s69, s69, s70\n\t"
"s_lshl_b32 s69, s69, 6\n\t"
"s_add_u32 s69, s69, 34816\n\t"
"s_lshl_b32 s70, s71, 3\n\t"
"s_add_u32 s69, s69, s70\n\t"
"v_mov_b32 v60, s69\n\t"
"v_mov_b32 v62, s26\n\t"
"v_mov_b32 v63, s27\n\t"
"s_mov_b64 s[76:77], exec\n\t"
"s_mov_b64 exec, 1\n\t"
"s_nop 1\n\t"
"global_store_dwordx2 v60, v[62:63], s[42:43] sc1\n\t"
"s_mov_b64 exec, s[76:77]\n\t"
"s_nop 1\n\t"
"s_add_u32 s69, s69, 64\n\t"
"v_mov_b32 v60, s69\n\t"
"v_mov_b32 v62, s26\n\t"
"v_mov_b32 v63, s27\n\t"
"s_mov_b64 s[76:77], exec\n\t"
"s_mov_b64 exec, 1\n\t"
"s_nop 1\n\t"
"global_store_dwordx2 v60, v[62:63], s[42:43] sc1\n\t"
"s_mov_b64 exec, s[76:77]\n\t"
"s_nop 1\n\t"
"v_mfma_f32_16x16x32_bf16 a[192:195], v[224:227], a[96:99], a[192:195]\n\t"
"ds_read_b64_tr_b16 v[64:65], v6 offset:40960\n\t"
"ds_read_b64_tr_b16 v[66:67], v6 offset:45056\n\t"
"v_mfma_f32_16x16x32_bf16 a[224:227], v[224:227], a[100:103], a[224:227]\n\t"
"ds_read_b64_tr_b16 v[68:69], v7 offset:40960\n\t"
"v_mfma_f32_16x16x32_bf16 a[196:199], v[228:231], a[96:99], a[196:199]\n\t"
"ds_read_b64_tr_b16 v[70:71], v7 offset:45056\n\t"
"s_waitcnt lgkmcnt(4)\n\t"
"v_mfma_f32_16x16x32_bf16 a[228:231], v[228:231], a[100:103], a[228:231]\n\t"
"ds_read_b64_tr_b16 v[72:73], v8 offset:40960\n\t"
"v_mfma_f32_16x16x32_bf16 a[200:203], v[232:235], a[96:99], a[200:203]\n\t"
"ds_read_b64_tr_b16 v[74:75], v8 offset:45056\n\t"
"v_mfma_f32_16x16x32_bf16 a[232:235], v[232:235], a[100:103], a[232:235]\n\t"
"ds_read_b64_tr_b16 v[76:77], v9 offset:40960\n\t"
"v_mfma_f32_16x16x32_bf16 a[204:207], v[236:239], a[96:99], a[204:207]\n\t"
"ds_read_b64_tr_b16 v[78:79], v9 offset:45056\n\t"
"v_mfma_f32_16x16x32_bf16 a[236:239], v[236:239], a[100:103], a[236:239]\n\t"
"ds_read_b64_tr_b16 v[80:81], v10 offset:40960\n\t"
"ds_read_b64_tr_b16 v[82:83], v10 offset:45056\n\t"
"v_mfma_f32_16x16x32_bf16 a[208:211], v[240:243], a[96:99], a[208:211]\n\t"
"ds_read_b64_tr_b16 v[84:85], v11 offset:40960\n\t"
"v_mfma_f32_16x16x32_bf16 a[240:243], v[240:243], a[100:103], a[240:243]\n\t"
"ds_read_b64_tr_b16 v[86:87], v11 offset:45056\n\t"
"v_mfma_f32_16x16x32_bf16 a[212:215], v[244:247], a[96:99], a[212:215]\n\t"
"ds_read_b64_tr_b16 v[88:89], v12 offset:40960\n\t"
"v_mfma_f32_16x16x32_bf16 a[244:247], v[244:247], a[100:103], a[244:247]\n\t"
"ds_read_b64_tr_b16 v[90:91], v12 offset:45056\n\t"
"v_mfma_f32_16x16x32_bf16 a[216:219], v[248:251], a[96:99], a[216:219]\n\t"
"ds_read_b64_tr_b16 v[92:93], v13 offset:40960\n\t"
"v_mfma_f32_16x16x32_bf16 a[248:251], v[248:251], a[100:103], a[248:251]\n\t"
"ds_read_b64_tr_b16 v[94:95], v13 offset:45056\n\t"
"s_waitcnt lgkmcnt(9)\n\t"
"v_mfma_f32_16x16x32_bf16 a[220:223], v[252:255], a[96:99], a[220:223]\n\t"
"v_mfma_f32_16x16x32_bf16 a[252:255], v[252:255], a[100:103], a[252:255]\n\t"
"v_mfma_f32_16x16x32_bf16 a[192:195], v[64:67], a[104:107], a[192:195]\n\t"
"ds_read_b64_tr_b16 v[224:225], v6 offset:49152\n\t"
"ds_read_b64_tr_b16 v[226:227], v6 offset:53248\n\t"
"v_mfma_f32_16x16x32_bf16 a[224:227], v[64:67], a[108:111], a[224:227]\n\t"
"ds_read_b64_tr_b16 v[228:229], v7 offset:49152\n\t"
"v_mfma_f32_16x16x32_bf16 a[196:199], v[68:71], a[104:107], a[196:199]\n\t"
"ds_read_b64_tr_b16 v[230:231], v7 offset:53248\n\t"
"v_mfma_f32_16x16x32_bf16 a[228:231], v[68:71], a[108:111], a[228:231]\n\t"
"ds_read_b64_tr_b16 v[232:233], v8 offset:49152\n\t"
"v_mfma_f32_16x16x32_bf16 a[200:203], v[72:75], a[104:107], a[200:203]\n\t"
"ds_read_b64_tr_b16 v[234:235], v8 offset:53248\n\t"
"v_mfma_f32_16x16x32_bf16 a[232:235], v[72:75], a[108:111], a[232:235]\n\t"
"ds_read_b64_tr_b16 v[236:237], v9 offset:49152\n\t"
"s_waitcnt lgkmcnt(8)\n\t"
"v_mfma_f32_16x16x32_bf16 a[204:207], v[76:79], a[104:107], a[204:207]\n\t"
"ds_read_b64_tr_b16 v[238:239], v9 offset:53248\n\t"
"v_mfma_f32_16x16x32_bf16 a[236:239], v[76:79], a[108:111], a[236:239]\n\t"
"ds_read_b64_tr_b16 v[240:241], v10 offset:49152\n\t"
"ds_read_b64_tr_b16 v[242:243], v10 offset:53248\n\t"
"v_mfma_f32_16x16x32_bf16 a[208:211], v[80:83], a[104:107], a[208:211]\n\t"
"ds_read_b64_tr_b16 v[244:245], v11 offset:49152\n\t"
"v_mfma_f32_16x16x32_bf16 a[240:243], v[80:83], a[108:111], a[240:243]\n\t"
"ds_read_b64_tr_b16 v[246:247], v11 offset:53248\n\t"
"v_mfma_f32_16x16x32_bf16 a[212:215], v[84:87], a[104:107], a[212:215]\n\t"
"ds_read_b64_tr_b16 v[248:249], v12 offset:49152\n\t"
"v_mfma_f32_16x16x32_bf16 a[244:247], v[84:87], a[108:111], a[244:247]\n\t"
"ds_read_b64_tr_b16 v[250:251], v12 offset:53248\n\t"
"v_mfma_f32_16x16x32_bf16 a[216:219], v[88:91], a[104:107], a[216:219]\n\t"
"ds_read_b64_tr_b16 v[252:253], v13 offset:49152\n\t"
"s_waitcnt lgkmcnt(9)\n\t"
"v_mfma_f32_16x16x32_bf16 a[248:251], v[88:91], a[108:111], a[248:251]\n\t"
"ds_read_b64_tr_b16 v[254:255], v13 offset:53248\n\t"
"v_mfma_f32_16x16x32_bf16 a[220:223], v[92:95], a[104:107], a[220:223]\n\t"
"v_mfma_f32_16x16x32_bf16 a[252:255], v[92:95], a[108:111], a[252:255]\n\t"
"v_mfma_f32_16x16x32_bf16 a[192:195], v[224:227], a[112:115], a[192:195]\n\t"
"ds_read_b64_tr_b16 v[64:65], v6 offset:57344\n\t"
"ds_read_b64_tr_b16 v[66:67], v6 offset:61440\n\t"
"v_mfma_f32_16x16x32_bf16 a[224:227], v[224:227], a[116:119], a[224:227]\n\t"
"ds_read_b64_tr_b16 v[68:69], v7 offset:57344\n\t"
"v_mfma_f32_16x16x32_bf16 a[196:199], v[228:231], a[112:115], a[196:199]\n\t"
"ds_read_b64_tr_b16 v[70:71], v7 offset:61440\n\t"
"v_mfma_f32_16x16x32_bf16 a[228:231], v[228:231], a[116:119], a[228:231]\n\t"
"ds_read_b64_tr_b16 v[72:73], v8 offset:57344\n\t"
"v_mfma_f32_16x16x32_bf16 a[200:203], v[232:235], a[112:115], a[200:203]\n\t"
"ds_read_b64_tr_b16 v[74:75], v8 offset:61440\n\t"
"s_waitcnt lgkmcnt(8)\n\t"
"v_mfma_f32_16x16x32_bf16 a[232:235], v[232:235], a[116:119], a[232:235]\n\t"
"ds_read_b64_tr_b16 v[76:77], v9 offset:57344\n\t"
"v_mfma_f32_16x16x32_bf16 a[204:207], v[236:239], a[112:115], a[204:207]\n\t"
"ds_read_b64_tr_b16 v[78:79], v9 offset:61440\n\t"
"v_mfma_f32_16x16x32_bf16 a[236:239], v[236:239], a[116:119], a[236:239]\n\t"
"ds_read_b64_tr_b16 v[80:81], v10 offset:57344\n\t"
"ds_read_b64_tr_b16 v[82:83], v10 offset:61440\n\t"
"v_mfma_f32_16x16x32_bf16 a[208:211], v[240:243], a[112:115], a[208:211]\n\t"
"ds_read_b64_tr_b16 v[84:85], v11 offset:57344\n\t"
"v_mfma_f32_16x16x32_bf16 a[240:243], v[240:243], a[116:119], a[240:243]\n\t"
"ds_read_b64_tr_b16 v[86:87], v11 offset:61440\n\t"
"v_mfma_f32_16x16x32_bf16 a[212:215], v[244:247], a[112:115], a[212:215]\n\t"
"ds_read_b64_tr_b16 v[88:89], v12 offset:57344\n\t"
"v_mfma_f32_16x16x32_bf16 a[244:247], v[244:247], a[116:119], a[244:247]\n\t"
"ds_read_b64_tr_b16 v[90:91], v12 offset:61440\n\t"
"s_waitcnt lgkmcnt(9)\n\t"
"v_mfma_f32_16x16x32_bf16 a[216:219], v[248:251], a[112:115], a[216:219]\n\t"
"ds_read_b64_tr_b16 v[92:93], v13 offset:57344\n\t"
"v_mfma_f32_16x16x32_bf16 a[248:251], v[248:251], a[116:119], a[248:251]\n\t"
"ds_read_b64_tr_b16 v[94:95], v13 offset:61440\n\t"
"v_mfma_f32_16x16x32_bf16 a[220:223], v[252:255], a[112:115], a[220:223]\n\t"
"v_mfma_f32_16x16x32_bf16 a[252:255], v[252:255], a[116:119], a[252:255]\n\t"
"v_mfma_f32_16x16x32_bf16 a[192:195], v[64:67], a[120:123], a[192:195]\n\t"
"v_mfma_f32_16x16x32_bf16 a[224:227], v[64:67], a[124:127], a[224:227]\n\t"
"v_mfma_f32_16x16x32_bf16 a[196:199], v[68:71], a[120:123], a[196:199]\n\t"
"v_mfma_f32_16x16x32_bf16 a[228:231], v[68:71], a[124:127], a[228:231]\n\t"
"s_waitcnt lgkmcnt(3)\n\t"
"v_mfma_f32_16x16x32_bf16 a[200:203], v[72:75], a[120:123], a[200:203]\n\t"
"v_mfma_f32_16x16x32_bf16 a[232:235], v[72:75], a[124:127], a[232:235]\n\t"
"v_mfma_f32_16x16x32_bf16 a[204:207], v[76:79], a[120:123], a[204:207]\n\t"
"v_mfma_f32_16x16x32_bf16 a[236:239], v[76:79], a[124:127], a[236:239]\n\t"
"v_mfma_f32_16x16x32_bf16 a[208:211], v[80:83], a[120:123], a[208:211]\n\t"
"v_mfma_f32_16x16x32_bf16 a[240:243], v[80:83], a[124:127], a[240:243]\n\t"
"v_mfma_f32_16x16x32_bf16 a[212:215], v[84:87], a[120:123], a[212:215]\n\t"
"v_mfma_f32_16x16x32_bf16 a[244:247], v[84:87], a[124:127], a[244:247]\n\t"
"s_waitcnt lgkmcnt(0)\n\t"
"v_mfma_f32_16x16x32_bf16 a[216:219], v[88:91], a[120:123], a[216:219]\n\t"
"v_mfma_f32_16x16x32_bf16 a[248:251], v[88:91], a[124:127], a[248:251]\n\t"
"v_mfma_f32_16x16x32_bf16 a[220:223], v[92:95], a[120:123], a[220:223]\n\t"
"v_mfma_f32_16x16x32_bf16 a[252:255], v[92:95], a[124:127], a[252:255]\n\t"
"s_nop 7\n\t"
"s_lshl_b32 s69, s74, 2\n\t"
"s_add_u32 s69, s69, s72\n\t"
"s_lshl_b32 s69, s69, 3\n\t"
"s_lshl_b32 s70, s39, 1\n\t"
"s_add_u32 s69, s69, s70\n\t"
"s_lshl_b32 s69, s69, 3\n\t"
"s_add_u32 s69, s69, s71\n\t"
"s_lshl_b32 s75, s69, 12\n\t"
"s_add_u32 s78, s75, 32768\n\t"
"v_accvgpr_read_b32 v32, a192\n\t"
"v_accvgpr_read_b32 v33, a193\n\t"
"v_accvgpr_read_b32 v34, a194\n\t"
"v_accvgpr_read_b32 v35, a195\n\t"
"v_accvgpr_read_b32 v36, a196\n\t"
"v_accvgpr_read_b32 v37, a197\n\t"
"v_accvgpr_read_b32 v38, a198\n\t"
"v_accvgpr_read_b32 v39, a199\n\t"
"v_cvt_pk_bf16_f32 v40, v32, v33\n\t"
"v_cvt_pk_bf16_f32 v41, v34, v35\n\t"
"v_cvt_pk_bf16_f32 v42, v36, v37\n\t"
"v_cvt_pk_bf16_f32 v43, v38, v39\n\t"
"buffer_store_dwordx4 v[40:43], v0, s[60:63], s75 offen offset:0 sc1\n\t"
"v_accvgpr_read_b32 v44, a200\n\t"
"v_accvgpr_read_b32 v45, a201\n\t"
"v_accvgpr_read_b32 v46, a202\n\t"
"v_accvgpr_read_b32 v47, a203\n\t"
"v_accvgpr_read_b32 v48, a204\n\t"
"v_accvgpr_read_b32 v49, a205\n\t"
"v_accvgpr_read_b32 v50, a206\n\t"
"v_accvgpr_read_b32 v51, a207\n\t"
"v_cvt_pk_bf16_f32 v52, v44, v45\n\t"
"v_cvt_pk_bf16_f32 v53, v46, v47\n\t"
"v_cvt_pk_bf16_f32 v54, v48, v49\n\t"
"v_cvt_pk_bf16_f32 v55, v50, v51\n\t"
"buffer_store_dwordx4 v[52:55], v0, s[60:63], s75 offen offset:1024 sc1\n\t"
"v_accvgpr_read_b32 v32, a208\n\t"
"v_accvgpr_read_b32 v33, a209\n\t"
"v_accvgpr_read_b32 v34, a210\n\t"
"v_accvgpr_read_b32 v35, a211\n\t"
"v_accvgpr_read_b32 v36, a212\n\t"
"v_accvgpr_read_b32 v37, a213\n\t"
"v_accvgpr_read_b32 v38, a214\n\t"
"v_accvgpr_read_b32 v39, a215\n\t"
"v_cvt_pk_bf16_f32 v40, v32, v33\n\t"
"v_cvt_pk_bf16_f32 v41, v34, v35\n\t"
"v_cvt_pk_bf16_f32 v42, v36, v37\n\t"
"v_cvt_pk_bf16_f32 v43, v38, v39\n\t"
"buffer_store_dwordx4 v[40:43], v0, s[60:63], s75 offen offset:2048 sc1\n\t"
"v_accvgpr_read_b32 v44, a216\n\t"
"v_accvgpr_read_b32 v45, a217\n\t"
"v_accvgpr_read_b32 v46, a218\n\t"
"v_accvgpr_read_b32 v47, a219\n\t"
"v_accvgpr_read_b32 v48, a220\n\t"
"v_accvgpr_read_b32 v49, a221\n\t"
"v_accvgpr_read_b32 v50, a222\n\t"
"v_accvgpr_read_b32 v51, a223\n\t"
"v_cvt_pk_bf16_f32 v52, v44, v45\n\t"
"v_cvt_pk_bf16_f32 v53, v46, v47\n\t"
"v_cvt_pk_bf16_f32 v54, v48, v49\n\t"
"v_cvt_pk_bf16_f32 v55, v50, v51\n\t"
"buffer_store_dwordx4 v[52:55], v0, s[60:63], s75 offen offset:3072 sc1\n\t"
"v_accvgpr_read_b32 v32, a224\n\t"
"v_accvgpr_read_b32 v33, a225\n\t"
"v_accvgpr_read_b32 v34, a226\n\t"
"v_accvgpr_read_b32 v35, a227\n\t"
"v_accvgpr_read_b32 v36, a228\n\t"
"v_accvgpr_read_b32 v37, a229\n\t"
"v_accvgpr_read_b32 v38, a230\n\t"
"v_accvgpr_read_b32 v39, a231\n\t"
"v_cvt_pk_bf16_f32 v40, v32, v33\n\t"
"v_cvt_pk_bf16_f32 v41, v34, v35\n\t"
"v_cvt_pk_bf16_f32 v42, v36, v37\n\t"
"v_cvt_pk_bf16_f32 v43, v38, v39\n\t"
"buffer_store_dwordx4 v[40:43], v0, s[60:63], s78 offen offset:0 sc1\n\t"
"v_accvgpr_read_b32 v44, a232\n\t"
"v_accvgpr_read_b32 v45, a233\n\t"
"v_accvgpr_read_b32 v46, a234\n\t"
"v_accvgpr_read_b32 v47, a235\n\t"
"v_accvgpr_read_b32 v48, a236\n\t"
"v_accvgpr_read_b32 v49, a237\n\t"
"v_accvgpr_read_b32 v50, a238\n\t"
"v_accvgpr_read_b32 v51, a239\n\t"
"v_cvt_pk_bf16_f32 v52, v44, v45\n\t"
"v_cvt_pk_bf16_f32 v53, v46, v47\n\t"
"v_cvt_pk_bf16_f32 v54, v48, v49\n\t"
"v_cvt_pk_bf16_f32 v55, v50, v51\n\t"
"buffer_store_dwordx4 v[52:55], v0, s[60:63], s78 offen offset:1024 sc1\n\t"
"v_accvgpr_read_b32 v32, a240\n\t"
"v_accvgpr_read_b32 v33, a241\n\t"
"v_accvgpr_read_b32 v34, a242\n\t"
"v_accvgpr_read_b32 v35, a243\n\t"
"v_accvgpr_read_b32 v36, a244\n\t"
"v_accvgpr_read_b32 v37, a245\n\t"
"v_accvgpr_read_b32 v38, a246\n\t"
"v_accvgpr_read_b32 v39, a247\n\t"
"v_cvt_pk_bf16_f32 v40, v32, v33\n\t"
"v_cvt_pk_bf16_f32 v41, v34, v35\n\t"
"v_cvt_pk_bf16_f32 v42, v36, v37\n\t"
"v_cvt_pk_bf16_f32 v43, v38, v39\n\t"
"buffer_store_dwordx4 v[40:43], v0, s[60:63], s78 offen offset:2048 sc1\n\t"
"v_accvgpr_read_b32 v44, a248\n\t"
"v_accvgpr_read_b32 v45, a249\n\t"
"v_accvgpr_read_b32 v46, a250\n\t"
"v_accvgpr_read_b32 v47, a251\n\t"
"v_accvgpr_read_b32 v48, a252\n\t"
"v_accvgpr_read_b32 v49, a253\n\t"
"v_accvgpr_read_b32 v50, a254\n\t"
"v_accvgpr_read_b32 v51, a255\n\t"
"v_cvt_pk_bf16_f32 v52, v44, v45\n\t"
"v_cvt_pk_bf16_f32 v53, v46, v47\n\t"
"v_cvt_pk_bf16_f32 v54, v48, v49\n\t"
"v_cvt_pk_bf16_f32 v55, v50, v51\n\t"
"buffer_store_dwordx4 v[52:55], v0, s[60:63], s78 offen offset:3072 sc1\n\t"
"s_lshl_b32 s69, s73, 2\n\t"
"s_add_u32 s69, s69, s72\n\t"
"s_lshl_b32 s69, s69, 3\n\t"
"s_add_u32 s69, s69, s71\n\t"
"s_lshl_b32 s70, s69, 6\n\t"
"s_add_u32 s70, s70, 34816\n\t"
"s_lshl_b32 s79, s69, 15\n\t"
"s_lshl_b32 s69, s39, 10\n\t"
"s_add_u32 s79, s79, s69\n\t"
"v_mbcnt_lo_u32_b32 v57, -1, 0\n\t"
"v_mbcnt_hi_u32_b32 v57, -1, v57\n\t"
"v_min_u32 v57, 7, v57\n\t"
"v_lshl_add_u32 v57, v57, 3, s70\n\t"
"global_load_dwordx2 v[58:59], v57, s[42:43] sc1\n\t"
"s_waitcnt vmcnt(0)\n\t"
"s_lshl_b32 s69, s74, 2\n\t"
"s_add_u32 s69, s69, s72\n\t"
"s_lshl_b32 s69, s69, 3\n\t"
"s_lshl_b32 s70, s39, 1\n\t"
"s_add_u32 s69, s69, s70\n\t"
"s_lshl_b32 s69, s69, 6\n\t"
"s_add_u32 s69, s69, 34816\n\t"
"s_lshl_b32 s70, s71, 3\n\t"
"s_add_u32 s69, s69, s70\n\t"
"v_mov_b32 v60, s69\n\t"
"v_mov_b32 v62, s26\n\t"
"v_mov_b32 v63, s27\n\t"
"s_mov_b64 s[76:77], exec\n\t"
"s_mov_b64 exec, 1\n\t"
"s_nop 1\n\t"
"global_store_dwordx2 v60, v[62:63], s[42:43] sc1\n\t"
"s_mov_b64 exec, s[76:77]\n\t"
"s_nop 1\n\t"
"s_add_u32 s69, s69, 64\n\t"
"v_mov_b32 v60, s69\n\t"
"v_mov_b32 v62, s26\n\t"
"v_mov_b32 v63, s27\n\t"
"s_mov_b64 s[76:77], exec\n\t"
"s_mov_b64 exec, 1\n\t"
"s_nop 1\n\t"
"global_store_dwordx2 v60, v[62:63], s[42:43] sc1\n\t"
"s_mov_b64 exec, s[76:77]\n\t"
"s_nop 1\n\t"
"s_mov_b32 s70, 0\n\t"
"s_nop 0\n\t"
"LFP40_%=:\n\t"
"v_cmp_eq_u64 vcc, s[26:27], v[58:59]\n\t"
"s_nop 1\n\t"
"s_cmp_eq_u64 vcc, exec\n\t"
"s_cbranch_scc1 LFO41_%=\n\t"
"s_sleep 1\n\t"
"global_load_dwordx2 v[58:59], v57, s[42:43] sc1\n\t"
"s_waitcnt vmcnt(0)\n\t"
"s_add_u32 s70, s70, 1\n\t"
"s_cmp_lt_u32 s70, 0x40000\n\t"
"s_cbranch_scc1 LFP40_%=\n\t"
"LFO41_%=:\n\t"
"s_add_u32 s69, s79, 0\n\t"
"buffer_load_dwordx4 v[96:99], v0, s[60:63], s69 offen offset:0 sc1\n\t"
"s_add_u32 s69, s79, 4096\n\t"
"buffer_load_dwordx4 v[100:103], v0, s[60:63], s69 offen offset:0 sc1\n\t"
"s_add_u32 s69, s79, 8192\n\t"
"buffer_load_dwordx4 v[104:107], v0, s[60:63], s69 offen offset:0 sc1\n\t"
"s_add_u32 s69, s79, 12288\n\t"
"buffer_load_dwordx4 v[108:111], v0, s[60:63], s69 offen offset:0 sc1\n\t"
"s_add_u32 s69, s79, 16384\n\t"
"buffer_load_dwordx4 v[112:115], v0, s[60:63], s69 offen offset:0 sc1\n\t"
"s_add_u32 s69, s79, 20480\n\t"
"buffer_load_dwordx4 v[116:119], v0, s[60:63], s69 offen offset:0 sc1\n\t"
"s_add_u32 s69, s79, 24576\n\t"
"buffer_load_dwordx4 v[120:123], v0, s[60:63], s69 offen offset:0 sc1\n\t"
"s_add_u32 s69, s79, 28672\n\t"
"buffer_load_dwordx4 v[124:127], v0, s[60:63], s69 offen offset:0 sc1\n\t"
"s_lshl_b32 s69, s74, 2\n\t"
"s_add_u32 s69, s69, s72\n\t"
"s_lshl_b32 s69, s69, 3\n\t"
"s_add_u32 s69, s69, s71\n\t"
"s_lshl_b32 s70, s69, 6\n\t"
"s_add_u32 s70, s70, 34816\n\t"
"s_lshl_b32 s79, s69, 15\n\t"
"s_lshl_b32 s69, s39, 10\n\t"
"s_add_u32 s79, s79, s69\n\t"
"v_mbcnt_lo_u32_b32 v57, -1, 0\n\t"
"v_mbcnt_hi_u32_b32 v57, -1, v57\n\t"
"v_min_u32 v57, 7, v57\n\t"
"v_lshl_add_u32 v57, v57, 3, s70\n\t"
"global_load_dwordx2 v[58:59], v57, s[42:43] sc1\n\t"
"s_waitcnt vmcnt(1)\n\t"
"v_lshlrev_b32 v128, 16, v96\n\t"
"v_and_b32 v129, 0xffff0000, v96\n\t"
"v_lshlrev_b32 v130, 16, v97\n\t"
"v_and_b32 v131, 0xffff0000, v97\n\t"
"v_lshlrev_b32 v132, 16, v98\n\t"
"v_and_b32 v133, 0xffff0000, v98\n\t"
"v_lshlrev_b32 v134, 16, v99\n\t"
"v_and_b32 v135, 0xffff0000, v99\n\t"
"v_lshlrev_b32 v136, 16, v100\n\t"
"v_and_b32 v137, 0xffff0000, v100\n\t"
"v_add_f32 v128, v128, v136\n\t"
"v_add_f32 v129, v129, v137\n\t"
"v_lshlrev_b32 v138, 16, v101\n\t"
"v_and_b32 v139, 0xffff0000, v101\n\t"
"v_add_f32 v130, v130, v138\n\t"
"v_add_f32 v131, v131, v139\n\t"
"v_lshlrev_b32 v136, 16, v102\n\t"
"v_and_b32 v137, 0xffff0000, v102\n\t"
"v_add_f32 v132, v132, v136\n\t"
"v_add_f32 v133, v133, v137\n\t"
"v_lshlrev_b32 v138, 16, v103\n\t"
"v_and_b32 v139, 0xffff0000, v103\n\t"
"v_add_f32 v134, v134, v138\n\t"
"v_add_f32 v135, v135, v139\n\t"
"v_lshlrev_b32 v136, 16, v104\n\t"
"v_and_b32 v137, 0xffff0000, v104\n\t"
"v_add_f32 v128, v128, v136\n\t"
"v_add_f32 v129, v129, v137\n\t"
"v_lshlrev_b32 v138, 16, v105\n\t"
"v_and_b32 v139, 0xffff0000, v105\n\t"
"v_add_f32 v130, v130, v138\n\t"
"v_add_f32 v131, v131, v139\n\t"
"v_lshlrev_b32 v136, 16, v106\n\t"
"v_and_b32 v137, 0xffff0000, v106\n\t"
"v_add_f32 v132, v132, v136\n\t"
"v_add_f32 v133, v133, v137\n\t"
"v_lshlrev_b32 v138, 16, v107\n\t"
"v_and_b32 v139, 0xffff0000, v107\n\t"
"v_add_f32 v134, v134, v138\n\t"
"v_add_f32 v135, v135, v139\n\t"
"v_lshlrev_b32 v136, 16, v108\n\t"
"v_and_b32 v137, 0xffff0000, v108\n\t"
"v_add_f32 v128, v128, v136\n\t"
"v_add_f32 v129, v129, v137\n\t"
"v_lshlrev_b32 v138, 16, v109\n\t"
"v_and_b32 v139, 0xffff0000, v109\n\t"
"v_add_f32 v130, v130, v138\n\t"
"v_add_f32 v131, v131, v139\n\t"
"v_lshlrev_b32 v136, 16, v110\n\t"
"v_and_b32 v137, 0xffff0000, v110\n\t"
"v_add_f32 v132, v132, v136\n\t"
"v_add_f32 v133, v133, v137\n\t"
"v_lshlrev_b32 v138, 16, v111\n\t"
"v_and_b32 v139, 0xffff0000, v111\n\t"
"v_add_f32 v134, v134, v138\n\t"
"v_add_f32 v135, v135, v139\n\t"
"v_lshlrev_b32 v136, 16, v112\n\t"
"v_and_b32 v137, 0xffff0000, v112\n\t"
"v_add_f32 v128, v128, v136\n\t"
"v_add_f32 v129, v129, v137\n\t"
"v_lshlrev_b32 v138, 16, v113\n\t"
"v_and_b32 v139, 0xffff0000, v113\n\t"
"v_add_f32 v130, v130, v138\n\t"
"v_add_f32 v131, v131, v139\n\t"
"v_lshlrev_b32 v136, 16, v114\n\t"
"v_and_b32 v137, 0xffff0000, v114\n\t"
"v_add_f32 v132, v132, v136\n\t"
"v_add_f32 v133, v133, v137\n\t"
"v_lshlrev_b32 v138, 16, v115\n\t"
"v_and_b32 v139, 0xffff0000, v115\n\t"
"v_add_f32 v134, v134, v138\n\t"
"v_add_f32 v135, v135, v139\n\t"
"v_lshlrev_b32 v136, 16, v116\n\t"
"v_and_b32 v137, 0xffff0000, v116\n\t"
"v_add_f32 v128, v128, v136\n\t"
"v_add_f32 v129, v129, v137\n\t"
"v_lshlrev_b32 v138, 16, v117\n\t"
"v_and_b32 v139, 0xffff0000, v117\n\t"
"v_add_f32 v130, v130, v138\n\t"
"v_add_f32 v131, v131, v139\n\t"
"v_lshlrev_b32 v136, 16, v118\n\t"
"v_and_b32 v137, 0xffff0000, v118\n\t"
"v_add_f32 v132, v132, v136\n\t"
"v_add_f32 v133, v133, v137\n\t"
"v_lshlrev_b32 v138, 16, v119\n\t"
"v_and_b32 v139, 0xffff0000, v119\n\t"
"v_add_f32 v134, v134, v138\n\t"
"v_add_f32 v135, v135, v139\n\t"
"v_lshlrev_b32 v136, 16, v120\n\t"
"v_and_b32 v137, 0xffff0000, v120\n\t"
"v_add_f32 v128, v128, v136\n\t"
"v_add_f32 v129, v129, v137\n\t"
"v_lshlrev_b32 v138, 16, v121\n\t"
"v_and_b32 v139, 0xffff0000, v121\n\t"
"v_add_f32 v130, v130, v138\n\t"
"v_add_f32 v131, v131, v139\n\t"
"v_lshlrev_b32 v136, 16, v122\n\t"
"v_and_b32 v137, 0xffff0000, v122\n\t"
"v_add_f32 v132, v132, v136\n\t"
"v_add_f32 v133, v133, v137\n\t"
"v_lshlrev_b32 v138, 16, v123\n\t"
"v_and_b32 v139, 0xffff0000, v123\n\t"
"v_add_f32 v134, v134, v138\n\t"
"v_add_f32 v135, v135, v139\n\t"
"v_lshlrev_b32 v136, 16, v124\n\t"
"v_and_b32 v137, 0xffff0000, v124\n\t"
"v_add_f32 v128, v128, v136\n\t"
"v_add_f32 v129, v129, v137\n\t"
"v_lshlrev_b32 v138, 16, v125\n\t"
"v_and_b32 v139, 0xffff0000, v125\n\t"
"v_add_f32 v130, v130, v138\n\t"
"v_add_f32 v131, v131, v139\n\t"
"v_lshlrev_b32 v136, 16, v126\n\t"
"v_and_b32 v137, 0xffff0000, v126\n\t"
"v_add_f32 v132, v132, v136\n\t"
"v_add_f32 v133, v133, v137\n\t"
"v_lshlrev_b32 v138, 16, v127\n\t"
"v_and_b32 v139, 0xffff0000, v127\n\t"
"v_add_f32 v134, v134, v138\n\t"
"v_add_f32 v135, v135, v139\n\t"
"s_lshl_b32 s68, s73, 19\n\t"
"s_lshl_b32 s69, s71, 16\n\t"
"s_add_u32 s68, s68, s69\n\t"
"s_lshl_b32 s69, s72, 9\n\t"
"s_add_u32 s68, s68, s69\n\t"
"s_lshl_b32 s69, s39, 7\n\t"
"s_add_u32 s68, s68, s69\n\t"
"buffer_store_dwordx4 v[128:131], v16, s[56:59], s68 offen offset:0\n\t"
"buffer_store_dwordx4 v[132:135], v16, s[56:59], s68 offen offset:64\n\t"
"s_mov_b32 s70, 0\n\t"
"s_waitcnt vmcnt(2)\n\t"
"LFP42_%=:\n\t"
"v_cmp_eq_u64 vcc, s[26:27], v[58:59]\n\t"
"s_nop 1\n\t"
"s_cmp_eq_u64 vcc, exec\n\t"
"s_cbranch_scc1 LFO43_%=\n\t"
"s_sleep 1\n\t"
"global_load_dwordx2 v[58:59], v57, s[42:43] sc1\n\t"
"s_waitcnt vmcnt(0)\n\t"
"s_add_u32 s70, s70, 1\n\t"
"s_cmp_lt_u32 s70, 0x40000\n\t"
"s_cbranch_scc1 LFP42_%=\n\t"
"LFO43_%=:\n\t"
"s_add_u32 s69, s79, 0\n\t"
"buffer_load_dwordx4 v[160:163], v0, s[60:63], s69 offen offset:0 sc1\n\t"
"s_add_u32 s69, s79, 4096\n\t"
"buffer_load_dwordx4 v[164:167], v0, s[60:63], s69 offen offset:0 sc1\n\t"
"s_add_u32 s69, s79, 8192\n\t"
"buffer_load_dwordx4 v[168:171], v0, s[60:63], s69 offen offset:0 sc1\n\t"
"s_add_u32 s69, s79, 12288\n\t"
"buffer_load_dwordx4 v[172:175], v0, s[60:63], s69 offen offset:0 sc1\n\t"
"s_add_u32 s69, s79, 16384\n\t"
"buffer_load_dwordx4 v[176:179], v0, s[60:63], s69 offen offset:0 sc1\n\t"
"s_add_u32 s69, s79, 20480\n\t"
"buffer_load_dwordx4 v[180:183], v0, s[60:63], s69 offen offset:0 sc1\n\t"
"s_add_u32 s69, s79, 24576\n\t"
"buffer_load_dwordx4 v[184:187], v0, s[60:63], s69 offen offset:0 sc1\n\t"
"s_add_u32 s69, s79, 28672\n\t"
"buffer_load_dwordx4 v[188:191], v0, s[60:63], s69 offen offset:0 sc1\n\t"
"s_or_b32 s69, s38, s39\n\t"
"s_cmp_lg_u32 s69, 0\n\t"
"s_cbranch_scc1 LEP44_%=\n\t"
"s_mov_b32 s69, 69632\n\t"
"v_mov_b32 v60, s69\n\t"
"v_mov_b32 v62, s26\n\t"
"v_mov_b32 v63, s27\n\t"
"s_mov_b64 s[76:77], exec\n\t"
"s_mov_b64 exec, 1\n\t"
"s_nop 1\n\t"
"global_store_dwordx2 v60, v[62:63], s[42:43] sc1\n\t"
"s_mov_b64 exec, s[76:77]\n\t"
"s_nop 1\n\t"
"LEP44_%=:\n\t"
"s_waitcnt vmcnt(0)\n\t"
"v_lshlrev_b32 v192, 16, v160\n\t"
"v_and_b32 v193, 0xffff0000, v160\n\t"
"v_lshlrev_b32 v194, 16, v161\n\t"
"v_and_b32 v195, 0xffff0000, v161\n\t"
"v_lshlrev_b32 v196, 16, v162\n\t"
"v_and_b32 v197, 0xffff0000, v162\n\t"
"v_lshlrev_b32 v198, 16, v163\n\t"
"v_and_b32 v199, 0xffff0000, v163\n\t"
"v_lshlrev_b32 v200, 16, v164\n\t"
"v_and_b32 v201, 0xffff0000, v164\n\t"
"v_add_f32 v192, v192, v200\n\t"
"v_add_f32 v193, v193, v201\n\t"
"v_lshlrev_b32 v202, 16, v165\n\t"
"v_and_b32 v203, 0xffff0000, v165\n\t"
"v_add_f32 v194, v194, v202\n\t"
"v_add_f32 v195, v195, v203\n\t"
"v_lshlrev_b32 v200, 16, v166\n\t"
"v_and_b32 v201, 0xffff0000, v166\n\t"
"v_add_f32 v196, v196, v200\n\t"
"v_add_f32 v197, v197, v201\n\t"
"v_lshlrev_b32 v202, 16, v167\n\t"
"v_and_b32 v203, 0xffff0000, v167\n\t"
"v_add_f32 v198, v198, v202\n\t"
"v_add_f32 v199, v199, v203\n\t"
"v_lshlrev_b32 v200, 16, v168\n\t"
"v_and_b32 v201, 0xffff0000, v168\n\t"
"v_add_f32 v192, v192, v200\n\t"
"v_add_f32 v193, v193, v201\n\t"
"v_lshlrev_b32 v202, 16, v169\n\t"
"v_and_b32 v203, 0xffff0000, v169\n\t"
"v_add_f32 v194, v194, v202\n\t"
"v_add_f32 v195, v195, v203\n\t"
"v_lshlrev_b32 v200, 16, v170\n\t"
"v_and_b32 v201, 0xffff0000, v170\n\t"
"v_add_f32 v196, v196, v200\n\t"
"v_add_f32 v197, v197, v201\n\t"
"v_lshlrev_b32 v202, 16, v171\n\t"
"v_and_b32 v203, 0xffff0000, v171\n\t"
"v_add_f32 v198, v198, v202\n\t"
"v_add_f32 v199, v199, v203\n\t"
"v_lshlrev_b32 v200, 16, v172\n\t"
"v_and_b32 v201, 0xffff0000, v172\n\t"
"v_add_f32 v192, v192, v200\n\t"
"v_add_f32 v193, v193, v201\n\t"
"v_lshlrev_b32 v202, 16, v173\n\t"
"v_and_b32 v203, 0xffff0000, v173\n\t"
"v_add_f32 v194, v194, v202\n\t"
"v_add_f32 v195, v195, v203\n\t"
"v_lshlrev_b32 v200, 16, v174\n\t"
"v_and_b32 v201, 0xffff0000, v174\n\t"
"v_add_f32 v196, v196, v200\n\t"
"v_add_f32 v197, v197, v201\n\t"
"v_lshlrev_b32 v202, 16, v175\n\t"
"v_and_b32 v203, 0xffff0000, v175\n\t"
"v_add_f32 v198, v198, v202\n\t"
"v_add_f32 v199, v199, v203\n\t"
"v_lshlrev_b32 v200, 16, v176\n\t"
"v_and_b32 v201, 0xffff0000, v176\n\t"
"v_add_f32 v192, v192, v200\n\t"
"v_add_f32 v193, v193, v201\n\t"
"v_lshlrev_b32 v202, 16, v177\n\t"
"v_and_b32 v203, 0xffff0000, v177\n\t"
"v_add_f32 v194, v194, v202\n\t"
"v_add_f32 v195, v195, v203\n\t"
"v_lshlrev_b32 v200, 16, v178\n\t"
"v_and_b32 v201, 0xffff0000, v178\n\t"
"v_add_f32 v196, v196, v200\n\t"
"v_add_f32 v197, v197, v201\n\t"
"v_lshlrev_b32 v202, 16, v179\n\t"
"v_and_b32 v203, 0xffff0000, v179\n\t"
"v_add_f32 v198, v198, v202\n\t"
"v_add_f32 v199, v199, v203\n\t"
"v_lshlrev_b32 v200, 16, v180\n\t"
"v_and_b32 v201, 0xffff0000, v180\n\t"
"v_add_f32 v192, v192, v200\n\t"
"v_add_f32 v193, v193, v201\n\t"
"v_lshlrev_b32 v202, 16, v181\n\t"
"v_and_b32 v203, 0xffff0000, v181\n\t"
"v_add_f32 v194, v194, v202\n\t"
"v_add_f32 v195, v195, v203\n\t"
"v_lshlrev_b32 v200, 16, v182\n\t"
"v_and_b32 v201, 0xffff0000, v182\n\t"
"v_add_f32 v196, v196, v200\n\t"
"v_add_f32 v197, v197, v201\n\t"
"v_lshlrev_b32 v202, 16, v183\n\t"
"v_and_b32 v203, 0xffff0000, v183\n\t"
"v_add_f32 v198, v198, v202\n\t"
"v_add_f32 v199, v199, v203\n\t"
"v_lshlrev_b32 v200, 16, v184\n\t"
"v_and_b32 v201, 0xffff0000, v184\n\t"
"v_add_f32 v192, v192, v200\n\t"
"v_add_f32 v193, v193, v201\n\t"
"v_lshlrev_b32 v202, 16, v185\n\t"
"v_and_b32 v203, 0xffff0000, v185\n\t"
"v_add_f32 v194, v194, v202\n\t"
"v_add_f32 v195, v195, v203\n\t"
"v_lshlrev_b32 v200, 16, v186\n\t"
"v_and_b32 v201, 0xffff0000, v186\n\t"
"v_add_f32 v196, v196, v200\n\t"
"v_add_f32 v197, v197, v201\n\t"
"v_lshlrev_b32 v202, 16, v187\n\t"
"v_and_b32 v203, 0xffff0000, v187\n\t"
"v_add_f32 v198, v198, v202\n\t"
"v_add_f32 v199, v199, v203\n\t"
"v_lshlrev_b32 v200, 16, v188\n\t"
"v_and_b32 v201, 0xffff0000, v188\n\t"
"v_add_f32 v192, v192, v200\n\t"
"v_add_f32 v193, v193, v201\n\t"
"v_lshlrev_b32 v202, 16, v189\n\t"
"v_and_b32 v203, 0xffff0000, v189\n\t"
"v_add_f32 v194, v194, v202\n\t"
"v_add_f32 v195, v195, v203\n\t"
"v_lshlrev_b32 v200, 16, v190\n\t"
"v_and_b32 v201, 0xffff0000, v190\n\t"
"v_add_f32 v196, v196, v200\n\t"
"v_add_f32 v197, v197, v201\n\t"
"v_lshlrev_b32 v202, 16, v191\n\t"
"v_and_b32 v203, 0xffff0000, v191\n\t"
"v_add_f32 v198, v198, v202\n\t"
"v_add_f32 v199, v199, v203\n\t"
"s_lshl_b32 s68, s74, 19\n\t"
"s_lshl_b32 s69, s71, 16\n\t"
"s_add_u32 s68, s68, s69\n\t"
"s_lshl_b32 s69, s72, 9\n\t"
"s_add_u32 s68, s68, s69\n\t"
"s_lshl_b32 s69, s39, 7\n\t"
"s_add_u32 s68, s68, s69\n\t"
"buffer_store_dwordx4 v[192:195], v16, s[56:59], s68 offen offset:0\n\t"
"buffer_store_dwordx4 v[196:199], v16, s[56:59], s68 offen offset:64\n\t"
"s_waitcnt vmcnt(0) lgkmcnt(0)\n\t"
".Lmega_end:\n\t"
    :: "s"(ka), "s"(b), "s"(wid), "s"(32), "{v[212:227]}"(hr), "{v[228:243]}"(hi), "{v[244:245]}"(cc), "{v[246:253]}"(cn)
    :
"v0", "v1", "v2", "v3", "v4", "v5", "v6", "v7", "v8", "v9", "v10", "v11", "v12", "v13", "v14", "v15", "v16", "v17", "v18", "v19", "v20", "v21", "v22", "v23", "v24", "v25", "v26", "v27", "v28", "v29", "v30", "v31", "v32", "v33", "v34", "v35", "v36", "v37", "v38", "v39", "v40", "v41", "v42", "v43", "v44", "v45", "v46", "v47", "v48", "v49", "v50", "v51", "v52", "v53", "v54", "v55", "v56", "v57", "v58", "v59", "v60", "v61", "v62", "v63", "v64", "v65", "v66", "v67", "v68", "v69", "v70", "v71", "v72", "v73", "v74", "v75", "v76", "v77", "v78", "v79", "v80", "v81", "v82", "v83", "v84", "v85", "v86", "v87", "v88", "v89", "v90", "v91", "v92", "v93", "v94", "v95", "v96", "v97", "v98", "v99", "v100", "v101", "v102", "v103", "v104", "v105", "v106", "v107", "v108", "v109", "v110", "v111", "v112", "v113", "v114", "v115", "v116", "v117", "v118", "v119", "v120", "v121", "v122", "v123", "v124", "v125", "v126", "v127", "v128", "v129", "v130", "v131", "v132", "v133", "v134", "v135", "v136", "v137", "v138", "v139", "v140", "v141", "v142", "v143", "v144", "v145", "v146", "v147", "v148", "v149", "v150", "v151", "v152", "v153", "v154", "v155", "v156", "v157", "v158", "v159", "v160", "v161", "v162", "v163", "v164", "v165", "v166", "v167", "v168", "v169", "v170", "v171", "v172", "v173", "v174", "v175", "v176", "v177", "v178", "v179", "v180", "v181", "v182", "v183", "v184", "v185", "v186", "v187", "v188", "v189", "v190", "v191", "v192", "v193", "v194", "v195", "v196", "v197", "v198", "v199", "v200", "v201", "v202", "v203", "v204", "v205", "v206", "v207", "v208", "v209", "v210", "v211", "a0", "a1", "a2", "a3", "a4", "a5", "a6", "a7", "a8", "a9", "a10", "a11", "a12", "a13", "a14", "a15", "a16", "a17", "a18", "a19", "a20", "a21", "a22", "a23", "a24", "a25", "a26", "a27", "a28", "a29", "a30", "a31", "a32", "a33", "a34", "a35", "a36", "a37", "a38", "a39", "a40", "a41", "a42", "a43", "a44", "a45", "a46", "a47", "a48", "a49", "a50", "a51", "a52", "a53", "a54", "a55", "a56", "a57", "a58", "a59", "a60", "a61", "a62", "a63", "a64", "a65", "a66", "a67", "a68", "a69", "a70", "a71", "a72", "a73", "a74", "a75", "a76", "a77", "a78", "a79", "a80", "a81", "a82", "a83", "a84", "a85", "a86", "a87", "a88", "a89", "a90", "a91", "a92", "a93", "a94", "a95", "a96", "a97", "a98", "a99", "a100", "a101", "a102", "a103", "a104", "a105", "a106", "a107", "a108", "a109", "a110", "a111", "a112", "a113", "a114", "a115", "a116", "a117", "a118", "a119", "a120", "a121", "a122", "a123", "a124", "a125", "a126", "a127", "a128", "a129", "a130", "a131", "a132", "a133", "a134", "a135", "a136", "a137", "a138", "a139", "a140", "a141", "a142", "a143", "a144", "a145", "a146", "a147", "a148", "a149", "a150", "a151", "a152", "a153", "a154", "a155", "a156", "a157", "a158", "a159", "a160", "a161", "a162", "a163", "a164", "a165", "a166", "a167", "a168", "a169", "a170", "a171", "a172", "a173", "a174", "a175", "a176", "a177", "a178", "a179", "a180", "a181", "a182", "a183", "a184", "a185", "a186", "a187", "a188", "a189", "a190", "a191", "a192", "a193", "a194", "a195", "a196", "a197", "a198", "a199", "a200", "a201", "a202", "a203", "a204", "a205", "a206", "a207", "a208", "a209", "a210", "a211", "a212", "a213", "a214", "a215", "a216", "a217", "a218", "a219", "a220", "a221", "a222", "a223", "a224", "a225", "a226", "a227", "a228", "a229", "a230", "a231", "a232", "a233", "a234", "a235", "a236", "a237", "a238", "a239", "a240", "a241", "a242", "a243", "a244", "a245", "a246", "a247", "a248", "a249", "a250", "a251", "a252", "a253", "a254", "a255", "s36", "s37", "s38", "s39", "s40", "s41", "s42", "s43", "s44", "s45", "s46", "s47", "s48", "s49", "s50", "s51", "s52", "s53", "s54", "s55", "s56", "s57", "s58", "s59", "s60", "s61", "s62", "s63", "s64", "s65", "s66", "s67", "s68", "s69", "s70", "s71", "s72", "s73", "s74", "s75", "s76", "s77", "s78", "s79", "s80", "s81", "s82", "s83", "s84", "s85", "s86", "s87", "s88", "s89", "s90", "s91", "s92", "s93", "s94", "s95", "s96", "s97", "s98", "s99", "s26", "s27", "s28", "s29", "s30", "s31", "vcc", "scc", "memory"
    , "v254", "v255"
  );
}
extern "C" void kernel_launch(void* const* d_in, const int* in_sizes, int n_in, void* d_out, int out_size, void* d_ws, size_t ws_size, hipStream_t stream) {
  Params p;
  for (int i = 0; i < 15; ++i) p.p[i] = d_in[i];
#define WS_SHIFT_MB 0
  d_ws = (char*)d_ws + ((size_t)WS_SHIFT_MB << 20);
  p.p[15] = d_out; p.p[16] = d_ws; p.p[17] = d_ws;
  (void)hipFuncSetAttribute((const void*)mega_kernel, hipFuncAttributeMaxDynamicSharedMemorySize, 163840);
  mega_kernel<<<256, 256, 163840, stream>>>(p);
}
```
